```python
import torch

def _softmax(x, axis=-1):
    return torch.softmax(x, dim=axis)
def _gelu(x):
    return torch.nn.functional.gelu(x, approximate="tanh")
def _silu(x):
    return torch.nn.functional.silu(x)
def _sigmoid(x):
    return torch.sigmoid(x)
def _layernorm(x, axis=-1, eps=1e-5):
    m = x.mean(dim=axis, keepdim=True)
    v = x.var(dim=axis, keepdim=True, unbiased=False)
    return (x - m) / torch.sqrt(v + eps)
def _scatter_add(tgt, idx, src, axis=0):
    return tgt.clone().index_add_(axis, idx, src)
def _segment_sum(src, seg, n):
    out = torch.zeros((n,) + src.shape[1:], dtype=src.dtype)
    return out.index_add_(0, seg, src)
def _pad(x, pw):
    flat = tuple(v for pair in reversed(pw) for v in pair)
    return torch.nn.functional.pad(x, flat)

kernel_name = 'parallel_sum__compressed_kv_cla__mixer_token_mlp__prompt'

def setup_inputs():

    _w = (lambda a: (lambda t: t.to(torch.bfloat16) if t.is_floating_point() else t)(torch.as_tensor(a)))
    def _A_setup():
        B, H, Sq, Sk, D, M = 1, 32, 2048, 2048, 128, 640
        Dc = 4 * D
        return {
            "x": _w((lambda *s: torch.randn(*s, dtype=torch.bfloat16) * 0.05)(B, Sq, M)),
            "c_kv": _w((lambda *s: torch.randn(*s, dtype=torch.bfloat16) * 0.2)(B, Sk, Dc)),
            "w_q": _w((lambda *s: torch.randn(*s, dtype=torch.bfloat16) * 6.4)(M, H, D) * (M ** -0.5)),
            "w_uk": _w((lambda *s: torch.randn(*s, dtype=torch.bfloat16) * 6.4)(Dc, H, D) * (Dc ** -0.5)),
            "w_uv": _w((lambda *s: torch.randn(*s, dtype=torch.bfloat16) * 0.2)(Dc, H, D) * (Dc ** -0.5)),
            "w_o": _w((lambda *s: torch.randn(*s, dtype=torch.bfloat16) * 0.2)(H, D, M) * ((H * D) ** -0.5)),
        }
    _A_in = _A_setup()

    def _B_setup():
        B, S, D, F = 1, 2048, 640, 1728
        return {
            "x": _w((lambda *s: torch.randn(*s, dtype=torch.bfloat16) * 0.05)(B, S, D)),
            "w1": _w((lambda *s: torch.randn(*s, dtype=torch.bfloat16) * 0.4)(S, F) * (S ** -0.5)),
            "b1": _w((lambda *s: torch.randn(*s, dtype=torch.bfloat16) * 3.2)(F) * 0.02),
            "w2": _w((lambda *s: torch.randn(*s, dtype=torch.bfloat16) * 0.2)(F, S) * (F ** -0.5)),
            "b2": _w((lambda *s: torch.randn(*s, dtype=torch.bfloat16) * 0.8)(S) * 0.02),
        }
    _B_in = _B_setup()

    return {
        "A_x": _A_in["x"],
        "A_c_kv": _A_in["c_kv"],
        "A_w_q": _A_in["w_q"],
        "A_w_uk": _A_in["w_uk"],
        "A_w_uv": _A_in["w_uv"],
        "A_w_o": _A_in["w_o"],
        "B_x": _B_in["x"],
        "B_w1": _B_in["w1"],
        "B_b1": _B_in["b1"],
        "B_w2": _B_in["w2"],
        "B_b2": _B_in["b2"],
    }

def reference(A_x, A_c_kv, A_w_q, A_w_uk, A_w_uv, A_w_o, B_x, B_w1, B_b1, B_w2, B_b2):
    def _A_ref(x, c_kv, w_q, w_uk, w_uv, w_o):
        B, Sq, M = x.shape[0], x.shape[1], x.shape[2]
        Sk = c_kv.shape[1]
        H, D = w_q.shape[1], w_q.shape[2]
        q = torch.einsum("bqm,mhd->bhqd", x, w_q)
        k = torch.einsum("bkc,chd->bhkd", c_kv, w_uk)
        v = torch.einsum("bkc,chd->bhkd", c_kv, w_uv)
        scores = torch.einsum("bhqd,bhkd->bhqk", q, k) * (D ** -0.5)
        off = Sk - Sq
        qi = torch.arange(Sq)[:, None]
        kj = torch.arange(Sk)[None, :]
        cmask = kj <= (qi + off)
        scores = torch.where(cmask[None, None], scores, -1e9)
        p = _softmax(scores, axis=-1)
        ctx = torch.einsum("bhqk,bhkd->bhqd", p, v)
        return torch.einsum("bhqd,hdm->bqm", ctx, w_o)

    def _B_ref(x, w1, b1, w2, b2):
        B, S, D = x.shape
        h = _layernorm(x, axis=-1)
        u = torch.einsum("bsd,sf->bdf", h, w1) + b1
        u = _gelu(u)
        y = torch.einsum("bdf,fs->bds", u, w2) + b2
        return (lambda x, *d: x.permute(*d))(y, 0, 2, 1)

    x = A_x
    xn = _layernorm(x, axis=-1)
    return x + _A_ref(xn, A_c_kv, A_w_q, A_w_uk, A_w_uv, A_w_o) + _B_ref(xn, B_w1, B_b1, B_w2, B_b2)
```

```cpp
#include <hip/hip_runtime.h>
#include <hip/hip_bf16.h>
#include <stdint.h>

typedef unsigned short u16;
typedef short bf16x8 __attribute__((ext_vector_type(8)));
typedef short s16x4 __attribute__((ext_vector_type(4)));
typedef float f32x4 __attribute__((ext_vector_type(4)));
typedef float f32x16 __attribute__((ext_vector_type(16)));
typedef unsigned u32x4 __attribute__((ext_vector_type(4)));
typedef unsigned u32x2 __attribute__((ext_vector_type(2)));
#define LAS __attribute__((address_space(3)))

constexpr int S_ = 2048, M_ = 640, DC_ = 512, H_ = 32, D_ = 128, F_ = 1728, HD_ = 4096;

__device__ __forceinline__ float bf2f(unsigned b) { return __uint_as_float(b << 16); }
__device__ __forceinline__ unsigned cvt_pk(float lo, float hi) {
    unsigned r; asm volatile("v_cvt_pk_bf16_f32 %0, %1, %2" : "=v"(r) : "v"(lo), "v"(hi)); return r;
}
__device__ __forceinline__ float rbf(float x) { return __uint_as_float(cvt_pk(x, x) << 16); }
__device__ __forceinline__ bf16x8 cat4(s16x4 a, s16x4 b) { return __builtin_shufflevector(a, b, 0, 1, 2, 3, 4, 5, 6, 7); }
__device__ __forceinline__ s16x4 tr_read(const LAS unsigned char* p) { return __builtin_amdgcn_ds_read_tr16_b64_v4i16((LAS s16x4*)p); }

__device__ __forceinline__ int lane_id() { int l; asm volatile("v_mbcnt_lo_u32_b32 %0, -1, 0\n\tv_mbcnt_hi_u32_b32 %0, -1, %0" : "=v"(l)); return l; }
#define TRRD(dst, addr, off) asm volatile("ds_read_b64_tr_b16 %0, %1 offset:%2" : "=v"(dst) : "v"(addr), "i"(off) : "memory")
#define WAIT_L0_8(a, b, c, d, e, f, g, h) asm volatile("s_waitcnt lgkmcnt(0)" : "+v"(a), "+v"(b), "+v"(c), "+v"(d), "+v"(e), "+v"(f), "+v"(g), "+v"(h) :: "memory")
#define WAIT_V(n) asm volatile("s_waitcnt vmcnt(" #n ")" ::: "memory")
#define WAIT_L(n) asm volatile("s_waitcnt lgkmcnt(" #n ")" ::: "memory")
#define BAR __builtin_amdgcn_s_barrier()
#define SCHED __builtin_amdgcn_sched_barrier(0)
#define GLDS16(gptr, ldsptr) __builtin_amdgcn_global_load_lds((const unsigned*)(gptr), (LAS unsigned*)(ldsptr), 16, 0, 0)

__device__ __forceinline__ float wave_sum(float v) {
#pragma unroll
    for (int o = 32; o >= 1; o >>= 1) v += __shfl_xor(v, o);
    return v;
}
__device__ __forceinline__ void ln10(float (&v)[10]) {
    float s = 0.f;
#pragma unroll
    for (int i = 0; i < 10; ++i) s += v[i];
    const float mean = wave_sum(s) * (1.0f / 640.0f);
    float q = 0.f;
#pragma unroll
    for (int i = 0; i < 10; ++i) { const float d = v[i] - mean; q += d * d; }
    const float var = wave_sum(q) * (1.0f / 640.0f);
    const float mb = rbf(mean), vb = rbf(var);
    const float den = rbf(sqrtf(rbf(vb + 1e-5f)));
#pragma unroll
    for (int i = 0; i < 10; ++i) v[i] = rbf(rbf(v[i] - mb) / den);
}
__global__ __launch_bounds__(256) void k_ln(const u16* __restrict__ x, u16* __restrict__ xn, u16* __restrict__ hT) {
    __shared__ u16 tile[16][648];
    const int wave = threadIdx.x >> 6, lane = threadIdx.x & 63, row0 = blockIdx.x * 16;
#pragma unroll
    for (int rr = 0; rr < 4; ++rr) {
        const int r = wave * 4 + rr, row = row0 + r;
        float v[10];
#pragma unroll
        for (int i = 0; i < 5; ++i) { const unsigned w = *(const unsigned*)(x + (size_t)row * M_ + i * 128 + lane * 2); v[2 * i] = bf2f(w & 0xffffu); v[2 * i + 1] = bf2f(w >> 16); }
        ln10(v);
#pragma unroll
        for (int i = 0; i < 5; ++i) *(unsigned*)(xn + (size_t)row * M_ + i * 128 + lane * 2) = cvt_pk(v[2 * i], v[2 * i + 1]);
        ln10(v);
#pragma unroll
        for (int i = 0; i < 5; ++i) *(unsigned*)(&tile[r][i * 128 + lane * 2]) = cvt_pk(v[2 * i], v[2 * i + 1]);
    }
    __syncthreads();
#pragma unroll
    for (int i = 0; i < 5; ++i) {
        const int c = threadIdx.x + i * 256, d = c >> 1, half = c & 1;
        u32x4 w;
#pragma unroll
        for (int e = 0; e < 4; ++e) w[e] = (unsigned)tile[half * 8 + 2 * e][d] | ((unsigned)tile[half * 8 + 2 * e + 1][d] << 16);
        *(u32x4*)(hT + (size_t)d * S_ + row0 + half * 8) = w;
    }
}

namespace g1 {
constexpr int HTB = 128 * 64 * 2, STAGE_BYTES = 8 * HTB;
__device__ __forceinline__ int lds_byte(int r, int c) { const int st = (r >> 4) * 2 + (c >> 5), rr = r & 15, cc = c & 31, ob = rr * 64 + cc * 2; return st * 1024 + (ob ^ (((ob >> 9) & 1) << 5)); }
__device__ __forceinline__ void stage_rc(int b, int& R, int& C) { const int st = b / 1024, sb = b % 1024, swz = sb ^ (((sb >> 9) & 1) << 5); R = (st >> 1) * 16 + swz / 64; C = (st & 1) * 32 + (swz % 64) / 2; }
struct Unit { const char* a; const char* b; unsigned lda2, ldb2; int nt, type, m0, n0, ks; };
struct Ptrs { const u16 *xn, *ckv, *wq, *wuk, *wuv, *hT, *w1; };
__device__ __forceinline__ bool get_unit(int c, int i, const Ptrs& P, Unit& u) {
    const int xcd = c & 7, j = c >> 3;
    int kind, idx;
    if (j < 16) { if (i == 0) { kind = 0; idx = j; } else if (i == 1) { kind = 1; idx = j; } else return false; }
    else { if (i == 0) { kind = 1; idx = j; } else if (i == 1) { kind = 2; idx = (j - 16) * 8 + xcd; if (idx >= 84) return false; } else return false; }
    if (kind == 0) {
        const int pm = (xcd & 1) * 4 + (idx & 3), pn = (xcd >> 1) * 4 + (idx >> 2);
        u.m0 = pm * 256; u.n0 = pn * 256; u.lda2 = M_ * 2; u.ldb2 = HD_ * 2; u.nt = 10; u.type = 0; u.ks = 0;
        u.a = (const char*)P.xn + (size_t)u.m0 * (M_ * 2); u.b = (const char*)P.wq + (size_t)u.n0 * 2;
    } else if (kind == 1) {
        const int pm = (xcd & 1) * 4 + (idx & 3), pn = (xcd >> 1) * 8 + (idx >> 2);
        u.m0 = pm * 256; u.n0 = (pn & 15) * 256; u.lda2 = DC_ * 2; u.ldb2 = HD_ * 2; u.nt = 8; u.type = 1 + (pn >> 4); u.ks = 0;
        u.a = (const char*)P.ckv + (size_t)u.m0 * (DC_ * 2); u.b = (const char*)((pn >> 4) ? P.wuv : P.wuk) + (size_t)u.n0 * 2;
    } else {
        const int tile = idx % 21, ks = idx / 21, mi = tile % 3, ni = tile / 3;
        u.m0 = mi == 2 ? 384 : mi * 256; u.n0 = ni == 6 ? 1472 : ni * 256; u.lda2 = S_ * 2; u.ldb2 = F_ * 2; u.nt = 8; u.type = 3; u.ks = ks;
        u.a = (const char*)P.hT + (size_t)u.m0 * (S_ * 2) + (size_t)ks * 512 * 2; u.b = (const char*)P.w1 + (size_t)ks * 512 * (F_ * 2) + (size_t)u.n0 * 2;
    }
    return true;
}
}

__global__ __launch_bounds__(512, 2) void k_gemm1(const u16* __restrict__ xn, const u16* __restrict__ ckv, const u16* __restrict__ wq, const u16* __restrict__ wuk,
                                                  const u16* __restrict__ wuv, const u16* __restrict__ hT, const u16* __restrict__ w1,
                                                  u16* __restrict__ qo, u16* __restrict__ ko, u16* __restrict__ vo, float* __restrict__ upart) {
    using namespace g1;
    extern __shared__ __attribute__((aligned(16))) unsigned char shm_[];
    LAS unsigned char* lds = (LAS unsigned char*)shm_;
    const int tid = threadIdx.x, wid = __builtin_amdgcn_readfirstlane(tid >> 6), lane = tid & 63, wr = wid >> 2, wc = wid & 3, fr = lane & 15, fq = lane >> 4;
    const Ptrs P{xn, ckv, wq, wuk, wuv, hT, w1};
    int sR, sC; stage_rc(tid * 16, sR, sC);
    const int bK = tid >> 4, bCh = (tid & 15) ^ (((bK & 3) << 2) | ((bK >> 2) & 3));
    const unsigned ldsw = (unsigned)wid * 1024u;
    const int aoff = lds_byte(wr * 64 + fr, fq * 8);
    int boff[2][2];
    { const int q = (lane & 15) >> 2, p = lane & 3;
#pragma unroll
      for (int n = 0; n < 2; ++n)
#pragma unroll
        for (int t = 0; t < 2; ++t) boff[n][t] = (int)(uintptr_t)lds + 4 * HTB + 256 * (8 * fq + 4 * t + q) + 16 * ((4 * wc + 2 * n + (p >> 1)) ^ ((q << 2) | ((2 * fq + t) & 3))) + 8 * (p & 1); }
#define G1_SA(b, h) (((b) * 2 + (h)) * HTB)
#define G1_SB(b, h) ((4 + (b) * 2 + (h)) * HTB)
#define G1_STAGE(bufoff, gbase, voff, step2) do { GLDS16((const char*)(gbase) + (voff), lds + (bufoff) + ldsw); GLDS16((const char*)(gbase) + (step2) + (voff), lds + (bufoff) + ldsw + 8192); } while (0)
#define G1_LDA(dst, b, h) do { _Pragma("unroll") for (int m = 0; m < 4; ++m) _Pragma("unroll") for (int k = 0; k < 2; ++k) dst[m][k] = *(const LAS bf16x8*)(lds + G1_SA(b, h) + aoff + m * 2048 + k * 1024); } while (0)
#define G1_LDB(dst, b, h) do { _Pragma("unroll") for (int n = 0; n < 2; ++n) _Pragma("unroll") for (int k = 0; k < 2; ++k) { \
        TRRD(dst[n][k][0], boff[n][0], ((b) * 2 + (h)) * HTB + k * 8192); TRRD(dst[n][k][1], boff[n][1], ((b) * 2 + (h)) * HTB + k * 8192); } } while (0)
#define G1_WB(dst) WAIT_L0_8(dst[0][0][0], dst[0][0][1], dst[0][1][0], dst[0][1][1], dst[1][0][0], dst[1][0][1], dst[1][1][0], dst[1][1][1])
#define G1_MMA(ai, bj, At, Bt) do { __builtin_amdgcn_s_setprio(1); _Pragma("unroll") for (int m = 0; m < 4; ++m) _Pragma("unroll") for (int n = 0; n < 2; ++n) _Pragma("unroll") for (int k = 0; k < 2; ++k) \
        acc[ai][bj][m][n] = __builtin_amdgcn_mfma_f32_16x16x32_bf16(cat4(Bt[n][k][0], Bt[n][k][1]), At[m][k], acc[ai][bj][m][n], 0, 0, 0); __builtin_amdgcn_s_setprio(0); } while (0)
    Unit cur, nxt; int ui = 0;
    if (!get_unit(blockIdx.x, 0, P, cur)) return;
    f32x4 acc[2][2][4][2];
#pragma unroll
    for (int a = 0; a < 2; ++a)
#pragma unroll
        for (int b = 0; b < 2; ++b)
#pragma unroll
            for (int m = 0; m < 4; ++m)
#pragma unroll
                for (int n = 0; n < 2; ++n) acc[a][b][m][n] = (f32x4){0.f, 0.f, 0.f, 0.f};
    bf16x8 At[4][2]; s16x4 B0[2][2][2], B1[2][2][2];
    unsigned vAc = (unsigned)sR * cur.lda2 + (unsigned)sC * 2u, vBc = (unsigned)bK * cur.ldb2 + (unsigned)bCh * 16u;
    {
        const size_t a64 = (size_t)64 * cur.lda2, a128 = (size_t)128 * cur.lda2, b32 = (size_t)32 * cur.ldb2, bk = (size_t)64 * cur.ldb2;
        G1_STAGE(G1_SB(0, 0), cur.b, vBc, b32); G1_STAGE(G1_SA(0, 0), cur.a, vAc, a64); G1_STAGE(G1_SB(0, 1), cur.b + 256, vBc, b32); G1_STAGE(G1_SA(0, 1), cur.a + a128, vAc, a64);
        if (wr == 1) BAR;
        WAIT_V(4); BAR;
        G1_STAGE(G1_SB(1, 0), cur.b + bk, vBc, b32); G1_STAGE(G1_SA(1, 0), cur.a + 128, vAc, a64); G1_STAGE(G1_SB(1, 1), cur.b + bk + 256, vBc, b32);
        WAIT_V(6); BAR;
    }
    for (;;) {
        const bool has_next = get_unit(blockIdx.x, ui + 1, P, nxt);
        if (!has_next) nxt = cur;
        const unsigned vAn = (unsigned)sR * nxt.lda2 + (unsigned)sC * 2u, vBn = (unsigned)bK * nxt.ldb2 + (unsigned)bCh * 16u;
        const int nt = cur.nt;
        for (int t = 0; t < nt; t += 2) {
            const bool last = (t == nt - 2);
            const size_t ca64 = (size_t)64 * cur.lda2, ca128 = (size_t)128 * cur.lda2;
            const char* a1 = cur.a + (size_t)(t + 1) * 128;
            const unsigned l_a = last ? nxt.lda2 : cur.lda2, l_b = last ? nxt.ldb2 : cur.ldb2;
            const size_t a64 = (size_t)64 * l_a, a128 = (size_t)128 * l_a, b32 = (size_t)32 * l_b, bk = (size_t)64 * l_b;
            const char* a2 = last ? nxt.a : cur.a + (size_t)(t + 2) * 128;
            const char* b2 = last ? nxt.b : cur.b + (size_t)(t + 2) * bk;
            const char* a3 = a2 + 128; const char* b3 = b2 + bk;
            const unsigned vA = last ? vAn : vAc, vB = last ? vBn : vBc;
            G1_LDB(B0, 0, 0); SCHED; G1_LDA(At, 0, 0); G1_STAGE(G1_SA(1, 1), a1 + ca128, vAc, ca64);
            WAIT_L(8); BAR; G1_WB(B0); G1_MMA(0, 0, At, B0); BAR; SCHED;
            G1_LDB(B1, 0, 1); G1_STAGE(G1_SB(0, 0), b2, vB, b32);
            BAR; G1_WB(B1); G1_MMA(0, 1, At, B1); BAR;
            G1_LDA(At, 0, 1); G1_STAGE(G1_SA(0, 0), a2, vA, a64);
            BAR; WAIT_L(0); G1_MMA(1, 0, At, B0); BAR; SCHED;
            G1_STAGE(G1_SB(0, 1), b2 + 256, vB, b32);
            WAIT_V(6); BAR; G1_MMA(1, 1, At, B1); BAR;
            G1_LDB(B0, 1, 0); SCHED; G1_LDA(At, 1, 0); G1_STAGE(G1_SA(0, 1), a2 + a128, vA, a64);
            WAIT_L(8); BAR; G1_WB(B0); G1_MMA(0, 0, At, B0); BAR; SCHED;
            G1_LDB(B1, 1, 1); G1_STAGE(G1_SB(1, 0), b3, vB, b32);
            BAR; G1_WB(B1); G1_MMA(0, 1, At, B1); BAR;
            G1_LDA(At, 1, 1); G1_STAGE(G1_SA(1, 0), a3, vA, a64);
            BAR; WAIT_L(0); G1_MMA(1, 0, At, B0); BAR; SCHED;
            G1_STAGE(G1_SB(1, 1), b3 + 256, vB, b32);
            WAIT_V(6); BAR; G1_MMA(1, 1, At, B1); BAR;
        }
        if (cur.type < 3) {
            u16* ob = cur.type == 0 ? qo : (cur.type == 1 ? ko : vo);
            const float sc = cur.type == 0 ? 0.08838834764831845f * 1.4426950408889634f : 1.0f;
#pragma unroll
            for (int ai = 0; ai < 2; ++ai)
#pragma unroll
                for (int bj = 0; bj < 2; ++bj) {
                    u16* hb = ob + ((size_t)((cur.n0 >> 7) + bj) * S_ + cur.m0 + ai * 128 + wr * 64 + fr) * D_ + wc * 32 + fq * 4;
#pragma unroll
                    for (int m = 0; m < 4; ++m)
#pragma unroll
                        for (int n = 0; n < 2; ++n) { const f32x4 v = acc[ai][bj][m][n] * sc; u32x2 w; w.x = cvt_pk(v[0], v[1]); w.y = cvt_pk(v[2], v[3]); *(u32x2*)(hb + (size_t)m * 16 * D_ + n * 16) = w; }
                }
        } else {
            float* ub = upart + ((size_t)cur.ks * M_ + cur.m0 + wr * 64 + fr) * F_ + cur.n0 + wc * 32 + fq * 4;
#pragma unroll
            for (int ai = 0; ai < 2; ++ai)
#pragma unroll
                for (int bj = 0; bj < 2; ++bj)
#pragma unroll
                    for (int m = 0; m < 4; ++m)
#pragma unroll
                        for (int n = 0; n < 2; ++n) *(f32x4*)(ub + (size_t)(ai * 128 + m * 16) * F_ + bj * 128 + n * 16) = acc[ai][bj][m][n];
        }
        if (!has_next) break;
#pragma unroll
        for (int a = 0; a < 2; ++a)
#pragma unroll
            for (int b = 0; b < 2; ++b)
#pragma unroll
                for (int m = 0; m < 4; ++m)
#pragma unroll
                    for (int n = 0; n < 2; ++n) acc[a][b][m][n] = (f32x4){0.f, 0.f, 0.f, 0.f};
        cur = nxt; vAc = vAn; vBc = vBn; ++ui;
    }
    WAIT_V(0);
    if (wr == 0) BAR;
    BAR;
#undef G1_SA
#undef G1_SB
#undef G1_STAGE
#undef G1_LDA
#undef G1_LDB
#undef G1_WB
#undef G1_MMA
}

namespace at {
constexpr int KT = 16384, KOFF = 0, VOFF = 65536, XOFF = 131072, LDS_BYTES = 131072 + 4096;
constexpr float THR = 8.f;
__device__ __forceinline__ int crow(int r, int hi) { return (r & 3) + 8 * (r >> 2) + 4 * hi; }
}
__global__ __launch_bounds__(512, 2) void k_attn(const u16* __restrict__ Q, const u16* __restrict__ K, const u16* __restrict__ V, u16* __restrict__ ctxT,
                                                 const float* __restrict__ upart, const u16* __restrict__ b1, u16* __restrict__ gT) {
    using namespace at;
    extern __shared__ __attribute__((aligned(16))) unsigned char shm_[];
    LAS unsigned char* lds = (LAS unsigned char*)shm_;
    const int tid = threadIdx.x, wid0 = __builtin_amdgcn_readfirstlane(tid >> 6);
    for (int tile = blockIdx.x; tile < 270; tile += 256) {
        const int d0 = (tile % 10) * 64, f0 = (tile / 10) * 64;
        LAS u16* tl = (LAS u16*)lds;
        const int dr = tid >> 3, fc = (tid & 7) * 8;
        f32x4 s0 = {0.f, 0.f, 0.f, 0.f}, s1 = {0.f, 0.f, 0.f, 0.f};
#pragma unroll
        for (int ks = 0; ks < 4; ++ks) { const float* p = upart + ((size_t)ks * M_ + d0 + dr) * F_ + f0 + fc; s0 += *(const f32x4*)p; s1 += *(const f32x4*)(p + 4); }
        const u32x4 bw = *(const u32x4*)(b1 + f0 + fc);
#pragma unroll
        for (int e = 0; e < 8; ++e) {
            const float uu = rbf(e < 4 ? s0[e] : s1[e - 4]);
            const float bb = bf2f((e & 1) ? (bw[e >> 1] >> 16) : (bw[e >> 1] & 0xffffu));
            const float xg = rbf(uu + bb);
            const float inner = 0.7978845608028654f * (xg + 0.044715f * xg * xg * xg);
            const float g = 0.5f * xg * (1.0f + tanhf(inner));
            tl[(fc + e) * 72 + dr] = (u16)(cvt_pk(g, g) & 0xffffu);
        }
        __syncthreads();
        { const int fr_ = tid >> 3, dc = (tid & 7) * 8; const u32x4 w = *(const LAS u32x4*)(tl + fr_ * 72 + dc); *(u32x4*)(gT + (size_t)(f0 + fr_) * M_ + d0 + dc) = w; }
        __syncthreads();
    }
    const int c_ = blockIdx.x, xcd = c_ & 7, jj = c_ >> 3, head = xcd * 4 + (jj >> 3), pi = jj & 7;
    const char* Kh = (const char*)(K + (size_t)head * S_ * D_);
    const char* Vh = (const char*)(V + (size_t)head * S_ * D_);
    const u16* Qh = Q + (size_t)head * S_ * D_;
#define AT_STAGE_K(buf, t) do { const char* g_ = Kh + (size_t)(t) * 32768; _Pragma("unroll") for (int i_ = 0; i_ < 4; ++i_) GLDS16(g_ + i_ * 4096 + voffK, lds + KOFF + (buf) * 32768 + ldst + i_ * 4096); } while (0)
#define AT_STAGE_V(buf, t) do { const char* g_ = Vh + (size_t)(t) * 32768; _Pragma("unroll") for (int i_ = 0; i_ < 4; ++i_) GLDS16(g_ + i_ * 4096 + voffV, lds + VOFF + (buf) * 32768 + ldst + i_ * 4096); } while (0)

    for (int pass = 0; pass < 2; ++pass) {
        int wid = wid0; const int lane = lane_id();
        asm volatile("" : "+s"(wid));
        const int r32 = lane & 31, hi = lane >> 5, grp = wid >> 2, w4 = wid & 3;
        const unsigned voffK = (unsigned)((grp * 64 + w4 * 4 + (lane >> 4)) * 256 + (((lane & 15) ^ ((w4 * 4 + (lane >> 4)) & 15)) * 16));
        const unsigned voffV = (unsigned)((grp * 64 + (w4 >> 1) * 8 + ((lane >> 2) & 7)) * 256 + (((w4 & 1) * 2 + (lane >> 5)) * 32 + (lane & 3) * 8) * 2);
        const unsigned ldst = (unsigned)(grp * KT + w4 * 1024);
        const int kb0 = (int)(uintptr_t)lds + KOFF + grp * KT + 256 * r32 + 16 * (hi ^ (r32 & 15));
        const int vb = (int)(uintptr_t)lds + VOFF + grp * KT + (4 * hi + ((lane & 15) >> 2)) * 64 + 32 * ((lane >> 4) & 1) + 8 * (lane & 3);
        LAS float* al_l = (LAS float*)(lds + XOFF) + wid * 32;
        LAS float* ml_l = (LAS float*)(lds + XOFF + 1024);
        const bool dead0 = grp == 1 && w4 < 2;
        const int qb = pass == 0 ? 15 - pi : pi, ns = qb + 1;
        bf16x8 qr[8];
#pragma unroll
        for (int d0 = 0; d0 < 8; ++d0) qr[d0] = *(const bf16x8*)(Qh + (size_t)(qb * 128 + w4 * 32 + r32) * D_ + d0 * 16 + hi * 8);
        AT_STAGE_K(0, qb);
        float m_reg = 0.f, l_reg = 0.f; bool first = true;
        f32x16 o[4] = {}; f32x16 negm = {};
        f32x16 pA0, pA1, pB0, pB1; float alA = 1.f, alB = 1.f; bf16x8 pa[4];
        WAIT_V(0); BAR;
#define AT_QK(P0, P1, KB) do { _Pragma("unroll") for (int d0 = 0; d0 < 8; ++d0) { int ka_; \
            if (d0 == 0) ka_ = kb0; else asm volatile("v_xor_b32 %0, %1, %2" : "=v"(ka_) : "i"(32 * d0), "v"(kb0)); \
            const bf16x8 k0_ = *(const LAS bf16x8*)(uintptr_t)(ka_ + (KB) * 32768); const bf16x8 k1_ = *(const LAS bf16x8*)(uintptr_t)(ka_ + (KB) * 32768 + 8192); \
            P0 = __builtin_amdgcn_mfma_f32_32x32x16_bf16(k0_, qr[d0], d0 == 0 ? negm : P0, 0, 0, 0); P1 = __builtin_amdgcn_mfma_f32_32x32x16_bf16(k1_, qr[d0], d0 == 0 ? negm : P1, 0, 0, 0); } } while (0)
#define AT_PV(VB) do { _Pragma("unroll") for (int c = 0; c < 4; ++c) { s16x4 v_[8]; \
            _Pragma("unroll") for (int s = 0; s < 8; ++s) TRRD(v_[s], vb, (VB) * 32768 + (s * 4 + c) * 512); \
            WAIT_L0_8(v_[0], v_[1], v_[2], v_[3], v_[4], v_[5], v_[6], v_[7]); \
            _Pragma("unroll") for (int s = 0; s < 4; ++s) o[c] = __builtin_amdgcn_mfma_f32_32x32x16_bf16(pa[s], cat4(v_[2 * s], v_[2 * s + 1]), o[c], 0, 0, 0); } } while (0)
#define AT_ROWMAX(P0, P1) \
            float pm_ = P0[0]; _Pragma("unroll") for (int r = 1; r < 16; ++r) pm_ = fmaxf(pm_, P0[r]); _Pragma("unroll") for (int r = 0; r < 16; ++r) pm_ = fmaxf(pm_, P1[r]); \
            { auto rr_ = __builtin_amdgcn_permlane32_swap(__float_as_uint(pm_), __float_as_uint(pm_), false, false); pm_ = fmaxf(__uint_as_float(rr_[0]), __uint_as_float(rr_[1])); }
#define AT_PSM(P0, P1, AL) do { AT_ROWMAX(P0, P1) \
            if (__builtin_expect(!first && __all(pm_ <= THR), 1)) { AL = 1.f; } \
            else { const float dl_ = first ? pm_ : fmaxf(pm_, 0.f); AL = first ? 1.f : __builtin_amdgcn_exp2f(-dl_); m_reg += dl_; first = false; \
                _Pragma("unroll") for (int r = 0; r < 16; ++r) { P0[r] -= dl_; P1[r] -= dl_; negm[r] = -m_reg; } } \
            _Pragma("unroll") for (int r = 0; r < 16; ++r) P0[r] = __builtin_amdgcn_exp2f(P0[r]); } while (0)
#define AT_FSM(P0, P1, AL) do { \
            _Pragma("unroll") for (int r = 0; r < 16; ++r) P1[r] = __builtin_amdgcn_exp2f(P1[r]); \
            float ps_ = 0.f; _Pragma("unroll") for (int r = 0; r < 16; ++r) ps_ += P0[r]; _Pragma("unroll") for (int r = 0; r < 16; ++r) ps_ += P1[r]; \
            l_reg = l_reg * AL + ps_; \
            _Pragma("unroll") for (int s = 0; s < 2; ++s) { u32x4 w0_, w1_; _Pragma("unroll") for (int e = 0; e < 4; ++e) { w0_[e] = cvt_pk(P0[8 * s + 2 * e], P0[8 * s + 2 * e + 1]); w1_[e] = cvt_pk(P1[8 * s + 2 * e], P1[8 * s + 2 * e + 1]); } \
                pa[s] = *reinterpret_cast<bf16x8*>(&w0_); pa[2 + s] = *reinterpret_cast<bf16x8*>(&w1_); } } while (0)
#define AT_RESC(AL) do { if (__any((AL) < 1.f)) { const int l_ = lane_id(), h_ = l_ >> 5; if (h_ == 0) al_l[l_] = (AL); WAIT_L(0); \
            _Pragma("unroll") for (int c = 0; c < 4; ++c) _Pragma("unroll") for (int r = 0; r < 16; ++r) o[c][r] *= al_l[crow(r, h_)]; } } while (0)
#define AT_STEP(PX0, PX1, alX, PY0, PY1, alY, i, KB) do { \
            if ((i) + 1 < ns) AT_STAGE_K((KB) ^ 1, (i)); AT_STAGE_V(KB, (i) - 1); SCHED; \
            AT_QK(PX0, PX1, KB); AT_FSM(PY0, PY1, alY); SCHED; \
            AT_PV((KB) ^ 1); AT_PSM(PX0, PX1, alX); AT_RESC(alX); \
            WAIT_V(0); BAR; } while (0)
        if (1 < ns) AT_STAGE_K(1, 0);
        AT_STAGE_V(0, qb); SCHED;
        if (!dead0) {
            AT_QK(pA0, pA1, 0);
            const int dq_ = w4 * 32 + r32 - grp * 64 - 4 * hi; const float NEG_ = -__builtin_inff();
#pragma unroll
            for (int r = 0; r < 16; ++r) { const int cc_ = (r & 3) + 8 * (r >> 2); if (cc_ > dq_) pA0[r] = NEG_; if (cc_ + 32 > dq_) pA1[r] = NEG_; }
            AT_ROWMAX(pA0, pA1)
            m_reg = pm_; first = false;
#pragma unroll
            for (int r = 0; r < 16; ++r) { pA0[r] = __builtin_amdgcn_exp2f(pA0[r] - pm_); pA1[r] -= pm_; negm[r] = -pm_; }
        } else {
#pragma unroll
            for (int r = 0; r < 16; ++r) { pA0[r] = 0.f; pA1[r] = -__builtin_inff(); }
        }
        WAIT_V(0); BAR;
        int t = 1;
        for (; t + 1 < ns; t += 2) {
            AT_STEP(pB0, pB1, alB, pA0, pA1, alA, t, 1);
            AT_STEP(pA0, pA1, alA, pB0, pB1, alB, t + 1, 0);
        }
        if (t < ns) {
            AT_STEP(pB0, pB1, alB, pA0, pA1, alA, t, 1);
            AT_FSM(pB0, pB1, alB); SCHED; AT_PV(1);
        } else {
            AT_FSM(pA0, pA1, alA); SCHED; AT_PV(0);
        }
        {
        const int lane = lane_id(), r32 = lane & 31, hi = lane >> 5;
        { auto rr_ = __builtin_amdgcn_permlane32_swap(__float_as_uint(l_reg), __float_as_uint(l_reg), false, false); l_reg = __uint_as_float(rr_[0]) + __uint_as_float(rr_[1]); }
        if (hi == 0) { ml_l[(wid * 32 + r32) * 2] = l_reg > 0.f ? m_reg : -1e30f; ml_l[(wid * 32 + r32) * 2 + 1] = l_reg; }
        WAIT_L(0); BAR;
        {
            const int ow = wid ^ 4;
            const float m0 = ml_l[(wid * 32 + r32) * 2], l0 = ml_l[(wid * 32 + r32) * 2 + 1], m1 = ml_l[(ow * 32 + r32) * 2], l1 = ml_l[(ow * 32 + r32) * 2 + 1];
            const float mm = fmaxf(m0, m1), a0 = __builtin_amdgcn_exp2f(m0 - mm), a1 = __builtin_amdgcn_exp2f(m1 - mm);
            const float fct = a0 / (l0 * a0 + l1 * a1);
            if (hi == 0) al_l[r32] = fct;
            WAIT_L(0);
#pragma unroll
            for (int c = 0; c < 4; ++c)
#pragma unroll
                for (int r = 0; r < 16; ++r) o[c][r] *= al_l[crow(r, hi)];
            LAS float* ex = (LAS float*)(lds + VOFF) + wid * 2048;
#pragma unroll
            for (int cc = 0; cc < 2; ++cc)
#pragma unroll
                for (int r = 0; r < 16; ++r) ex[(cc * 16 + r) * 64 + lane] = grp == 0 ? o[2 + cc][r] : o[cc][r];
            WAIT_L(0); BAR;
            const LAS float* ey = (const LAS float*)(lds + VOFF) + ow * 2048;
            u16* ob = ctxT + (size_t)(head * 128 + grp * 64 + r32) * S_ + qb * 128 + w4 * 32 + 4 * hi;
#pragma unroll
            for (int cc = 0; cc < 2; ++cc) {
                float v[16];
#pragma unroll
                for (int r = 0; r < 16; ++r) v[r] = (grp == 0 ? o[cc][r] : o[2 + cc][r]) + ey[(cc * 16 + r) * 64 + lane];
#pragma unroll
                for (int k4 = 0; k4 < 4; ++k4) { u32x2 w; w.x = cvt_pk(v[4 * k4], v[4 * k4 + 1]); w.y = cvt_pk(v[4 * k4 + 2], v[4 * k4 + 3]); *(u32x2*)(ob + (size_t)cc * 32 * S_ + 8 * k4) = w; }
            }
            WAIT_L(0); BAR;
        }
        }
#undef AT_QK
#undef AT_PV
#undef AT_PSM
#undef AT_ROWMAX
#undef AT_FSM
#undef AT_RESC
#undef AT_STEP
    }
#undef AT_STAGE_K
#undef AT_STAGE_V
}

namespace g3 {
constexpr int NST = 8, STG = 18432, LDS_BYTES = NST * STG, NK1 = 64, NKT = 91;
}
__global__ __launch_bounds__(512, 2) void k_out(const u16* __restrict__ ctxT, const u16* __restrict__ wo, const u16* __restrict__ w2, const u16* __restrict__ gT,
                                                const u16* __restrict__ x, const u16* __restrict__ b2, u16* __restrict__ out) {
    using namespace g3;
    extern __shared__ __attribute__((aligned(16))) unsigned char shm_[];
    LAS unsigned char* lds = (LAS unsigned char*)shm_;
    const int tid = threadIdx.x, wid = __builtin_amdgcn_readfirstlane(tid >> 6), lane = tid & 63, fr = lane & 15, fq = lane >> 4;
    const int wm = wid & 3, kh = wid >> 2;
    const int c_ = blockIdx.x, xcd = c_ & 7, jj = c_ >> 3;
    const int s0 = (xcd * 4 + (jj & 3)) * 64, n0 = (jj >> 2) * 80;
    unsigned voffA, voffB0, voffB1;
    { const int k = wid * 8 + (lane >> 3), f = (k & 2) | ((k >> 1) & 4); voffA = (unsigned)(k * (S_ * 2) + (s0 + (((lane & 7) ^ f) * 8)) * 2); }
    { const int st = wid * 4 + (lane >> 4), kb = st / 5, nb = st % 5, k = kb * 8 + (((lane >> 1) & 7) ^ ((kb & 1) << 2)); voffB0 = (unsigned)(k * (M_ * 2) + (n0 + nb * 16 + (lane & 1) * 8) * 2); }
    { const int st = (8 + (wid & 1)) * 4 + (lane >> 4), kb = st / 5, nb = st % 5, k = kb * 8 + (((lane >> 1) & 7) ^ ((kb & 1) << 2)); voffB1 = (unsigned)(k * (M_ * 2) + (n0 + nb * 16 + (lane & 1) * 8) * 2); }
    auto stage = [&](int kt) {
        const int slot = kt & (NST - 1);
        const char* ga = kt < NK1 ? (const char*)ctxT + (size_t)kt * 64 * (S_ * 2) : (const char*)w2 + (size_t)(kt - NK1) * 64 * (S_ * 2);
        const char* gb = kt < NK1 ? (const char*)wo + (size_t)kt * 64 * (M_ * 2) : (const char*)gT + (size_t)(kt - NK1) * 64 * (M_ * 2);
        GLDS16(ga + voffA, lds + slot * STG + wid * 1024);
        GLDS16(gb + voffB0, lds + slot * STG + 8192 + wid * 1024);
        if (wid < 2) GLDS16(gb + voffB1, lds + slot * STG + 8192 + (8 + wid) * 1024);
    };
    int aoff[2], boff[2];
    { const int q = fr >> 2, p = lane & 3;
#pragma unroll
      for (int t = 0; t < 2; ++t) { const int k = 32 * kh + 8 * fq + 4 * t + q, f = (k & 2) | ((k >> 1) & 4);
          aoff[t] = (int)(uintptr_t)lds + 128 * k + 16 * ((2 * wm + (p >> 1)) ^ f) + 8 * (p & 1);
          const int kb = 4 * kh + fq; boff[t] = (int)(uintptr_t)lds + 8192 + kb * 5 * 256 + (((4 * t + q) ^ ((kb & 1) << 2)) * 32) + 8 * p; } }
    f32x4 acc[5];
#pragma unroll
    for (int n = 0; n < 5; ++n) acc[n] = (f32x4){0.f, 0.f, 0.f, 0.f};
#pragma unroll
    for (int kt = 0; kt < NST - 1; ++kt) stage(kt);
    for (int kt = 0; kt < NKT; ++kt) {
        if (kt + NST - 2 < NKT) { if (wid < 2) WAIT_V(18); else WAIT_V(12); } else WAIT_V(0);
        BAR;
        if (kt + NST - 1 < NKT) stage(kt + NST - 1);
        const int so = (kt & (NST - 1)) * STG;
        const int a0_ = aoff[0] + so, a1_ = aoff[1] + so, b0_ = boff[0] + so, b1_ = boff[1] + so;
        s16x4 af[2], bfr[5][2];
        TRRD(af[0], a0_, 0); TRRD(af[1], a1_, 0);
#pragma unroll
        for (int n = 0; n < 5; ++n) { TRRD(bfr[n][0], b0_, n * 256); TRRD(bfr[n][1], b1_, n * 256); }
        asm volatile("s_waitcnt lgkmcnt(0)" : "+v"(af[0]), "+v"(af[1]), "+v"(bfr[0][0]), "+v"(bfr[0][1]), "+v"(bfr[1][0]), "+v"(bfr[1][1]), "+v"(bfr[2][0]), "+v"(bfr[2][1]), "+v"(bfr[3][0]), "+v"(bfr[3][1]), "+v"(bfr[4][0]), "+v"(bfr[4][1]) :: "memory");
#pragma unroll
        for (int n = 0; n < 5; ++n) acc[n] = __builtin_amdgcn_mfma_f32_16x16x32_bf16(cat4(bfr[n][0], bfr[n][1]), cat4(af[0], af[1]), acc[n], 0, 0, 0);
    }
    WAIT_L(0); BAR;
    LAS f32x4* ex = (LAS f32x4*)lds;
    if (kh == 1) {
#pragma unroll
        for (int n = 0; n < 5; ++n) ex[(wm * 5 + n) * 64 + lane] = acc[n];
    }
    WAIT_L(0); BAR;
    if (kh == 0) {
        const int s = s0 + wm * 16 + fr;
        const float bias = bf2f(b2[s]);
#pragma unroll
        for (int n = 0; n < 5; ++n) {
            const f32x4 v = acc[n] + ex[(wm * 5 + n) * 64 + lane];
            const size_t o = (size_t)s * M_ + n0 + n * 16 + fq * 4;
            const u32x2 xw = *(const u32x2*)(x + o);
            u32x2 w;
            w.x = cvt_pk(v[0] + bias + bf2f(xw.x & 0xffffu), v[1] + bias + bf2f(xw.x >> 16));
            w.y = cvt_pk(v[2] + bias + bf2f(xw.y & 0xffffu), v[3] + bias + bf2f(xw.y >> 16));
            *(u32x2*)(out + o) = w;
        }
    }
}

extern "C" void kernel_launch(void* const* d_in, const int* in_sizes, int n_in, void* d_out, int out_size, void* d_ws, size_t ws_size,
                              hipStream_t stream) {
    const u16* x = (const u16*)d_in[0];
    const u16* ckv = (const u16*)d_in[1];
    const u16* wq = (const u16*)d_in[2];
    const u16* wuk = (const u16*)d_in[3];
    const u16* wuv = (const u16*)d_in[4];
    const u16* wo = (const u16*)d_in[5];
    const u16* w1 = (const u16*)d_in[7];
    const u16* b1 = (const u16*)d_in[8];
    const u16* w2 = (const u16*)d_in[9];
    const u16* b2 = (const u16*)d_in[10];
    char* ws = (char*)d_ws;
    size_t off = 0;
    u16* xn = (u16*)(ws + off); off += (size_t)S_ * M_ * 2;
    u16* hT = (u16*)(ws + off); off += (size_t)S_ * M_ * 2;
    u16* q = (u16*)(ws + off); off += (size_t)H_ * S_ * D_ * 2;
    u16* k = (u16*)(ws + off); off += (size_t)H_ * S_ * D_ * 2;
    u16* v = (u16*)(ws + off); off += (size_t)H_ * S_ * D_ * 2;
    float* upart = (float*)(ws + off); off += (size_t)4 * M_ * F_ * 4;
    u16* gT = (u16*)(ws + off); off += (size_t)F_ * M_ * 2;
    u16* ctxT = (u16*)(ws + off); off += (size_t)HD_ * S_ * 2;
    (void)hipFuncSetAttribute((const void*)k_gemm1, hipFuncAttributeMaxDynamicSharedMemorySize, g1::STAGE_BYTES);
    (void)hipFuncSetAttribute((const void*)k_attn, hipFuncAttributeMaxDynamicSharedMemorySize, at::LDS_BYTES);
    (void)hipFuncSetAttribute((const void*)k_out, hipFuncAttributeMaxDynamicSharedMemorySize, g3::LDS_BYTES);
    k_ln<<<dim3(S_ / 16), dim3(256), 0, stream>>>(x, xn, hT);
    k_gemm1<<<dim3(256), dim3(512), g1::STAGE_BYTES, stream>>>(xn, ckv, wq, wuk, wuv, hT, w1, q, k, v, upart);
    k_attn<<<dim3(256), dim3(512), at::LDS_BYTES, stream>>>(q, k, v, ctxT, upart, b1, gT);
    k_out<<<dim3(256), dim3(512), g3::LDS_BYTES, stream>>>(ctxT, wo, w2, gT, x, b2, (u16*)d_out);
}
```

```cpp
#include <hip/hip_runtime.h>
#include <hip/hip_bf16.h>
#include <stdint.h>

typedef unsigned short u16;
typedef short bf16x8 __attribute__((ext_vector_type(8)));
typedef short s16x4 __attribute__((ext_vector_type(4)));
typedef float f32x4 __attribute__((ext_vector_type(4)));
typedef float f32x16 __attribute__((ext_vector_type(16)));
typedef unsigned u32x4 __attribute__((ext_vector_type(4)));
typedef unsigned u32x2 __attribute__((ext_vector_type(2)));
#define LAS __attribute__((address_space(3)))

constexpr int S_ = 2048, M_ = 640, DC_ = 512, H_ = 32, D_ = 128, F_ = 1728, HD_ = 4096;

__device__ __forceinline__ float bf2f(unsigned b) { return __uint_as_float(b << 16); }
__device__ __forceinline__ unsigned cvt_pk(float lo, float hi) {
    unsigned r; asm volatile("v_cvt_pk_bf16_f32 %0, %1, %2" : "=v"(r) : "v"(lo), "v"(hi)); return r;
}
__device__ __forceinline__ float rbf(float x) { return __uint_as_float(cvt_pk(x, x) << 16); }
__device__ __forceinline__ bf16x8 cat4(s16x4 a, s16x4 b) { return __builtin_shufflevector(a, b, 0, 1, 2, 3, 4, 5, 6, 7); }
__device__ __forceinline__ s16x4 tr_read(const LAS unsigned char* p) { return __builtin_amdgcn_ds_read_tr16_b64_v4i16((LAS s16x4*)p); }

__device__ __forceinline__ int lane_id() { int l; asm volatile("v_mbcnt_lo_u32_b32 %0, -1, 0\n\tv_mbcnt_hi_u32_b32 %0, -1, %0" : "=v"(l)); return l; }
#define TRRD(dst, addr, off) asm volatile("ds_read_b64_tr_b16 %0, %1 offset:%2" : "=v"(dst) : "v"(addr), "i"(off) : "memory")
#define WAIT_L0_8(a, b, c, d, e, f, g, h) asm volatile("s_waitcnt lgkmcnt(0)" : "+v"(a), "+v"(b), "+v"(c), "+v"(d), "+v"(e), "+v"(f), "+v"(g), "+v"(h) :: "memory")
#define WAIT_V(n) asm volatile("s_waitcnt vmcnt(" #n ")" ::: "memory")
#define WAIT_L(n) asm volatile("s_waitcnt lgkmcnt(" #n ")" ::: "memory")
#define BAR __builtin_amdgcn_s_barrier()
#define SCHED __builtin_amdgcn_sched_barrier(0)
#define GLDS16(gptr, ldsptr) __builtin_amdgcn_global_load_lds((const unsigned*)(gptr), (LAS unsigned*)(ldsptr), 16, 0, 0)

template <int CTRL> __device__ __forceinline__ float dpp_add(float v) {
    return v + __int_as_float(__builtin_amdgcn_update_dpp(0, __float_as_int(v), CTRL, 0xf, 0xf, true));
}
__device__ __forceinline__ float wave_sum(float v) {
    v = dpp_add<0xB1>(v);
    v = dpp_add<0x4E>(v);
    v = dpp_add<0x141>(v);
    v = dpp_add<0x140>(v);
    { auto r = __builtin_amdgcn_permlane16_swap(__float_as_uint(v), __float_as_uint(v), false, false); v = __uint_as_float(r[0]) + __uint_as_float(r[1]); }
    { auto r = __builtin_amdgcn_permlane32_swap(__float_as_uint(v), __float_as_uint(v), false, false); v = __uint_as_float(r[0]) + __uint_as_float(r[1]); }
    return v;
}
__device__ __forceinline__ void ln10(float (&v)[10]) {
    float s = 0.f;
#pragma unroll
    for (int i = 0; i < 10; ++i) s += v[i];
    const float mean = wave_sum(s) * (1.0f / 640.0f);
    float q = 0.f;
#pragma unroll
    for (int i = 0; i < 10; ++i) { const float d = v[i] - mean; q += d * d; }
    const float var = wave_sum(q) * (1.0f / 640.0f);
    const float mb = rbf(mean), vb = rbf(var);
    const float den = rbf(sqrtf(rbf(vb + 1e-5f)));
#pragma unroll
    for (int i = 0; i < 10; ++i) v[i] = rbf(rbf(v[i] - mb) / den);
}
__global__ __launch_bounds__(512) void k_ln(const u16* __restrict__ x, u16* __restrict__ xn, u16* __restrict__ hT) {
    __shared__ u16 tile[8][648];
    const int wave = threadIdx.x >> 6, lane = threadIdx.x & 63, row0 = ((blockIdx.x & 7) * 32 + (blockIdx.x >> 3)) * 8;
    {
        const int row = row0 + wave;
        float v[10];
#pragma unroll
        for (int i = 0; i < 5; ++i) { const unsigned w = *(const unsigned*)(x + (size_t)row * M_ + i * 128 + lane * 2); v[2 * i] = bf2f(w & 0xffffu); v[2 * i + 1] = bf2f(w >> 16); }
        ln10(v);
#pragma unroll
        for (int i = 0; i < 5; ++i) *(unsigned*)(xn + (size_t)row * M_ + i * 128 + lane * 2) = cvt_pk(v[2 * i], v[2 * i + 1]);
        ln10(v);
#pragma unroll
        for (int i = 0; i < 5; ++i) *(unsigned*)(&tile[wave][i * 128 + lane * 2]) = cvt_pk(v[2 * i], v[2 * i + 1]);
    }
    __syncthreads();
#pragma unroll
    for (int i = 0; i < 2; ++i) {
        const int d = threadIdx.x + i * 512;
        if (d < M_) {
            u32x4 w;
#pragma unroll
            for (int e = 0; e < 4; ++e) w[e] = (unsigned)tile[2 * e][d] | ((unsigned)tile[2 * e + 1][d] << 16);
            *(u32x4*)(hT + (size_t)d * S_ + row0) = w;
        }
    }
}

namespace g1 {
constexpr int HTB = 128 * 64 * 2, STAGE_BYTES = 8 * HTB;
__device__ __forceinline__ int lds_byte(int r, int c) { const int st = (r >> 4) * 2 + (c >> 5), rr = r & 15, cc = c & 31, ob = rr * 64 + cc * 2; return st * 1024 + (ob ^ (((ob >> 9) & 1) << 5)); }
__device__ __forceinline__ void stage_rc(int b, int& R, int& C) { const int st = b / 1024, sb = b % 1024, swz = sb ^ (((sb >> 9) & 1) << 5); R = (st >> 1) * 16 + swz / 64; C = (st & 1) * 32 + (swz % 64) / 2; }
struct Unit { const char* a; const char* b; unsigned lda2, ldb2; int nt, type, m0, n0, ks; };
struct Ptrs { const u16 *xn, *ckv, *wq, *wuk, *wuv, *hT, *w1; };
__device__ __forceinline__ bool get_unit(int c, int i, const Ptrs& P, Unit& u) {
    const int xcd = c & 7, j = c >> 3;
    int kind, idx;
    if (j < 16) { if (i == 0) { kind = 0; idx = j; } else if (i == 1) { kind = 1; idx = j; } else return false; }
    else { if (i == 0) { kind = 1; idx = j; } else if (i == 1) { kind = 2; idx = (j - 16) * 8 + xcd; if (idx >= 84) return false; } else return false; }
    if (kind == 0) {
        const int pm = (xcd & 1) * 4 + (idx & 3), pn = (xcd >> 1) * 4 + (idx >> 2);
        u.m0 = pm * 256; u.n0 = pn * 256; u.lda2 = M_ * 2; u.ldb2 = HD_ * 2; u.nt = 10; u.type = 0; u.ks = 0;
        u.a = (const char*)P.xn + (size_t)u.m0 * (M_ * 2); u.b = (const char*)P.wq + (size_t)u.n0 * 2;
    } else if (kind == 1) {
        const int pm = (xcd & 1) * 4 + (idx & 3), pn = (xcd >> 1) * 8 + (idx >> 2);
        u.m0 = pm * 256; u.n0 = (pn & 15) * 256; u.lda2 = DC_ * 2; u.ldb2 = HD_ * 2; u.nt = 8; u.type = 1 + (pn >> 4); u.ks = 0;
        u.a = (const char*)P.ckv + (size_t)u.m0 * (DC_ * 2); u.b = (const char*)((pn >> 4) ? P.wuv : P.wuk) + (size_t)u.n0 * 2;
    } else {
        const int tile = idx % 21, ks = idx / 21, mi = tile % 3, ni = tile / 3;
        u.m0 = mi == 2 ? 384 : mi * 256; u.n0 = ni == 6 ? 1472 : ni * 256; u.lda2 = S_ * 2; u.ldb2 = F_ * 2; u.nt = 8; u.type = 3; u.ks = ks;
        u.a = (const char*)P.hT + (size_t)u.m0 * (S_ * 2) + (size_t)ks * 512 * 2; u.b = (const char*)P.w1 + (size_t)ks * 512 * (F_ * 2) + (size_t)u.n0 * 2;
    }
    return true;
}
}

__global__ __launch_bounds__(512, 2) void k_gemm1(const u16* __restrict__ xn, const u16* __restrict__ ckv, const u16* __restrict__ wq, const u16* __restrict__ wuk,
                                                  const u16* __restrict__ wuv, const u16* __restrict__ hT, const u16* __restrict__ w1,
                                                  u16* __restrict__ qo, u16* __restrict__ ko, u16* __restrict__ vo, float* __restrict__ upart) {
    using namespace g1;
    extern __shared__ __attribute__((aligned(16))) unsigned char shm_[];
    LAS unsigned char* lds = (LAS unsigned char*)shm_;
    const int tid = threadIdx.x, wid = __builtin_amdgcn_readfirstlane(tid >> 6), lane = tid & 63, wr = wid >> 2, wc = wid & 3, fr = lane & 15, fq = lane >> 4;
    const Ptrs P{xn, ckv, wq, wuk, wuv, hT, w1};
    int sR, sC; stage_rc(tid * 16, sR, sC);
    const int bK = tid >> 4, bCh = (tid & 15) ^ (((bK & 3) << 2) | ((bK >> 2) & 3));
    const unsigned ldsw = (unsigned)wid * 1024u;
    const int aoff = lds_byte(wr * 64 + fr, fq * 8);
    int boff[2][2];
    { const int q = (lane & 15) >> 2, p = lane & 3;
#pragma unroll
      for (int n = 0; n < 2; ++n)
#pragma unroll
        for (int t = 0; t < 2; ++t) boff[n][t] = (int)(uintptr_t)lds + 4 * HTB + 256 * (8 * fq + 4 * t + q) + 16 * ((4 * wc + 2 * n + (p >> 1)) ^ ((q << 2) | ((2 * fq + t) & 3))) + 8 * (p & 1); }
#define G1_SA(b, h) (((b) * 2 + (h)) * HTB)
#define G1_SB(b, h) ((4 + (b) * 2 + (h)) * HTB)
#define G1_STAGE(bufoff, gbase, voff, step2) do { GLDS16((const char*)(gbase) + (voff), lds + (bufoff) + ldsw); GLDS16((const char*)(gbase) + (step2) + (voff), lds + (bufoff) + ldsw + 8192); } while (0)
#define G1_LDA(dst, b, h) do { _Pragma("unroll") for (int m = 0; m < 4; ++m) _Pragma("unroll") for (int k = 0; k < 2; ++k) dst[m][k] = *(const LAS bf16x8*)(lds + G1_SA(b, h) + aoff + m * 2048 + k * 1024); } while (0)
#define G1_LDB(dst, b, h) do { _Pragma("unroll") for (int n = 0; n < 2; ++n) _Pragma("unroll") for (int k = 0; k < 2; ++k) { \
        TRRD(dst[n][k][0], boff[n][0], ((b) * 2 + (h)) * HTB + k * 8192); TRRD(dst[n][k][1], boff[n][1], ((b) * 2 + (h)) * HTB + k * 8192); } } while (0)
#define G1_WB(dst) WAIT_L0_8(dst[0][0][0], dst[0][0][1], dst[0][1][0], dst[0][1][1], dst[1][0][0], dst[1][0][1], dst[1][1][0], dst[1][1][1])
#define G1_MMA(ai, bj, At, Bt) do { __builtin_amdgcn_s_setprio(1); _Pragma("unroll") for (int m = 0; m < 4; ++m) _Pragma("unroll") for (int n = 0; n < 2; ++n) _Pragma("unroll") for (int k = 0; k < 2; ++k) \
        acc[ai][bj][m][n] = __builtin_amdgcn_mfma_f32_16x16x32_bf16(cat4(Bt[n][k][0], Bt[n][k][1]), At[m][k], acc[ai][bj][m][n], 0, 0, 0); __builtin_amdgcn_s_setprio(0); } while (0)
    Unit cur, nxt; int ui = 0;
    if (!get_unit(blockIdx.x, 0, P, cur)) return;
    f32x4 acc[2][2][4][2];
#pragma unroll
    for (int a = 0; a < 2; ++a)
#pragma unroll
        for (int b = 0; b < 2; ++b)
#pragma unroll
            for (int m = 0; m < 4; ++m)
#pragma unroll
                for (int n = 0; n < 2; ++n) acc[a][b][m][n] = (f32x4){0.f, 0.f, 0.f, 0.f};
    bf16x8 At[4][2]; s16x4 B0[2][2][2], B1[2][2][2];
    unsigned vAc = (unsigned)sR * cur.lda2 + (unsigned)sC * 2u, vBc = (unsigned)bK * cur.ldb2 + (unsigned)bCh * 16u;
    {
        const size_t a64 = (size_t)64 * cur.lda2, a128 = (size_t)128 * cur.lda2, b32 = (size_t)32 * cur.ldb2, bk = (size_t)64 * cur.ldb2;
        G1_STAGE(G1_SB(0, 0), cur.b, vBc, b32); G1_STAGE(G1_SA(0, 0), cur.a, vAc, a64); G1_STAGE(G1_SB(0, 1), cur.b + 256, vBc, b32); G1_STAGE(G1_SA(0, 1), cur.a + a128, vAc, a64);
        if (wr == 1) BAR;
        WAIT_V(4); BAR;
        G1_STAGE(G1_SB(1, 0), cur.b + bk, vBc, b32); G1_STAGE(G1_SA(1, 0), cur.a + 128, vAc, a64); G1_STAGE(G1_SB(1, 1), cur.b + bk + 256, vBc, b32);
        WAIT_V(6); BAR;
    }
    for (;;) {
        const bool has_next = get_unit(blockIdx.x, ui + 1, P, nxt);
        if (!has_next) nxt = cur;
        const unsigned vAn = (unsigned)sR * nxt.lda2 + (unsigned)sC * 2u, vBn = (unsigned)bK * nxt.ldb2 + (unsigned)bCh * 16u;
        const int nt = cur.nt;
        for (int t = 0; t < nt; t += 2) {
            const bool last = (t == nt - 2);
            const size_t ca64 = (size_t)64 * cur.lda2, ca128 = (size_t)128 * cur.lda2;
            const char* a1 = cur.a + (size_t)(t + 1) * 128;
            const unsigned l_a = last ? nxt.lda2 : cur.lda2, l_b = last ? nxt.ldb2 : cur.ldb2;
            const size_t a64 = (size_t)64 * l_a, a128 = (size_t)128 * l_a, b32 = (size_t)32 * l_b, bk = (size_t)64 * l_b;
            const char* a2 = last ? nxt.a : cur.a + (size_t)(t + 2) * 128;
            const char* b2 = last ? nxt.b : cur.b + (size_t)(t + 2) * bk;
            const char* a3 = a2 + 128; const char* b3 = b2 + bk;
            const unsigned vA = last ? vAn : vAc, vB = last ? vBn : vBc;
            G1_LDB(B0, 0, 0); SCHED; G1_LDA(At, 0, 0); G1_STAGE(G1_SA(1, 1), a1 + ca128, vAc, ca64);
            WAIT_L(8); BAR; G1_WB(B0); G1_MMA(0, 0, At, B0); BAR; SCHED;
            G1_LDB(B1, 0, 1); G1_STAGE(G1_SB(0, 0), b2, vB, b32);
            BAR; G1_WB(B1); G1_MMA(0, 1, At, B1); BAR;
            G1_LDA(At, 0, 1); G1_STAGE(G1_SA(0, 0), a2, vA, a64);
            BAR; WAIT_L(0); G1_MMA(1, 0, At, B0); BAR; SCHED;
            G1_STAGE(G1_SB(0, 1), b2 + 256, vB, b32);
            WAIT_V(6); BAR; G1_MMA(1, 1, At, B1); BAR;
            G1_LDB(B0, 1, 0); SCHED; G1_LDA(At, 1, 0); G1_STAGE(G1_SA(0, 1), a2 + a128, vA, a64);
            WAIT_L(8); BAR; G1_WB(B0); G1_MMA(0, 0, At, B0); BAR; SCHED;
            G1_LDB(B1, 1, 1); G1_STAGE(G1_SB(1, 0), b3, vB, b32);
            BAR; G1_WB(B1); G1_MMA(0, 1, At, B1); BAR;
            G1_LDA(At, 1, 1); G1_STAGE(G1_SA(1, 0), a3, vA, a64);
            BAR; WAIT_L(0); G1_MMA(1, 0, At, B0); BAR; SCHED;
            G1_STAGE(G1_SB(1, 1), b3 + 256, vB, b32);
            WAIT_V(6); BAR; G1_MMA(1, 1, At, B1); BAR;
        }
        if (cur.type < 3) {
            u16* ob = cur.type == 0 ? qo : (cur.type == 1 ? ko : vo);
            const float sc = cur.type == 0 ? 0.08838834764831845f * 1.4426950408889634f : 1.0f;
#pragma unroll
            for (int ai = 0; ai < 2; ++ai)
#pragma unroll
                for (int bj = 0; bj < 2; ++bj) {
                    u16* hb = ob + (size_t)((cur.n0 >> 7) + bj) * S_ * D_ + (size_t)(((cur.m0 + ai * 128 + wr * 64) >> 4) * 4 + wc) * 512 + fr * 32 + ((fq & 1) * 2 + (fq >> 1)) * 8;
#pragma unroll
                    for (int m = 0; m < 4; ++m) {
                        const f32x4 v0 = acc[ai][bj][m][0] * sc, v1 = acc[ai][bj][m][1] * sc;
                        const auto r0 = __builtin_amdgcn_permlane16_swap(cvt_pk(v0[0], v0[1]), cvt_pk(v1[0], v1[1]), false, false);
                        const auto r1 = __builtin_amdgcn_permlane16_swap(cvt_pk(v0[2], v0[3]), cvt_pk(v1[2], v1[3]), false, false);
                        u32x4 w; w[0] = r0[0]; w[1] = r1[0]; w[2] = r0[1]; w[3] = r1[1];
                        *(u32x4*)(hb + m * 2048) = w;
                    }
                }
        } else {
            float* ub = upart + (size_t)cur.ks * M_ * F_ + (size_t)(((cur.m0 + wr * 64) >> 4) * 108 + ((cur.n0 + wc * 32) >> 4)) * 256 + fr * 16 + fq * 4;
#pragma unroll
            for (int ai = 0; ai < 2; ++ai)
#pragma unroll
                for (int bj = 0; bj < 2; ++bj)
#pragma unroll
                    for (int m = 0; m < 4; ++m)
#pragma unroll
                        for (int n = 0; n < 2; ++n) *(f32x4*)(ub + (size_t)((ai * 8 + m) * 108 + bj * 8 + n) * 256) = acc[ai][bj][m][n];
        }
        if (!has_next) break;
#pragma unroll
        for (int a = 0; a < 2; ++a)
#pragma unroll
            for (int b = 0; b < 2; ++b)
#pragma unroll
                for (int m = 0; m < 4; ++m)
#pragma unroll
                    for (int n = 0; n < 2; ++n) acc[a][b][m][n] = (f32x4){0.f, 0.f, 0.f, 0.f};
        cur = nxt; vAc = vAn; vBc = vBn; ++ui;
    }
    WAIT_V(0);
    if (wr == 0) BAR;
    BAR;
#undef G1_SA
#undef G1_SB
#undef G1_STAGE
#undef G1_LDA
#undef G1_LDB
#undef G1_WB
#undef G1_MMA
}

namespace at {
constexpr int KT = 16384, KOFF = 0, VOFF = 65536, XOFF = 131072, LDS_BYTES = 131072 + 4096;
constexpr float THR = 8.f;
__device__ __forceinline__ int crow(int r, int hi) { return (r & 3) + 8 * (r >> 2) + 4 * hi; }
}
__global__ __launch_bounds__(512, 2) void k_attn(const u16* __restrict__ Q, const u16* __restrict__ K, const u16* __restrict__ V, u16* __restrict__ ctxT,
                                                 const float* __restrict__ upart, const u16* __restrict__ b1, u16* __restrict__ gT) {
    using namespace at;
    extern __shared__ __attribute__((aligned(16))) unsigned char shm_[];
    LAS unsigned char* lds = (LAS unsigned char*)shm_;
    const int tid = threadIdx.x, wid0 = __builtin_amdgcn_readfirstlane(tid >> 6);
    for (int tile = blockIdx.x; tile < 270; tile += 256) {
        const int d0 = (tile % 10) * 64, f0 = (tile / 10) * 64;
        LAS u16* tl = (LAS u16*)lds;
        const int dr = tid >> 3, fc = (tid & 7) * 8;
        f32x4 s0 = {0.f, 0.f, 0.f, 0.f}, s1 = {0.f, 0.f, 0.f, 0.f};
#pragma unroll
        for (int ks = 0; ks < 4; ++ks) { const float* p = upart + (size_t)ks * M_ * F_ + (size_t)(((d0 + dr) >> 4) * 108 + ((f0 + fc) >> 4)) * 256 + (dr & 15) * 16 + (fc & 15); s0 += *(const f32x4*)p; s1 += *(const f32x4*)(p + 4); }
        const u32x4 bw = *(const u32x4*)(b1 + f0 + fc);
#pragma unroll
        for (int e = 0; e < 8; ++e) {
            const float uu = rbf(e < 4 ? s0[e] : s1[e - 4]);
            const float bb = bf2f((e & 1) ? (bw[e >> 1] >> 16) : (bw[e >> 1] & 0xffffu));
            const float xg = rbf(uu + bb);
            const float inner = 0.7978845608028654f * (xg + 0.044715f * xg * xg * xg);
            const float g = 0.5f * xg * (1.0f + tanhf(inner));
            tl[(fc + e) * 72 + dr] = (u16)(cvt_pk(g, g) & 0xffffu);
        }
        __syncthreads();
        { const int fr_ = tid >> 3, dc = (tid & 7) * 8; const u32x4 w = *(const LAS u32x4*)(tl + fr_ * 72 + dc); *(u32x4*)(gT + (size_t)(f0 + fr_) * M_ + d0 + dc) = w; }
        __syncthreads();
    }
    const int c_ = blockIdx.x, xcd = c_ & 7, jj = c_ >> 3, head = xcd * 4 + (jj >> 3), pi = jj & 7;
    const char* Kh = (const char*)(K + (size_t)head * S_ * D_);
    const char* Vh = (const char*)(V + (size_t)head * S_ * D_);
    const u16* Qh = Q + (size_t)head * S_ * D_;
#define AT_STAGE_K(buf, t) do { const char* g_ = Kh + (size_t)(t) * 32768; _Pragma("unroll") for (int i_ = 0; i_ < 4; ++i_) GLDS16(g_ + i_ * 4096 + voffK, lds + KOFF + (buf) * 32768 + ldst + i_ * 4096); } while (0)
#define AT_STAGE_V(buf, t) do { const char* g_ = Vh + (size_t)(t) * 32768; _Pragma("unroll") for (int i_ = 0; i_ < 4; ++i_) GLDS16(g_ + i_ * 4096 + voffV, lds + VOFF + (buf) * 32768 + ldst + i_ * 4096); } while (0)

    for (int pass = 0; pass < 2; ++pass) {
        int wid = wid0; const int lane = lane_id();
        asm volatile("" : "+s"(wid));
        const int r32 = lane & 31, hi = lane >> 5, grp = wid >> 2, w4 = wid & 3;
        const int krow_ = w4 * 4 + (lane >> 4), ksc_ = (lane & 15) ^ krow_;
        const unsigned voffK = (unsigned)((grp * 16 + (ksc_ >> 2)) * 1024 + krow_ * 64 + (ksc_ & 3) * 16);
        const unsigned voffV = (unsigned)((grp * 16 + (w4 & 1) * 2 + (lane >> 5)) * 1024 + ((w4 >> 1) * 8 + ((lane >> 2) & 7)) * 64 + (lane & 3) * 16);
        const unsigned ldst = (unsigned)(grp * KT + w4 * 1024);
        const int kb0 = (int)(uintptr_t)lds + KOFF + grp * KT + 256 * r32 + 16 * (hi ^ (r32 & 15));
        const int vb = (int)(uintptr_t)lds + VOFF + grp * KT + (4 * hi + ((lane & 15) >> 2)) * 64 + 32 * ((lane >> 4) & 1) + 8 * (lane & 3);
        LAS float* al_l = (LAS float*)(lds + XOFF) + wid * 32;
        LAS float* ml_l = (LAS float*)(lds + XOFF + 1024);
        const bool dead0 = grp == 1 && w4 < 2;
        const int qb = pass == 0 ? 15 - pi : pi, ns = qb + 1;
        bf16x8 qr[8];
#pragma unroll
        for (int d0 = 0; d0 < 8; ++d0) qr[d0] = *(const bf16x8*)(Qh + (size_t)((qb * 8 + w4 * 2 + (r32 >> 4)) * 4 + (d0 >> 1)) * 512 + (r32 & 15) * 32 + ((d0 & 1) * 2 + hi) * 8);
        AT_STAGE_K(0, qb);
        float m_reg = 0.f, l_reg = 0.f; bool first = true;
        f32x16 o[4] = {}; f32x16 negm = {};
        f32x16 pA0, pA1, pB0, pB1; float alA = 1.f, alB = 1.f; bf16x8 pa[4];
        WAIT_V(0); BAR;
#define AT_QK(P0, P1, KB) do { _Pragma("unroll") for (int d0 = 0; d0 < 8; ++d0) { int ka_; \
            if (d0 == 0) ka_ = kb0; else asm volatile("v_xor_b32 %0, %1, %2" : "=v"(ka_) : "i"(32 * d0), "v"(kb0)); \
            const bf16x8 k0_ = *(const LAS bf16x8*)(uintptr_t)(ka_ + (KB) * 32768); const bf16x8 k1_ = *(const LAS bf16x8*)(uintptr_t)(ka_ + (KB) * 32768 + 8192); \
            P0 = __builtin_amdgcn_mfma_f32_32x32x16_bf16(k0_, qr[d0], d0 == 0 ? negm : P0, 0, 0, 0); P1 = __builtin_amdgcn_mfma_f32_32x32x16_bf16(k1_, qr[d0], d0 == 0 ? negm : P1, 0, 0, 0); } } while (0)
#define AT_PV(VB) do { _Pragma("unroll") for (int c = 0; c < 4; ++c) { s16x4 v_[8]; \
            _Pragma("unroll") for (int s = 0; s < 8; ++s) TRRD(v_[s], vb, (VB) * 32768 + (s * 4 + c) * 512); \
            WAIT_L0_8(v_[0], v_[1], v_[2], v_[3], v_[4], v_[5], v_[6], v_[7]); \
            _Pragma("unroll") for (int s = 0; s < 4; ++s) o[c] = __builtin_amdgcn_mfma_f32_32x32x16_bf16(pa[s], cat4(v_[2 * s], v_[2 * s + 1]), o[c], 0, 0, 0); } } while (0)
#define AT_ROWMAX(P0, P1) \
            float pm_ = P0[0]; _Pragma("unroll") for (int r = 1; r < 16; ++r) pm_ = fmaxf(pm_, P0[r]); _Pragma("unroll") for (int r = 0; r < 16; ++r) pm_ = fmaxf(pm_, P1[r]); \
            { auto rr_ = __builtin_amdgcn_permlane32_swap(__float_as_uint(pm_), __float_as_uint(pm_), false, false); pm_ = fmaxf(__uint_as_float(rr_[0]), __uint_as_float(rr_[1])); }
#define AT_PSM(P0, P1, AL) do { AT_ROWMAX(P0, P1) \
            if (__builtin_expect(!first && __all(pm_ <= THR), 1)) { AL = 1.f; } \
            else { const float dl_ = first ? pm_ : fmaxf(pm_, 0.f); AL = first ? 1.f : __builtin_amdgcn_exp2f(-dl_); m_reg += dl_; first = false; \
                _Pragma("unroll") for (int r = 0; r < 16; ++r) { P0[r] -= dl_; P1[r] -= dl_; negm[r] = -m_reg; } } \
            _Pragma("unroll") for (int r = 0; r < 16; ++r) P0[r] = __builtin_amdgcn_exp2f(P0[r]); } while (0)
#define AT_FSM(P0, P1, AL) do { \
            _Pragma("unroll") for (int r = 0; r < 16; ++r) P1[r] = __builtin_amdgcn_exp2f(P1[r]); \
            float ps_ = 0.f; _Pragma("unroll") for (int r = 0; r < 16; ++r) ps_ += P0[r]; _Pragma("unroll") for (int r = 0; r < 16; ++r) ps_ += P1[r]; \
            l_reg = l_reg * AL + ps_; \
            _Pragma("unroll") for (int s = 0; s < 2; ++s) { u32x4 w0_, w1_; _Pragma("unroll") for (int e = 0; e < 4; ++e) { w0_[e] = cvt_pk(P0[8 * s + 2 * e], P0[8 * s + 2 * e + 1]); w1_[e] = cvt_pk(P1[8 * s + 2 * e], P1[8 * s + 2 * e + 1]); } \
                pa[s] = *reinterpret_cast<bf16x8*>(&w0_); pa[2 + s] = *reinterpret_cast<bf16x8*>(&w1_); } } while (0)
#define AT_RESC(AL) do { if (__any((AL) < 1.f)) { const int l_ = lane_id(), h_ = l_ >> 5; if (h_ == 0) al_l[l_] = (AL); WAIT_L(0); \
            _Pragma("unroll") for (int c = 0; c < 4; ++c) _Pragma("unroll") for (int r = 0; r < 16; ++r) o[c][r] *= al_l[crow(r, h_)]; } } while (0)
#define KRD(sl, d0, KB) do { int ka_; if ((d0) == 0) ka_ = kb0; else asm volatile("v_xor_b32 %0, %1, %2" : "=v"(ka_) : "i"(32 * (d0)), "v"(kb0)); \
            asm volatile("ds_read_b128 %0, %1 offset:%2" : "=v"(kf[sl][0]) : "v"(ka_), "i"((KB) * 32768) : "memory"); \
            asm volatile("ds_read_b128 %0, %1 offset:%2" : "=v"(kf[sl][1]) : "v"(ka_), "i"((KB) * 32768 + 8192) : "memory"); } while (0)
#define KWAIT(sl, n) asm volatile("s_waitcnt lgkmcnt(" #n ")" : "+v"(kf[sl][0]), "+v"(kf[sl][1]) :: "memory")
#define VRD(i, VB) do { TRRD(vf[(i) & 3][0], vb, (VB) * 32768 + ((((i) & 3) * 2) * 4 + ((i) >> 2)) * 512); TRRD(vf[(i) & 3][1], vb, (VB) * 32768 + ((((i) & 3) * 2 + 1) * 4 + ((i) >> 2)) * 512); } while (0)
#define VWAIT(i, n) asm volatile("s_waitcnt lgkmcnt(" #n ")" : "+v"(vf[(i) & 3][0]), "+v"(vf[(i) & 3][1]) :: "memory")
#define QKA(PX0, d0) PX0 = __builtin_amdgcn_mfma_f32_32x32x16_bf16(kf[(d0) % 3][0], qr[d0], (d0) == 0 ? negm : PX0, 0, 0, 0)
#define QKB(PX1, d0) PX1 = __builtin_amdgcn_mfma_f32_32x32x16_bf16(kf[(d0) % 3][1], qr[d0], (d0) == 0 ? negm : PX1, 0, 0, 0)
#define PVM(i) o[(i) >> 2] = __builtin_amdgcn_mfma_f32_32x32x16_bf16(pa[(i) & 3], cat4(vf[(i) & 3][0], vf[(i) & 3][1]), o[(i) >> 2], 0, 0, 0)
#define EXPR(P, a, b) do { _Pragma("unroll") for (int r_ = (a); r_ < (b); ++r_) { float e_ = __builtin_amdgcn_exp2f(P[r_]); asm volatile("" : "+v"(e_)); P[r_] = e_; } } while (0)
#define SUMR(acc, P, a, b) do { _Pragma("unroll") for (int r_ = (a); r_ < (b); ++r_) acc += P[r_]; asm volatile("" : "+v"(acc)); } while (0)
#define MAXR(acc, P, a, b) do { _Pragma("unroll") for (int r_ = (a); r_ < (b); r_ += 2) acc = fmaxf(fmaxf(acc, P[r_]), P[r_ + 1]); } while (0)
#define PKR(W, P, a) do { _Pragma("unroll") for (int e_ = 0; e_ < 4; ++e_) W[e_] = cvt_pk(P[(a) + 2 * e_], P[(a) + 2 * e_ + 1]); } while (0)
#define GK(i_) GLDS16(gk_ + (i_) * 4096 + voffK, lds + KOFF + kbuf_ * 32768 + ldst + (i_) * 4096)
#define GV(i_) GLDS16(gv_ + (i_) * 4096 + voffV, lds + VOFF + vbuf_ * 32768 + ldst + (i_) * 4096)
#define AT_STEP(PX0, PX1, alX, PY0, PY1, alY, i, KB) do { \
            bf16x8 kf[3][2]; s16x4 vf[4][2]; u32x4 w0_, w1_, w2_, w3_; float s0_ = 0.f, s1_ = 0.f; \
            constexpr int kbuf_ = (KB) ^ 1, vbuf_ = (KB); const char* gk_ = Kh + (size_t)((i) < qb ? (i) : qb) * 32768; const char* gv_ = Vh + (size_t)((i) - 1) * 32768; \
              \
            KRD(0, 0, KB); KRD(1, 1, KB); SCHED; \
            EXPR(PY1, 0, 8); PKR(w0_, PY0, 0); SUMR(s0_, PY0, 0, 4); SCHED; \
            KWAIT(0, 2); QKA(PX0, 0); KRD(2, 2, KB); EXPR(PY1, 8, 10); SCHED; \
            QKB(PX1, 0); GK(0); EXPR(PY1, 10, 12); SCHED; \
            KWAIT(1, 2); QKA(PX0, 1); KRD(0, 3, KB); EXPR(PY1, 12, 14); SCHED; \
            QKB(PX1, 1); GK(1); EXPR(PY1, 14, 16); SCHED; \
            KWAIT(2, 2); QKA(PX0, 2); KRD(1, 4, KB); SUMR(s0_, PY0, 4, 8); SCHED; \
            QKB(PX1, 2); GK(2); PKR(w1_, PY0, 8); SCHED; \
            KWAIT(0, 2); QKA(PX0, 3); KRD(2, 5, KB); SUMR(s0_, PY0, 8, 12); SCHED; \
            QKB(PX1, 3); GK(3); SUMR(s0_, PY0, 12, 16); SCHED; \
            KWAIT(1, 2); QKA(PX0, 4); KRD(0, 6, KB); SUMR(s1_, PY1, 0, 4); SCHED; \
            QKB(PX1, 4); GV(0); PKR(w2_, PY1, 0); SCHED; \
            KWAIT(2, 2); QKA(PX0, 5); KRD(1, 7, KB); SUMR(s1_, PY1, 4, 8); SCHED; \
            QKB(PX1, 5); GV(1); PKR(w3_, PY1, 8); SCHED; \
            KWAIT(0, 2); QKA(PX0, 6); VRD(0, (KB) ^ 1); VRD(1, (KB) ^ 1); SUMR(s1_, PY1, 8, 12); SCHED; \
            QKB(PX1, 6); GV(2); SUMR(s1_, PY1, 12, 16); SCHED; \
            KWAIT(1, 4); QKA(PX0, 7); VRD(2, (KB) ^ 1); l_reg = l_reg * alY + (s0_ + s1_); asm volatile("" : "+v"(l_reg)); SCHED; \
            QKB(PX1, 7); VRD(3, (KB) ^ 1); GV(3); SCHED; \
            pa[0] = *reinterpret_cast<bf16x8*>(&w0_); pa[1] = *reinterpret_cast<bf16x8*>(&w1_); pa[2] = *reinterpret_cast<bf16x8*>(&w2_); pa[3] = *reinterpret_cast<bf16x8*>(&w3_); \
              \
            float pm_, pn_; \
            VWAIT(0, 6); PVM(0); VRD(4, (KB) ^ 1); SCHED; \
            VWAIT(1, 6); PVM(1); VRD(5, (KB) ^ 1); pm_ = fmaxf(PX0[0], PX0[1]); MAXR(pm_, PX0, 2, 10); SCHED; \
            VWAIT(2, 6); PVM(2); VRD(6, (KB) ^ 1); MAXR(pm_, PX0, 10, 16); pn_ = fmaxf(PX1[0], PX1[1]); MAXR(pn_, PX1, 2, 4); SCHED; \
            VWAIT(3, 6); PVM(3); VRD(7, (KB) ^ 1); MAXR(pn_, PX1, 4, 12); SCHED; \
            VWAIT(4, 6); PVM(4); VRD(8, (KB) ^ 1); MAXR(pn_, PX1, 12, 16); pm_ = fmaxf(pm_, pn_); \
            { auto rr_ = __builtin_amdgcn_permlane32_swap(__float_as_uint(pm_), __float_as_uint(pm_), false, false); pm_ = fmaxf(__uint_as_float(rr_[0]), __uint_as_float(rr_[1])); } SCHED; \
            VWAIT(5, 6); PVM(5); VRD(9, (KB) ^ 1); SCHED; \
            if (__builtin_expect(!first && __all(pm_ <= THR), 1)) { alX = 1.f; } \
            else { const float dl_ = first ? pm_ : fmaxf(pm_, 0.f); alX = first ? 1.f : __builtin_amdgcn_exp2f(-dl_); m_reg += dl_; first = false; \
                _Pragma("unroll") for (int r = 0; r < 16; ++r) { PX0[r] -= dl_; PX1[r] -= dl_; negm[r] = -m_reg; } } \
            SCHED; \
            VWAIT(6, 6); PVM(6); VRD(10, (KB) ^ 1); EXPR(PX0, 0, 2); SCHED; \
            VWAIT(7, 6); PVM(7); VRD(11, (KB) ^ 1); EXPR(PX0, 2, 4); SCHED; \
            VWAIT(8, 6); PVM(8); VRD(12, (KB) ^ 1); EXPR(PX0, 4, 6); SCHED; \
            VWAIT(9, 6); PVM(9); VRD(13, (KB) ^ 1); EXPR(PX0, 6, 8); SCHED; \
            VWAIT(10, 6); PVM(10); VRD(14, (KB) ^ 1); EXPR(PX0, 8, 10); SCHED; \
            VWAIT(11, 6); PVM(11); VRD(15, (KB) ^ 1); EXPR(PX0, 10, 12); SCHED; \
            VWAIT(12, 6); PVM(12); EXPR(PX0, 12, 14); SCHED; \
            VWAIT(13, 4); PVM(13); EXPR(PX0, 14, 16); SCHED; \
            VWAIT(14, 2); PVM(14); SCHED; \
            VWAIT(15, 0); PVM(15); SCHED; \
            AT_RESC(alX); \
            WAIT_V(0); BAR; } while (0)
        if (1 < ns) AT_STAGE_K(1, 0);
        AT_STAGE_V(0, qb); SCHED;
        if (!dead0) {
            AT_QK(pA0, pA1, 0);
            const int dq_ = w4 * 32 + r32 - grp * 64 - 4 * hi; const float NEG_ = -__builtin_inff();
#pragma unroll
            for (int r = 0; r < 16; ++r) { const int cc_ = (r & 3) + 8 * (r >> 2); if (cc_ > dq_) pA0[r] = NEG_; if (cc_ + 32 > dq_) pA1[r] = NEG_; }
            AT_ROWMAX(pA0, pA1)
            m_reg = pm_; first = false;
#pragma unroll
            for (int r = 0; r < 16; ++r) { pA0[r] = __builtin_amdgcn_exp2f(pA0[r] - pm_); pA1[r] -= pm_; negm[r] = -pm_; }
        } else {
#pragma unroll
            for (int r = 0; r < 16; ++r) { pA0[r] = 0.f; pA1[r] = -__builtin_inff(); }
        }
        WAIT_V(0); BAR;
        int t = 1;
        for (; t + 1 < ns; t += 2) {
            AT_STEP(pB0, pB1, alB, pA0, pA1, alA, t, 1);
            AT_STEP(pA0, pA1, alA, pB0, pB1, alB, t + 1, 0);
        }
        if (t < ns) {
            AT_STEP(pB0, pB1, alB, pA0, pA1, alA, t, 1);
            AT_FSM(pB0, pB1, alB); SCHED; AT_PV(1);
        } else {
            AT_FSM(pA0, pA1, alA); SCHED; AT_PV(0);
        }
        {
        const int lane = lane_id(), r32 = lane & 31, hi = lane >> 5;
        { auto rr_ = __builtin_amdgcn_permlane32_swap(__float_as_uint(l_reg), __float_as_uint(l_reg), false, false); l_reg = __uint_as_float(rr_[0]) + __uint_as_float(rr_[1]); }
        if (hi == 0) { ml_l[(wid * 32 + r32) * 2] = l_reg > 0.f ? m_reg : -1e30f; ml_l[(wid * 32 + r32) * 2 + 1] = l_reg; }
        WAIT_L(0); BAR;
        {
            const int ow = wid ^ 4;
            const float m0 = ml_l[(wid * 32 + r32) * 2], l0 = ml_l[(wid * 32 + r32) * 2 + 1], m1 = ml_l[(ow * 32 + r32) * 2], l1 = ml_l[(ow * 32 + r32) * 2 + 1];
            const float mm = fmaxf(m0, m1), a0 = __builtin_amdgcn_exp2f(m0 - mm), a1 = __builtin_amdgcn_exp2f(m1 - mm);
            const float fct = a0 / (l0 * a0 + l1 * a1);
            if (hi == 0) al_l[r32] = fct;
            WAIT_L(0);
#pragma unroll
            for (int c = 0; c < 4; ++c)
#pragma unroll
                for (int r = 0; r < 16; ++r) o[c][r] *= al_l[crow(r, hi)];
            LAS float* ex = (LAS float*)(lds + VOFF) + wid * 2048;
#pragma unroll
            for (int cc = 0; cc < 2; ++cc)
#pragma unroll
                for (int r = 0; r < 16; ++r) ex[(cc * 16 + r) * 64 + lane] = grp == 0 ? o[2 + cc][r] : o[cc][r];
            WAIT_L(0); BAR;
            const LAS float* ey = (const LAS float*)(lds + VOFF) + ow * 2048;
            u16* ob = ctxT + ((size_t)(head * 4 + grp * 2) * 128 + qb * 8 + w4 * 2) * 512 + r32 * 16 + hi * 8;
#pragma unroll
            for (int cc = 0; cc < 2; ++cc) {
                float v[16];
#pragma unroll
                for (int r = 0; r < 16; ++r) v[r] = (grp == 0 ? o[cc][r] : o[2 + cc][r]) + ey[(cc * 16 + r) * 64 + lane];
#pragma unroll
                for (int kk = 0; kk < 2; ++kk) {
                    const auto rx = __builtin_amdgcn_permlane32_swap(cvt_pk(v[8 * kk], v[8 * kk + 1]), cvt_pk(v[8 * kk + 4], v[8 * kk + 5]), false, false);
                    const auto ry = __builtin_amdgcn_permlane32_swap(cvt_pk(v[8 * kk + 2], v[8 * kk + 3]), cvt_pk(v[8 * kk + 6], v[8 * kk + 7]), false, false);
                    u32x4 w; w[0] = rx[0]; w[1] = ry[0]; w[2] = rx[1]; w[3] = ry[1];
                    *(u32x4*)(ob + (size_t)(cc * 128 + kk) * 512) = w;
                }
            }
            WAIT_L(0); BAR;
        }
        }
#undef AT_QK
#undef AT_PV
#undef AT_PSM
#undef AT_ROWMAX
#undef AT_FSM
#undef AT_RESC
#undef AT_STEP
#undef KRD
#undef KWAIT
#undef VRD
#undef VWAIT
#undef QKA
#undef QKB
#undef PVM
#undef EXPR
#undef SUMR
#undef MAXR
#undef PKR
#undef GK
#undef GV
    }
#undef AT_STAGE_K
#undef AT_STAGE_V
}

namespace g3 {
constexpr int NST = 4, STG = 36864, LDS_BYTES = NST * STG, NK1 = 64, NKT = 91, KSPL = 23;
}
__global__ __launch_bounds__(512, 2) void k_out(const u16* __restrict__ ctxT, const u16* __restrict__ wo, const u16* __restrict__ w2, const u16* __restrict__ gT,
                                                float* __restrict__ part) {
    using namespace g3;
    extern __shared__ __attribute__((aligned(16))) unsigned char shm_[];
    LAS unsigned char* lds = (LAS unsigned char*)shm_;
    const int tid = threadIdx.x, wid = __builtin_amdgcn_readfirstlane(tid >> 6), lane = tid & 63, fr = lane & 15, fq = lane >> 4;
    const int wm = wid & 3, wn = wid >> 2;
    const int c_ = blockIdx.x, xcd = c_ & 7, jj = c_ >> 3;
    const int z = xcd >> 1, st = (xcd & 1) * 8 + (jj & 7), nt = jj >> 3, tile = st * 4 + nt;
    const int s0 = st * 128, n0 = nt * 160;
    const int kt0 = z * KSPL, kt1 = (kt0 + KSPL < NKT) ? kt0 + KSPL : NKT, nk = kt1 - kt0;
    unsigned voffC, voffW, voffB, voffB8;
    { const int k = (wid & 3) * 16 + (lane >> 2), ch = (lane & 3) ^ ((lane >> 5) << 1), sb = wid >> 2;
      voffC = (unsigned)(((k >> 5) * 128 + (s0 >> 4) + sb * 2 + (ch >> 1)) * 1024 + (k & 31) * 32 + (ch & 1) * 16);
      voffW = (unsigned)(k * (S_ * 2) + (s0 + sb * 32 + ch * 8) * 2);
      voffB = (unsigned)(k * (M_ * 2) + (n0 + sb * 32 + ch * 8) * 2);
      voffB8 = (unsigned)(k * (M_ * 2) + (n0 + 128 + ch * 8) * 2); }
        auto stage = [&](int i) {
        const int kt = kt0 + i;
        LAS unsigned char* dst = lds + (i & (NST - 1)) * STG + wid * 1024;
        if (kt < NK1) { const char* ga = (const char*)ctxT + (size_t)kt * 262144 + voffC; GLDS16(ga, dst); GLDS16(ga + 4096, dst + 8192); }
        else { const char* ga = (const char*)w2 + (size_t)(kt - NK1) * 262144 + voffW; GLDS16(ga, dst); GLDS16(ga + 128, dst + 8192); }
        const char* gb = kt < NK1 ? (const char*)wo + (size_t)kt * 81920 : (const char*)gT + (size_t)(kt - NK1) * 81920;
        GLDS16(gb + voffB, dst + 16384); GLDS16(gb + voffB + 128, dst + 24576);
        if (wid < 4) GLDS16(gb + voffB8, dst + 32768);
    };
    int aE, aO, bE, bO;
    { const int q = fr >> 2, p = lane & 3, x0 = (int)(uintptr_t)lds + (8 * fq + q) * 64 + (2 * (fq & 1) + (p >> 1)) * 16 + (p & 1) * 8, x1 = x0 ^ 32;
      aE = x0 + wm * 4096; aO = x1 + wm * 4096;
      bE = wn ? x1 + 6 * 4096 : x0 + 4 * 4096; bO = wn ? x0 + 7 * 4096 : x1 + 4 * 4096; }
    f32x4 acc[2][5];
#pragma unroll
    for (int i = 0; i < 2; ++i)
#pragma unroll
        for (int n = 0; n < 5; ++n) acc[i][n] = (f32x4){0.f, 0.f, 0.f, 0.f};
    s16x4 fa[2][2][2], fb[2][5][2];
    stage(0); stage(1); stage(2);
    if (wid < 4) WAIT_V(10); else WAIT_V(8);
    BAR;
    if (wn == 1) BAR;
    for (int it = 0; it < nk; ++it) {
        const int so_ = (it & (NST - 1)) * STG; const int a0_ = aE + so_, a1_ = aO + so_, b0_ = bE + so_, b1_ = bO + so_;
#pragma unroll
        for (int kh = 0; kh < 2; ++kh) {
#pragma unroll
            for (int t = 0; t < 2; ++t) { TRRD(fa[kh][0][t], a0_, kh * 2048 + t * 256); TRRD(fa[kh][1][t], a1_, kh * 2048 + t * 256); }
#pragma unroll
            for (int n = 0; n < 5; ++n)
#pragma unroll
                for (int t = 0; t < 2; ++t) TRRD(fb[kh][n][t], (n & 1) ? b1_ : b0_, (n >> 1) * 4096 + kh * 2048 + t * 256);
        }
        if (it + 3 < nk) { stage(it + 3); if (wid < 4) WAIT_V(10); else WAIT_V(8); } else WAIT_V(0);
#pragma unroll
        for (int kh = 0; kh < 2; ++kh)
            asm volatile("s_waitcnt lgkmcnt(0)" : "+v"(fa[kh][0][0]), "+v"(fa[kh][0][1]), "+v"(fa[kh][1][0]), "+v"(fa[kh][1][1]), "+v"(fb[kh][0][0]), "+v"(fb[kh][0][1]), "+v"(fb[kh][1][0]), "+v"(fb[kh][1][1]),
                         "+v"(fb[kh][2][0]), "+v"(fb[kh][2][1]), "+v"(fb[kh][3][0]), "+v"(fb[kh][3][1]), "+v"(fb[kh][4][0]), "+v"(fb[kh][4][1]) :: "memory");
        SCHED; BAR; SCHED;
        __builtin_amdgcn_s_setprio(1);
#pragma unroll
        for (int kh = 0; kh < 2; ++kh)
#pragma unroll
            for (int n = 0; n < 5; ++n)
#pragma unroll
                for (int i = 0; i < 2; ++i)
                    acc[i][n] = __builtin_amdgcn_mfma_f32_16x16x32_bf16(cat4(fb[kh][n][0], fb[kh][n][1]), cat4(fa[kh][i][0], fa[kh][i][1]), acc[i][n], 0, 0, 0);
        __builtin_amdgcn_s_setprio(0);
        SCHED; BAR; SCHED;
    }
    if (wn == 0) BAR;
    float* pt = part + ((size_t)(tile * 4 + z) * 8 + wid) * 2560 + lane * 4;
#pragma unroll
    for (int i = 0; i < 2; ++i)
#pragma unroll
        for (int n = 0; n < 5; ++n) *(f32x4*)(pt + (i * 5 + n) * 256) = acc[i][n];
}

__global__ __launch_bounds__(512) void k_red(const float* __restrict__ part, const u16* __restrict__ x, const u16* __restrict__ b2, u16* __restrict__ out) {
    const int tile = blockIdx.x / 10, j = blockIdx.x % 10, wid = threadIdx.x >> 6, lane = threadIdx.x & 63, fr = lane & 15, fq = lane >> 4;
    const float* pt = part + ((size_t)tile * 4 * 8 + wid) * 2560 + j * 256 + lane * 4;
    const f32x4 p0 = *(const f32x4*)pt, p1 = *(const f32x4*)(pt + 8 * 2560), p2 = *(const f32x4*)(pt + 16 * 2560), p3 = *(const f32x4*)(pt + 24 * 2560);
    const int s = (tile >> 2) * 128 + (wid & 3) * 32 + (j / 5) * 16 + fr;
    const size_t o = (size_t)s * M_ + (tile & 3) * 160 + (wid >> 2) * 80 + (j % 5) * 16 + fq * 4;
    const float bias = bf2f(b2[s]);
    const u32x2 xw = *(const u32x2*)(x + o);
    const f32x4 v = ((p0 + p1) + p2) + p3;
    u32x2 w;
    w.x = cvt_pk(v[0] + bias + bf2f(xw.x & 0xffffu), v[1] + bias + bf2f(xw.x >> 16));
    w.y = cvt_pk(v[2] + bias + bf2f(xw.y & 0xffffu), v[3] + bias + bf2f(xw.y >> 16));
    *(u32x2*)(out + o) = w;
}

extern "C" void kernel_launch(void* const* d_in, const int* in_sizes, int n_in, void* d_out, int out_size, void* d_ws, size_t ws_size,
                              hipStream_t stream) {
    const u16* x = (const u16*)d_in[0];
    const u16* ckv = (const u16*)d_in[1];
    const u16* wq = (const u16*)d_in[2];
    const u16* wuk = (const u16*)d_in[3];
    const u16* wuv = (const u16*)d_in[4];
    const u16* wo = (const u16*)d_in[5];
    const u16* w1 = (const u16*)d_in[7];
    const u16* b1 = (const u16*)d_in[8];
    const u16* w2 = (const u16*)d_in[9];
    const u16* b2 = (const u16*)d_in[10];
    char* ws = (char*)d_ws;
    size_t off = 0;
    u16* xn = (u16*)(ws + off); off += (size_t)S_ * M_ * 2;
    u16* hT = (u16*)(ws + off); off += (size_t)S_ * M_ * 2;
    u16* q = (u16*)(ws + off); off += (size_t)H_ * S_ * D_ * 2;
    u16* k = (u16*)(ws + off); off += (size_t)H_ * S_ * D_ * 2;
    u16* v = (u16*)(ws + off); off += (size_t)H_ * S_ * D_ * 2;
    float* upart = (float*)(ws + off); off += (size_t)4 * M_ * F_ * 4;
    u16* gT = (u16*)(ws + off); off += (size_t)F_ * M_ * 2;
    u16* ctxT = (u16*)(ws + off); off += (size_t)HD_ * S_ * 2;
    float* part = (float*)(ws + off); off += (size_t)4 * S_ * M_ * 4;
    (void)hipFuncSetAttribute((const void*)k_gemm1, hipFuncAttributeMaxDynamicSharedMemorySize, g1::STAGE_BYTES);
    (void)hipFuncSetAttribute((const void*)k_attn, hipFuncAttributeMaxDynamicSharedMemorySize, at::LDS_BYTES);
    (void)hipFuncSetAttribute((const void*)k_out, hipFuncAttributeMaxDynamicSharedMemorySize, g3::LDS_BYTES);
    k_ln<<<dim3(S_ / 8), dim3(512), 0, stream>>>(x, xn, hT);
    k_gemm1<<<dim3(256), dim3(512), g1::STAGE_BYTES, stream>>>(xn, ckv, wq, wuk, wuv, hT, w1, q, k, v, upart);
    k_attn<<<dim3(256), dim3(512), at::LDS_BYTES, stream>>>(q, k, v, ctxT, upart, b1, gT);
    k_out<<<dim3(256), dim3(512), g3::LDS_BYTES, stream>>>(ctxT, wo, w2, gT, part);
    k_red<<<dim3(640), dim3(512), 0, stream>>>(part, x, b2, (u16*)d_out);
}
```

```cpp
#include <hip/hip_runtime.h>
#include <hip/hip_bf16.h>
#include <stdint.h>

typedef unsigned short u16;
typedef short bf16x8 __attribute__((ext_vector_type(8)));
typedef short s16x4 __attribute__((ext_vector_type(4)));
typedef float f32x4 __attribute__((ext_vector_type(4)));
typedef float f32x16 __attribute__((ext_vector_type(16)));
typedef unsigned u32x4 __attribute__((ext_vector_type(4)));
typedef unsigned u32x2 __attribute__((ext_vector_type(2)));
#define LAS __attribute__((address_space(3)))

constexpr int S_ = 2048, M_ = 640, DC_ = 512, H_ = 32, D_ = 128, F_ = 1728, HD_ = 4096;

__device__ __forceinline__ float bf2f(unsigned b) { return __uint_as_float(b << 16); }
__device__ __forceinline__ unsigned cvt_pk(float lo, float hi) {
    unsigned r; asm volatile("v_cvt_pk_bf16_f32 %0, %1, %2" : "=v"(r) : "v"(lo), "v"(hi)); return r;
}
__device__ __forceinline__ float rbf(float x) { return __uint_as_float(cvt_pk(x, x) << 16); }
__device__ __forceinline__ bf16x8 cat4(s16x4 a, s16x4 b) { return __builtin_shufflevector(a, b, 0, 1, 2, 3, 4, 5, 6, 7); }
__device__ __forceinline__ s16x4 tr_read(const LAS unsigned char* p) { return __builtin_amdgcn_ds_read_tr16_b64_v4i16((LAS s16x4*)p); }

__device__ __forceinline__ int lane_id() { int l; asm volatile("v_mbcnt_lo_u32_b32 %0, -1, 0\n\tv_mbcnt_hi_u32_b32 %0, -1, %0" : "=v"(l)); return l; }
#define TRRD(dst, addr, off) asm volatile("ds_read_b64_tr_b16 %0, %1 offset:%2" : "=v"(dst) : "v"(addr), "i"(off) : "memory")
#define WAIT_L0_8(a, b, c, d, e, f, g, h) asm volatile("s_waitcnt lgkmcnt(0)" : "+v"(a), "+v"(b), "+v"(c), "+v"(d), "+v"(e), "+v"(f), "+v"(g), "+v"(h) :: "memory")
#define WAIT_V(n) asm volatile("s_waitcnt vmcnt(" #n ")" ::: "memory")
#define WAIT_L(n) asm volatile("s_waitcnt lgkmcnt(" #n ")" ::: "memory")
#define BAR __builtin_amdgcn_s_barrier()
#define SCHED __builtin_amdgcn_sched_barrier(0)
#define GLDS16(gptr, ldsptr) __builtin_amdgcn_global_load_lds((const unsigned*)(gptr), (LAS unsigned*)(ldsptr), 16, 0, 0)

template <int CTRL> __device__ __forceinline__ float dpp_add(float v) {
    return v + __int_as_float(__builtin_amdgcn_update_dpp(0, __float_as_int(v), CTRL, 0xf, 0xf, true));
}
__device__ __forceinline__ float wave_sum(float v) {
    v = dpp_add<0xB1>(v);
    v = dpp_add<0x4E>(v);
    v = dpp_add<0x141>(v);
    v = dpp_add<0x140>(v);
    { auto r = __builtin_amdgcn_permlane16_swap(__float_as_uint(v), __float_as_uint(v), false, false); v = __uint_as_float(r[0]) + __uint_as_float(r[1]); }
    { auto r = __builtin_amdgcn_permlane32_swap(__float_as_uint(v), __float_as_uint(v), false, false); v = __uint_as_float(r[0]) + __uint_as_float(r[1]); }
    return v;
}
__device__ __forceinline__ void ln10(float (&v)[10]) {
    float s = 0.f;
#pragma unroll
    for (int i = 0; i < 10; ++i) s += v[i];
    const float mean = wave_sum(s) * (1.0f / 640.0f);
    float q = 0.f;
#pragma unroll
    for (int i = 0; i < 10; ++i) { const float d = v[i] - mean; q += d * d; }
    const float var = wave_sum(q) * (1.0f / 640.0f);
    const float mb = rbf(mean), vb = rbf(var);
    const float den = rbf(sqrtf(rbf(vb + 1e-5f)));
#pragma unroll
    for (int i = 0; i < 10; ++i) v[i] = rbf(rbf(v[i] - mb) / den);
}
__global__ __launch_bounds__(512) void k_ln(const u16* __restrict__ x, u16* __restrict__ xn, u16* __restrict__ hT) {
    __shared__ u16 tile[8][648];
    const int wave = threadIdx.x >> 6, lane = threadIdx.x & 63, row0 = ((blockIdx.x & 7) * 32 + (blockIdx.x >> 3)) * 8;
    {
        const int row = row0 + wave;
        float v[10];
#pragma unroll
        for (int i = 0; i < 5; ++i) { const unsigned w = *(const unsigned*)(x + (size_t)row * M_ + i * 128 + lane * 2); v[2 * i] = bf2f(w & 0xffffu); v[2 * i + 1] = bf2f(w >> 16); }
        ln10(v);
#pragma unroll
        for (int i = 0; i < 5; ++i) *(unsigned*)(xn + (size_t)row * M_ + i * 128 + lane * 2) = cvt_pk(v[2 * i], v[2 * i + 1]);
        ln10(v);
#pragma unroll
        for (int i = 0; i < 5; ++i) *(unsigned*)(&tile[wave][i * 128 + lane * 2]) = cvt_pk(v[2 * i], v[2 * i + 1]);
    }
    __syncthreads();
#pragma unroll
    for (int i = 0; i < 2; ++i) {
        const int d = threadIdx.x + i * 512;
        if (d < M_) {
            u32x4 w;
#pragma unroll
            for (int e = 0; e < 4; ++e) w[e] = (unsigned)tile[2 * e][d] | ((unsigned)tile[2 * e + 1][d] << 16);
            *(u32x4*)(hT + (size_t)d * S_ + row0) = w;
        }
    }
}

namespace g1 {
constexpr int HTB = 128 * 64 * 2, STAGE_BYTES = 8 * HTB;
__device__ __forceinline__ int lds_byte(int r, int c) { const int st = (r >> 4) * 2 + (c >> 5), rr = r & 15, cc = c & 31, ob = rr * 64 + cc * 2; return st * 1024 + (ob ^ (((ob >> 9) & 1) << 5)); }
__device__ __forceinline__ void stage_rc(int b, int& R, int& C) { const int st = b / 1024, sb = b % 1024, swz = sb ^ (((sb >> 9) & 1) << 5); R = (st >> 1) * 16 + swz / 64; C = (st & 1) * 32 + (swz % 64) / 2; }
struct Unit { const char* a; const char* b; unsigned lda2, ldb2; int nt, type, m0, n0, ks; };
struct Ptrs { const u16 *xn, *ckv, *wq, *wuk, *wuv, *hT, *w1; };
__device__ __forceinline__ bool get_unit(int c, int i, const Ptrs& P, Unit& u) {
    const int xcd = c & 7, j = c >> 3;
    int kind, idx;
    if (j < 16) { if (i == 0) { kind = 0; idx = j; } else if (i == 1) { kind = 1; idx = j; } else return false; }
    else {
        const int jj = j - 16, nU = xcd < 2 ? 6 : 5, r = jj - nU;
        if (r < 0) { if (i == 0) { kind = 2; idx = jj * 8 + xcd; } else return false; }
        else { kind = 1; if (i == 0) idx = 16 + r; else if (i == 1 && r < nU) idx = 32 - nU + r; else return false; }
    }
    if (kind == 0) {
        const int pm = (xcd & 1) * 4 + (idx & 3), pn = (xcd >> 1) * 4 + (idx >> 2);
        u.m0 = pm * 256; u.n0 = pn * 256; u.lda2 = M_ * 2; u.ldb2 = HD_ * 2; u.nt = 10; u.type = 0; u.ks = 0;
        u.a = (const char*)P.xn + (size_t)u.m0 * (M_ * 2); u.b = (const char*)P.wq + (size_t)u.n0 * 2;
    } else if (kind == 1) {
        const int pm = (xcd & 1) * 4 + (idx & 3), pn = (xcd >> 1) * 8 + (idx >> 2);
        u.m0 = pm * 256; u.n0 = (pn & 15) * 256; u.lda2 = DC_ * 2; u.ldb2 = HD_ * 2; u.nt = 8; u.type = 1 + (pn >> 4); u.ks = 0;
        u.a = (const char*)P.ckv + (size_t)u.m0 * (DC_ * 2); u.b = (const char*)((pn >> 4) ? P.wuv : P.wuk) + (size_t)u.n0 * 2;
    } else {
        const int tile = idx % 21, ks = idx / 21, mi = tile % 3, ni = tile / 3;
        u.m0 = mi == 2 ? 384 : mi * 256; u.n0 = ni == 6 ? 1472 : ni * 256; u.lda2 = S_ * 2; u.ldb2 = F_ * 2; u.nt = 16; u.type = 3; u.ks = ks;
        u.a = (const char*)P.hT + (size_t)u.m0 * (S_ * 2) + (size_t)ks * 1024 * 2; u.b = (const char*)P.w1 + (size_t)ks * 1024 * (F_ * 2) + (size_t)u.n0 * 2;
    }
    return true;
}
}

__global__ __launch_bounds__(512, 2) void k_gemm1(const u16* __restrict__ xn, const u16* __restrict__ ckv, const u16* __restrict__ wq, const u16* __restrict__ wuk,
                                                  const u16* __restrict__ wuv, const u16* __restrict__ hT, const u16* __restrict__ w1,
                                                  u16* __restrict__ qo, u16* __restrict__ ko, u16* __restrict__ vo, float* __restrict__ upart) {
    using namespace g1;
    extern __shared__ __attribute__((aligned(16))) unsigned char shm_[];
    LAS unsigned char* lds = (LAS unsigned char*)shm_;
    const int tid = threadIdx.x, wid = __builtin_amdgcn_readfirstlane(tid >> 6), lane = tid & 63, wr = wid >> 2, wc = wid & 3, fr = lane & 15, fq = lane >> 4;
    const Ptrs P{xn, ckv, wq, wuk, wuv, hT, w1};
    int sR, sC; stage_rc(tid * 16, sR, sC);
    const int bK = tid >> 4, bCh = (tid & 15) ^ (((bK & 3) << 2) | ((bK >> 2) & 3));
    const unsigned ldsw = (unsigned)wid * 1024u;
    const int aoff = lds_byte(wr * 64 + fr, fq * 8);
    int boff[2][2];
    { const int q = (lane & 15) >> 2, p = lane & 3;
#pragma unroll
      for (int n = 0; n < 2; ++n)
#pragma unroll
        for (int t = 0; t < 2; ++t) boff[n][t] = (int)(uintptr_t)lds + 4 * HTB + 256 * (8 * fq + 4 * t + q) + 16 * ((4 * wc + 2 * n + (p >> 1)) ^ ((q << 2) | ((2 * fq + t) & 3))) + 8 * (p & 1); }
#define G1_SA(b, h) (((b) * 2 + (h)) * HTB)
#define G1_SB(b, h) ((4 + (b) * 2 + (h)) * HTB)
#define G1_STAGE(bufoff, gbase, voff, step2) do { GLDS16((const char*)(gbase) + (voff), lds + (bufoff) + ldsw); GLDS16((const char*)(gbase) + (step2) + (voff), lds + (bufoff) + ldsw + 8192); } while (0)
#define G1_LDA(dst, b, h) do { _Pragma("unroll") for (int m = 0; m < 4; ++m) _Pragma("unroll") for (int k = 0; k < 2; ++k) dst[m][k] = *(const LAS bf16x8*)(lds + G1_SA(b, h) + aoff + m * 2048 + k * 1024); } while (0)
#define G1_LDB(dst, b, h) do { _Pragma("unroll") for (int n = 0; n < 2; ++n) _Pragma("unroll") for (int k = 0; k < 2; ++k) { \
        TRRD(dst[n][k][0], boff[n][0], ((b) * 2 + (h)) * HTB + k * 8192); TRRD(dst[n][k][1], boff[n][1], ((b) * 2 + (h)) * HTB + k * 8192); } } while (0)
#define G1_WB(dst) WAIT_L0_8(dst[0][0][0], dst[0][0][1], dst[0][1][0], dst[0][1][1], dst[1][0][0], dst[1][0][1], dst[1][1][0], dst[1][1][1])
#define G1_MMA(ai, bj, At, Bt) do { __builtin_amdgcn_s_setprio(1); _Pragma("unroll") for (int m = 0; m < 4; ++m) _Pragma("unroll") for (int n = 0; n < 2; ++n) _Pragma("unroll") for (int k = 0; k < 2; ++k) \
        acc[ai][bj][m][n] = __builtin_amdgcn_mfma_f32_16x16x32_bf16(cat4(Bt[n][k][0], Bt[n][k][1]), At[m][k], acc[ai][bj][m][n], 0, 0, 0); __builtin_amdgcn_s_setprio(0); } while (0)
    Unit cur, nxt; int ui = 0;
    if (!get_unit(blockIdx.x, 0, P, cur)) return;
    f32x4 acc[2][2][4][2];
#pragma unroll
    for (int a = 0; a < 2; ++a)
#pragma unroll
        for (int b = 0; b < 2; ++b)
#pragma unroll
            for (int m = 0; m < 4; ++m)
#pragma unroll
                for (int n = 0; n < 2; ++n) acc[a][b][m][n] = (f32x4){0.f, 0.f, 0.f, 0.f};
    bf16x8 At[4][2]; s16x4 B0[2][2][2], B1[2][2][2];
    unsigned vAc = (unsigned)sR * cur.lda2 + (unsigned)sC * 2u, vBc = (unsigned)bK * cur.ldb2 + (unsigned)bCh * 16u;
    {
        const size_t a64 = (size_t)64 * cur.lda2, a128 = (size_t)128 * cur.lda2, b32 = (size_t)32 * cur.ldb2, bk = (size_t)64 * cur.ldb2;
        G1_STAGE(G1_SB(0, 0), cur.b, vBc, b32); G1_STAGE(G1_SA(0, 0), cur.a, vAc, a64); G1_STAGE(G1_SB(0, 1), cur.b + 256, vBc, b32); G1_STAGE(G1_SA(0, 1), cur.a + a128, vAc, a64);
        if (wr == 1) BAR;
        WAIT_V(4); BAR;
        G1_STAGE(G1_SB(1, 0), cur.b + bk, vBc, b32); G1_STAGE(G1_SA(1, 0), cur.a + 128, vAc, a64); G1_STAGE(G1_SB(1, 1), cur.b + bk + 256, vBc, b32);
        WAIT_V(6); BAR;
    }
    for (;;) {
        const bool has_next = get_unit(blockIdx.x, ui + 1, P, nxt);
        if (!has_next) nxt = cur;
        const unsigned vAn = (unsigned)sR * nxt.lda2 + (unsigned)sC * 2u, vBn = (unsigned)bK * nxt.ldb2 + (unsigned)bCh * 16u;
        const int nt = cur.nt;
        for (int t = 0; t < nt; t += 2) {
            const bool last = (t == nt - 2);
            const size_t ca64 = (size_t)64 * cur.lda2, ca128 = (size_t)128 * cur.lda2;
            const char* a1 = cur.a + (size_t)(t + 1) * 128;
            const unsigned l_a = last ? nxt.lda2 : cur.lda2, l_b = last ? nxt.ldb2 : cur.ldb2;
            const size_t a64 = (size_t)64 * l_a, a128 = (size_t)128 * l_a, b32 = (size_t)32 * l_b, bk = (size_t)64 * l_b;
            const char* a2 = last ? nxt.a : cur.a + (size_t)(t + 2) * 128;
            const char* b2 = last ? nxt.b : cur.b + (size_t)(t + 2) * bk;
            const char* a3 = a2 + 128; const char* b3 = b2 + bk;
            const unsigned vA = last ? vAn : vAc, vB = last ? vBn : vBc;
            G1_LDB(B0, 0, 0); SCHED; G1_LDA(At, 0, 0); G1_STAGE(G1_SA(1, 1), a1 + ca128, vAc, ca64);
            WAIT_L(8); BAR; G1_WB(B0); G1_MMA(0, 0, At, B0); BAR; SCHED;
            G1_LDB(B1, 0, 1); G1_STAGE(G1_SB(0, 0), b2, vB, b32);
            BAR; G1_WB(B1); G1_MMA(0, 1, At, B1); BAR;
            G1_LDA(At, 0, 1); G1_STAGE(G1_SA(0, 0), a2, vA, a64);
            BAR; WAIT_L(0); G1_MMA(1, 0, At, B0); BAR; SCHED;
            G1_STAGE(G1_SB(0, 1), b2 + 256, vB, b32);
            WAIT_V(6); BAR; G1_MMA(1, 1, At, B1); BAR;
            G1_LDB(B0, 1, 0); SCHED; G1_LDA(At, 1, 0); G1_STAGE(G1_SA(0, 1), a2 + a128, vA, a64);
            WAIT_L(8); BAR; G1_WB(B0); G1_MMA(0, 0, At, B0); BAR; SCHED;
            G1_LDB(B1, 1, 1); G1_STAGE(G1_SB(1, 0), b3, vB, b32);
            BAR; G1_WB(B1); G1_MMA(0, 1, At, B1); BAR;
            G1_LDA(At, 1, 1); G1_STAGE(G1_SA(1, 0), a3, vA, a64);
            BAR; WAIT_L(0); G1_MMA(1, 0, At, B0); BAR; SCHED;
            G1_STAGE(G1_SB(1, 1), b3 + 256, vB, b32);
            WAIT_V(6); BAR; G1_MMA(1, 1, At, B1); BAR;
        }
        if (cur.type < 3) {
            u16* ob = cur.type == 0 ? qo : (cur.type == 1 ? ko : vo);
            const float sc = cur.type == 0 ? 0.08838834764831845f * 1.4426950408889634f : 1.0f;
#pragma unroll
            for (int ai = 0; ai < 2; ++ai)
#pragma unroll
                for (int bj = 0; bj < 2; ++bj) {
                    u16* hb = ob + (size_t)((cur.n0 >> 7) + bj) * S_ * D_ + (size_t)(((cur.m0 + ai * 128 + wr * 64) >> 4) * 4 + wc) * 512 + fr * 32 + ((fq & 1) * 2 + (fq >> 1)) * 8;
#pragma unroll
                    for (int m = 0; m < 4; ++m) {
                        const f32x4 v0 = acc[ai][bj][m][0] * sc, v1 = acc[ai][bj][m][1] * sc;
                        const auto r0 = __builtin_amdgcn_permlane16_swap(cvt_pk(v0[0], v0[1]), cvt_pk(v1[0], v1[1]), false, false);
                        const auto r1 = __builtin_amdgcn_permlane16_swap(cvt_pk(v0[2], v0[3]), cvt_pk(v1[2], v1[3]), false, false);
                        u32x4 w; w[0] = r0[0]; w[1] = r1[0]; w[2] = r0[1]; w[3] = r1[1];
                        *(u32x4*)(hb + m * 2048) = w;
                    }
                }
        } else {
            float* ub = upart + (size_t)cur.ks * M_ * F_ + (size_t)(((cur.m0 + wr * 64) >> 4) * 108 + ((cur.n0 + wc * 32) >> 4)) * 256 + fr * 16 + fq * 4;
#pragma unroll
            for (int ai = 0; ai < 2; ++ai)
#pragma unroll
                for (int bj = 0; bj < 2; ++bj)
#pragma unroll
                    for (int m = 0; m < 4; ++m)
#pragma unroll
                        for (int n = 0; n < 2; ++n) *(f32x4*)(ub + (size_t)((ai * 8 + m) * 108 + bj * 8 + n) * 256) = acc[ai][bj][m][n];
        }
        if (!has_next) break;
#pragma unroll
        for (int a = 0; a < 2; ++a)
#pragma unroll
            for (int b = 0; b < 2; ++b)
#pragma unroll
                for (int m = 0; m < 4; ++m)
#pragma unroll
                    for (int n = 0; n < 2; ++n) acc[a][b][m][n] = (f32x4){0.f, 0.f, 0.f, 0.f};
        cur = nxt; vAc = vAn; vBc = vBn; ++ui;
    }
    WAIT_V(0);
    if (wr == 0) BAR;
    BAR;
#undef G1_SA
#undef G1_SB
#undef G1_STAGE
#undef G1_LDA
#undef G1_LDB
#undef G1_WB
#undef G1_MMA
}

namespace at {
constexpr int KT = 16384, KOFF = 0, VOFF = 65536, XOFF = 131072, UOFF = 131072 + 4096, LDS_BYTES = 131072 + 4096 + 10240;
constexpr float THR = 8.f;
__device__ __forceinline__ int crow(int r, int hi) { return (r & 3) + 8 * (r >> 2) + 4 * hi; }
}
__global__ __launch_bounds__(512, 2) void k_attn(const u16* __restrict__ Q, const u16* __restrict__ K, const u16* __restrict__ V, u16* __restrict__ ctxT,
                                                 const float* __restrict__ upart, const u16* __restrict__ b1, u16* __restrict__ gT) {
    using namespace at;
    extern __shared__ __attribute__((aligned(16))) unsigned char shm_[];
    LAS unsigned char* lds = (LAS unsigned char*)shm_;
    const int tid = threadIdx.x, wid0 = __builtin_amdgcn_readfirstlane(tid >> 6);
    const int c_ = blockIdx.x, xcd = c_ & 7, jj = c_ >> 3, head = xcd * 4 + (jj >> 3), pi = jj & 7;
    const char* Kh = (const char*)(K + (size_t)head * S_ * D_);
    const char* Vh = (const char*)(V + (size_t)head * S_ * D_);
    const char* Qh = (const char*)(Q + (size_t)head * S_ * D_);
#define AT_STAGE_K(buf, t) do { const char* g_ = Kh + (size_t)(t) * 32768; _Pragma("unroll") for (int i_ = 0; i_ < 4; ++i_) GLDS16(g_ + i_ * 4096 + voffK, lds + KOFF + (buf) * 32768 + ldst + i_ * 4096); } while (0)
#define AT_STAGE_V(buf, t) do { const char* g_ = Vh + (size_t)(t) * 32768; _Pragma("unroll") for (int i_ = 0; i_ < 4; ++i_) GLDS16(g_ + i_ * 4096 + voffV, lds + VOFF + (buf) * 32768 + ldst + i_ * 4096); } while (0)
#define AT_LOADQ(dst, qb_) do { const char* qp_ = Qh + (size_t)(((qb_) * 8 + w4 * 2 + (r32 >> 4)) * 4096 + (r32 & 15) * 64 + hi * 16); \
        _Pragma("unroll") for (int d0 = 0; d0 < 8; ++d0) asm volatile("global_load_dwordx4 %0, %1, off offset:%2" : "=v"(dst[d0]) : "v"(qp_), "i"((d0 >> 1) * 1024 + (d0 & 1) * 32) : "memory"); } while (0)
#define AT_BINDQ(dst) asm volatile("" : "+v"(dst[0]), "+v"(dst[1]), "+v"(dst[2]), "+v"(dst[3]), "+v"(dst[4]), "+v"(dst[5]), "+v"(dst[6]), "+v"(dst[7]) :: "memory")
#define AT_LANEC \
        const int r32 = lane & 31, hi = lane >> 5, grp = wid >> 2, w4 = wid & 3; \
        const int krow_ = w4 * 4 + (lane >> 4), ksc_ = (lane & 15) ^ krow_; \
        const unsigned voffK = (unsigned)((grp * 16 + (ksc_ >> 2)) * 1024 + krow_ * 64 + (ksc_ & 3) * 16); \
        const unsigned voffV = (unsigned)((grp * 16 + (w4 & 1) * 2 + (lane >> 5)) * 1024 + ((w4 >> 1) * 8 + ((lane >> 2) & 7)) * 64 + (lane & 3) * 16); \
        const unsigned ldst = (unsigned)(grp * KT + w4 * 1024);
    bf16x8 qr[8];
    {
        const int wid = wid0, lane = tid & 63;
        AT_LANEC
        AT_LOADQ(qr, 15 - pi);
        AT_STAGE_K(0, 15 - pi); AT_STAGE_K(1, 0); AT_STAGE_V(0, 15 - pi);
        LAS u16* tl = (LAS u16*)(lds + UOFF) + wid * 640;
        for (int item = c_ * 8 + wid; item < 2160; item = (wid == 0 && c_ < 112 && item < 2048) ? 2048 + c_ : 4096) {
            const int fb = item / 20, dp = item % 20;
            const float* pu = upart + (size_t)(dp * 2 * 108 + fb) * 256 + lane * 4;
            const f32x4 a0 = *(const f32x4*)pu, a1 = *(const f32x4*)(pu + M_ * F_), c0 = *(const f32x4*)(pu + 108 * 256), c1 = *(const f32x4*)(pu + M_ * F_ + 108 * 256);
            const u32x2 bw = *(const u32x2*)(b1 + fb * 16 + (lane & 3) * 4);
#pragma unroll
            for (int e = 0; e < 8; ++e) {
                const float uu = rbf(e < 4 ? a0[e] + a1[e] : c0[e - 4] + c1[e - 4]);
                const unsigned bq = bw[(e >> 1) & 1];
                const float xg = rbf(uu + bf2f((e & 1) ? (bq >> 16) : (bq & 0xffffu)));
                const float inner = 0.7978845608028654f * (xg + 0.044715f * xg * xg * xg);
                const float g = xg * __builtin_amdgcn_rcpf(1.0f + __builtin_amdgcn_exp2f(inner * -2.8853900817779268f));
                tl[((lane & 3) * 4 + (e & 3)) * 40 + (e >> 2) * 16 + (lane >> 2)] = (u16)(cvt_pk(g, g) & 0xffffu);
            }
            WAIT_L(0);
            const u32x4 w = *(const LAS u32x4*)(tl + (lane >> 2) * 40 + (lane & 3) * 8);
            *(u32x4*)(gT + (size_t)item * 512 + lane * 8) = w;
            WAIT_L(0);
        }
        WAIT_V(1); AT_BINDQ(qr);
        BAR;
    }
    for (int pass = 0; pass < 2; ++pass) {
        int wid = wid0; const int lane = lane_id();
        asm volatile("" : "+s"(wid));
        AT_LANEC
        const int kb0 = (int)(uintptr_t)lds + KOFF + grp * KT + 256 * r32 + 16 * (hi ^ (r32 & 15));
        const int vb = (int)(uintptr_t)lds + VOFF + grp * KT + (4 * hi + ((lane & 15) >> 2)) * 64 + 32 * ((lane >> 4) & 1) + 8 * (lane & 3);
        LAS float* al_l = (LAS float*)(lds + XOFF) + wid * 32;
        LAS float* ml_l = (LAS float*)(lds + XOFF + 1024);
        const bool dead0 = grp == 1 && w4 < 2;
        const int qb = pass == 0 ? 15 - pi : pi, ns = qb + 1;
        float m_reg = 0.f, l_reg = 0.f; bool first = true;
        f32x16 o[4] = {}; f32x16 negm = {};
        f32x16 pA0, pA1, pB0, pB1; float alA = 1.f, alB = 1.f; bf16x8 pa[4];
#define AT_QK(P0, P1, KB) do { _Pragma("unroll") for (int d0 = 0; d0 < 8; ++d0) { int ka_; \
            if (d0 == 0) ka_ = kb0; else asm volatile("v_xor_b32 %0, %1, %2" : "=v"(ka_) : "i"(32 * d0), "v"(kb0)); \
            const bf16x8 k0_ = *(const LAS bf16x8*)(uintptr_t)(ka_ + (KB) * 32768); const bf16x8 k1_ = *(const LAS bf16x8*)(uintptr_t)(ka_ + (KB) * 32768 + 8192); \
            P0 = __builtin_amdgcn_mfma_f32_32x32x16_bf16(k0_, qr[d0], d0 == 0 ? negm : P0, 0, 0, 0); P1 = __builtin_amdgcn_mfma_f32_32x32x16_bf16(k1_, qr[d0], d0 == 0 ? negm : P1, 0, 0, 0); } } while (0)
#define AT_PV(VB) do { _Pragma("unroll") for (int c = 0; c < 4; ++c) { s16x4 v_[8]; \
            _Pragma("unroll") for (int s = 0; s < 8; ++s) TRRD(v_[s], vb, (VB) * 32768 + (s * 4 + c) * 512); \
            WAIT_L0_8(v_[0], v_[1], v_[2], v_[3], v_[4], v_[5], v_[6], v_[7]); \
            _Pragma("unroll") for (int s = 0; s < 4; ++s) o[c] = __builtin_amdgcn_mfma_f32_32x32x16_bf16(pa[s], cat4(v_[2 * s], v_[2 * s + 1]), o[c], 0, 0, 0); } } while (0)
#define AT_ROWMAX(P0, P1) \
            float pm_ = P0[0]; _Pragma("unroll") for (int r = 1; r < 16; ++r) pm_ = fmaxf(pm_, P0[r]); _Pragma("unroll") for (int r = 0; r < 16; ++r) pm_ = fmaxf(pm_, P1[r]); \
            { auto rr_ = __builtin_amdgcn_permlane32_swap(__float_as_uint(pm_), __float_as_uint(pm_), false, false); pm_ = fmaxf(__uint_as_float(rr_[0]), __uint_as_float(rr_[1])); }
#define AT_PSM(P0, P1, AL) do { AT_ROWMAX(P0, P1) \
            if (__builtin_expect(!first && __all(pm_ <= THR), 1)) { AL = 1.f; } \
            else { const float dl_ = first ? pm_ : fmaxf(pm_, 0.f); AL = first ? 1.f : __builtin_amdgcn_exp2f(-dl_); m_reg += dl_; first = false; \
                _Pragma("unroll") for (int r = 0; r < 16; ++r) { P0[r] -= dl_; P1[r] -= dl_; negm[r] = -m_reg; } } \
            _Pragma("unroll") for (int r = 0; r < 16; ++r) P0[r] = __builtin_amdgcn_exp2f(P0[r]); } while (0)
#define AT_FSM(P0, P1, AL) do { \
            _Pragma("unroll") for (int r = 0; r < 16; ++r) P1[r] = __builtin_amdgcn_exp2f(P1[r]); \
            float ps_ = 0.f; _Pragma("unroll") for (int r = 0; r < 16; ++r) ps_ += P0[r]; _Pragma("unroll") for (int r = 0; r < 16; ++r) ps_ += P1[r]; \
            l_reg = l_reg * AL + ps_; \
            _Pragma("unroll") for (int s = 0; s < 2; ++s) { u32x4 w0_, w1_; _Pragma("unroll") for (int e = 0; e < 4; ++e) { w0_[e] = cvt_pk(P0[8 * s + 2 * e], P0[8 * s + 2 * e + 1]); w1_[e] = cvt_pk(P1[8 * s + 2 * e], P1[8 * s + 2 * e + 1]); } \
                pa[s] = *reinterpret_cast<bf16x8*>(&w0_); pa[2 + s] = *reinterpret_cast<bf16x8*>(&w1_); } } while (0)
#define AT_RESC(AL) do { if (__any((AL) < 1.f)) { const int l_ = lane_id(), h_ = l_ >> 5; if (h_ == 0) al_l[l_] = (AL); WAIT_L(0); \
            _Pragma("unroll") for (int c = 0; c < 4; ++c) _Pragma("unroll") for (int r = 0; r < 16; ++r) o[c][r] *= al_l[crow(r, h_)]; } } while (0)
#define KRD(sl, d0, KB) do { int ka_; if ((d0) == 0) ka_ = kb0; else asm volatile("v_xor_b32 %0, %1, %2" : "=v"(ka_) : "i"(32 * (d0)), "v"(kb0)); \
            asm volatile("ds_read_b128 %0, %1 offset:%2" : "=v"(kf[sl][0]) : "v"(ka_), "i"((KB) * 32768) : "memory"); \
            asm volatile("ds_read_b128 %0, %1 offset:%2" : "=v"(kf[sl][1]) : "v"(ka_), "i"((KB) * 32768 + 8192) : "memory"); } while (0)
#define KWAIT(sl, n) asm volatile("s_waitcnt lgkmcnt(" #n ")" : "+v"(kf[sl][0]), "+v"(kf[sl][1]) :: "memory")
#define VRD(i, VB) do { TRRD(vf[(i) & 3][0], vb, (VB) * 32768 + ((((i) & 3) * 2) * 4 + ((i) >> 2)) * 512); TRRD(vf[(i) & 3][1], vb, (VB) * 32768 + ((((i) & 3) * 2 + 1) * 4 + ((i) >> 2)) * 512); } while (0)
#define VWAIT(i, n) asm volatile("s_waitcnt lgkmcnt(" #n ")" : "+v"(vf[(i) & 3][0]), "+v"(vf[(i) & 3][1]) :: "memory")
#define QKA(PX0, d0) PX0 = __builtin_amdgcn_mfma_f32_32x32x16_bf16(kf[(d0) % 3][0], qr[d0], (d0) == 0 ? negm : PX0, 0, 0, 0)
#define QKB(PX1, d0) PX1 = __builtin_amdgcn_mfma_f32_32x32x16_bf16(kf[(d0) % 3][1], qr[d0], (d0) == 0 ? negm : PX1, 0, 0, 0)
#define PVM(i) o[(i) >> 2] = __builtin_amdgcn_mfma_f32_32x32x16_bf16(pa[(i) & 3], cat4(vf[(i) & 3][0], vf[(i) & 3][1]), o[(i) >> 2], 0, 0, 0)
#define EXPR(P, a, b) do { _Pragma("unroll") for (int r_ = (a); r_ < (b); ++r_) { float e_ = __builtin_amdgcn_exp2f(P[r_]); asm volatile("" : "+v"(e_)); P[r_] = e_; } } while (0)
#define SUMR(acc, P, a, b) do { _Pragma("unroll") for (int r_ = (a); r_ < (b); ++r_) acc += P[r_]; asm volatile("" : "+v"(acc)); } while (0)
#define MAXR(acc, P, a, b) do { _Pragma("unroll") for (int r_ = (a); r_ < (b); r_ += 2) acc = fmaxf(fmaxf(acc, P[r_]), P[r_ + 1]); } while (0)
#define PKR(W, P, a) do { _Pragma("unroll") for (int e_ = 0; e_ < 4; ++e_) W[e_] = cvt_pk(P[(a) + 2 * e_], P[(a) + 2 * e_ + 1]); } while (0)
#define GK(i_) GLDS16(gk_ + (i_) * 4096 + voffK, lds + KOFF + kbuf_ * 32768 + ldst + (i_) * 4096)
#define GV(i_) GLDS16(gv_ + (i_) * 4096 + voffV, lds + VOFF + vbuf_ * 32768 + ldst + (i_) * 4096)
#define AT_STEP(PX0, PX1, alX, PY0, PY1, alY, i, KB) do { \
            bf16x8 kf[3][2]; s16x4 vf[4][2]; u32x4 w0_, w1_, w2_, w3_; float s0_ = 0.f, s1_ = 0.f; \
            constexpr int kbuf_ = (KB) ^ 1, vbuf_ = (KB); const char* gk_ = Kh + (size_t)((i) < qb ? (i) : qb) * 32768; const char* gv_ = Vh + (size_t)((i) - 1) * 32768; \
              \
            KRD(0, 0, KB); KRD(1, 1, KB); SCHED; \
            EXPR(PY1, 0, 8); PKR(w0_, PY0, 0); SUMR(s0_, PY0, 0, 4); SCHED; \
            KWAIT(0, 2); QKA(PX0, 0); KRD(2, 2, KB); EXPR(PY1, 8, 10); SCHED; \
            QKB(PX1, 0); GK(0); EXPR(PY1, 10, 12); SCHED; \
            KWAIT(1, 2); QKA(PX0, 1); KRD(0, 3, KB); EXPR(PY1, 12, 14); SCHED; \
            QKB(PX1, 1); GK(1); EXPR(PY1, 14, 16); SCHED; \
            KWAIT(2, 2); QKA(PX0, 2); KRD(1, 4, KB); SUMR(s0_, PY0, 4, 8); SCHED; \
            QKB(PX1, 2); GK(2); PKR(w1_, PY0, 8); SCHED; \
            KWAIT(0, 2); QKA(PX0, 3); KRD(2, 5, KB); SUMR(s0_, PY0, 8, 12); SCHED; \
            QKB(PX1, 3); GK(3); SUMR(s0_, PY0, 12, 16); SCHED; \
            KWAIT(1, 2); QKA(PX0, 4); KRD(0, 6, KB); SUMR(s1_, PY1, 0, 4); SCHED; \
            QKB(PX1, 4); GV(0); PKR(w2_, PY1, 0); SCHED; \
            KWAIT(2, 2); QKA(PX0, 5); KRD(1, 7, KB); SUMR(s1_, PY1, 4, 8); SCHED; \
            QKB(PX1, 5); GV(1); PKR(w3_, PY1, 8); SCHED; \
            KWAIT(0, 2); QKA(PX0, 6); VRD(0, (KB) ^ 1); VRD(1, (KB) ^ 1); SUMR(s1_, PY1, 8, 12); SCHED; \
            QKB(PX1, 6); GV(2); SUMR(s1_, PY1, 12, 16); SCHED; \
            KWAIT(1, 4); QKA(PX0, 7); VRD(2, (KB) ^ 1); l_reg = l_reg * alY + (s0_ + s1_); asm volatile("" : "+v"(l_reg)); SCHED; \
            QKB(PX1, 7); VRD(3, (KB) ^ 1); GV(3); SCHED; \
            pa[0] = *reinterpret_cast<bf16x8*>(&w0_); pa[1] = *reinterpret_cast<bf16x8*>(&w1_); pa[2] = *reinterpret_cast<bf16x8*>(&w2_); pa[3] = *reinterpret_cast<bf16x8*>(&w3_); \
              \
            float pm_, pn_; \
            VWAIT(0, 6); PVM(0); VRD(4, (KB) ^ 1); SCHED; \
            VWAIT(1, 6); PVM(1); VRD(5, (KB) ^ 1); pm_ = fmaxf(PX0[0], PX0[1]); MAXR(pm_, PX0, 2, 10); SCHED; \
            VWAIT(2, 6); PVM(2); VRD(6, (KB) ^ 1); MAXR(pm_, PX0, 10, 16); pn_ = fmaxf(PX1[0], PX1[1]); MAXR(pn_, PX1, 2, 4); SCHED; \
            VWAIT(3, 6); PVM(3); VRD(7, (KB) ^ 1); MAXR(pn_, PX1, 4, 12); SCHED; \
            VWAIT(4, 6); PVM(4); VRD(8, (KB) ^ 1); MAXR(pn_, PX1, 12, 16); pm_ = fmaxf(pm_, pn_); \
            { auto rr_ = __builtin_amdgcn_permlane32_swap(__float_as_uint(pm_), __float_as_uint(pm_), false, false); pm_ = fmaxf(__uint_as_float(rr_[0]), __uint_as_float(rr_[1])); } SCHED; \
            VWAIT(5, 6); PVM(5); VRD(9, (KB) ^ 1); SCHED; \
            if (__builtin_expect(!first && __all(pm_ <= THR), 1)) { alX = 1.f; } \
            else { const float dl_ = first ? pm_ : fmaxf(pm_, 0.f); alX = first ? 1.f : __builtin_amdgcn_exp2f(-dl_); m_reg += dl_; first = false; \
                _Pragma("unroll") for (int r = 0; r < 16; ++r) { PX0[r] -= dl_; PX1[r] -= dl_; negm[r] = -m_reg; } } \
            SCHED; \
            VWAIT(6, 6); PVM(6); VRD(10, (KB) ^ 1); EXPR(PX0, 0, 2); SCHED; \
            VWAIT(7, 6); PVM(7); VRD(11, (KB) ^ 1); EXPR(PX0, 2, 4); SCHED; \
            VWAIT(8, 6); PVM(8); VRD(12, (KB) ^ 1); EXPR(PX0, 4, 6); SCHED; \
            VWAIT(9, 6); PVM(9); VRD(13, (KB) ^ 1); EXPR(PX0, 6, 8); SCHED; \
            VWAIT(10, 6); PVM(10); VRD(14, (KB) ^ 1); EXPR(PX0, 8, 10); SCHED; \
            VWAIT(11, 6); PVM(11); VRD(15, (KB) ^ 1); EXPR(PX0, 10, 12); SCHED; \
            VWAIT(12, 6); PVM(12); EXPR(PX0, 12, 14); SCHED; \
            VWAIT(13, 4); PVM(13); EXPR(PX0, 14, 16); SCHED; \
            VWAIT(14, 2); PVM(14); SCHED; \
            VWAIT(15, 0); PVM(15); SCHED; \
            AT_RESC(alX); \
            WAIT_V(0); BAR; } while (0)
        if (pass == 1) AT_STAGE_V(0, qb);
        SCHED;
        if (!dead0) {
            AT_QK(pA0, pA1, 0);
            const int dq_ = w4 * 32 + r32 - grp * 64 - 4 * hi; const float NEG_ = -__builtin_inff();
#pragma unroll
            for (int r = 0; r < 16; ++r) { const int cc_ = (r & 3) + 8 * (r >> 2); if (cc_ > dq_) pA0[r] = NEG_; if (cc_ + 32 > dq_) pA1[r] = NEG_; }
            AT_ROWMAX(pA0, pA1)
            m_reg = pm_; first = false;
#pragma unroll
            for (int r = 0; r < 16; ++r) { pA0[r] = __builtin_amdgcn_exp2f(pA0[r] - pm_); pA1[r] -= pm_; negm[r] = -pm_; }
        } else {
#pragma unroll
            for (int r = 0; r < 16; ++r) { pA0[r] = 0.f; pA1[r] = -__builtin_inff(); }
        }
        WAIT_V(0); BAR;
        int t = 1;
        for (; t + 1 < ns; t += 2) {
            AT_STEP(pB0, pB1, alB, pA0, pA1, alA, t, 1);
            AT_STEP(pA0, pA1, alA, pB0, pB1, alB, t + 1, 0);
        }
#define AT_PREF do { { const int lane = lane_id(), r32 = lane & 31, hi = lane >> 5; AT_LOADQ(qr, pi); } if (pass == 0) { AT_STAGE_K(0, pi); if (pi > 0) AT_STAGE_K(1, 0); } SCHED; } while (0)
        if (t < ns) {
            AT_STEP(pB0, pB1, alB, pA0, pA1, alA, t, 1);
            AT_PREF; AT_FSM(pB0, pB1, alB); SCHED; AT_PV(1);
        } else {
            AT_PREF; AT_FSM(pA0, pA1, alA); SCHED; AT_PV(0);
        }
        {
        const int lane = lane_id(), r32 = lane & 31, hi = lane >> 5;
        { auto rr_ = __builtin_amdgcn_permlane32_swap(__float_as_uint(l_reg), __float_as_uint(l_reg), false, false); l_reg = __uint_as_float(rr_[0]) + __uint_as_float(rr_[1]); }
        if (hi == 0) { ml_l[(wid * 32 + r32) * 2] = l_reg > 0.f ? m_reg : -1e30f; ml_l[(wid * 32 + r32) * 2 + 1] = l_reg; }
        WAIT_L(0); BAR;
        {
            const int ow = wid ^ 4;
            const float m0 = ml_l[(wid * 32 + r32) * 2], l0 = ml_l[(wid * 32 + r32) * 2 + 1], m1 = ml_l[(ow * 32 + r32) * 2], l1 = ml_l[(ow * 32 + r32) * 2 + 1];
            const float mm = fmaxf(m0, m1), a0 = __builtin_amdgcn_exp2f(m0 - mm), a1 = __builtin_amdgcn_exp2f(m1 - mm);
            const float fct = a0 / (l0 * a0 + l1 * a1);
            if (hi == 0) al_l[r32] = fct;
            WAIT_L(0);
#pragma unroll
            for (int c = 0; c < 4; ++c)
#pragma unroll
                for (int r = 0; r < 16; ++r) o[c][r] *= al_l[crow(r, hi)];
            LAS float* ex = (LAS float*)(lds + VOFF) + wid * 2048;
#pragma unroll
            for (int cc = 0; cc < 2; ++cc)
#pragma unroll
                for (int r = 0; r < 16; ++r) ex[(cc * 16 + r) * 64 + lane] = grp == 0 ? o[2 + cc][r] : o[cc][r];
            WAIT_L(0); BAR;
            const LAS float* ey = (const LAS float*)(lds + VOFF) + ow * 2048;
            u16* ob = ctxT + ((size_t)(head * 4 + grp * 2) * 128 + qb * 8 + w4 * 2) * 512 + r32 * 16 + hi * 8;
#pragma unroll
            for (int cc = 0; cc < 2; ++cc) {
                float v[16];
#pragma unroll
                for (int r = 0; r < 16; ++r) v[r] = (grp == 0 ? o[cc][r] : o[2 + cc][r]) + ey[(cc * 16 + r) * 64 + lane];
#pragma unroll
                for (int kk = 0; kk < 2; ++kk) {
                    const auto rx = __builtin_amdgcn_permlane32_swap(cvt_pk(v[8 * kk], v[8 * kk + 1]), cvt_pk(v[8 * kk + 4], v[8 * kk + 5]), false, false);
                    const auto ry = __builtin_amdgcn_permlane32_swap(cvt_pk(v[8 * kk + 2], v[8 * kk + 3]), cvt_pk(v[8 * kk + 6], v[8 * kk + 7]), false, false);
                    u32x4 w; w[0] = rx[0]; w[1] = ry[0]; w[2] = rx[1]; w[3] = ry[1];
                    *(u32x4*)(ob + (size_t)(cc * 128 + kk) * 512) = w;
                }
            }
            WAIT_V(4); AT_BINDQ(qr);
            WAIT_L(0); BAR;
        }
        }
#undef AT_PREF
#undef AT_QK
#undef AT_PV
#undef AT_PSM
#undef AT_ROWMAX
#undef AT_FSM
#undef AT_RESC
#undef AT_STEP
#undef KRD
#undef KWAIT
#undef VRD
#undef VWAIT
#undef QKA
#undef QKB
#undef PVM
#undef EXPR
#undef SUMR
#undef MAXR
#undef PKR
#undef GK
#undef GV
    }
#undef AT_STAGE_K
#undef AT_STAGE_V
#undef AT_LOADQ
#undef AT_BINDQ
#undef AT_LANEC
}

namespace g3 {
constexpr int NST = 4, STG = 36864, LDS_BYTES = NST * STG, NK1 = 64, NKT = 91, KSPL = 23;
}
__global__ __launch_bounds__(512, 2) void k_out(const u16* __restrict__ ctxT, const u16* __restrict__ wo, const u16* __restrict__ w2, const u16* __restrict__ gT,
                                                float* __restrict__ part) {
    using namespace g3;
    extern __shared__ __attribute__((aligned(16))) unsigned char shm_[];
    LAS unsigned char* lds = (LAS unsigned char*)shm_;
    const int tid = threadIdx.x, wid = __builtin_amdgcn_readfirstlane(tid >> 6), lane = tid & 63, fr = lane & 15, fq = lane >> 4;
    const int c_ = blockIdx.x, xcd = c_ & 7, jj = c_ >> 3;
    const int z = xcd >> 1, st = (xcd & 1) * 8 + (jj & 7), nt = jj >> 3, tile = st * 4 + nt;
    const int s0 = st * 128, n0 = nt * 160;
    const int kt0 = z * KSPL, kt1 = (kt0 + KSPL < NKT) ? kt0 + KSPL : NKT, nk = kt1 - kt0;
    if (wid >= 4) {
        const int lw = wid - 4, k = lw * 16 + (lane >> 2), ch = (lane & 3) ^ ((lane >> 5) << 1);
        const unsigned voffC = (unsigned)(((k >> 5) * 128 + (s0 >> 4) + (ch >> 1)) * 1024 + (k & 31) * 32 + (ch & 1) * 16);
        const unsigned voffW = (unsigned)(k * (S_ * 2) + (s0 + ch * 8) * 2);
        const unsigned voffB = (unsigned)(k * (M_ * 2) + (n0 + ch * 8) * 2);
        const unsigned voffG = (unsigned)((lw * 20 + nt * 5) * 1024 + (lane >> 2) * 64 + ch * 16);
        auto stage = [&](int i) {
            const int kt = kt0 + i;
            LAS unsigned char* dst = lds + (i & (NST - 1)) * STG + lw * 1024;
            if (kt < NK1) { const char* ga = (const char*)ctxT + (size_t)kt * 262144 + voffC; const char* gb = (const char*)wo + (size_t)kt * 81920 + voffB;
#pragma unroll
                for (int j = 0; j < 4; ++j) GLDS16(ga + j * 2048, dst + j * 4096);
#pragma unroll
                for (int j = 0; j < 5; ++j) GLDS16(gb + j * 64, dst + 16384 + j * 4096); }
            else { const char* ga = (const char*)w2 + (size_t)(kt - NK1) * 262144 + voffW; const char* gb = (const char*)gT + (size_t)(kt - NK1) * 81920 + voffG;
#pragma unroll
                for (int j = 0; j < 4; ++j) GLDS16(ga + j * 64, dst + j * 4096);
#pragma unroll
                for (int j = 0; j < 5; ++j) GLDS16(gb + j * 1024, dst + 16384 + j * 4096); }
        };
        stage(0); stage(1); stage(2); stage(3);
        WAIT_V(27); BAR;
        WAIT_V(18); BAR;
        for (int it = 0; it < nk; ++it) {
            if (it + 4 < nk) { stage(it + 4); WAIT_V(18); } else if (it + 3 < nk) WAIT_V(9); else WAIT_V(0);
            BAR;
        }
        return;
    }
    const int wm = wid & 1, wn = wid >> 1;
    int aE, aO, bE, bO;
    { const int q = fr >> 2, p = lane & 3, x0 = (int)(uintptr_t)lds + (8 * fq + q) * 64 + (2 * (fq & 1) + (p >> 1)) * 16 + (p & 1) * 8, x1 = x0 ^ 32;
      aE = x0 + wm * 8192; aO = x1 + wm * 8192;
      bE = wn ? x1 + 6 * 4096 : x0 + 4 * 4096; bO = wn ? x0 + 7 * 4096 : x1 + 4 * 4096; }
    f32x4 acc[4][5];
#pragma unroll
    for (int i = 0; i < 4; ++i)
#pragma unroll
        for (int n = 0; n < 5; ++n) acc[i][n] = (f32x4){0.f, 0.f, 0.f, 0.f};
    s16x4 fa[2][2][4][2], fb[2][2][5][2];
#define O_RA(B_, kh, i) do { TRRD(fa[B_][kh][i][0], ((i) & 1) ? a1_ : a0_, ((i) >> 1) * 4096 + (kh) * 2048); TRRD(fa[B_][kh][i][1], ((i) & 1) ? a1_ : a0_, ((i) >> 1) * 4096 + (kh) * 2048 + 256); } while (0)
#define O_RB(B_, kh, n) do { TRRD(fb[B_][kh][n][0], ((n) & 1) ? b1_ : b0_, ((n) >> 1) * 4096 + (kh) * 2048); TRRD(fb[B_][kh][n][1], ((n) & 1) ? b1_ : b0_, ((n) >> 1) * 4096 + (kh) * 2048 + 256); } while (0)
#define O_ADDR(i) const int so_ = ((i) & (NST - 1)) * STG; const int a0_ = aE + so_, a1_ = aO + so_, b0_ = bE + so_, b1_ = bO + so_
#define O_WT(B_) do { _Pragma("unroll") for (int kh = 0; kh < 2; ++kh) { \
        asm volatile("s_waitcnt lgkmcnt(0)" : "+v"(fa[B_][kh][0][0]), "+v"(fa[B_][kh][0][1]), "+v"(fa[B_][kh][1][0]), "+v"(fa[B_][kh][1][1]), "+v"(fa[B_][kh][2][0]), "+v"(fa[B_][kh][2][1]), "+v"(fa[B_][kh][3][0]), "+v"(fa[B_][kh][3][1]) :: "memory"); \
        asm volatile("" : "+v"(fb[B_][kh][0][0]), "+v"(fb[B_][kh][0][1]), "+v"(fb[B_][kh][1][0]), "+v"(fb[B_][kh][1][1]), "+v"(fb[B_][kh][2][0]), "+v"(fb[B_][kh][2][1]), "+v"(fb[B_][kh][3][0]), "+v"(fb[B_][kh][3][1]), "+v"(fb[B_][kh][4][0]), "+v"(fb[B_][kh][4][1]) :: "memory"); } } while (0)
#define O_M(B_, kh, n, i) acc[i][n] = __builtin_amdgcn_mfma_f32_16x16x32_bf16(cat4(fb[B_][kh][n][0], fb[B_][kh][n][1]), cat4(fa[B_][kh][i][0], fa[B_][kh][i][1]), acc[i][n], 0, 0, 0)
#define O_G(B_, kh, n, R0, R1) do { O_M(B_, kh, n, 0); O_M(B_, kh, n, 1); R0; SCHED; O_M(B_, kh, n, 2); O_M(B_, kh, n, 3); R1; SCHED; } while (0)
#define O_IT(B_, i) do { O_ADDR((i) + 1); constexpr int N_ = (B_) ^ 1; \
        O_G(B_, 0, 0, O_RA(N_, 0, 0), O_RA(N_, 0, 1)); \
        O_G(B_, 0, 1, O_RA(N_, 0, 2), O_RA(N_, 0, 3)); \
        O_G(B_, 0, 2, O_RB(N_, 0, 0); O_RB(N_, 0, 1), O_RB(N_, 0, 2)); \
        O_G(B_, 0, 3, O_RB(N_, 0, 3); O_RB(N_, 0, 4), O_RA(N_, 1, 0)); \
        O_G(B_, 0, 4, O_RA(N_, 1, 1); O_RA(N_, 1, 2), O_RA(N_, 1, 3)); \
        O_G(B_, 1, 0, O_RB(N_, 1, 0); O_RB(N_, 1, 1), O_RB(N_, 1, 2)); \
        O_G(B_, 1, 1, O_RB(N_, 1, 3), O_RB(N_, 1, 4)); \
        O_G(B_, 1, 2, , ); O_G(B_, 1, 3, , ); O_G(B_, 1, 4, , ); \
        O_WT(N_); BAR; SCHED; } while (0)
    BAR;
    { O_ADDR(0);
#pragma unroll
      for (int kh = 0; kh < 2; ++kh) {
        O_RA(0, kh, 0); O_RA(0, kh, 1); O_RA(0, kh, 2); O_RA(0, kh, 3); O_RB(0, kh, 0); O_RB(0, kh, 1); O_RB(0, kh, 2); O_RB(0, kh, 3); O_RB(0, kh, 4); }
      O_WT(0); }
    BAR; SCHED;
    __builtin_amdgcn_s_setprio(1);
    int it = 0;
    for (; it + 1 < nk; it += 2) { O_IT(0, it); O_IT(1, it + 1); }
    if (it < nk) O_IT(0, it);
    __builtin_amdgcn_s_setprio(0);
    float* pt = part + ((size_t)(tile * 4 + z) * 4 + wid) * 5120 + lane * 4;
#pragma unroll
    for (int i = 0; i < 4; ++i)
#pragma unroll
        for (int n = 0; n < 5; ++n) *(f32x4*)(pt + (i * 5 + n) * 256) = acc[i][n];
#undef O_RA
#undef O_RB
#undef O_ADDR
#undef O_WT
#undef O_M
#undef O_G
#undef O_IT
}

__global__ __launch_bounds__(512) void k_red(const float* __restrict__ part, const u16* __restrict__ x, const u16* __restrict__ b2, u16* __restrict__ out) {
    const int tile = blockIdx.x / 10, f = (blockIdx.x % 10) * 8 + (threadIdx.x >> 6), w = f / 20, j = f % 20, lane = threadIdx.x & 63, fr = lane & 15, fq = lane >> 4;
    const float* pt = part + (size_t)tile * 4 * 20480 + f * 256 + lane * 4;
    const f32x4 p0 = *(const f32x4*)pt, p1 = *(const f32x4*)(pt + 20480), p2 = *(const f32x4*)(pt + 2 * 20480), p3 = *(const f32x4*)(pt + 3 * 20480);
    const int s = (tile >> 2) * 128 + (w & 1) * 64 + (j / 5) * 16 + fr;
    const size_t o = (size_t)s * M_ + (tile & 3) * 160 + (w >> 1) * 80 + (j % 5) * 16 + fq * 4;
    const float bias = bf2f(b2[s]);
    const u32x2 xw = *(const u32x2*)(x + o);
    const f32x4 v = ((p0 + p1) + p2) + p3;
    u32x2 wv;
    wv.x = cvt_pk(v[0] + bias + bf2f(xw.x & 0xffffu), v[1] + bias + bf2f(xw.x >> 16));
    wv.y = cvt_pk(v[2] + bias + bf2f(xw.y & 0xffffu), v[3] + bias + bf2f(xw.y >> 16));
    *(u32x2*)(out + o) = wv;
}

extern "C" void kernel_launch(void* const* d_in, const int* in_sizes, int n_in, void* d_out, int out_size, void* d_ws, size_t ws_size,
                              hipStream_t stream) {
    const u16* x = (const u16*)d_in[0];
    const u16* ckv = (const u16*)d_in[1];
    const u16* wq = (const u16*)d_in[2];
    const u16* wuk = (const u16*)d_in[3];
    const u16* wuv = (const u16*)d_in[4];
    const u16* wo = (const u16*)d_in[5];
    const u16* w1 = (const u16*)d_in[7];
    const u16* b1 = (const u16*)d_in[8];
    const u16* w2 = (const u16*)d_in[9];
    const u16* b2 = (const u16*)d_in[10];
    char* ws = (char*)d_ws;
    size_t off = 0;
    u16* xn = (u16*)(ws + off); off += (size_t)S_ * M_ * 2;
    u16* hT = (u16*)(ws + off); off += (size_t)S_ * M_ * 2;
    u16* q = (u16*)(ws + off); off += (size_t)H_ * S_ * D_ * 2;
    u16* k = (u16*)(ws + off); off += (size_t)H_ * S_ * D_ * 2;
    u16* v = (u16*)(ws + off); off += (size_t)H_ * S_ * D_ * 2;
    float* upart = (float*)(ws + off); off += (size_t)4 * M_ * F_ * 4;
    u16* gT = (u16*)(ws + off); off += (size_t)F_ * M_ * 2;
    u16* ctxT = (u16*)(ws + off); off += (size_t)HD_ * S_ * 2;
    float* part = (float*)(ws + off); off += (size_t)4 * S_ * M_ * 4;
    (void)hipFuncSetAttribute((const void*)k_gemm1, hipFuncAttributeMaxDynamicSharedMemorySize, g1::STAGE_BYTES);
    (void)hipFuncSetAttribute((const void*)k_attn, hipFuncAttributeMaxDynamicSharedMemorySize, at::LDS_BYTES);
    (void)hipFuncSetAttribute((const void*)k_out, hipFuncAttributeMaxDynamicSharedMemorySize, g3::LDS_BYTES);
    k_ln<<<dim3(S_ / 8), dim3(512), 0, stream>>>(x, xn, hT);
    k_gemm1<<<dim3(256), dim3(512), g1::STAGE_BYTES, stream>>>(xn, ckv, wq, wuk, wuv, hT, w1, q, k, v, upart);
    k_attn<<<dim3(256), dim3(512), at::LDS_BYTES, stream>>>(q, k, v, ctxT, upart, b1, gT);
    k_out<<<dim3(256), dim3(512), g3::LDS_BYTES, stream>>>(ctxT, wo, w2, gT, part);
    k_red<<<dim3(640), dim3(512), 0, stream>>>(part, x, b2, (u16*)d_out);
}
```

```cpp
#include <hip/hip_runtime.h>
#include <hip/hip_bf16.h>
#include <stdint.h>

typedef unsigned short u16;
typedef short bf16x8 __attribute__((ext_vector_type(8)));
typedef short s16x4 __attribute__((ext_vector_type(4)));
typedef float f32x4 __attribute__((ext_vector_type(4)));
typedef float f32x16 __attribute__((ext_vector_type(16)));
typedef unsigned u32x4 __attribute__((ext_vector_type(4)));
typedef unsigned u32x2 __attribute__((ext_vector_type(2)));
#define LAS __attribute__((address_space(3)))

constexpr int S_ = 2048, M_ = 640, DC_ = 512, H_ = 32, D_ = 128, F_ = 1728, HD_ = 4096;

__device__ __forceinline__ float bf2f(unsigned b) { return __uint_as_float(b << 16); }
__device__ __forceinline__ unsigned cvt_pk(float lo, float hi) {
    unsigned r; asm volatile("v_cvt_pk_bf16_f32 %0, %1, %2" : "=v"(r) : "v"(lo), "v"(hi)); return r;
}
__device__ __forceinline__ float rbf(float x) { return __uint_as_float(cvt_pk(x, x) << 16); }
__device__ __forceinline__ bf16x8 cat4(s16x4 a, s16x4 b) { return __builtin_shufflevector(a, b, 0, 1, 2, 3, 4, 5, 6, 7); }
__device__ __forceinline__ s16x4 tr_read(const LAS unsigned char* p) { return __builtin_amdgcn_ds_read_tr16_b64_v4i16((LAS s16x4*)p); }

__device__ __forceinline__ int lane_id() { int l; asm volatile("v_mbcnt_lo_u32_b32 %0, -1, 0\n\tv_mbcnt_hi_u32_b32 %0, -1, %0" : "=v"(l)); return l; }
#define TRRD(dst, addr, off) asm volatile("ds_read_b64_tr_b16 %0, %1 offset:%2" : "=v"(dst) : "v"(addr), "i"(off) : "memory")
#define WAIT_L0_8(a, b, c, d, e, f, g, h) asm volatile("s_waitcnt lgkmcnt(0)" : "+v"(a), "+v"(b), "+v"(c), "+v"(d), "+v"(e), "+v"(f), "+v"(g), "+v"(h) :: "memory")
#define WAIT_V(n) asm volatile("s_waitcnt vmcnt(" #n ")" ::: "memory")
#define WAIT_L(n) asm volatile("s_waitcnt lgkmcnt(" #n ")" ::: "memory")
#define BAR __builtin_amdgcn_s_barrier()
#define SCHED __builtin_amdgcn_sched_barrier(0)
#define GLDS16(gptr, ldsptr) __builtin_amdgcn_global_load_lds((const unsigned*)(gptr), (LAS unsigned*)(ldsptr), 16, 0, 0)

template <int CTRL> __device__ __forceinline__ float dpp_add(float v) {
    return v + __int_as_float(__builtin_amdgcn_update_dpp(0, __float_as_int(v), CTRL, 0xf, 0xf, true));
}
__device__ __forceinline__ float wave_sum(float v) {
    v = dpp_add<0xB1>(v);
    v = dpp_add<0x4E>(v);
    v = dpp_add<0x141>(v);
    v = dpp_add<0x140>(v);
    { auto r = __builtin_amdgcn_permlane16_swap(__float_as_uint(v), __float_as_uint(v), false, false); v = __uint_as_float(r[0]) + __uint_as_float(r[1]); }
    { auto r = __builtin_amdgcn_permlane32_swap(__float_as_uint(v), __float_as_uint(v), false, false); v = __uint_as_float(r[0]) + __uint_as_float(r[1]); }
    return v;
}
typedef float f32p __attribute__((ext_vector_type(2)));
typedef __bf16 bf16p __attribute__((ext_vector_type(2)));
__device__ __forceinline__ unsigned cvp(f32p v) { return __builtin_bit_cast(unsigned, __builtin_convertvector(v, bf16p)); }
__device__ __forceinline__ f32p unp(unsigned w) { return (f32p){__uint_as_float(w << 16), __uint_as_float(w & 0xffff0000u)}; }
template <int N> struct IC { static constexpr int value = N; };
template <class H> __device__ __forceinline__ void ln2(unsigned (&w)[2][5], H&& hook) {
    f32p v[2][5]; float mean[2], var[2], den[2], r[2], mb[2];
#pragma unroll
    for (int e = 0; e < 2; ++e) {
#pragma unroll
        for (int i = 0; i < 5; ++i) v[e][i] = unp(w[e][i]);
        const f32p s = ((v[e][0] + v[e][1]) + (v[e][2] + v[e][3])) + v[e][4];
        mean[e] = s[0] + s[1];
    }
    hook(IC<0>{}, mean[0], mean[1]);
#pragma unroll
    for (int e = 0; e < 2; ++e) mean[e] = wave_sum(mean[e]) * (1.0f / 640.0f);
    hook(IC<1>{}, mean[0], mean[1]);
#pragma unroll
    for (int e = 0; e < 2; ++e) {
        f32p q = {0.f, 0.f};
#pragma unroll
        for (int i = 0; i < 5; ++i) { const f32p d = v[e][i] - mean[e]; q = __builtin_elementwise_fma(d, d, q); }
        var[e] = q[0] + q[1];
    }
    hook(IC<2>{}, var[0], var[1]);
#pragma unroll
    for (int e = 0; e < 2; ++e) var[e] = wave_sum(var[e]) * (1.0f / 640.0f);
    hook(IC<3>{}, var[0], var[1]);
#pragma unroll
    for (int e = 0; e < 2; ++e) {
        const f32p mv = unp(cvp((f32p){mean[e], var[e]}));
        const float ve = unp(cvp((f32p){mv[1] + 1e-5f, 0.f}))[0];
        den[e] = unp(cvp((f32p){__builtin_amdgcn_sqrtf(ve), 0.f}))[0]; r[e] = __builtin_amdgcn_rcpf(den[e]); mb[e] = mv[0];
    }
    hook(IC<4>{}, r[0], r[1]);
#pragma unroll
    for (int i = 0; i < 5; ++i) {
#pragma unroll
        for (int e = 0; e < 2; ++e) {
            const f32p a = unp(cvp(v[e][i] - mb[e])), q0 = a * r[e];
            w[e][i] = cvp(__builtin_elementwise_fma(__builtin_elementwise_fma(-q0, (f32p){den[e], den[e]}, a), (f32p){r[e], r[e]}, q0));
        }
        if (i == 1) { hook(IC<5>{}, w[1][1], mb[0]); }
        if (i == 2) { hook(IC<6>{}, w[1][2], mb[0]); }
        if (i == 3) { hook(IC<7>{}, w[1][3], mb[0]); }
    }
}

#define ACL_LO "a0","a1","a2","a3","a4","a5","a6","a7","a8","a9","a10","a11","a12","a13","a14","a15","a16","a17","a18","a19","a20","a21","a22","a23","a24","a25","a26","a27","a28","a29","a30","a31","a32","a33","a34","a35","a36","a37","a38","a39","a40","a41","a42","a43","a44","a45","a46","a47","a48","a49","a50","a51","a52","a53","a54","a55","a56","a57","a58","a59","a60","a61","a62","a63","a64","a65","a66","a67","a68","a69","a70","a71","a72","a73","a74","a75","a76","a77","a78","a79","a80","a81","a82","a83","a84","a85","a86","a87","a88","a89","a90","a91","a92","a93","a94","a95","a96","a97","a98","a99","a100","a101","a102","a103","a104","a105","a106","a107","a108","a109","a110","a111","a112","a113","a114","a115","a116","a117","a118","a119","a120","a121","a122","a123","a124","a125","a126","a127"
#define ACL_HI "a128","a129","a130","a131","a132","a133","a134","a135","a136","a137","a138","a139","a140","a141","a142","a143","a144","a145","a146","a147","a148","a149","a150","a151","a152","a153","a154","a155","a156","a157","a158","a159","a160","a161","a162","a163","a164","a165","a166","a167","a168","a169","a170","a171","a172","a173","a174","a175","a176","a177","a178","a179","a180","a181","a182","a183","a184","a185","a186","a187","a188","a189","a190","a191","a192","a193","a194","a195","a196","a197","a198","a199","a200","a201","a202","a203","a204","a205","a206","a207","a208","a209","a210","a211","a212","a213","a214","a215","a216","a217","a218","a219","a220","a221","a222","a223","a224","a225","a226","a227","a228","a229","a230","a231","a232","a233","a234","a235","a236","a237","a238","a239","a240","a241","a242","a243","a244","a245","a246","a247","a248","a249","a250","a251","a252","a253","a254","a255"
#define VCL9 "v0","v1","v2","v3","v4","v5","v6","v7","v8","v9","v10","v11","v12","v13","v14","v15","v16","v17","v18","v19","v20","v21","v22","v23","v24","v25","v26","v27","v28","v29","v30","v31","v32","v33","v34","v35","v36","v37","v38","v39","v40","v41","v42","v43","v44","v45","v46","v47","v48","v49","v50","v51","v52","v53","v54","v55","v56","v57","v58","v59","v60","v61","v62","v63","v64","v65","v66","v67","v68","v69","v70","v71","v72","v73","v74","v75","v76","v77","v78","v79","v80","v81","v82","v83","v84","v85","v86","v87","v88","v89","v90","v91","v92","v93","v94","v95","v96","v97","v98","v99","v100","v101","v102","v103","v104","v105","v106","v107","v108","v109","v110","v111","v112","v113","v114","v115","v116","v117","v118","v119","v120","v121","v122","v123","v124","v125","v126","v127","v128","v129","v130","v131","v132","v133","v134","v135","v136","v137","v138","v139","v140","v141","v142","v143","v144","v145","v146","v147","v148","v149","v150","v151","v152","v153","v154","v155","v156","v157","v158","v159","v160","v161","v162","v163","v164","v165","v166","v167","v168","v169","v170","v171","v172","v173","v174","v175","v176","v177","v178","v179","v180","v181","v182","v183","v184","v185","v186","v187","v188","v189","v190","v191","v192","v193","v194","v195","v196","v197","v198","v199","v200","v201","v202","v203","v204","v205","v206","v207","v208","v209","v210","v211","v212","v213","v214","v215","v216","v217","v218","v219","v220","v221","v222","v223","v245","v246","v247","v248","v249","v250","v251","v252","v253"
#define NKP 0
#define KPE 0
#define G10_PRO_KV0 \
    "s_add_u32 m0, %[m0w], 0x10000\ns_add_u32 s81, %[sw], 0x0\nbuffer_load_dwordx4 v240, %[srdW], s81 offen offset:0 lds\nbuffer_load_dwordx4 v241, %[srdW], s81 offen offset:1024 lds\n" \
    "buffer_load_dwordx4 v242, %[srdW], s81 offen offset:2048 lds\nbuffer_load_dwordx4 v243, %[srdW], s81 offen offset:3072 lds\ns_add_u32 m0, %[m0w], 0x12000\ns_add_u32 s81, %[sw], 0x20000\n" \
    "buffer_load_dwordx4 v240, %[srdW], s81 offen offset:0 lds\nbuffer_load_dwordx4 v241, %[srdW], s81 offen offset:1024 lds\nbuffer_load_dwordx4 v242, %[srdW], s81 offen offset:2048 lds\n" \
    "buffer_load_dwordx4 v243, %[srdW], s81 offen offset:3072 lds\ns_add_u32 %[sw], %[sw], 0x80000\ns_add_u32 m0, %[m0x], 0x0\ns_add_u32 s80, %[sx], 0x0\nbuffer_load_dwordx4 v236, %[srdX], s80 offen offset:0 lds\n" \
    "buffer_load_dwordx4 v237, %[srdX], s80 offen offset:1024 lds\nbuffer_load_dwordx4 v238, %[srdX], s80 offen offset:2048 lds\nbuffer_load_dwordx4 v239, %[srdX], s80 offen offset:3072 lds\ns_add_u32 m0, %[m0x], 0x1000\n" \
    "s_add_u32 s80, %[sx], 0x8000\nbuffer_load_dwordx4 v236, %[srdX], s80 offen offset:0 lds\nbuffer_load_dwordx4 v237, %[srdX], s80 offen offset:1024 lds\nbuffer_load_dwordx4 v238, %[srdX], s80 offen offset:2048 lds\n" \
    "buffer_load_dwordx4 v239, %[srdX], s80 offen offset:3072 lds\ns_add_u32 %[sx], %[sx], 0x80\n"
#define G10_PRO_KV1 \
    "s_add_u32 m0, %[m0w], 0x18000\ns_add_u32 s81, %[sw], 0x0\nbuffer_load_dwordx4 v240, %[srdW], s81 offen offset:0 lds\nbuffer_load_dwordx4 v241, %[srdW], s81 offen offset:1024 lds\n" \
    "buffer_load_dwordx4 v242, %[srdW], s81 offen offset:2048 lds\nbuffer_load_dwordx4 v243, %[srdW], s81 offen offset:3072 lds\ns_add_u32 m0, %[m0w], 0x1a000\ns_add_u32 s81, %[sw], 0x20000\n" \
    "buffer_load_dwordx4 v240, %[srdW], s81 offen offset:0 lds\nbuffer_load_dwordx4 v241, %[srdW], s81 offen offset:1024 lds\nbuffer_load_dwordx4 v242, %[srdW], s81 offen offset:2048 lds\n" \
    "buffer_load_dwordx4 v243, %[srdW], s81 offen offset:3072 lds\ns_add_u32 %[sw], %[sw], 0x80000\ns_add_u32 m0, %[m0x], 0x8000\ns_add_u32 s80, %[sx], 0x0\nbuffer_load_dwordx4 v236, %[srdX], s80 offen offset:0 lds\n" \
    "buffer_load_dwordx4 v237, %[srdX], s80 offen offset:1024 lds\nbuffer_load_dwordx4 v238, %[srdX], s80 offen offset:2048 lds\nbuffer_load_dwordx4 v239, %[srdX], s80 offen offset:3072 lds\ns_add_u32 m0, %[m0x], 0x9000\n" \
    "s_add_u32 s80, %[sx], 0x8000\nbuffer_load_dwordx4 v236, %[srdX], s80 offen offset:0 lds\nbuffer_load_dwordx4 v237, %[srdX], s80 offen offset:1024 lds\nbuffer_load_dwordx4 v238, %[srdX], s80 offen offset:2048 lds\n" \
    "buffer_load_dwordx4 v239, %[srdX], s80 offen offset:3072 lds\ns_add_u32 %[sx], %[sx], 0x80\n"
#define G10_MAIN \
    "s_memrealtime s[90:91]\ns_waitcnt vmcnt(28)\ns_barrier\nds_read_b128 a[32:35], v224 offset:0\nds_read_b128 a[36:39], v224 offset:2048\nds_read_b128 a[40:43], v224 offset:4096\nds_read_b128 a[44:47], v224 offset:6144\n" \
    "ds_read_b128 a[48:51], v224 offset:8192\nds_read_b128 a[52:55], v224 offset:10240\nds_read_b128 a[56:59], v224 offset:12288\nds_read_b128 a[60:63], v224 offset:14336\nds_read_b64_tr_b16 a[64:65], v228 offset:0\n" \
    "ds_read_b64_tr_b16 a[66:67], v228 offset:2048\nds_read_b64_tr_b16 a[68:69], v229 offset:0\nds_read_b64_tr_b16 a[70:71], v229 offset:2048\nds_read_b64_tr_b16 a[72:73], v230 offset:0\n" \
    "ds_read_b64_tr_b16 a[74:75], v230 offset:2048\nds_read_b64_tr_b16 a[76:77], v231 offset:0\nds_read_b64_tr_b16 a[78:79], v231 offset:2048\nds_read_b64_tr_b16 a[80:81], v232 offset:0\n" \
    "ds_read_b64_tr_b16 a[82:83], v232 offset:2048\nds_read_b64_tr_b16 a[84:85], v233 offset:0\nds_read_b64_tr_b16 a[86:87], v233 offset:2048\nds_read_b64_tr_b16 a[88:89], v234 offset:0\n" \
    "ds_read_b64_tr_b16 a[90:91], v234 offset:2048\nds_read_b64_tr_b16 a[92:93], v235 offset:0\nds_read_b64_tr_b16 a[94:95], v235 offset:2048\ns_mov_b32 s84, 0x10000\ns_mov_b32 s85, 0x18000\ns_mov_b32 s86, 0x20000\n" \
    "s_mov_b32 %[cnt], 8\ns_waitcnt lgkmcnt(0)\nv_mfma_f32_16x16x32_bf16 a[0:3], a[64:67], a[32:35], 0\nv_mov_b32_e32 v246, s90\nv_mov_b32_e32 v247, s91\nds_read_b128 a[96:99], v225 offset:0\n" \
    "v_mfma_f32_16x16x32_bf16 a[4:7], a[68:71], a[32:35], 0\nbuffer_load_dwordx4 a[160:163], v244, %[srdP], %[sp] offen\ns_add_u32 %[sp], %[sp], %[spst]\nds_read_b128 a[100:103], v225 offset:2048\ns_mov_b32 s83, %[sw]\n" \
    "v_mfma_f32_16x16x32_bf16 a[8:11], a[64:67], a[36:39], 0\nbuffer_load_dwordx4 a[164:167], v244, %[srdP], %[sp] offen\ns_add_u32 %[sp], %[sp], %[spst]\ns_add_u32 %[sw], %[sw], 0x80000\n" \
    "v_mfma_f32_16x16x32_bf16 a[12:15], a[68:71], a[36:39], 0\nds_read_b128 a[104:107], v225 offset:4096\ns_add_u32 m0, %[m0w], s86\ns_add_u32 s81, s83, 0x0\nbuffer_load_dwordx4 v240, %[srdW], s81 offen offset:0 lds\n" \
    "v_mfma_f32_16x16x32_bf16 a[16:19], a[64:67], a[40:43], 0\nv_mfma_f32_16x16x32_bf16 a[20:23], a[68:71], a[40:43], 0\nds_read_b128 a[108:111], v225 offset:6144\nv_mfma_f32_16x16x32_bf16 a[24:27], a[64:67], a[44:47], 0\n" \
    "v_mfma_f32_16x16x32_bf16 a[28:31], a[68:71], a[44:47], 0\nds_read_b128 a[112:115], v225 offset:8192\nv_mfma_f32_16x16x32_bf16 v[0:3], a[64:67], a[48:51], 0\nv_mfma_f32_16x16x32_bf16 v[4:7], a[68:71], a[48:51], 0\n" \
    "ds_read_b128 a[116:119], v225 offset:10240\nbuffer_load_dwordx4 v241, %[srdW], s81 offen offset:1024 lds\nv_mfma_f32_16x16x32_bf16 v[8:11], a[64:67], a[52:55], 0\n" \
    "v_mfma_f32_16x16x32_bf16 v[12:15], a[68:71], a[52:55], 0\nds_read_b128 a[120:123], v225 offset:12288\nv_mfma_f32_16x16x32_bf16 v[16:19], a[64:67], a[56:59], 0\nv_mfma_f32_16x16x32_bf16 v[20:23], a[68:71], a[56:59], 0\n" \
    "ds_read_b128 a[124:127], v225 offset:14336\nv_mfma_f32_16x16x32_bf16 v[24:27], a[64:67], a[60:63], 0\nv_mfma_f32_16x16x32_bf16 v[28:31], a[68:71], a[60:63], 0\nds_read_b64_tr_b16 a[128:129], v228 offset:16384\n" \
    "buffer_load_dwordx4 v242, %[srdW], s81 offen offset:2048 lds\nv_mfma_f32_16x16x32_bf16 v[32:35], a[72:75], a[32:35], 0\nds_read_b64_tr_b16 a[130:131], v228 offset:18432\n" \
    "v_mfma_f32_16x16x32_bf16 v[36:39], a[76:79], a[32:35], 0\nv_mfma_f32_16x16x32_bf16 v[40:43], a[72:75], a[36:39], 0\nds_read_b64_tr_b16 a[132:133], v229 offset:16384\n" \
    "v_mfma_f32_16x16x32_bf16 v[44:47], a[76:79], a[36:39], 0\nv_mfma_f32_16x16x32_bf16 v[48:51], a[72:75], a[40:43], 0\nds_read_b64_tr_b16 a[134:135], v229 offset:18432\n" \
    "v_mfma_f32_16x16x32_bf16 v[52:55], a[76:79], a[40:43], 0\nbuffer_load_dwordx4 v243, %[srdW], s81 offen offset:3072 lds\nv_mfma_f32_16x16x32_bf16 v[56:59], a[72:75], a[44:47], 0\n" \
    "ds_read_b64_tr_b16 a[136:137], v230 offset:16384\nv_mfma_f32_16x16x32_bf16 v[60:63], a[76:79], a[44:47], 0\nv_mfma_f32_16x16x32_bf16 v[64:67], a[72:75], a[48:51], 0\nds_read_b64_tr_b16 a[138:139], v230 offset:18432\n" \
    "v_mfma_f32_16x16x32_bf16 v[68:71], a[76:79], a[48:51], 0\nv_mfma_f32_16x16x32_bf16 v[72:75], a[72:75], a[52:55], 0\nds_read_b64_tr_b16 a[140:141], v231 offset:16384\n" \
    "v_mfma_f32_16x16x32_bf16 v[76:79], a[76:79], a[52:55], 0\nv_mfma_f32_16x16x32_bf16 v[80:83], a[72:75], a[56:59], 0\nds_read_b64_tr_b16 a[142:143], v231 offset:18432\ns_add_u32 m0, m0, 0x2000\n" \
    "s_add_u32 s81, s83, 0x20000\nbuffer_load_dwordx4 v240, %[srdW], s81 offen offset:0 lds\nv_mfma_f32_16x16x32_bf16 v[84:87], a[76:79], a[56:59], 0\nv_mfma_f32_16x16x32_bf16 v[88:91], a[72:75], a[60:63], 0\n" \
    "ds_read_b64_tr_b16 a[144:145], v232 offset:16384\nv_mfma_f32_16x16x32_bf16 v[92:95], a[76:79], a[60:63], 0\nds_read_b64_tr_b16 a[146:147], v232 offset:18432\nv_mfma_f32_16x16x32_bf16 v[96:99], a[80:83], a[32:35], 0\n" \
    "v_mfma_f32_16x16x32_bf16 v[100:103], a[84:87], a[32:35], 0\nds_read_b64_tr_b16 a[148:149], v233 offset:16384\nv_mfma_f32_16x16x32_bf16 v[104:107], a[80:83], a[36:39], 0\n" \
    "buffer_load_dwordx4 v241, %[srdW], s81 offen offset:1024 lds\nv_mfma_f32_16x16x32_bf16 v[108:111], a[84:87], a[36:39], 0\nds_read_b64_tr_b16 a[150:151], v233 offset:18432\n" \
    "v_mfma_f32_16x16x32_bf16 v[112:115], a[80:83], a[40:43], 0\nv_mfma_f32_16x16x32_bf16 v[116:119], a[84:87], a[40:43], 0\nds_read_b64_tr_b16 a[152:153], v234 offset:16384\n" \
    "v_mfma_f32_16x16x32_bf16 v[120:123], a[80:83], a[44:47], 0\nv_mfma_f32_16x16x32_bf16 v[124:127], a[84:87], a[44:47], 0\nds_read_b64_tr_b16 a[154:155], v234 offset:18432\n" \
    "v_mfma_f32_16x16x32_bf16 v[128:131], a[80:83], a[48:51], 0\nbuffer_load_dwordx4 v242, %[srdW], s81 offen offset:2048 lds\nv_mfma_f32_16x16x32_bf16 v[132:135], a[84:87], a[48:51], 0\n" \
    "ds_read_b64_tr_b16 a[156:157], v235 offset:16384\nv_mfma_f32_16x16x32_bf16 v[136:139], a[80:83], a[52:55], 0\nv_mfma_f32_16x16x32_bf16 v[140:143], a[84:87], a[52:55], 0\nds_read_b64_tr_b16 a[158:159], v235 offset:18432\n" \
    "v_mfma_f32_16x16x32_bf16 v[144:147], a[80:83], a[56:59], 0\nv_mfma_f32_16x16x32_bf16 v[148:151], a[84:87], a[56:59], 0\nv_mfma_f32_16x16x32_bf16 v[152:155], a[80:83], a[60:63], 0\n" \
    "buffer_load_dwordx4 v243, %[srdW], s81 offen offset:3072 lds\nv_mfma_f32_16x16x32_bf16 v[156:159], a[84:87], a[60:63], 0\nv_mfma_f32_16x16x32_bf16 v[160:163], a[88:91], a[32:35], 0\n" \
    "v_mfma_f32_16x16x32_bf16 v[164:167], a[92:95], a[32:35], 0\nv_mfma_f32_16x16x32_bf16 v[168:171], a[88:91], a[36:39], 0\nv_mfma_f32_16x16x32_bf16 v[172:175], a[92:95], a[36:39], 0\n" \
    "v_mfma_f32_16x16x32_bf16 v[176:179], a[88:91], a[40:43], 0\nv_mfma_f32_16x16x32_bf16 v[180:183], a[92:95], a[40:43], 0\nv_mfma_f32_16x16x32_bf16 v[184:187], a[88:91], a[44:47], 0\n" \
    "v_mfma_f32_16x16x32_bf16 v[188:191], a[92:95], a[44:47], 0\nv_mfma_f32_16x16x32_bf16 v[192:195], a[88:91], a[48:51], 0\nv_mfma_f32_16x16x32_bf16 v[196:199], a[92:95], a[48:51], 0\n" \
    "v_mfma_f32_16x16x32_bf16 v[200:203], a[88:91], a[52:55], 0\nv_mfma_f32_16x16x32_bf16 v[204:207], a[92:95], a[52:55], 0\nv_mfma_f32_16x16x32_bf16 v[208:211], a[88:91], a[56:59], 0\n" \
    "v_mfma_f32_16x16x32_bf16 v[212:215], a[92:95], a[56:59], 0\nv_mfma_f32_16x16x32_bf16 v[216:219], a[88:91], a[60:63], 0\nv_mfma_f32_16x16x32_bf16 v[220:223], a[92:95], a[60:63], 0\ns_waitcnt vmcnt(10) lgkmcnt(0)\n" \
    "s_barrier\nL_al%=:\nv_mfma_f32_16x16x32_bf16 a[0:3], a[128:131], a[96:99], a[0:3]\nbuffer_store_dwordx2 v[246:247], off, %[srdF], %[fso] sc0 sc1\ns_mov_b32 %[fso], 0x70000000\nv_accvgpr_read_b32 v245, a236\n" \
    "s_mov_b32 s83, %[sx]\nv_mfma_f32_16x16x32_bf16 a[4:7], a[132:135], a[96:99], a[4:7]\ns_sub_u32 s81, s85, s84\ns_mov_b32 s87, s84\ns_add_u32 m0, %[m0x], 0x0\ns_add_u32 s80, s83, 0x0\n" \
    "buffer_load_dwordx4 v236, %[srdX], s80 offen offset:0 lds\nv_mfma_f32_16x16x32_bf16 a[8:11], a[128:131], a[100:103], a[8:11]\ns_mov_b32 s84, s85\ns_mov_b32 s85, s86\n" \
    "v_mfma_f32_16x16x32_bf16 a[12:15], a[132:135], a[100:103], a[12:15]\ns_add_u32 %[sx], %[sx], 0x80\ns_mov_b32 s86, s87\nv_mfma_f32_16x16x32_bf16 a[16:19], a[128:131], a[104:107], a[16:19]\nv_add_u32_e32 v228, s81, v228\n" \
    "ds_read_b128 a[32:35], v224 offset:32768\nv_mfma_f32_16x16x32_bf16 a[20:23], a[132:135], a[104:107], a[20:23]\nv_add_u32_e32 v229, s81, v229\nbuffer_load_dwordx4 v237, %[srdX], s80 offen offset:1024 lds\n" \
    "v_mfma_f32_16x16x32_bf16 a[24:27], a[128:131], a[108:111], a[24:27]\nv_add_u32_e32 v230, s81, v230\nds_read_b128 a[36:39], v224 offset:34816\nv_mfma_f32_16x16x32_bf16 a[28:31], a[132:135], a[108:111], a[28:31]\n" \
    "v_add_u32_e32 v231, s81, v231\nv_mfma_f32_16x16x32_bf16 v[0:3], a[128:131], a[112:115], v[0:3]\nv_add_u32_e32 v232, s81, v232\nds_read_b128 a[40:43], v224 offset:36864\n" \
    "v_mfma_f32_16x16x32_bf16 v[4:7], a[132:135], a[112:115], v[4:7]\nv_add_u32_e32 v233, s81, v233\nv_mfma_f32_16x16x32_bf16 v[8:11], a[128:131], a[116:119], v[8:11]\nv_add_u32_e32 v234, s81, v234\n" \
    "ds_read_b128 a[44:47], v224 offset:38912\nbuffer_load_dwordx4 v238, %[srdX], s80 offen offset:2048 lds\nv_mfma_f32_16x16x32_bf16 v[12:15], a[132:135], a[116:119], v[12:15]\nv_add_u32_e32 v235, s81, v235\n" \
    "v_mfma_f32_16x16x32_bf16 v[16:19], a[128:131], a[120:123], v[16:19]\nds_read_b128 a[48:51], v224 offset:40960\ns_sub_u32 %[cnt], %[cnt], 1\nv_mfma_f32_16x16x32_bf16 v[20:23], a[132:135], a[120:123], v[20:23]\n" \
    "v_mfma_f32_16x16x32_bf16 v[24:27], a[128:131], a[124:127], v[24:27]\nds_read_b128 a[52:55], v224 offset:43008\nbuffer_load_dwordx4 v239, %[srdX], s80 offen offset:3072 lds\n" \
    "v_mfma_f32_16x16x32_bf16 v[28:31], a[132:135], a[124:127], v[28:31]\nv_mfma_f32_16x16x32_bf16 v[32:35], a[136:139], a[96:99], v[32:35]\nds_read_b128 a[56:59], v224 offset:45056\n" \
    "v_mfma_f32_16x16x32_bf16 v[36:39], a[140:143], a[96:99], v[36:39]\nv_mfma_f32_16x16x32_bf16 v[40:43], a[136:139], a[100:103], v[40:43]\nds_read_b128 a[60:63], v224 offset:47104\n" \
    "v_mfma_f32_16x16x32_bf16 v[44:47], a[140:143], a[100:103], v[44:47]\ns_add_u32 m0, %[m0x], 0x1000\ns_add_u32 s80, s83, 0x8000\nbuffer_load_dwordx4 v236, %[srdX], s80 offen offset:0 lds\n" \
    "v_mfma_f32_16x16x32_bf16 v[48:51], a[136:139], a[104:107], v[48:51]\nds_read_b64_tr_b16 a[64:65], v228 offset:0\nv_mfma_f32_16x16x32_bf16 v[52:55], a[140:143], a[104:107], v[52:55]\n" \
    "ds_read_b64_tr_b16 a[66:67], v228 offset:2048\nv_mfma_f32_16x16x32_bf16 v[56:59], a[136:139], a[108:111], v[56:59]\nv_mfma_f32_16x16x32_bf16 v[60:63], a[140:143], a[108:111], v[60:63]\n" \
    "ds_read_b64_tr_b16 a[68:69], v229 offset:0\nbuffer_load_dwordx4 v237, %[srdX], s80 offen offset:1024 lds\nv_mfma_f32_16x16x32_bf16 v[64:67], a[136:139], a[112:115], v[64:67]\n" \
    "ds_read_b64_tr_b16 a[70:71], v229 offset:2048\nv_mfma_f32_16x16x32_bf16 v[68:71], a[140:143], a[112:115], v[68:71]\nv_mfma_f32_16x16x32_bf16 v[72:75], a[136:139], a[116:119], v[72:75]\n" \
    "ds_read_b64_tr_b16 a[72:73], v230 offset:0\nv_mfma_f32_16x16x32_bf16 v[76:79], a[140:143], a[116:119], v[76:79]\nds_read_b64_tr_b16 a[74:75], v230 offset:2048\n" \
    "v_mfma_f32_16x16x32_bf16 v[80:83], a[136:139], a[120:123], v[80:83]\nbuffer_load_dwordx4 v238, %[srdX], s80 offen offset:2048 lds\nv_mfma_f32_16x16x32_bf16 v[84:87], a[140:143], a[120:123], v[84:87]\n" \
    "ds_read_b64_tr_b16 a[76:77], v231 offset:0\nv_mfma_f32_16x16x32_bf16 v[88:91], a[136:139], a[124:127], v[88:91]\nds_read_b64_tr_b16 a[78:79], v231 offset:2048\n" \
    "v_mfma_f32_16x16x32_bf16 v[92:95], a[140:143], a[124:127], v[92:95]\nv_mfma_f32_16x16x32_bf16 v[96:99], a[144:147], a[96:99], v[96:99]\nds_read_b64_tr_b16 a[80:81], v232 offset:0\n" \
    "buffer_load_dwordx4 v239, %[srdX], s80 offen offset:3072 lds\nv_mfma_f32_16x16x32_bf16 v[100:103], a[148:151], a[96:99], v[100:103]\nv_mfma_f32_16x16x32_bf16 v[104:107], a[144:147], a[100:103], v[104:107]\n" \
    "ds_read_b64_tr_b16 a[82:83], v232 offset:2048\nv_mfma_f32_16x16x32_bf16 v[108:111], a[148:151], a[100:103], v[108:111]\nds_read_b64_tr_b16 a[84:85], v233 offset:0\n" \
    "v_mfma_f32_16x16x32_bf16 v[112:115], a[144:147], a[104:107], v[112:115]\nv_mfma_f32_16x16x32_bf16 v[116:119], a[148:151], a[104:107], v[116:119]\nds_read_b64_tr_b16 a[86:87], v233 offset:2048\n" \
    "v_mfma_f32_16x16x32_bf16 v[120:123], a[144:147], a[108:111], v[120:123]\nds_read_b64_tr_b16 a[88:89], v234 offset:0\nv_mfma_f32_16x16x32_bf16 v[124:127], a[148:151], a[108:111], v[124:127]\n" \
    "v_mfma_f32_16x16x32_bf16 v[128:131], a[144:147], a[112:115], v[128:131]\nds_read_b64_tr_b16 a[90:91], v234 offset:2048\nv_mfma_f32_16x16x32_bf16 v[132:135], a[148:151], a[112:115], v[132:135]\n" \
    "ds_read_b64_tr_b16 a[92:93], v235 offset:0\nv_mfma_f32_16x16x32_bf16 v[136:139], a[144:147], a[116:119], v[136:139]\nv_mfma_f32_16x16x32_bf16 v[140:143], a[148:151], a[116:119], v[140:143]\n" \
    "ds_read_b64_tr_b16 a[94:95], v235 offset:2048\nv_mfma_f32_16x16x32_bf16 v[144:147], a[144:147], a[120:123], v[144:147]\nv_mfma_f32_16x16x32_bf16 v[148:151], a[148:151], a[120:123], v[148:151]\n" \
    "v_mfma_f32_16x16x32_bf16 v[152:155], a[144:147], a[124:127], v[152:155]\nv_mfma_f32_16x16x32_bf16 v[156:159], a[148:151], a[124:127], v[156:159]\nv_mfma_f32_16x16x32_bf16 v[160:163], a[152:155], a[96:99], v[160:163]\n" \
    "v_mfma_f32_16x16x32_bf16 v[164:167], a[156:159], a[96:99], v[164:167]\nv_mfma_f32_16x16x32_bf16 v[168:171], a[152:155], a[100:103], v[168:171]\nv_mfma_f32_16x16x32_bf16 v[172:175], a[156:159], a[100:103], v[172:175]\n" \
    "v_mfma_f32_16x16x32_bf16 v[176:179], a[152:155], a[104:107], v[176:179]\nv_mfma_f32_16x16x32_bf16 v[180:183], a[156:159], a[104:107], v[180:183]\nv_mfma_f32_16x16x32_bf16 v[184:187], a[152:155], a[108:111], v[184:187]\n" \
    "v_mfma_f32_16x16x32_bf16 v[188:191], a[156:159], a[108:111], v[188:191]\nv_mfma_f32_16x16x32_bf16 v[192:195], a[152:155], a[112:115], v[192:195]\nv_mfma_f32_16x16x32_bf16 v[196:199], a[156:159], a[112:115], v[196:199]\n" \
    "v_mfma_f32_16x16x32_bf16 v[200:203], a[152:155], a[116:119], v[200:203]\nv_mfma_f32_16x16x32_bf16 v[204:207], a[156:159], a[116:119], v[204:207]\nv_mfma_f32_16x16x32_bf16 v[208:211], a[152:155], a[120:123], v[208:211]\n" \
    "v_mfma_f32_16x16x32_bf16 v[212:215], a[156:159], a[120:123], v[212:215]\nv_mfma_f32_16x16x32_bf16 v[216:219], a[152:155], a[124:127], v[216:219]\nv_mfma_f32_16x16x32_bf16 v[220:223], a[156:159], a[124:127], v[220:223]\n" \
    "s_waitcnt lgkmcnt(0)\nv_mfma_f32_16x16x32_bf16 a[0:3], a[64:67], a[32:35], a[0:3]\nbuffer_load_dwordx2 v[248:249], v245, %[srdF], %[flo] offen sc0 sc1\nds_read_b128 a[96:99], v225 offset:32768\n" \
    "v_mfma_f32_16x16x32_bf16 a[4:7], a[68:71], a[32:35], a[4:7]\nbuffer_load_dwordx4 a[160:163], v244, %[srdP], %[sp] offen\ns_add_u32 %[sp], %[sp], %[spst]\nds_read_b128 a[100:103], v225 offset:34816\ns_mov_b32 s83, %[sw]\n" \
    "v_mfma_f32_16x16x32_bf16 a[8:11], a[64:67], a[36:39], a[8:11]\nbuffer_load_dwordx4 a[164:167], v244, %[srdP], %[sp] offen\ns_add_u32 %[sp], %[sp], %[spst]\ns_add_u32 %[sw], %[sw], 0x80000\n" \
    "v_mfma_f32_16x16x32_bf16 a[12:15], a[68:71], a[36:39], a[12:15]\nds_read_b128 a[104:107], v225 offset:36864\ns_add_u32 m0, %[m0w], s86\ns_add_u32 s81, s83, 0x0\nbuffer_load_dwordx4 v240, %[srdW], s81 offen offset:0 lds\n" \
    "v_mfma_f32_16x16x32_bf16 a[16:19], a[64:67], a[40:43], a[16:19]\nv_mfma_f32_16x16x32_bf16 a[20:23], a[68:71], a[40:43], a[20:23]\nds_read_b128 a[108:111], v225 offset:38912\n" \
    "v_mfma_f32_16x16x32_bf16 a[24:27], a[64:67], a[44:47], a[24:27]\nv_mfma_f32_16x16x32_bf16 a[28:31], a[68:71], a[44:47], a[28:31]\nds_read_b128 a[112:115], v225 offset:40960\n" \
    "v_mfma_f32_16x16x32_bf16 v[0:3], a[64:67], a[48:51], v[0:3]\nv_mfma_f32_16x16x32_bf16 v[4:7], a[68:71], a[48:51], v[4:7]\nds_read_b128 a[116:119], v225 offset:43008\n" \
    "buffer_load_dwordx4 v241, %[srdW], s81 offen offset:1024 lds\nv_mfma_f32_16x16x32_bf16 v[8:11], a[64:67], a[52:55], v[8:11]\nv_mfma_f32_16x16x32_bf16 v[12:15], a[68:71], a[52:55], v[12:15]\n" \
    "ds_read_b128 a[120:123], v225 offset:45056\nv_mfma_f32_16x16x32_bf16 v[16:19], a[64:67], a[56:59], v[16:19]\nv_mfma_f32_16x16x32_bf16 v[20:23], a[68:71], a[56:59], v[20:23]\nds_read_b128 a[124:127], v225 offset:47104\n" \
    "v_mfma_f32_16x16x32_bf16 v[24:27], a[64:67], a[60:63], v[24:27]\nv_mfma_f32_16x16x32_bf16 v[28:31], a[68:71], a[60:63], v[28:31]\nds_read_b64_tr_b16 a[128:129], v228 offset:16384\n" \
    "buffer_load_dwordx4 v242, %[srdW], s81 offen offset:2048 lds\nv_mfma_f32_16x16x32_bf16 v[32:35], a[72:75], a[32:35], v[32:35]\nds_read_b64_tr_b16 a[130:131], v228 offset:18432\n" \
    "v_mfma_f32_16x16x32_bf16 v[36:39], a[76:79], a[32:35], v[36:39]\nv_mfma_f32_16x16x32_bf16 v[40:43], a[72:75], a[36:39], v[40:43]\nds_read_b64_tr_b16 a[132:133], v229 offset:16384\n" \
    "v_mfma_f32_16x16x32_bf16 v[44:47], a[76:79], a[36:39], v[44:47]\nv_mfma_f32_16x16x32_bf16 v[48:51], a[72:75], a[40:43], v[48:51]\nds_read_b64_tr_b16 a[134:135], v229 offset:18432\n" \
    "v_mfma_f32_16x16x32_bf16 v[52:55], a[76:79], a[40:43], v[52:55]\nbuffer_load_dwordx4 v243, %[srdW], s81 offen offset:3072 lds\nv_mfma_f32_16x16x32_bf16 v[56:59], a[72:75], a[44:47], v[56:59]\n" \
    "ds_read_b64_tr_b16 a[136:137], v230 offset:16384\nv_mfma_f32_16x16x32_bf16 v[60:63], a[76:79], a[44:47], v[60:63]\nv_mfma_f32_16x16x32_bf16 v[64:67], a[72:75], a[48:51], v[64:67]\n" \
    "ds_read_b64_tr_b16 a[138:139], v230 offset:18432\nv_mfma_f32_16x16x32_bf16 v[68:71], a[76:79], a[48:51], v[68:71]\nv_mfma_f32_16x16x32_bf16 v[72:75], a[72:75], a[52:55], v[72:75]\n" \
    "ds_read_b64_tr_b16 a[140:141], v231 offset:16384\nv_mfma_f32_16x16x32_bf16 v[76:79], a[76:79], a[52:55], v[76:79]\nv_mfma_f32_16x16x32_bf16 v[80:83], a[72:75], a[56:59], v[80:83]\n" \
    "ds_read_b64_tr_b16 a[142:143], v231 offset:18432\ns_add_u32 m0, m0, 0x2000\ns_add_u32 s81, s83, 0x20000\nbuffer_load_dwordx4 v240, %[srdW], s81 offen offset:0 lds\n" \
    "v_mfma_f32_16x16x32_bf16 v[84:87], a[76:79], a[56:59], v[84:87]\nv_mfma_f32_16x16x32_bf16 v[88:91], a[72:75], a[60:63], v[88:91]\nds_read_b64_tr_b16 a[144:145], v232 offset:16384\n" \
    "v_mfma_f32_16x16x32_bf16 v[92:95], a[76:79], a[60:63], v[92:95]\nds_read_b64_tr_b16 a[146:147], v232 offset:18432\nv_mfma_f32_16x16x32_bf16 v[96:99], a[80:83], a[32:35], v[96:99]\n" \
    "v_mfma_f32_16x16x32_bf16 v[100:103], a[84:87], a[32:35], v[100:103]\nds_read_b64_tr_b16 a[148:149], v233 offset:16384\nv_mfma_f32_16x16x32_bf16 v[104:107], a[80:83], a[36:39], v[104:107]\n" \
    "buffer_load_dwordx4 v241, %[srdW], s81 offen offset:1024 lds\nv_mfma_f32_16x16x32_bf16 v[108:111], a[84:87], a[36:39], v[108:111]\nds_read_b64_tr_b16 a[150:151], v233 offset:18432\n" \
    "v_mfma_f32_16x16x32_bf16 v[112:115], a[80:83], a[40:43], v[112:115]\nv_mfma_f32_16x16x32_bf16 v[116:119], a[84:87], a[40:43], v[116:119]\nds_read_b64_tr_b16 a[152:153], v234 offset:16384\n" \
    "v_mfma_f32_16x16x32_bf16 v[120:123], a[80:83], a[44:47], v[120:123]\nv_mfma_f32_16x16x32_bf16 v[124:127], a[84:87], a[44:47], v[124:127]\nds_read_b64_tr_b16 a[154:155], v234 offset:18432\n" \
    "v_mfma_f32_16x16x32_bf16 v[128:131], a[80:83], a[48:51], v[128:131]\nbuffer_load_dwordx4 v242, %[srdW], s81 offen offset:2048 lds\nv_mfma_f32_16x16x32_bf16 v[132:135], a[84:87], a[48:51], v[132:135]\n" \
    "ds_read_b64_tr_b16 a[156:157], v235 offset:16384\nv_mfma_f32_16x16x32_bf16 v[136:139], a[80:83], a[52:55], v[136:139]\nv_mfma_f32_16x16x32_bf16 v[140:143], a[84:87], a[52:55], v[140:143]\n" \
    "ds_read_b64_tr_b16 a[158:159], v235 offset:18432\nv_mfma_f32_16x16x32_bf16 v[144:147], a[80:83], a[56:59], v[144:147]\nv_mfma_f32_16x16x32_bf16 v[148:151], a[84:87], a[56:59], v[148:151]\n" \
    "v_mfma_f32_16x16x32_bf16 v[152:155], a[80:83], a[60:63], v[152:155]\nbuffer_load_dwordx4 v243, %[srdW], s81 offen offset:3072 lds\nv_mfma_f32_16x16x32_bf16 v[156:159], a[84:87], a[60:63], v[156:159]\n" \
    "v_mfma_f32_16x16x32_bf16 v[160:163], a[88:91], a[32:35], v[160:163]\nv_mfma_f32_16x16x32_bf16 v[164:167], a[92:95], a[32:35], v[164:167]\nv_mfma_f32_16x16x32_bf16 v[168:171], a[88:91], a[36:39], v[168:171]\n" \
    "v_mfma_f32_16x16x32_bf16 v[172:175], a[92:95], a[36:39], v[172:175]\nv_mfma_f32_16x16x32_bf16 v[176:179], a[88:91], a[40:43], v[176:179]\nv_mfma_f32_16x16x32_bf16 v[180:183], a[92:95], a[40:43], v[180:183]\n" \
    "v_mfma_f32_16x16x32_bf16 v[184:187], a[88:91], a[44:47], v[184:187]\nv_mfma_f32_16x16x32_bf16 v[188:191], a[92:95], a[44:47], v[188:191]\nv_mfma_f32_16x16x32_bf16 v[192:195], a[88:91], a[48:51], v[192:195]\n" \
    "v_mfma_f32_16x16x32_bf16 v[196:199], a[92:95], a[48:51], v[196:199]\nv_mfma_f32_16x16x32_bf16 v[200:203], a[88:91], a[52:55], v[200:203]\nv_mfma_f32_16x16x32_bf16 v[204:207], a[92:95], a[52:55], v[204:207]\n" \
    "v_mfma_f32_16x16x32_bf16 v[208:211], a[88:91], a[56:59], v[208:211]\nv_mfma_f32_16x16x32_bf16 v[212:215], a[92:95], a[56:59], v[212:215]\nv_mfma_f32_16x16x32_bf16 v[216:219], a[88:91], a[60:63], v[216:219]\n" \
    "v_mfma_f32_16x16x32_bf16 v[220:223], a[92:95], a[60:63], v[220:223]\ns_waitcnt vmcnt(10) lgkmcnt(0)\ns_barrier\ns_cmp_eq_u32 %[cnt], 3\ns_cbranch_scc1 L_ax%=\n" \
    "v_mfma_f32_16x16x32_bf16 a[0:3], a[128:131], a[96:99], a[0:3]\ns_mov_b32 s83, %[sx]\nv_mfma_f32_16x16x32_bf16 a[4:7], a[132:135], a[96:99], a[4:7]\ns_sub_u32 s81, s85, s84\ns_mov_b32 s87, s84\n" \
    "s_add_u32 m0, %[m0x], 0x8000\ns_add_u32 s80, s83, 0x0\nbuffer_load_dwordx4 v236, %[srdX], s80 offen offset:0 lds\nv_mfma_f32_16x16x32_bf16 a[8:11], a[128:131], a[100:103], a[8:11]\ns_mov_b32 s84, s85\n" \
    "s_mov_b32 s85, s86\nv_mfma_f32_16x16x32_bf16 a[12:15], a[132:135], a[100:103], a[12:15]\ns_add_u32 %[sx], %[sx], 0x80\ns_mov_b32 s86, s87\nv_mfma_f32_16x16x32_bf16 a[16:19], a[128:131], a[104:107], a[16:19]\n" \
    "v_add_u32_e32 v228, s81, v228\nds_read_b128 a[32:35], v224 offset:0\nv_mfma_f32_16x16x32_bf16 a[20:23], a[132:135], a[104:107], a[20:23]\nv_add_u32_e32 v229, s81, v229\n" \
    "buffer_load_dwordx4 v237, %[srdX], s80 offen offset:1024 lds\nv_mfma_f32_16x16x32_bf16 a[24:27], a[128:131], a[108:111], a[24:27]\nv_add_u32_e32 v230, s81, v230\nds_read_b128 a[36:39], v224 offset:2048\n" \
    "v_mfma_f32_16x16x32_bf16 a[28:31], a[132:135], a[108:111], a[28:31]\nv_add_u32_e32 v231, s81, v231\nv_mfma_f32_16x16x32_bf16 v[0:3], a[128:131], a[112:115], v[0:3]\nv_add_u32_e32 v232, s81, v232\n" \
    "ds_read_b128 a[40:43], v224 offset:4096\nv_mfma_f32_16x16x32_bf16 v[4:7], a[132:135], a[112:115], v[4:7]\nv_add_u32_e32 v233, s81, v233\nv_mfma_f32_16x16x32_bf16 v[8:11], a[128:131], a[116:119], v[8:11]\n" \
    "v_add_u32_e32 v234, s81, v234\nds_read_b128 a[44:47], v224 offset:6144\nbuffer_load_dwordx4 v238, %[srdX], s80 offen offset:2048 lds\nv_mfma_f32_16x16x32_bf16 v[12:15], a[132:135], a[116:119], v[12:15]\n" \
    "v_add_u32_e32 v235, s81, v235\nv_mfma_f32_16x16x32_bf16 v[16:19], a[128:131], a[120:123], v[16:19]\nds_read_b128 a[48:51], v224 offset:8192\ns_sub_u32 %[cnt], %[cnt], 1\n" \
    "v_mfma_f32_16x16x32_bf16 v[20:23], a[132:135], a[120:123], v[20:23]\nv_mfma_f32_16x16x32_bf16 v[24:27], a[128:131], a[124:127], v[24:27]\nds_read_b128 a[52:55], v224 offset:10240\n" \
    "buffer_load_dwordx4 v239, %[srdX], s80 offen offset:3072 lds\nv_mfma_f32_16x16x32_bf16 v[28:31], a[132:135], a[124:127], v[28:31]\nv_mfma_f32_16x16x32_bf16 v[32:35], a[136:139], a[96:99], v[32:35]\n" \
    "ds_read_b128 a[56:59], v224 offset:12288\nv_mfma_f32_16x16x32_bf16 v[36:39], a[140:143], a[96:99], v[36:39]\nv_mfma_f32_16x16x32_bf16 v[40:43], a[136:139], a[100:103], v[40:43]\nds_read_b128 a[60:63], v224 offset:14336\n" \
    "v_mfma_f32_16x16x32_bf16 v[44:47], a[140:143], a[100:103], v[44:47]\ns_add_u32 m0, %[m0x], 0x9000\ns_add_u32 s80, s83, 0x8000\nbuffer_load_dwordx4 v236, %[srdX], s80 offen offset:0 lds\n" \
    "v_mfma_f32_16x16x32_bf16 v[48:51], a[136:139], a[104:107], v[48:51]\nds_read_b64_tr_b16 a[64:65], v228 offset:0\nv_mfma_f32_16x16x32_bf16 v[52:55], a[140:143], a[104:107], v[52:55]\n" \
    "ds_read_b64_tr_b16 a[66:67], v228 offset:2048\nv_mfma_f32_16x16x32_bf16 v[56:59], a[136:139], a[108:111], v[56:59]\nv_mfma_f32_16x16x32_bf16 v[60:63], a[140:143], a[108:111], v[60:63]\n" \
    "ds_read_b64_tr_b16 a[68:69], v229 offset:0\nbuffer_load_dwordx4 v237, %[srdX], s80 offen offset:1024 lds\nv_mfma_f32_16x16x32_bf16 v[64:67], a[136:139], a[112:115], v[64:67]\n" \
    "ds_read_b64_tr_b16 a[70:71], v229 offset:2048\nv_mfma_f32_16x16x32_bf16 v[68:71], a[140:143], a[112:115], v[68:71]\nv_mfma_f32_16x16x32_bf16 v[72:75], a[136:139], a[116:119], v[72:75]\n" \
    "ds_read_b64_tr_b16 a[72:73], v230 offset:0\nv_mfma_f32_16x16x32_bf16 v[76:79], a[140:143], a[116:119], v[76:79]\nds_read_b64_tr_b16 a[74:75], v230 offset:2048\n" \
    "v_mfma_f32_16x16x32_bf16 v[80:83], a[136:139], a[120:123], v[80:83]\nbuffer_load_dwordx4 v238, %[srdX], s80 offen offset:2048 lds\nv_mfma_f32_16x16x32_bf16 v[84:87], a[140:143], a[120:123], v[84:87]\n" \
    "ds_read_b64_tr_b16 a[76:77], v231 offset:0\nv_mfma_f32_16x16x32_bf16 v[88:91], a[136:139], a[124:127], v[88:91]\nds_read_b64_tr_b16 a[78:79], v231 offset:2048\n" \
    "v_mfma_f32_16x16x32_bf16 v[92:95], a[140:143], a[124:127], v[92:95]\nv_mfma_f32_16x16x32_bf16 v[96:99], a[144:147], a[96:99], v[96:99]\nds_read_b64_tr_b16 a[80:81], v232 offset:0\n" \
    "buffer_load_dwordx4 v239, %[srdX], s80 offen offset:3072 lds\nv_mfma_f32_16x16x32_bf16 v[100:103], a[148:151], a[96:99], v[100:103]\nv_mfma_f32_16x16x32_bf16 v[104:107], a[144:147], a[100:103], v[104:107]\n" \
    "ds_read_b64_tr_b16 a[82:83], v232 offset:2048\nv_mfma_f32_16x16x32_bf16 v[108:111], a[148:151], a[100:103], v[108:111]\nds_read_b64_tr_b16 a[84:85], v233 offset:0\n" \
    "v_mfma_f32_16x16x32_bf16 v[112:115], a[144:147], a[104:107], v[112:115]\nv_mfma_f32_16x16x32_bf16 v[116:119], a[148:151], a[104:107], v[116:119]\nds_read_b64_tr_b16 a[86:87], v233 offset:2048\n" \
    "v_mfma_f32_16x16x32_bf16 v[120:123], a[144:147], a[108:111], v[120:123]\nds_read_b64_tr_b16 a[88:89], v234 offset:0\nv_mfma_f32_16x16x32_bf16 v[124:127], a[148:151], a[108:111], v[124:127]\n" \
    "v_mfma_f32_16x16x32_bf16 v[128:131], a[144:147], a[112:115], v[128:131]\nds_read_b64_tr_b16 a[90:91], v234 offset:2048\nv_mfma_f32_16x16x32_bf16 v[132:135], a[148:151], a[112:115], v[132:135]\n" \
    "ds_read_b64_tr_b16 a[92:93], v235 offset:0\nv_mfma_f32_16x16x32_bf16 v[136:139], a[144:147], a[116:119], v[136:139]\nv_mfma_f32_16x16x32_bf16 v[140:143], a[148:151], a[116:119], v[140:143]\n" \
    "ds_read_b64_tr_b16 a[94:95], v235 offset:2048\nv_mfma_f32_16x16x32_bf16 v[144:147], a[144:147], a[120:123], v[144:147]\nv_mfma_f32_16x16x32_bf16 v[148:151], a[148:151], a[120:123], v[148:151]\n" \
    "v_mfma_f32_16x16x32_bf16 v[152:155], a[144:147], a[124:127], v[152:155]\nv_mfma_f32_16x16x32_bf16 v[156:159], a[148:151], a[124:127], v[156:159]\nv_mfma_f32_16x16x32_bf16 v[160:163], a[152:155], a[96:99], v[160:163]\n" \
    "v_mfma_f32_16x16x32_bf16 v[164:167], a[156:159], a[96:99], v[164:167]\nv_mfma_f32_16x16x32_bf16 v[168:171], a[152:155], a[100:103], v[168:171]\nv_mfma_f32_16x16x32_bf16 v[172:175], a[156:159], a[100:103], v[172:175]\n" \
    "v_mfma_f32_16x16x32_bf16 v[176:179], a[152:155], a[104:107], v[176:179]\nv_mfma_f32_16x16x32_bf16 v[180:183], a[156:159], a[104:107], v[180:183]\nv_mfma_f32_16x16x32_bf16 v[184:187], a[152:155], a[108:111], v[184:187]\n" \
    "v_mfma_f32_16x16x32_bf16 v[188:191], a[156:159], a[108:111], v[188:191]\nv_mfma_f32_16x16x32_bf16 v[192:195], a[152:155], a[112:115], v[192:195]\nv_mfma_f32_16x16x32_bf16 v[196:199], a[156:159], a[112:115], v[196:199]\n" \
    "v_mfma_f32_16x16x32_bf16 v[200:203], a[152:155], a[116:119], v[200:203]\nv_mfma_f32_16x16x32_bf16 v[204:207], a[156:159], a[116:119], v[204:207]\nv_mfma_f32_16x16x32_bf16 v[208:211], a[152:155], a[120:123], v[208:211]\n" \
    "v_mfma_f32_16x16x32_bf16 v[212:215], a[156:159], a[120:123], v[212:215]\nv_mfma_f32_16x16x32_bf16 v[216:219], a[152:155], a[124:127], v[216:219]\nv_mfma_f32_16x16x32_bf16 v[220:223], a[156:159], a[124:127], v[220:223]\n" \
    "s_waitcnt lgkmcnt(0)\nv_mfma_f32_16x16x32_bf16 a[0:3], a[64:67], a[32:35], a[0:3]\nds_read_b128 a[96:99], v225 offset:0\nv_mfma_f32_16x16x32_bf16 a[4:7], a[68:71], a[32:35], a[4:7]\n" \
    "buffer_load_dwordx4 a[160:163], v244, %[srdP], %[sp] offen\ns_add_u32 %[sp], %[sp], %[spst]\nds_read_b128 a[100:103], v225 offset:2048\ns_mov_b32 s83, %[sw]\n" \
    "v_mfma_f32_16x16x32_bf16 a[8:11], a[64:67], a[36:39], a[8:11]\nbuffer_load_dwordx4 a[164:167], v244, %[srdP], %[sp] offen\ns_add_u32 %[sp], %[sp], %[spst]\ns_add_u32 %[sw], %[sw], 0x80000\n" \
    "v_mfma_f32_16x16x32_bf16 a[12:15], a[68:71], a[36:39], a[12:15]\nds_read_b128 a[104:107], v225 offset:4096\ns_add_u32 m0, %[m0w], s86\ns_add_u32 s81, s83, 0x0\nbuffer_load_dwordx4 v240, %[srdW], s81 offen offset:0 lds\n" \
    "v_mfma_f32_16x16x32_bf16 a[16:19], a[64:67], a[40:43], a[16:19]\nv_mfma_f32_16x16x32_bf16 a[20:23], a[68:71], a[40:43], a[20:23]\nds_read_b128 a[108:111], v225 offset:6144\n" \
    "v_mfma_f32_16x16x32_bf16 a[24:27], a[64:67], a[44:47], a[24:27]\nv_mfma_f32_16x16x32_bf16 a[28:31], a[68:71], a[44:47], a[28:31]\nds_read_b128 a[112:115], v225 offset:8192\n" \
    "v_mfma_f32_16x16x32_bf16 v[0:3], a[64:67], a[48:51], v[0:3]\nv_mfma_f32_16x16x32_bf16 v[4:7], a[68:71], a[48:51], v[4:7]\nds_read_b128 a[116:119], v225 offset:10240\n" \
    "buffer_load_dwordx4 v241, %[srdW], s81 offen offset:1024 lds\nv_mfma_f32_16x16x32_bf16 v[8:11], a[64:67], a[52:55], v[8:11]\nv_mfma_f32_16x16x32_bf16 v[12:15], a[68:71], a[52:55], v[12:15]\n" \
    "ds_read_b128 a[120:123], v225 offset:12288\nv_mfma_f32_16x16x32_bf16 v[16:19], a[64:67], a[56:59], v[16:19]\nv_mfma_f32_16x16x32_bf16 v[20:23], a[68:71], a[56:59], v[20:23]\nds_read_b128 a[124:127], v225 offset:14336\n" \
    "v_mfma_f32_16x16x32_bf16 v[24:27], a[64:67], a[60:63], v[24:27]\nv_mfma_f32_16x16x32_bf16 v[28:31], a[68:71], a[60:63], v[28:31]\nds_read_b64_tr_b16 a[128:129], v228 offset:16384\n" \
    "buffer_load_dwordx4 v242, %[srdW], s81 offen offset:2048 lds\nv_mfma_f32_16x16x32_bf16 v[32:35], a[72:75], a[32:35], v[32:35]\nds_read_b64_tr_b16 a[130:131], v228 offset:18432\n" \
    "v_mfma_f32_16x16x32_bf16 v[36:39], a[76:79], a[32:35], v[36:39]\nv_mfma_f32_16x16x32_bf16 v[40:43], a[72:75], a[36:39], v[40:43]\nds_read_b64_tr_b16 a[132:133], v229 offset:16384\n" \
    "v_mfma_f32_16x16x32_bf16 v[44:47], a[76:79], a[36:39], v[44:47]\nv_mfma_f32_16x16x32_bf16 v[48:51], a[72:75], a[40:43], v[48:51]\nds_read_b64_tr_b16 a[134:135], v229 offset:18432\n" \
    "v_mfma_f32_16x16x32_bf16 v[52:55], a[76:79], a[40:43], v[52:55]\nbuffer_load_dwordx4 v243, %[srdW], s81 offen offset:3072 lds\nv_mfma_f32_16x16x32_bf16 v[56:59], a[72:75], a[44:47], v[56:59]\n" \
    "ds_read_b64_tr_b16 a[136:137], v230 offset:16384\nv_mfma_f32_16x16x32_bf16 v[60:63], a[76:79], a[44:47], v[60:63]\nv_mfma_f32_16x16x32_bf16 v[64:67], a[72:75], a[48:51], v[64:67]\n" \
    "ds_read_b64_tr_b16 a[138:139], v230 offset:18432\nv_mfma_f32_16x16x32_bf16 v[68:71], a[76:79], a[48:51], v[68:71]\nv_mfma_f32_16x16x32_bf16 v[72:75], a[72:75], a[52:55], v[72:75]\n" \
    "ds_read_b64_tr_b16 a[140:141], v231 offset:16384\nv_mfma_f32_16x16x32_bf16 v[76:79], a[76:79], a[52:55], v[76:79]\nv_mfma_f32_16x16x32_bf16 v[80:83], a[72:75], a[56:59], v[80:83]\n" \
    "ds_read_b64_tr_b16 a[142:143], v231 offset:18432\ns_add_u32 m0, m0, 0x2000\ns_add_u32 s81, s83, 0x20000\nbuffer_load_dwordx4 v240, %[srdW], s81 offen offset:0 lds\n" \
    "v_mfma_f32_16x16x32_bf16 v[84:87], a[76:79], a[56:59], v[84:87]\nv_mfma_f32_16x16x32_bf16 v[88:91], a[72:75], a[60:63], v[88:91]\nds_read_b64_tr_b16 a[144:145], v232 offset:16384\n" \
    "v_mfma_f32_16x16x32_bf16 v[92:95], a[76:79], a[60:63], v[92:95]\nds_read_b64_tr_b16 a[146:147], v232 offset:18432\nv_mfma_f32_16x16x32_bf16 v[96:99], a[80:83], a[32:35], v[96:99]\n" \
    "v_mfma_f32_16x16x32_bf16 v[100:103], a[84:87], a[32:35], v[100:103]\nds_read_b64_tr_b16 a[148:149], v233 offset:16384\nv_mfma_f32_16x16x32_bf16 v[104:107], a[80:83], a[36:39], v[104:107]\n" \
    "buffer_load_dwordx4 v241, %[srdW], s81 offen offset:1024 lds\nv_mfma_f32_16x16x32_bf16 v[108:111], a[84:87], a[36:39], v[108:111]\nds_read_b64_tr_b16 a[150:151], v233 offset:18432\n" \
    "v_mfma_f32_16x16x32_bf16 v[112:115], a[80:83], a[40:43], v[112:115]\nv_mfma_f32_16x16x32_bf16 v[116:119], a[84:87], a[40:43], v[116:119]\nds_read_b64_tr_b16 a[152:153], v234 offset:16384\n" \
    "v_mfma_f32_16x16x32_bf16 v[120:123], a[80:83], a[44:47], v[120:123]\nv_mfma_f32_16x16x32_bf16 v[124:127], a[84:87], a[44:47], v[124:127]\nds_read_b64_tr_b16 a[154:155], v234 offset:18432\n" \
    "v_mfma_f32_16x16x32_bf16 v[128:131], a[80:83], a[48:51], v[128:131]\nbuffer_load_dwordx4 v242, %[srdW], s81 offen offset:2048 lds\nv_mfma_f32_16x16x32_bf16 v[132:135], a[84:87], a[48:51], v[132:135]\n" \
    "ds_read_b64_tr_b16 a[156:157], v235 offset:16384\nv_mfma_f32_16x16x32_bf16 v[136:139], a[80:83], a[52:55], v[136:139]\nv_mfma_f32_16x16x32_bf16 v[140:143], a[84:87], a[52:55], v[140:143]\n" \
    "ds_read_b64_tr_b16 a[158:159], v235 offset:18432\nv_mfma_f32_16x16x32_bf16 v[144:147], a[80:83], a[56:59], v[144:147]\nv_mfma_f32_16x16x32_bf16 v[148:151], a[84:87], a[56:59], v[148:151]\n" \
    "v_mfma_f32_16x16x32_bf16 v[152:155], a[80:83], a[60:63], v[152:155]\nbuffer_load_dwordx4 v243, %[srdW], s81 offen offset:3072 lds\nv_mfma_f32_16x16x32_bf16 v[156:159], a[84:87], a[60:63], v[156:159]\n" \
    "v_mfma_f32_16x16x32_bf16 v[160:163], a[88:91], a[32:35], v[160:163]\nv_mfma_f32_16x16x32_bf16 v[164:167], a[92:95], a[32:35], v[164:167]\nv_mfma_f32_16x16x32_bf16 v[168:171], a[88:91], a[36:39], v[168:171]\n" \
    "v_mfma_f32_16x16x32_bf16 v[172:175], a[92:95], a[36:39], v[172:175]\nv_mfma_f32_16x16x32_bf16 v[176:179], a[88:91], a[40:43], v[176:179]\nv_mfma_f32_16x16x32_bf16 v[180:183], a[92:95], a[40:43], v[180:183]\n" \
    "v_mfma_f32_16x16x32_bf16 v[184:187], a[88:91], a[44:47], v[184:187]\nv_mfma_f32_16x16x32_bf16 v[188:191], a[92:95], a[44:47], v[188:191]\nv_mfma_f32_16x16x32_bf16 v[192:195], a[88:91], a[48:51], v[192:195]\n" \
    "v_mfma_f32_16x16x32_bf16 v[196:199], a[92:95], a[48:51], v[196:199]\nv_mfma_f32_16x16x32_bf16 v[200:203], a[88:91], a[52:55], v[200:203]\nv_mfma_f32_16x16x32_bf16 v[204:207], a[92:95], a[52:55], v[204:207]\n" \
    "v_mfma_f32_16x16x32_bf16 v[208:211], a[88:91], a[56:59], v[208:211]\nv_mfma_f32_16x16x32_bf16 v[212:215], a[92:95], a[56:59], v[212:215]\nv_mfma_f32_16x16x32_bf16 v[216:219], a[88:91], a[60:63], v[216:219]\n" \
    "v_mfma_f32_16x16x32_bf16 v[220:223], a[92:95], a[60:63], v[220:223]\ns_waitcnt vmcnt(10) lgkmcnt(0)\ns_barrier\ns_branch L_al%=\nL_ax%=:\nv_mfma_f32_16x16x32_bf16 a[0:3], a[128:131], a[96:99], a[0:3]\n" \
    "v_cmp_ge_u64_e64 s[90:91], v[248:249], %[tlo]\nv_cmp_lt_u64_e64 s[92:93], v[248:249], %[thi]\nv_accvgpr_read_b32 v245, a237\nv_accvgpr_read_b32 v251, a238\ns_nop 0\ns_and_b64 s[90:91], s[90:91], s[92:93]\n" \
    "s_cmp_eq_u64 s[90:91], -1\ns_cselect_b32 s88, %[sel], 0\nv_mov_b32_e32 v250, s88\nds_write_b32 v251, v250\ns_waitcnt lgkmcnt(0)\ns_barrier\nds_read_b128 v[246:249], v245\ns_mov_b32 s83, %[sx]\n" \
    "v_mfma_f32_16x16x32_bf16 a[4:7], a[132:135], a[96:99], a[4:7]\ns_add_u32 m0, %[m0x], 0x8000\ns_add_u32 s80, s83, 0x0\nbuffer_load_dwordx4 v236, %[srdX], s80 offen offset:0 lds\n" \
    "v_mfma_f32_16x16x32_bf16 a[8:11], a[128:131], a[100:103], a[8:11]\nv_mfma_f32_16x16x32_bf16 a[12:15], a[132:135], a[100:103], a[12:15]\ns_add_u32 %[sx], %[sx], 0x80\n" \
    "v_mfma_f32_16x16x32_bf16 a[16:19], a[128:131], a[104:107], a[16:19]\nv_add_u32_e32 v228, 0xffff0000, v228\nds_read_b128 a[32:35], v224 offset:0\nv_mfma_f32_16x16x32_bf16 a[20:23], a[132:135], a[104:107], a[20:23]\n" \
    "v_add_u32_e32 v229, 0xffff0000, v229\nbuffer_load_dwordx4 v237, %[srdX], s80 offen offset:1024 lds\nv_mfma_f32_16x16x32_bf16 a[24:27], a[128:131], a[108:111], a[24:27]\nv_add_u32_e32 v230, 0xffff0000, v230\n" \
    "ds_read_b128 a[36:39], v224 offset:2048\nv_mfma_f32_16x16x32_bf16 a[28:31], a[132:135], a[108:111], a[28:31]\nv_add_u32_e32 v231, 0xffff0000, v231\nv_mfma_f32_16x16x32_bf16 v[0:3], a[128:131], a[112:115], v[0:3]\n" \
    "v_add_u32_e32 v232, 0xffff0000, v232\nds_read_b128 a[40:43], v224 offset:4096\nv_mfma_f32_16x16x32_bf16 v[4:7], a[132:135], a[112:115], v[4:7]\nv_add_u32_e32 v233, 0xffff0000, v233\n" \
    "v_mfma_f32_16x16x32_bf16 v[8:11], a[128:131], a[116:119], v[8:11]\nv_add_u32_e32 v234, 0xffff0000, v234\nds_read_b128 a[44:47], v224 offset:6144\nbuffer_load_dwordx4 v238, %[srdX], s80 offen offset:2048 lds\n" \
    "v_mfma_f32_16x16x32_bf16 v[12:15], a[132:135], a[116:119], v[12:15]\nv_add_u32_e32 v235, 0xffff0000, v235\nv_mfma_f32_16x16x32_bf16 v[16:19], a[128:131], a[120:123], v[16:19]\nds_read_b128 a[48:51], v224 offset:8192\n" \
    "v_mfma_f32_16x16x32_bf16 v[20:23], a[132:135], a[120:123], v[20:23]\nv_mfma_f32_16x16x32_bf16 v[24:27], a[128:131], a[124:127], v[24:27]\nds_read_b128 a[52:55], v224 offset:10240\n" \
    "buffer_load_dwordx4 v239, %[srdX], s80 offen offset:3072 lds\nv_mfma_f32_16x16x32_bf16 v[28:31], a[132:135], a[124:127], v[28:31]\nv_mfma_f32_16x16x32_bf16 v[32:35], a[136:139], a[96:99], v[32:35]\n" \
    "ds_read_b128 a[56:59], v224 offset:12288\nv_mfma_f32_16x16x32_bf16 v[36:39], a[140:143], a[96:99], v[36:39]\nv_mfma_f32_16x16x32_bf16 v[40:43], a[136:139], a[100:103], v[40:43]\nds_read_b128 a[60:63], v224 offset:14336\n" \
    "v_mfma_f32_16x16x32_bf16 v[44:47], a[140:143], a[100:103], v[44:47]\ns_add_u32 m0, %[m0x], 0x9000\ns_add_u32 s80, s83, 0x8000\nbuffer_load_dwordx4 v236, %[srdX], s80 offen offset:0 lds\n" \
    "v_mfma_f32_16x16x32_bf16 v[48:51], a[136:139], a[104:107], v[48:51]\nds_read_b64_tr_b16 a[64:65], v228 offset:0\nv_mfma_f32_16x16x32_bf16 v[52:55], a[140:143], a[104:107], v[52:55]\n" \
    "ds_read_b64_tr_b16 a[66:67], v228 offset:2048\nv_mfma_f32_16x16x32_bf16 v[56:59], a[136:139], a[108:111], v[56:59]\nv_mfma_f32_16x16x32_bf16 v[60:63], a[140:143], a[108:111], v[60:63]\n" \
    "ds_read_b64_tr_b16 a[68:69], v229 offset:0\nbuffer_load_dwordx4 v237, %[srdX], s80 offen offset:1024 lds\nv_mfma_f32_16x16x32_bf16 v[64:67], a[136:139], a[112:115], v[64:67]\n" \
    "ds_read_b64_tr_b16 a[70:71], v229 offset:2048\nv_mfma_f32_16x16x32_bf16 v[68:71], a[140:143], a[112:115], v[68:71]\nv_mfma_f32_16x16x32_bf16 v[72:75], a[136:139], a[116:119], v[72:75]\n" \
    "ds_read_b64_tr_b16 a[72:73], v230 offset:0\nv_mfma_f32_16x16x32_bf16 v[76:79], a[140:143], a[116:119], v[76:79]\nds_read_b64_tr_b16 a[74:75], v230 offset:2048\n" \
    "v_mfma_f32_16x16x32_bf16 v[80:83], a[136:139], a[120:123], v[80:83]\nbuffer_load_dwordx4 v238, %[srdX], s80 offen offset:2048 lds\nv_mfma_f32_16x16x32_bf16 v[84:87], a[140:143], a[120:123], v[84:87]\n" \
    "ds_read_b64_tr_b16 a[76:77], v231 offset:0\nv_mfma_f32_16x16x32_bf16 v[88:91], a[136:139], a[124:127], v[88:91]\nds_read_b64_tr_b16 a[78:79], v231 offset:2048\n" \
    "v_mfma_f32_16x16x32_bf16 v[92:95], a[140:143], a[124:127], v[92:95]\nv_mfma_f32_16x16x32_bf16 v[96:99], a[144:147], a[96:99], v[96:99]\nds_read_b64_tr_b16 a[80:81], v232 offset:0\n" \
    "buffer_load_dwordx4 v239, %[srdX], s80 offen offset:3072 lds\nv_mfma_f32_16x16x32_bf16 v[100:103], a[148:151], a[96:99], v[100:103]\nv_mfma_f32_16x16x32_bf16 v[104:107], a[144:147], a[100:103], v[104:107]\n" \
    "ds_read_b64_tr_b16 a[82:83], v232 offset:2048\nv_mfma_f32_16x16x32_bf16 v[108:111], a[148:151], a[100:103], v[108:111]\nds_read_b64_tr_b16 a[84:85], v233 offset:0\n" \
    "v_mfma_f32_16x16x32_bf16 v[112:115], a[144:147], a[104:107], v[112:115]\nv_mfma_f32_16x16x32_bf16 v[116:119], a[148:151], a[104:107], v[116:119]\nds_read_b64_tr_b16 a[86:87], v233 offset:2048\n" \
    "v_mfma_f32_16x16x32_bf16 v[120:123], a[144:147], a[108:111], v[120:123]\nds_read_b64_tr_b16 a[88:89], v234 offset:0\nv_mfma_f32_16x16x32_bf16 v[124:127], a[148:151], a[108:111], v[124:127]\n" \
    "v_mfma_f32_16x16x32_bf16 v[128:131], a[144:147], a[112:115], v[128:131]\nds_read_b64_tr_b16 a[90:91], v234 offset:2048\nv_mfma_f32_16x16x32_bf16 v[132:135], a[148:151], a[112:115], v[132:135]\n" \
    "ds_read_b64_tr_b16 a[92:93], v235 offset:0\nv_mfma_f32_16x16x32_bf16 v[136:139], a[144:147], a[116:119], v[136:139]\nv_mfma_f32_16x16x32_bf16 v[140:143], a[148:151], a[116:119], v[140:143]\n" \
    "ds_read_b64_tr_b16 a[94:95], v235 offset:2048\nv_mfma_f32_16x16x32_bf16 v[144:147], a[144:147], a[120:123], v[144:147]\nv_mfma_f32_16x16x32_bf16 v[148:151], a[148:151], a[120:123], v[148:151]\n" \
    "v_mfma_f32_16x16x32_bf16 v[152:155], a[144:147], a[124:127], v[152:155]\nv_mfma_f32_16x16x32_bf16 v[156:159], a[148:151], a[124:127], v[156:159]\nv_mfma_f32_16x16x32_bf16 v[160:163], a[152:155], a[96:99], v[160:163]\n" \
    "v_mfma_f32_16x16x32_bf16 v[164:167], a[156:159], a[96:99], v[164:167]\nv_mfma_f32_16x16x32_bf16 v[168:171], a[152:155], a[100:103], v[168:171]\nv_mfma_f32_16x16x32_bf16 v[172:175], a[156:159], a[100:103], v[172:175]\n" \
    "v_mfma_f32_16x16x32_bf16 v[176:179], a[152:155], a[104:107], v[176:179]\nv_mfma_f32_16x16x32_bf16 v[180:183], a[156:159], a[104:107], v[180:183]\nv_mfma_f32_16x16x32_bf16 v[184:187], a[152:155], a[108:111], v[184:187]\n" \
    "v_mfma_f32_16x16x32_bf16 v[188:191], a[156:159], a[108:111], v[188:191]\nv_mfma_f32_16x16x32_bf16 v[192:195], a[152:155], a[112:115], v[192:195]\nv_mfma_f32_16x16x32_bf16 v[196:199], a[156:159], a[112:115], v[196:199]\n" \
    "v_mfma_f32_16x16x32_bf16 v[200:203], a[152:155], a[116:119], v[200:203]\nv_mfma_f32_16x16x32_bf16 v[204:207], a[156:159], a[116:119], v[204:207]\nv_mfma_f32_16x16x32_bf16 v[208:211], a[152:155], a[120:123], v[208:211]\n" \
    "v_mfma_f32_16x16x32_bf16 v[212:215], a[156:159], a[120:123], v[212:215]\nv_mfma_f32_16x16x32_bf16 v[216:219], a[152:155], a[124:127], v[216:219]\nv_mfma_f32_16x16x32_bf16 v[220:223], a[156:159], a[124:127], v[220:223]\n" \
    "s_waitcnt lgkmcnt(0)\nv_and_b32_e32 v246, v246, v247\nv_and_b32_e32 v248, v248, v249\nv_and_b32_e32 v246, v246, v248\ns_nop 1\nv_readfirstlane_b32 %[sel], v246\ns_nop 3\ns_cmp_eq_u32 %[sel], 1\ns_cbranch_scc1 L_fQ%=\n" \
    "s_cmp_eq_u32 %[sel], 2\ns_cbranch_scc1 L_fU%=\nv_mfma_f32_16x16x32_bf16 a[0:3], a[64:67], a[32:35], a[0:3]\nds_read_b128 a[96:99], v225 offset:0\nv_mfma_f32_16x16x32_bf16 a[4:7], a[68:71], a[32:35], a[4:7]\n" \
    "ds_read_b128 a[100:103], v225 offset:2048\nv_mfma_f32_16x16x32_bf16 a[8:11], a[64:67], a[36:39], a[8:11]\nv_mfma_f32_16x16x32_bf16 a[12:15], a[68:71], a[36:39], a[12:15]\nds_read_b128 a[104:107], v225 offset:4096\n" \
    "v_mfma_f32_16x16x32_bf16 a[16:19], a[64:67], a[40:43], a[16:19]\nv_mfma_f32_16x16x32_bf16 a[20:23], a[68:71], a[40:43], a[20:23]\nds_read_b128 a[108:111], v225 offset:6144\n" \
    "v_mfma_f32_16x16x32_bf16 a[24:27], a[64:67], a[44:47], a[24:27]\nv_mfma_f32_16x16x32_bf16 a[28:31], a[68:71], a[44:47], a[28:31]\nds_read_b128 a[112:115], v225 offset:8192\n" \
    "v_mfma_f32_16x16x32_bf16 v[0:3], a[64:67], a[48:51], v[0:3]\nv_mfma_f32_16x16x32_bf16 v[4:7], a[68:71], a[48:51], v[4:7]\nds_read_b128 a[116:119], v225 offset:10240\n" \
    "v_mfma_f32_16x16x32_bf16 v[8:11], a[64:67], a[52:55], v[8:11]\nv_mfma_f32_16x16x32_bf16 v[12:15], a[68:71], a[52:55], v[12:15]\nds_read_b128 a[120:123], v225 offset:12288\n" \
    "v_mfma_f32_16x16x32_bf16 v[16:19], a[64:67], a[56:59], v[16:19]\nv_mfma_f32_16x16x32_bf16 v[20:23], a[68:71], a[56:59], v[20:23]\nds_read_b128 a[124:127], v225 offset:14336\n" \
    "v_mfma_f32_16x16x32_bf16 v[24:27], a[64:67], a[60:63], v[24:27]\nv_mfma_f32_16x16x32_bf16 v[28:31], a[68:71], a[60:63], v[28:31]\nds_read_b64_tr_b16 a[128:129], v228 offset:16384\n" \
    "v_mfma_f32_16x16x32_bf16 v[32:35], a[72:75], a[32:35], v[32:35]\nds_read_b64_tr_b16 a[130:131], v228 offset:18432\nv_mfma_f32_16x16x32_bf16 v[36:39], a[76:79], a[32:35], v[36:39]\n" \
    "v_mfma_f32_16x16x32_bf16 v[40:43], a[72:75], a[36:39], v[40:43]\nds_read_b64_tr_b16 a[132:133], v229 offset:16384\nv_mfma_f32_16x16x32_bf16 v[44:47], a[76:79], a[36:39], v[44:47]\n" \
    "v_mfma_f32_16x16x32_bf16 v[48:51], a[72:75], a[40:43], v[48:51]\nds_read_b64_tr_b16 a[134:135], v229 offset:18432\nv_mfma_f32_16x16x32_bf16 v[52:55], a[76:79], a[40:43], v[52:55]\n" \
    "v_mfma_f32_16x16x32_bf16 v[56:59], a[72:75], a[44:47], v[56:59]\nds_read_b64_tr_b16 a[136:137], v230 offset:16384\nv_mfma_f32_16x16x32_bf16 v[60:63], a[76:79], a[44:47], v[60:63]\n" \
    "v_mfma_f32_16x16x32_bf16 v[64:67], a[72:75], a[48:51], v[64:67]\nds_read_b64_tr_b16 a[138:139], v230 offset:18432\nv_mfma_f32_16x16x32_bf16 v[68:71], a[76:79], a[48:51], v[68:71]\n" \
    "v_mfma_f32_16x16x32_bf16 v[72:75], a[72:75], a[52:55], v[72:75]\nds_read_b64_tr_b16 a[140:141], v231 offset:16384\nv_mfma_f32_16x16x32_bf16 v[76:79], a[76:79], a[52:55], v[76:79]\n" \
    "v_mfma_f32_16x16x32_bf16 v[80:83], a[72:75], a[56:59], v[80:83]\nds_read_b64_tr_b16 a[142:143], v231 offset:18432\nv_mfma_f32_16x16x32_bf16 v[84:87], a[76:79], a[56:59], v[84:87]\n" \
    "v_mfma_f32_16x16x32_bf16 v[88:91], a[72:75], a[60:63], v[88:91]\nds_read_b64_tr_b16 a[144:145], v232 offset:16384\nv_mfma_f32_16x16x32_bf16 v[92:95], a[76:79], a[60:63], v[92:95]\n" \
    "ds_read_b64_tr_b16 a[146:147], v232 offset:18432\nv_mfma_f32_16x16x32_bf16 v[96:99], a[80:83], a[32:35], v[96:99]\nv_mfma_f32_16x16x32_bf16 v[100:103], a[84:87], a[32:35], v[100:103]\n" \
    "ds_read_b64_tr_b16 a[148:149], v233 offset:16384\nv_mfma_f32_16x16x32_bf16 v[104:107], a[80:83], a[36:39], v[104:107]\nv_mfma_f32_16x16x32_bf16 v[108:111], a[84:87], a[36:39], v[108:111]\n" \
    "ds_read_b64_tr_b16 a[150:151], v233 offset:18432\nv_mfma_f32_16x16x32_bf16 v[112:115], a[80:83], a[40:43], v[112:115]\nv_mfma_f32_16x16x32_bf16 v[116:119], a[84:87], a[40:43], v[116:119]\n" \
    "ds_read_b64_tr_b16 a[152:153], v234 offset:16384\nv_mfma_f32_16x16x32_bf16 v[120:123], a[80:83], a[44:47], v[120:123]\nv_mfma_f32_16x16x32_bf16 v[124:127], a[84:87], a[44:47], v[124:127]\n" \
    "ds_read_b64_tr_b16 a[154:155], v234 offset:18432\nv_mfma_f32_16x16x32_bf16 v[128:131], a[80:83], a[48:51], v[128:131]\nv_mfma_f32_16x16x32_bf16 v[132:135], a[84:87], a[48:51], v[132:135]\n" \
    "ds_read_b64_tr_b16 a[156:157], v235 offset:16384\nv_mfma_f32_16x16x32_bf16 v[136:139], a[80:83], a[52:55], v[136:139]\nv_mfma_f32_16x16x32_bf16 v[140:143], a[84:87], a[52:55], v[140:143]\n" \
    "ds_read_b64_tr_b16 a[158:159], v235 offset:18432\nv_mfma_f32_16x16x32_bf16 v[144:147], a[80:83], a[56:59], v[144:147]\nv_mfma_f32_16x16x32_bf16 v[148:151], a[84:87], a[56:59], v[148:151]\n" \
    "v_mfma_f32_16x16x32_bf16 v[152:155], a[80:83], a[60:63], v[152:155]\nv_mfma_f32_16x16x32_bf16 v[156:159], a[84:87], a[60:63], v[156:159]\nv_mfma_f32_16x16x32_bf16 v[160:163], a[88:91], a[32:35], v[160:163]\n" \
    "v_mfma_f32_16x16x32_bf16 v[164:167], a[92:95], a[32:35], v[164:167]\nv_mfma_f32_16x16x32_bf16 v[168:171], a[88:91], a[36:39], v[168:171]\nv_mfma_f32_16x16x32_bf16 v[172:175], a[92:95], a[36:39], v[172:175]\n" \
    "v_mfma_f32_16x16x32_bf16 v[176:179], a[88:91], a[40:43], v[176:179]\nv_mfma_f32_16x16x32_bf16 v[180:183], a[92:95], a[40:43], v[180:183]\nv_mfma_f32_16x16x32_bf16 v[184:187], a[88:91], a[44:47], v[184:187]\n" \
    "v_mfma_f32_16x16x32_bf16 v[188:191], a[92:95], a[44:47], v[188:191]\nv_mfma_f32_16x16x32_bf16 v[192:195], a[88:91], a[48:51], v[192:195]\nv_mfma_f32_16x16x32_bf16 v[196:199], a[92:95], a[48:51], v[196:199]\n" \
    "v_mfma_f32_16x16x32_bf16 v[200:203], a[88:91], a[52:55], v[200:203]\nv_mfma_f32_16x16x32_bf16 v[204:207], a[92:95], a[52:55], v[204:207]\nv_mfma_f32_16x16x32_bf16 v[208:211], a[88:91], a[56:59], v[208:211]\n" \
    "v_mfma_f32_16x16x32_bf16 v[212:215], a[92:95], a[56:59], v[212:215]\nv_mfma_f32_16x16x32_bf16 v[216:219], a[88:91], a[60:63], v[216:219]\nv_mfma_f32_16x16x32_bf16 v[220:223], a[92:95], a[60:63], v[220:223]\n" \
    "s_waitcnt vmcnt(0) lgkmcnt(0)\ns_barrier\nv_mfma_f32_16x16x32_bf16 a[0:3], a[128:131], a[96:99], a[0:3]\nv_mfma_f32_16x16x32_bf16 a[4:7], a[132:135], a[96:99], a[4:7]\n" \
    "v_mfma_f32_16x16x32_bf16 a[8:11], a[128:131], a[100:103], a[8:11]\nv_mfma_f32_16x16x32_bf16 a[12:15], a[132:135], a[100:103], a[12:15]\nv_mfma_f32_16x16x32_bf16 a[16:19], a[128:131], a[104:107], a[16:19]\n" \
    "v_add_u32_e32 v228, 0x8000, v228\nds_read_b128 a[32:35], v224 offset:32768\nv_mfma_f32_16x16x32_bf16 a[20:23], a[132:135], a[104:107], a[20:23]\nv_add_u32_e32 v229, 0x8000, v229\n" \
    "v_mfma_f32_16x16x32_bf16 a[24:27], a[128:131], a[108:111], a[24:27]\nv_add_u32_e32 v230, 0x8000, v230\nds_read_b128 a[36:39], v224 offset:34816\nv_mfma_f32_16x16x32_bf16 a[28:31], a[132:135], a[108:111], a[28:31]\n" \
    "v_add_u32_e32 v231, 0x8000, v231\nv_mfma_f32_16x16x32_bf16 v[0:3], a[128:131], a[112:115], v[0:3]\nv_add_u32_e32 v232, 0x8000, v232\nds_read_b128 a[40:43], v224 offset:36864\n" \
    "v_mfma_f32_16x16x32_bf16 v[4:7], a[132:135], a[112:115], v[4:7]\nv_add_u32_e32 v233, 0x8000, v233\nv_mfma_f32_16x16x32_bf16 v[8:11], a[128:131], a[116:119], v[8:11]\nv_add_u32_e32 v234, 0x8000, v234\n" \
    "ds_read_b128 a[44:47], v224 offset:38912\nv_mfma_f32_16x16x32_bf16 v[12:15], a[132:135], a[116:119], v[12:15]\nv_add_u32_e32 v235, 0x8000, v235\nv_mfma_f32_16x16x32_bf16 v[16:19], a[128:131], a[120:123], v[16:19]\n" \
    "ds_read_b128 a[48:51], v224 offset:40960\nv_mfma_f32_16x16x32_bf16 v[20:23], a[132:135], a[120:123], v[20:23]\nv_mfma_f32_16x16x32_bf16 v[24:27], a[128:131], a[124:127], v[24:27]\n" \
    "ds_read_b128 a[52:55], v224 offset:43008\nv_mfma_f32_16x16x32_bf16 v[28:31], a[132:135], a[124:127], v[28:31]\nv_mfma_f32_16x16x32_bf16 v[32:35], a[136:139], a[96:99], v[32:35]\nds_read_b128 a[56:59], v224 offset:45056\n" \
    "v_mfma_f32_16x16x32_bf16 v[36:39], a[140:143], a[96:99], v[36:39]\nv_mfma_f32_16x16x32_bf16 v[40:43], a[136:139], a[100:103], v[40:43]\nds_read_b128 a[60:63], v224 offset:47104\n" \
    "v_mfma_f32_16x16x32_bf16 v[44:47], a[140:143], a[100:103], v[44:47]\nv_mfma_f32_16x16x32_bf16 v[48:51], a[136:139], a[104:107], v[48:51]\nds_read_b64_tr_b16 a[64:65], v228 offset:0\n" \
    "v_mfma_f32_16x16x32_bf16 v[52:55], a[140:143], a[104:107], v[52:55]\nds_read_b64_tr_b16 a[66:67], v228 offset:2048\nv_mfma_f32_16x16x32_bf16 v[56:59], a[136:139], a[108:111], v[56:59]\n" \
    "v_mfma_f32_16x16x32_bf16 v[60:63], a[140:143], a[108:111], v[60:63]\nds_read_b64_tr_b16 a[68:69], v229 offset:0\nv_mfma_f32_16x16x32_bf16 v[64:67], a[136:139], a[112:115], v[64:67]\n" \
    "ds_read_b64_tr_b16 a[70:71], v229 offset:2048\nv_mfma_f32_16x16x32_bf16 v[68:71], a[140:143], a[112:115], v[68:71]\nv_mfma_f32_16x16x32_bf16 v[72:75], a[136:139], a[116:119], v[72:75]\n" \
    "ds_read_b64_tr_b16 a[72:73], v230 offset:0\nv_mfma_f32_16x16x32_bf16 v[76:79], a[140:143], a[116:119], v[76:79]\nds_read_b64_tr_b16 a[74:75], v230 offset:2048\n" \
    "v_mfma_f32_16x16x32_bf16 v[80:83], a[136:139], a[120:123], v[80:83]\nv_mfma_f32_16x16x32_bf16 v[84:87], a[140:143], a[120:123], v[84:87]\nds_read_b64_tr_b16 a[76:77], v231 offset:0\n" \
    "v_mfma_f32_16x16x32_bf16 v[88:91], a[136:139], a[124:127], v[88:91]\nds_read_b64_tr_b16 a[78:79], v231 offset:2048\nv_mfma_f32_16x16x32_bf16 v[92:95], a[140:143], a[124:127], v[92:95]\n" \
    "v_mfma_f32_16x16x32_bf16 v[96:99], a[144:147], a[96:99], v[96:99]\nds_read_b64_tr_b16 a[80:81], v232 offset:0\nv_mfma_f32_16x16x32_bf16 v[100:103], a[148:151], a[96:99], v[100:103]\n" \
    "v_mfma_f32_16x16x32_bf16 v[104:107], a[144:147], a[100:103], v[104:107]\nds_read_b64_tr_b16 a[82:83], v232 offset:2048\nv_mfma_f32_16x16x32_bf16 v[108:111], a[148:151], a[100:103], v[108:111]\n" \
    "ds_read_b64_tr_b16 a[84:85], v233 offset:0\nv_mfma_f32_16x16x32_bf16 v[112:115], a[144:147], a[104:107], v[112:115]\nv_mfma_f32_16x16x32_bf16 v[116:119], a[148:151], a[104:107], v[116:119]\n" \
    "ds_read_b64_tr_b16 a[86:87], v233 offset:2048\nv_mfma_f32_16x16x32_bf16 v[120:123], a[144:147], a[108:111], v[120:123]\nds_read_b64_tr_b16 a[88:89], v234 offset:0\n" \
    "v_mfma_f32_16x16x32_bf16 v[124:127], a[148:151], a[108:111], v[124:127]\nv_mfma_f32_16x16x32_bf16 v[128:131], a[144:147], a[112:115], v[128:131]\nds_read_b64_tr_b16 a[90:91], v234 offset:2048\n" \
    "v_mfma_f32_16x16x32_bf16 v[132:135], a[148:151], a[112:115], v[132:135]\nds_read_b64_tr_b16 a[92:93], v235 offset:0\nv_mfma_f32_16x16x32_bf16 v[136:139], a[144:147], a[116:119], v[136:139]\n" \
    "v_mfma_f32_16x16x32_bf16 v[140:143], a[148:151], a[116:119], v[140:143]\nds_read_b64_tr_b16 a[94:95], v235 offset:2048\nv_mfma_f32_16x16x32_bf16 v[144:147], a[144:147], a[120:123], v[144:147]\n" \
    "v_mfma_f32_16x16x32_bf16 v[148:151], a[148:151], a[120:123], v[148:151]\nv_mfma_f32_16x16x32_bf16 v[152:155], a[144:147], a[124:127], v[152:155]\nv_mfma_f32_16x16x32_bf16 v[156:159], a[148:151], a[124:127], v[156:159]\n" \
    "v_mfma_f32_16x16x32_bf16 v[160:163], a[152:155], a[96:99], v[160:163]\nv_mfma_f32_16x16x32_bf16 v[164:167], a[156:159], a[96:99], v[164:167]\nv_mfma_f32_16x16x32_bf16 v[168:171], a[152:155], a[100:103], v[168:171]\n" \
    "v_mfma_f32_16x16x32_bf16 v[172:175], a[156:159], a[100:103], v[172:175]\nv_mfma_f32_16x16x32_bf16 v[176:179], a[152:155], a[104:107], v[176:179]\nv_mfma_f32_16x16x32_bf16 v[180:183], a[156:159], a[104:107], v[180:183]\n" \
    "v_mfma_f32_16x16x32_bf16 v[184:187], a[152:155], a[108:111], v[184:187]\nv_mfma_f32_16x16x32_bf16 v[188:191], a[156:159], a[108:111], v[188:191]\nv_mfma_f32_16x16x32_bf16 v[192:195], a[152:155], a[112:115], v[192:195]\n" \
    "v_mfma_f32_16x16x32_bf16 v[196:199], a[156:159], a[112:115], v[196:199]\nv_mfma_f32_16x16x32_bf16 v[200:203], a[152:155], a[116:119], v[200:203]\nv_mfma_f32_16x16x32_bf16 v[204:207], a[156:159], a[116:119], v[204:207]\n" \
    "v_mfma_f32_16x16x32_bf16 v[208:211], a[152:155], a[120:123], v[208:211]\nv_mfma_f32_16x16x32_bf16 v[212:215], a[156:159], a[120:123], v[212:215]\nv_mfma_f32_16x16x32_bf16 v[216:219], a[152:155], a[124:127], v[216:219]\n" \
    "v_mfma_f32_16x16x32_bf16 v[220:223], a[156:159], a[124:127], v[220:223]\ns_waitcnt lgkmcnt(0)\nv_mfma_f32_16x16x32_bf16 a[0:3], a[64:67], a[32:35], a[0:3]\nds_read_b128 a[96:99], v225 offset:32768\n" \
    "v_mfma_f32_16x16x32_bf16 a[4:7], a[68:71], a[32:35], a[4:7]\nds_read_b128 a[100:103], v225 offset:34816\nv_mfma_f32_16x16x32_bf16 a[8:11], a[64:67], a[36:39], a[8:11]\n" \
    "v_mfma_f32_16x16x32_bf16 a[12:15], a[68:71], a[36:39], a[12:15]\nds_read_b128 a[104:107], v225 offset:36864\nv_mfma_f32_16x16x32_bf16 a[16:19], a[64:67], a[40:43], a[16:19]\n" \
    "v_mfma_f32_16x16x32_bf16 a[20:23], a[68:71], a[40:43], a[20:23]\nds_read_b128 a[108:111], v225 offset:38912\nv_mfma_f32_16x16x32_bf16 a[24:27], a[64:67], a[44:47], a[24:27]\n" \
    "v_mfma_f32_16x16x32_bf16 a[28:31], a[68:71], a[44:47], a[28:31]\nds_read_b128 a[112:115], v225 offset:40960\nv_mfma_f32_16x16x32_bf16 v[0:3], a[64:67], a[48:51], v[0:3]\n" \
    "v_mfma_f32_16x16x32_bf16 v[4:7], a[68:71], a[48:51], v[4:7]\nds_read_b128 a[116:119], v225 offset:43008\nv_mfma_f32_16x16x32_bf16 v[8:11], a[64:67], a[52:55], v[8:11]\n" \
    "v_mfma_f32_16x16x32_bf16 v[12:15], a[68:71], a[52:55], v[12:15]\nds_read_b128 a[120:123], v225 offset:45056\nv_mfma_f32_16x16x32_bf16 v[16:19], a[64:67], a[56:59], v[16:19]\n" \
    "v_mfma_f32_16x16x32_bf16 v[20:23], a[68:71], a[56:59], v[20:23]\nds_read_b128 a[124:127], v225 offset:47104\nv_mfma_f32_16x16x32_bf16 v[24:27], a[64:67], a[60:63], v[24:27]\n" \
    "v_mfma_f32_16x16x32_bf16 v[28:31], a[68:71], a[60:63], v[28:31]\nds_read_b64_tr_b16 a[128:129], v228 offset:16384\nv_mfma_f32_16x16x32_bf16 v[32:35], a[72:75], a[32:35], v[32:35]\n" \
    "ds_read_b64_tr_b16 a[130:131], v228 offset:18432\nv_mfma_f32_16x16x32_bf16 v[36:39], a[76:79], a[32:35], v[36:39]\nv_mfma_f32_16x16x32_bf16 v[40:43], a[72:75], a[36:39], v[40:43]\n" \
    "ds_read_b64_tr_b16 a[132:133], v229 offset:16384\nv_mfma_f32_16x16x32_bf16 v[44:47], a[76:79], a[36:39], v[44:47]\nv_mfma_f32_16x16x32_bf16 v[48:51], a[72:75], a[40:43], v[48:51]\n" \
    "ds_read_b64_tr_b16 a[134:135], v229 offset:18432\nv_mfma_f32_16x16x32_bf16 v[52:55], a[76:79], a[40:43], v[52:55]\nv_mfma_f32_16x16x32_bf16 v[56:59], a[72:75], a[44:47], v[56:59]\n" \
    "ds_read_b64_tr_b16 a[136:137], v230 offset:16384\nv_mfma_f32_16x16x32_bf16 v[60:63], a[76:79], a[44:47], v[60:63]\nv_mfma_f32_16x16x32_bf16 v[64:67], a[72:75], a[48:51], v[64:67]\n" \
    "ds_read_b64_tr_b16 a[138:139], v230 offset:18432\nv_mfma_f32_16x16x32_bf16 v[68:71], a[76:79], a[48:51], v[68:71]\nv_mfma_f32_16x16x32_bf16 v[72:75], a[72:75], a[52:55], v[72:75]\n" \
    "ds_read_b64_tr_b16 a[140:141], v231 offset:16384\nv_mfma_f32_16x16x32_bf16 v[76:79], a[76:79], a[52:55], v[76:79]\nv_mfma_f32_16x16x32_bf16 v[80:83], a[72:75], a[56:59], v[80:83]\n" \
    "ds_read_b64_tr_b16 a[142:143], v231 offset:18432\nv_mfma_f32_16x16x32_bf16 v[84:87], a[76:79], a[56:59], v[84:87]\nv_mfma_f32_16x16x32_bf16 v[88:91], a[72:75], a[60:63], v[88:91]\n" \
    "ds_read_b64_tr_b16 a[144:145], v232 offset:16384\nv_mfma_f32_16x16x32_bf16 v[92:95], a[76:79], a[60:63], v[92:95]\nds_read_b64_tr_b16 a[146:147], v232 offset:18432\n" \
    "v_mfma_f32_16x16x32_bf16 v[96:99], a[80:83], a[32:35], v[96:99]\nv_mfma_f32_16x16x32_bf16 v[100:103], a[84:87], a[32:35], v[100:103]\nds_read_b64_tr_b16 a[148:149], v233 offset:16384\n" \
    "v_mfma_f32_16x16x32_bf16 v[104:107], a[80:83], a[36:39], v[104:107]\nv_mfma_f32_16x16x32_bf16 v[108:111], a[84:87], a[36:39], v[108:111]\nds_read_b64_tr_b16 a[150:151], v233 offset:18432\n" \
    "v_mfma_f32_16x16x32_bf16 v[112:115], a[80:83], a[40:43], v[112:115]\nv_mfma_f32_16x16x32_bf16 v[116:119], a[84:87], a[40:43], v[116:119]\nds_read_b64_tr_b16 a[152:153], v234 offset:16384\n" \
    "v_mfma_f32_16x16x32_bf16 v[120:123], a[80:83], a[44:47], v[120:123]\nv_mfma_f32_16x16x32_bf16 v[124:127], a[84:87], a[44:47], v[124:127]\nds_read_b64_tr_b16 a[154:155], v234 offset:18432\n" \
    "v_mfma_f32_16x16x32_bf16 v[128:131], a[80:83], a[48:51], v[128:131]\nv_mfma_f32_16x16x32_bf16 v[132:135], a[84:87], a[48:51], v[132:135]\nds_read_b64_tr_b16 a[156:157], v235 offset:16384\n" \
    "v_mfma_f32_16x16x32_bf16 v[136:139], a[80:83], a[52:55], v[136:139]\nv_mfma_f32_16x16x32_bf16 v[140:143], a[84:87], a[52:55], v[140:143]\nds_read_b64_tr_b16 a[158:159], v235 offset:18432\n" \
    "v_mfma_f32_16x16x32_bf16 v[144:147], a[80:83], a[56:59], v[144:147]\nv_mfma_f32_16x16x32_bf16 v[148:151], a[84:87], a[56:59], v[148:151]\nv_mfma_f32_16x16x32_bf16 v[152:155], a[80:83], a[60:63], v[152:155]\n" \
    "v_mfma_f32_16x16x32_bf16 v[156:159], a[84:87], a[60:63], v[156:159]\nv_mfma_f32_16x16x32_bf16 v[160:163], a[88:91], a[32:35], v[160:163]\nv_mfma_f32_16x16x32_bf16 v[164:167], a[92:95], a[32:35], v[164:167]\n" \
    "v_mfma_f32_16x16x32_bf16 v[168:171], a[88:91], a[36:39], v[168:171]\nv_mfma_f32_16x16x32_bf16 v[172:175], a[92:95], a[36:39], v[172:175]\nv_mfma_f32_16x16x32_bf16 v[176:179], a[88:91], a[40:43], v[176:179]\n" \
    "v_mfma_f32_16x16x32_bf16 v[180:183], a[92:95], a[40:43], v[180:183]\nv_mfma_f32_16x16x32_bf16 v[184:187], a[88:91], a[44:47], v[184:187]\nv_mfma_f32_16x16x32_bf16 v[188:191], a[92:95], a[44:47], v[188:191]\n" \
    "v_mfma_f32_16x16x32_bf16 v[192:195], a[88:91], a[48:51], v[192:195]\nv_mfma_f32_16x16x32_bf16 v[196:199], a[92:95], a[48:51], v[196:199]\nv_mfma_f32_16x16x32_bf16 v[200:203], a[88:91], a[52:55], v[200:203]\n" \
    "v_mfma_f32_16x16x32_bf16 v[204:207], a[92:95], a[52:55], v[204:207]\nv_mfma_f32_16x16x32_bf16 v[208:211], a[88:91], a[56:59], v[208:211]\nv_mfma_f32_16x16x32_bf16 v[212:215], a[92:95], a[56:59], v[212:215]\n" \
    "v_mfma_f32_16x16x32_bf16 v[216:219], a[88:91], a[60:63], v[216:219]\nv_mfma_f32_16x16x32_bf16 v[220:223], a[92:95], a[60:63], v[220:223]\ns_waitcnt vmcnt(63) lgkmcnt(0)\ns_barrier\n" \
    "v_mfma_f32_16x16x32_bf16 a[0:3], a[128:131], a[96:99], a[0:3]\nv_mfma_f32_16x16x32_bf16 a[4:7], a[132:135], a[96:99], a[4:7]\nv_mfma_f32_16x16x32_bf16 a[8:11], a[128:131], a[100:103], a[8:11]\n" \
    "v_mfma_f32_16x16x32_bf16 a[12:15], a[132:135], a[100:103], a[12:15]\nv_mfma_f32_16x16x32_bf16 a[16:19], a[128:131], a[104:107], a[16:19]\nv_mfma_f32_16x16x32_bf16 a[20:23], a[132:135], a[104:107], a[20:23]\n" \
    "v_mfma_f32_16x16x32_bf16 a[24:27], a[128:131], a[108:111], a[24:27]\nv_mfma_f32_16x16x32_bf16 a[28:31], a[132:135], a[108:111], a[28:31]\nv_accvgpr_read_b32 v246, a0\nv_accvgpr_read_b32 v247, a1\n" \
    "v_mfma_f32_16x16x32_bf16 v[0:3], a[128:131], a[112:115], v[0:3]\nv_accvgpr_read_b32 v248, a2\nv_accvgpr_read_b32 v249, a3\nv_mfma_f32_16x16x32_bf16 v[4:7], a[132:135], a[112:115], v[4:7]\nv_accvgpr_read_b32 v250, a4\n" \
    "v_accvgpr_read_b32 v251, a5\nv_mfma_f32_16x16x32_bf16 v[8:11], a[128:131], a[116:119], v[8:11]\nv_accvgpr_read_b32 v252, a6\nv_accvgpr_read_b32 v253, a7\n" \
    "v_mfma_f32_16x16x32_bf16 v[12:15], a[132:135], a[116:119], v[12:15]\nv_cvt_pk_bf16_f32 v246, v246, v247\nv_cvt_pk_bf16_f32 v247, v248, v249\nv_mfma_f32_16x16x32_bf16 v[16:19], a[128:131], a[120:123], v[16:19]\n" \
    "v_cvt_pk_bf16_f32 v248, v250, v251\nv_cvt_pk_bf16_f32 v249, v252, v253\nv_mfma_f32_16x16x32_bf16 v[20:23], a[132:135], a[120:123], v[20:23]\ns_add_u32 s82, %[so0], 0x0\ns_nop 0\nv_permlane16_swap_b32_e32 v246, v248\n" \
    "v_mfma_f32_16x16x32_bf16 v[24:27], a[128:131], a[124:127], v[24:27]\nv_permlane16_swap_b32_e32 v247, v249\nbuffer_store_dwordx4 v[246:249], v244, %[srdO], s82 offen sc1\n" \
    "v_mfma_f32_16x16x32_bf16 v[28:31], a[132:135], a[124:127], v[28:31]\ns_nop 1\nv_accvgpr_read_b32 v246, a8\nv_accvgpr_read_b32 v247, a9\nv_mfma_f32_16x16x32_bf16 v[32:35], a[136:139], a[96:99], v[32:35]\n" \
    "v_accvgpr_read_b32 v248, a10\nv_accvgpr_read_b32 v249, a11\nv_mfma_f32_16x16x32_bf16 v[36:39], a[140:143], a[96:99], v[36:39]\nv_accvgpr_read_b32 v250, a12\nv_accvgpr_read_b32 v251, a13\n" \
    "v_mfma_f32_16x16x32_bf16 v[40:43], a[136:139], a[100:103], v[40:43]\nv_accvgpr_read_b32 v252, a14\nv_accvgpr_read_b32 v253, a15\nv_mfma_f32_16x16x32_bf16 v[44:47], a[140:143], a[100:103], v[44:47]\n" \
    "v_cvt_pk_bf16_f32 v246, v246, v247\nv_cvt_pk_bf16_f32 v247, v248, v249\nv_mfma_f32_16x16x32_bf16 v[48:51], a[136:139], a[104:107], v[48:51]\nv_cvt_pk_bf16_f32 v248, v250, v251\nv_cvt_pk_bf16_f32 v249, v252, v253\n" \
    "v_mfma_f32_16x16x32_bf16 v[52:55], a[140:143], a[104:107], v[52:55]\ns_add_u32 s82, %[so0], 0x1000\ns_nop 0\nv_permlane16_swap_b32_e32 v246, v248\nv_mfma_f32_16x16x32_bf16 v[56:59], a[136:139], a[108:111], v[56:59]\n" \
    "v_permlane16_swap_b32_e32 v247, v249\nbuffer_store_dwordx4 v[246:249], v244, %[srdO], s82 offen sc1\nv_mfma_f32_16x16x32_bf16 v[60:63], a[140:143], a[108:111], v[60:63]\ns_nop 1\nv_accvgpr_read_b32 v246, a16\n" \
    "v_accvgpr_read_b32 v247, a17\nv_mfma_f32_16x16x32_bf16 v[64:67], a[136:139], a[112:115], v[64:67]\nv_accvgpr_read_b32 v248, a18\nv_accvgpr_read_b32 v249, a19\n" \
    "v_mfma_f32_16x16x32_bf16 v[68:71], a[140:143], a[112:115], v[68:71]\nv_accvgpr_read_b32 v250, a20\nv_accvgpr_read_b32 v251, a21\nv_mfma_f32_16x16x32_bf16 v[72:75], a[136:139], a[116:119], v[72:75]\n" \
    "v_accvgpr_read_b32 v252, a22\nv_accvgpr_read_b32 v253, a23\nv_mfma_f32_16x16x32_bf16 v[76:79], a[140:143], a[116:119], v[76:79]\nv_cvt_pk_bf16_f32 v246, v246, v247\nv_cvt_pk_bf16_f32 v247, v248, v249\n" \
    "v_mfma_f32_16x16x32_bf16 v[80:83], a[136:139], a[120:123], v[80:83]\nv_cvt_pk_bf16_f32 v248, v250, v251\nv_cvt_pk_bf16_f32 v249, v252, v253\nv_mfma_f32_16x16x32_bf16 v[84:87], a[140:143], a[120:123], v[84:87]\n" \
    "s_add_u32 s82, %[so0], 0x2000\ns_nop 0\nv_permlane16_swap_b32_e32 v246, v248\nv_mfma_f32_16x16x32_bf16 v[88:91], a[136:139], a[124:127], v[88:91]\nv_permlane16_swap_b32_e32 v247, v249\n" \
    "buffer_store_dwordx4 v[246:249], v244, %[srdO], s82 offen sc1\nv_mfma_f32_16x16x32_bf16 v[92:95], a[140:143], a[124:127], v[92:95]\ns_nop 1\nv_accvgpr_read_b32 v246, a24\nv_accvgpr_read_b32 v247, a25\n" \
    "v_mfma_f32_16x16x32_bf16 v[96:99], a[144:147], a[96:99], v[96:99]\nv_accvgpr_read_b32 v248, a26\nv_accvgpr_read_b32 v249, a27\nv_mfma_f32_16x16x32_bf16 v[100:103], a[148:151], a[96:99], v[100:103]\n" \
    "v_accvgpr_read_b32 v250, a28\nv_accvgpr_read_b32 v251, a29\nv_mfma_f32_16x16x32_bf16 v[104:107], a[144:147], a[100:103], v[104:107]\nv_accvgpr_read_b32 v252, a30\nv_accvgpr_read_b32 v253, a31\n" \
    "v_mfma_f32_16x16x32_bf16 v[108:111], a[148:151], a[100:103], v[108:111]\nv_cvt_pk_bf16_f32 v246, v246, v247\nv_cvt_pk_bf16_f32 v247, v248, v249\nv_mfma_f32_16x16x32_bf16 v[112:115], a[144:147], a[104:107], v[112:115]\n" \
    "v_cvt_pk_bf16_f32 v248, v250, v251\nv_cvt_pk_bf16_f32 v249, v252, v253\nv_mfma_f32_16x16x32_bf16 v[116:119], a[148:151], a[104:107], v[116:119]\ns_add_u32 s82, %[so0], 0x3000\ns_nop 0\n" \
    "v_permlane16_swap_b32_e32 v246, v248\nv_mfma_f32_16x16x32_bf16 v[120:123], a[144:147], a[108:111], v[120:123]\nv_permlane16_swap_b32_e32 v247, v249\nbuffer_store_dwordx4 v[246:249], v244, %[srdO], s82 offen sc1\n" \
    "v_mfma_f32_16x16x32_bf16 v[124:127], a[148:151], a[108:111], v[124:127]\ns_nop 1\nv_cvt_pk_bf16_f32 v0, v0, v1\nv_cvt_pk_bf16_f32 v1, v2, v3\nv_mfma_f32_16x16x32_bf16 v[128:131], a[144:147], a[112:115], v[128:131]\n" \
    "v_cvt_pk_bf16_f32 v2, v4, v5\nv_cvt_pk_bf16_f32 v3, v6, v7\nv_mfma_f32_16x16x32_bf16 v[132:135], a[148:151], a[112:115], v[132:135]\ns_add_u32 s82, %[so0], 0x4000\ns_nop 0\nv_permlane16_swap_b32_e32 v0, v2\n" \
    "v_mfma_f32_16x16x32_bf16 v[136:139], a[144:147], a[116:119], v[136:139]\nv_permlane16_swap_b32_e32 v1, v3\nbuffer_store_dwordx4 v[0:3], v244, %[srdO], s82 offen sc1\n" \
    "v_mfma_f32_16x16x32_bf16 v[140:143], a[148:151], a[116:119], v[140:143]\nv_cvt_pk_bf16_f32 v8, v8, v9\nv_cvt_pk_bf16_f32 v9, v10, v11\nv_mfma_f32_16x16x32_bf16 v[144:147], a[144:147], a[120:123], v[144:147]\n" \
    "v_cvt_pk_bf16_f32 v10, v12, v13\nv_cvt_pk_bf16_f32 v11, v14, v15\nv_mfma_f32_16x16x32_bf16 v[148:151], a[148:151], a[120:123], v[148:151]\ns_add_u32 s82, %[so0], 0x5000\ns_nop 0\nv_permlane16_swap_b32_e32 v8, v10\n" \
    "v_mfma_f32_16x16x32_bf16 v[152:155], a[144:147], a[124:127], v[152:155]\nv_permlane16_swap_b32_e32 v9, v11\nbuffer_store_dwordx4 v[8:11], v244, %[srdO], s82 offen sc1\n" \
    "v_mfma_f32_16x16x32_bf16 v[156:159], a[148:151], a[124:127], v[156:159]\nv_cvt_pk_bf16_f32 v16, v16, v17\nv_cvt_pk_bf16_f32 v17, v18, v19\nv_mfma_f32_16x16x32_bf16 v[160:163], a[152:155], a[96:99], v[160:163]\n" \
    "v_cvt_pk_bf16_f32 v18, v20, v21\nv_cvt_pk_bf16_f32 v19, v22, v23\nv_mfma_f32_16x16x32_bf16 v[164:167], a[156:159], a[96:99], v[164:167]\ns_add_u32 s82, %[so0], 0x6000\ns_nop 0\nv_permlane16_swap_b32_e32 v16, v18\n" \
    "v_mfma_f32_16x16x32_bf16 v[168:171], a[152:155], a[100:103], v[168:171]\nv_permlane16_swap_b32_e32 v17, v19\nbuffer_store_dwordx4 v[16:19], v244, %[srdO], s82 offen sc1\n" \
    "v_mfma_f32_16x16x32_bf16 v[172:175], a[156:159], a[100:103], v[172:175]\nv_cvt_pk_bf16_f32 v24, v24, v25\nv_cvt_pk_bf16_f32 v25, v26, v27\nv_mfma_f32_16x16x32_bf16 v[176:179], a[152:155], a[104:107], v[176:179]\n" \
    "v_cvt_pk_bf16_f32 v26, v28, v29\nv_cvt_pk_bf16_f32 v27, v30, v31\nv_mfma_f32_16x16x32_bf16 v[180:183], a[156:159], a[104:107], v[180:183]\ns_add_u32 s82, %[so0], 0x7000\ns_nop 0\nv_permlane16_swap_b32_e32 v24, v26\n" \
    "v_mfma_f32_16x16x32_bf16 v[184:187], a[152:155], a[108:111], v[184:187]\nv_permlane16_swap_b32_e32 v25, v27\nbuffer_store_dwordx4 v[24:27], v244, %[srdO], s82 offen sc1\n" \
    "v_mfma_f32_16x16x32_bf16 v[188:191], a[156:159], a[108:111], v[188:191]\nv_cvt_pk_bf16_f32 v32, v32, v33\nv_cvt_pk_bf16_f32 v33, v34, v35\nv_mfma_f32_16x16x32_bf16 v[192:195], a[152:155], a[112:115], v[192:195]\n" \
    "v_cvt_pk_bf16_f32 v34, v36, v37\nv_cvt_pk_bf16_f32 v35, v38, v39\nv_mfma_f32_16x16x32_bf16 v[196:199], a[156:159], a[112:115], v[196:199]\ns_add_u32 s82, %[so1], 0x0\ns_nop 0\nv_permlane16_swap_b32_e32 v32, v34\n" \
    "v_mfma_f32_16x16x32_bf16 v[200:203], a[152:155], a[116:119], v[200:203]\nv_permlane16_swap_b32_e32 v33, v35\nbuffer_store_dwordx4 v[32:35], v244, %[srdO], s82 offen sc1\n" \
    "v_mfma_f32_16x16x32_bf16 v[204:207], a[156:159], a[116:119], v[204:207]\nv_cvt_pk_bf16_f32 v40, v40, v41\nv_cvt_pk_bf16_f32 v41, v42, v43\nv_mfma_f32_16x16x32_bf16 v[208:211], a[152:155], a[120:123], v[208:211]\n" \
    "v_cvt_pk_bf16_f32 v42, v44, v45\nv_cvt_pk_bf16_f32 v43, v46, v47\nv_mfma_f32_16x16x32_bf16 v[212:215], a[156:159], a[120:123], v[212:215]\ns_add_u32 s82, %[so1], 0x1000\ns_nop 0\nv_permlane16_swap_b32_e32 v40, v42\n" \
    "v_mfma_f32_16x16x32_bf16 v[216:219], a[152:155], a[124:127], v[216:219]\nv_permlane16_swap_b32_e32 v41, v43\nbuffer_store_dwordx4 v[40:43], v244, %[srdO], s82 offen sc1\n" \
    "v_mfma_f32_16x16x32_bf16 v[220:223], a[156:159], a[124:127], v[220:223]\nv_cvt_pk_bf16_f32 v48, v48, v49\nv_cvt_pk_bf16_f32 v49, v50, v51\ns_nop 7\ns_nop 3\nv_cvt_pk_bf16_f32 v50, v52, v53\n" \
    "v_cvt_pk_bf16_f32 v51, v54, v55\ns_add_u32 s82, %[so1], 0x2000\ns_nop 0\nv_permlane16_swap_b32_e32 v48, v50\nv_permlane16_swap_b32_e32 v49, v51\nbuffer_store_dwordx4 v[48:51], v244, %[srdO], s82 offen sc1\n" \
    "v_cvt_pk_bf16_f32 v56, v56, v57\nv_cvt_pk_bf16_f32 v57, v58, v59\nv_cvt_pk_bf16_f32 v58, v60, v61\nv_cvt_pk_bf16_f32 v59, v62, v63\ns_add_u32 s82, %[so1], 0x3000\ns_nop 0\nv_permlane16_swap_b32_e32 v56, v58\n" \
    "v_permlane16_swap_b32_e32 v57, v59\nbuffer_store_dwordx4 v[56:59], v244, %[srdO], s82 offen sc1\nv_cvt_pk_bf16_f32 v64, v64, v65\nv_cvt_pk_bf16_f32 v65, v66, v67\nv_cvt_pk_bf16_f32 v66, v68, v69\n" \
    "v_cvt_pk_bf16_f32 v67, v70, v71\ns_add_u32 s82, %[so1], 0x4000\ns_nop 0\nv_permlane16_swap_b32_e32 v64, v66\nv_permlane16_swap_b32_e32 v65, v67\nbuffer_store_dwordx4 v[64:67], v244, %[srdO], s82 offen sc1\n" \
    "v_cvt_pk_bf16_f32 v72, v72, v73\nv_cvt_pk_bf16_f32 v73, v74, v75\nv_cvt_pk_bf16_f32 v74, v76, v77\nv_cvt_pk_bf16_f32 v75, v78, v79\ns_add_u32 s82, %[so1], 0x5000\ns_nop 0\nv_permlane16_swap_b32_e32 v72, v74\n" \
    "v_permlane16_swap_b32_e32 v73, v75\nbuffer_store_dwordx4 v[72:75], v244, %[srdO], s82 offen sc1\nv_cvt_pk_bf16_f32 v80, v80, v81\nv_cvt_pk_bf16_f32 v81, v82, v83\nv_cvt_pk_bf16_f32 v82, v84, v85\n" \
    "v_cvt_pk_bf16_f32 v83, v86, v87\ns_add_u32 s82, %[so1], 0x6000\ns_nop 0\nv_permlane16_swap_b32_e32 v80, v82\nv_permlane16_swap_b32_e32 v81, v83\nbuffer_store_dwordx4 v[80:83], v244, %[srdO], s82 offen sc1\n" \
    "v_cvt_pk_bf16_f32 v88, v88, v89\nv_cvt_pk_bf16_f32 v89, v90, v91\nv_cvt_pk_bf16_f32 v90, v92, v93\nv_cvt_pk_bf16_f32 v91, v94, v95\ns_add_u32 s82, %[so1], 0x7000\ns_nop 0\nv_permlane16_swap_b32_e32 v88, v90\n" \
    "v_permlane16_swap_b32_e32 v89, v91\nbuffer_store_dwordx4 v[88:91], v244, %[srdO], s82 offen sc1\nv_cvt_pk_bf16_f32 v96, v96, v97\nv_cvt_pk_bf16_f32 v97, v98, v99\nv_cvt_pk_bf16_f32 v98, v100, v101\n" \
    "v_cvt_pk_bf16_f32 v99, v102, v103\ns_add_u32 s82, %[so2], 0x0\ns_nop 0\nv_permlane16_swap_b32_e32 v96, v98\nv_permlane16_swap_b32_e32 v97, v99\nbuffer_store_dwordx4 v[96:99], v244, %[srdO], s82 offen sc1\n" \
    "v_cvt_pk_bf16_f32 v104, v104, v105\nv_cvt_pk_bf16_f32 v105, v106, v107\nv_cvt_pk_bf16_f32 v106, v108, v109\nv_cvt_pk_bf16_f32 v107, v110, v111\ns_add_u32 s82, %[so2], 0x1000\ns_nop 0\n" \
    "v_permlane16_swap_b32_e32 v104, v106\nv_permlane16_swap_b32_e32 v105, v107\nbuffer_store_dwordx4 v[104:107], v244, %[srdO], s82 offen sc1\nv_cvt_pk_bf16_f32 v112, v112, v113\nv_cvt_pk_bf16_f32 v113, v114, v115\n" \
    "v_cvt_pk_bf16_f32 v114, v116, v117\nv_cvt_pk_bf16_f32 v115, v118, v119\ns_add_u32 s82, %[so2], 0x2000\ns_nop 0\nv_permlane16_swap_b32_e32 v112, v114\nv_permlane16_swap_b32_e32 v113, v115\n" \
    "buffer_store_dwordx4 v[112:115], v244, %[srdO], s82 offen sc1\nv_cvt_pk_bf16_f32 v120, v120, v121\nv_cvt_pk_bf16_f32 v121, v122, v123\nv_cvt_pk_bf16_f32 v122, v124, v125\nv_cvt_pk_bf16_f32 v123, v126, v127\n" \
    "s_add_u32 s82, %[so2], 0x3000\ns_nop 0\nv_permlane16_swap_b32_e32 v120, v122\nv_permlane16_swap_b32_e32 v121, v123\nbuffer_store_dwordx4 v[120:123], v244, %[srdO], s82 offen sc1\nv_cvt_pk_bf16_f32 v128, v128, v129\n" \
    "v_cvt_pk_bf16_f32 v129, v130, v131\nv_cvt_pk_bf16_f32 v130, v132, v133\nv_cvt_pk_bf16_f32 v131, v134, v135\ns_add_u32 s82, %[so2], 0x4000\ns_nop 0\nv_permlane16_swap_b32_e32 v128, v130\n" \
    "v_permlane16_swap_b32_e32 v129, v131\nbuffer_store_dwordx4 v[128:131], v244, %[srdO], s82 offen sc1\nv_cvt_pk_bf16_f32 v136, v136, v137\nv_cvt_pk_bf16_f32 v137, v138, v139\nv_cvt_pk_bf16_f32 v138, v140, v141\n" \
    "v_cvt_pk_bf16_f32 v139, v142, v143\ns_add_u32 s82, %[so2], 0x5000\ns_nop 0\nv_permlane16_swap_b32_e32 v136, v138\nv_permlane16_swap_b32_e32 v137, v139\nbuffer_store_dwordx4 v[136:139], v244, %[srdO], s82 offen sc1\n" \
    "v_cvt_pk_bf16_f32 v144, v144, v145\nv_cvt_pk_bf16_f32 v145, v146, v147\nv_cvt_pk_bf16_f32 v146, v148, v149\nv_cvt_pk_bf16_f32 v147, v150, v151\ns_add_u32 s82, %[so2], 0x6000\ns_nop 0\n" \
    "v_permlane16_swap_b32_e32 v144, v146\nv_permlane16_swap_b32_e32 v145, v147\nbuffer_store_dwordx4 v[144:147], v244, %[srdO], s82 offen sc1\nv_cvt_pk_bf16_f32 v152, v152, v153\nv_cvt_pk_bf16_f32 v153, v154, v155\n" \
    "v_cvt_pk_bf16_f32 v154, v156, v157\nv_cvt_pk_bf16_f32 v155, v158, v159\ns_add_u32 s82, %[so2], 0x7000\ns_nop 0\nv_permlane16_swap_b32_e32 v152, v154\nv_permlane16_swap_b32_e32 v153, v155\n" \
    "buffer_store_dwordx4 v[152:155], v244, %[srdO], s82 offen sc1\nv_cvt_pk_bf16_f32 v160, v160, v161\nv_cvt_pk_bf16_f32 v161, v162, v163\nv_cvt_pk_bf16_f32 v162, v164, v165\nv_cvt_pk_bf16_f32 v163, v166, v167\n" \
    "s_add_u32 s82, %[so3], 0x0\ns_nop 0\nv_permlane16_swap_b32_e32 v160, v162\nv_permlane16_swap_b32_e32 v161, v163\nbuffer_store_dwordx4 v[160:163], v244, %[srdO], s82 offen sc1\nv_cvt_pk_bf16_f32 v168, v168, v169\n" \
    "v_cvt_pk_bf16_f32 v169, v170, v171\nv_cvt_pk_bf16_f32 v170, v172, v173\nv_cvt_pk_bf16_f32 v171, v174, v175\ns_add_u32 s82, %[so3], 0x1000\ns_nop 0\nv_permlane16_swap_b32_e32 v168, v170\n" \
    "v_permlane16_swap_b32_e32 v169, v171\nbuffer_store_dwordx4 v[168:171], v244, %[srdO], s82 offen sc1\nv_cvt_pk_bf16_f32 v176, v176, v177\nv_cvt_pk_bf16_f32 v177, v178, v179\nv_cvt_pk_bf16_f32 v178, v180, v181\n" \
    "v_cvt_pk_bf16_f32 v179, v182, v183\ns_add_u32 s82, %[so3], 0x2000\ns_nop 0\nv_permlane16_swap_b32_e32 v176, v178\nv_permlane16_swap_b32_e32 v177, v179\nbuffer_store_dwordx4 v[176:179], v244, %[srdO], s82 offen sc1\n" \
    "v_cvt_pk_bf16_f32 v184, v184, v185\nv_cvt_pk_bf16_f32 v185, v186, v187\nv_cvt_pk_bf16_f32 v186, v188, v189\nv_cvt_pk_bf16_f32 v187, v190, v191\ns_add_u32 s82, %[so3], 0x3000\ns_nop 0\n" \
    "v_permlane16_swap_b32_e32 v184, v186\nv_permlane16_swap_b32_e32 v185, v187\nbuffer_store_dwordx4 v[184:187], v244, %[srdO], s82 offen sc1\nv_cvt_pk_bf16_f32 v192, v192, v193\nv_cvt_pk_bf16_f32 v193, v194, v195\n" \
    "v_cvt_pk_bf16_f32 v194, v196, v197\nv_cvt_pk_bf16_f32 v195, v198, v199\ns_add_u32 s82, %[so3], 0x4000\ns_nop 0\nv_permlane16_swap_b32_e32 v192, v194\nv_permlane16_swap_b32_e32 v193, v195\n" \
    "buffer_store_dwordx4 v[192:195], v244, %[srdO], s82 offen sc1\nv_cvt_pk_bf16_f32 v200, v200, v201\nv_cvt_pk_bf16_f32 v201, v202, v203\nv_cvt_pk_bf16_f32 v202, v204, v205\nv_cvt_pk_bf16_f32 v203, v206, v207\n" \
    "s_add_u32 s82, %[so3], 0x5000\ns_nop 0\nv_permlane16_swap_b32_e32 v200, v202\nv_permlane16_swap_b32_e32 v201, v203\nbuffer_store_dwordx4 v[200:203], v244, %[srdO], s82 offen sc1\nv_cvt_pk_bf16_f32 v208, v208, v209\n" \
    "v_cvt_pk_bf16_f32 v209, v210, v211\nv_cvt_pk_bf16_f32 v210, v212, v213\nv_cvt_pk_bf16_f32 v211, v214, v215\ns_add_u32 s82, %[so3], 0x6000\ns_nop 0\nv_permlane16_swap_b32_e32 v208, v210\n" \
    "v_permlane16_swap_b32_e32 v209, v211\nbuffer_store_dwordx4 v[208:211], v244, %[srdO], s82 offen sc1\nv_cvt_pk_bf16_f32 v216, v216, v217\nv_cvt_pk_bf16_f32 v217, v218, v219\nv_cvt_pk_bf16_f32 v218, v220, v221\n" \
    "v_cvt_pk_bf16_f32 v219, v222, v223\ns_add_u32 s82, %[so3], 0x7000\ns_nop 0\nv_permlane16_swap_b32_e32 v216, v218\nv_permlane16_swap_b32_e32 v217, v219\nbuffer_store_dwordx4 v[216:219], v244, %[srdO], s82 offen sc1\n" \
    "s_branch L_end%=\nL_fQ%=:\nv_mfma_f32_16x16x32_bf16 a[0:3], a[64:67], a[32:35], a[0:3]\nv_accvgpr_read_b32 v240, a253\nv_accvgpr_read_b32 v241, a254\nv_accvgpr_read_b32 v242, a255\nds_read_b128 a[96:99], v225 offset:0\n" \
    "v_mfma_f32_16x16x32_bf16 a[4:7], a[68:71], a[32:35], a[4:7]\nds_read_b128 a[100:103], v225 offset:2048\ns_mov_b32 s83, %[sw2]\nv_mfma_f32_16x16x32_bf16 a[8:11], a[64:67], a[36:39], a[8:11]\n" \
    "s_add_u32 %[sw2], %[sw2], 0x80000\nv_mfma_f32_16x16x32_bf16 a[12:15], a[68:71], a[36:39], a[12:15]\nds_read_b128 a[104:107], v225 offset:4096\ns_add_u32 m0, %[m0w2], 0x20000\ns_add_u32 s81, s83, 0x0\n" \
    "buffer_load_dwordx4 v240, %[srdW2], s81 offen offset:0 lds\nv_mfma_f32_16x16x32_bf16 a[16:19], a[64:67], a[40:43], a[16:19]\nv_mfma_f32_16x16x32_bf16 a[20:23], a[68:71], a[40:43], a[20:23]\n" \
    "ds_read_b128 a[108:111], v225 offset:6144\nv_mfma_f32_16x16x32_bf16 a[24:27], a[64:67], a[44:47], a[24:27]\nv_mfma_f32_16x16x32_bf16 a[28:31], a[68:71], a[44:47], a[28:31]\nds_read_b128 a[112:115], v225 offset:8192\n" \
    "v_mfma_f32_16x16x32_bf16 v[0:3], a[64:67], a[48:51], v[0:3]\nv_mfma_f32_16x16x32_bf16 v[4:7], a[68:71], a[48:51], v[4:7]\nds_read_b128 a[116:119], v225 offset:10240\n" \
    "v_mfma_f32_16x16x32_bf16 v[8:11], a[64:67], a[52:55], v[8:11]\nv_mfma_f32_16x16x32_bf16 v[12:15], a[68:71], a[52:55], v[12:15]\nds_read_b128 a[120:123], v225 offset:12288\n" \
    "buffer_load_dwordx4 v241, %[srdW2], s81 offen offset:1024 lds\nv_mfma_f32_16x16x32_bf16 v[16:19], a[64:67], a[56:59], v[16:19]\nv_mfma_f32_16x16x32_bf16 v[20:23], a[68:71], a[56:59], v[20:23]\n" \
    "ds_read_b128 a[124:127], v225 offset:14336\nv_mfma_f32_16x16x32_bf16 v[24:27], a[64:67], a[60:63], v[24:27]\nv_mfma_f32_16x16x32_bf16 v[28:31], a[68:71], a[60:63], v[28:31]\n" \
    "ds_read_b64_tr_b16 a[128:129], v228 offset:16384\nv_mfma_f32_16x16x32_bf16 v[32:35], a[72:75], a[32:35], v[32:35]\nds_read_b64_tr_b16 a[130:131], v228 offset:18432\n" \
    "v_mfma_f32_16x16x32_bf16 v[36:39], a[76:79], a[32:35], v[36:39]\nv_mfma_f32_16x16x32_bf16 v[40:43], a[72:75], a[36:39], v[40:43]\nds_read_b64_tr_b16 a[132:133], v229 offset:16384\n" \
    "v_mfma_f32_16x16x32_bf16 v[44:47], a[76:79], a[36:39], v[44:47]\nbuffer_load_dwordx4 v242, %[srdW2], s81 offen offset:2048 lds\nv_mfma_f32_16x16x32_bf16 v[48:51], a[72:75], a[40:43], v[48:51]\n" \
    "ds_read_b64_tr_b16 a[134:135], v229 offset:18432\nv_mfma_f32_16x16x32_bf16 v[52:55], a[76:79], a[40:43], v[52:55]\nv_mfma_f32_16x16x32_bf16 v[56:59], a[72:75], a[44:47], v[56:59]\n" \
    "ds_read_b64_tr_b16 a[136:137], v230 offset:16384\nv_mfma_f32_16x16x32_bf16 v[60:63], a[76:79], a[44:47], v[60:63]\nv_mfma_f32_16x16x32_bf16 v[64:67], a[72:75], a[48:51], v[64:67]\n" \
    "ds_read_b64_tr_b16 a[138:139], v230 offset:18432\nv_mfma_f32_16x16x32_bf16 v[68:71], a[76:79], a[48:51], v[68:71]\nv_mfma_f32_16x16x32_bf16 v[72:75], a[72:75], a[52:55], v[72:75]\n" \
    "ds_read_b64_tr_b16 a[140:141], v231 offset:16384\nv_mfma_f32_16x16x32_bf16 v[76:79], a[76:79], a[52:55], v[76:79]\nv_mfma_f32_16x16x32_bf16 v[80:83], a[72:75], a[56:59], v[80:83]\n" \
    "ds_read_b64_tr_b16 a[142:143], v231 offset:18432\ns_add_u32 m0, %[m0w2], 0x21800\ns_add_u32 s81, s83, 0x20000\nbuffer_load_dwordx4 v240, %[srdW2], s81 offen offset:0 lds\n" \
    "v_mfma_f32_16x16x32_bf16 v[84:87], a[76:79], a[56:59], v[84:87]\nv_mfma_f32_16x16x32_bf16 v[88:91], a[72:75], a[60:63], v[88:91]\nds_read_b64_tr_b16 a[144:145], v232 offset:16384\n" \
    "v_mfma_f32_16x16x32_bf16 v[92:95], a[76:79], a[60:63], v[92:95]\nds_read_b64_tr_b16 a[146:147], v232 offset:18432\nv_mfma_f32_16x16x32_bf16 v[96:99], a[80:83], a[32:35], v[96:99]\n" \
    "v_mfma_f32_16x16x32_bf16 v[100:103], a[84:87], a[32:35], v[100:103]\nds_read_b64_tr_b16 a[148:149], v233 offset:16384\nv_mfma_f32_16x16x32_bf16 v[104:107], a[80:83], a[36:39], v[104:107]\n" \
    "v_mfma_f32_16x16x32_bf16 v[108:111], a[84:87], a[36:39], v[108:111]\nds_read_b64_tr_b16 a[150:151], v233 offset:18432\nv_mfma_f32_16x16x32_bf16 v[112:115], a[80:83], a[40:43], v[112:115]\n" \
    "buffer_load_dwordx4 v241, %[srdW2], s81 offen offset:1024 lds\nv_mfma_f32_16x16x32_bf16 v[116:119], a[84:87], a[40:43], v[116:119]\nds_read_b64_tr_b16 a[152:153], v234 offset:16384\n" \
    "v_mfma_f32_16x16x32_bf16 v[120:123], a[80:83], a[44:47], v[120:123]\nv_mfma_f32_16x16x32_bf16 v[124:127], a[84:87], a[44:47], v[124:127]\nds_read_b64_tr_b16 a[154:155], v234 offset:18432\n" \
    "v_mfma_f32_16x16x32_bf16 v[128:131], a[80:83], a[48:51], v[128:131]\nv_mfma_f32_16x16x32_bf16 v[132:135], a[84:87], a[48:51], v[132:135]\nds_read_b64_tr_b16 a[156:157], v235 offset:16384\n" \
    "v_mfma_f32_16x16x32_bf16 v[136:139], a[80:83], a[52:55], v[136:139]\nv_mfma_f32_16x16x32_bf16 v[140:143], a[84:87], a[52:55], v[140:143]\nds_read_b64_tr_b16 a[158:159], v235 offset:18432\n" \
    "v_mfma_f32_16x16x32_bf16 v[144:147], a[80:83], a[56:59], v[144:147]\nbuffer_load_dwordx4 v242, %[srdW2], s81 offen offset:2048 lds\nv_mfma_f32_16x16x32_bf16 v[148:151], a[84:87], a[56:59], v[148:151]\n" \
    "v_mfma_f32_16x16x32_bf16 v[152:155], a[80:83], a[60:63], v[152:155]\nv_mfma_f32_16x16x32_bf16 v[156:159], a[84:87], a[60:63], v[156:159]\nv_mfma_f32_16x16x32_bf16 v[160:163], a[88:91], a[32:35], v[160:163]\n" \
    "v_mfma_f32_16x16x32_bf16 v[164:167], a[92:95], a[32:35], v[164:167]\nv_mfma_f32_16x16x32_bf16 v[168:171], a[88:91], a[36:39], v[168:171]\nv_mfma_f32_16x16x32_bf16 v[172:175], a[92:95], a[36:39], v[172:175]\n" \
    "v_mfma_f32_16x16x32_bf16 v[176:179], a[88:91], a[40:43], v[176:179]\nv_mfma_f32_16x16x32_bf16 v[180:183], a[92:95], a[40:43], v[180:183]\nv_mfma_f32_16x16x32_bf16 v[184:187], a[88:91], a[44:47], v[184:187]\n" \
    "v_mfma_f32_16x16x32_bf16 v[188:191], a[92:95], a[44:47], v[188:191]\nv_mfma_f32_16x16x32_bf16 v[192:195], a[88:91], a[48:51], v[192:195]\nv_mfma_f32_16x16x32_bf16 v[196:199], a[92:95], a[48:51], v[196:199]\n" \
    "v_mfma_f32_16x16x32_bf16 v[200:203], a[88:91], a[52:55], v[200:203]\nv_mfma_f32_16x16x32_bf16 v[204:207], a[92:95], a[52:55], v[204:207]\nv_mfma_f32_16x16x32_bf16 v[208:211], a[88:91], a[56:59], v[208:211]\n" \
    "v_mfma_f32_16x16x32_bf16 v[212:215], a[92:95], a[56:59], v[212:215]\nv_mfma_f32_16x16x32_bf16 v[216:219], a[88:91], a[60:63], v[216:219]\nv_mfma_f32_16x16x32_bf16 v[220:223], a[92:95], a[60:63], v[220:223]\n" \
    "s_waitcnt vmcnt(6) lgkmcnt(0)\ns_barrier\nv_mfma_f32_16x16x32_bf16 a[0:3], a[128:131], a[96:99], a[0:3]\nv_accvgpr_read_b32 v236, a249\nv_accvgpr_read_b32 v237, a250\nv_accvgpr_read_b32 v238, a251\n" \
    "v_accvgpr_read_b32 v239, a252\ns_mov_b32 s83, %[sx2]\nv_mfma_f32_16x16x32_bf16 a[4:7], a[132:135], a[96:99], a[4:7]\ns_add_u32 m0, %[m0x20], 0x0\ns_add_u32 s80, s83, 0x0\n" \
    "buffer_load_dwordx4 v236, %[srdX2], s80 offen offset:0 sc1 lds\nv_mfma_f32_16x16x32_bf16 a[8:11], a[128:131], a[100:103], a[8:11]\nv_mfma_f32_16x16x32_bf16 a[12:15], a[132:135], a[100:103], a[12:15]\n" \
    "s_add_u32 %[sx2], %[sx2], 0x80\nv_mfma_f32_16x16x32_bf16 a[16:19], a[128:131], a[104:107], a[16:19]\nv_add_u32_e32 v228, 0x8000, v228\nds_read_b128 a[32:35], v224 offset:32768\n" \
    "v_mfma_f32_16x16x32_bf16 a[20:23], a[132:135], a[104:107], a[20:23]\nv_add_u32_e32 v229, 0x8000, v229\nbuffer_load_dwordx4 v237, %[srdX2], s80 offen offset:1024 sc1 lds\n" \
    "v_mfma_f32_16x16x32_bf16 a[24:27], a[128:131], a[108:111], a[24:27]\nv_add_u32_e32 v230, 0x8000, v230\nds_read_b128 a[36:39], v224 offset:34816\nv_mfma_f32_16x16x32_bf16 a[28:31], a[132:135], a[108:111], a[28:31]\n" \
    "v_add_u32_e32 v231, 0x8000, v231\nv_mfma_f32_16x16x32_bf16 v[0:3], a[128:131], a[112:115], v[0:3]\nv_add_u32_e32 v232, 0x8000, v232\nds_read_b128 a[40:43], v224 offset:36864\n" \
    "v_mfma_f32_16x16x32_bf16 v[4:7], a[132:135], a[112:115], v[4:7]\nv_add_u32_e32 v233, 0x8000, v233\nv_mfma_f32_16x16x32_bf16 v[8:11], a[128:131], a[116:119], v[8:11]\nv_add_u32_e32 v234, 0x8000, v234\n" \
    "ds_read_b128 a[44:47], v224 offset:38912\nbuffer_load_dwordx4 v238, %[srdX2], s80 offen offset:2048 sc1 lds\nv_mfma_f32_16x16x32_bf16 v[12:15], a[132:135], a[116:119], v[12:15]\nv_add_u32_e32 v235, 0x8000, v235\n" \
    "v_mfma_f32_16x16x32_bf16 v[16:19], a[128:131], a[120:123], v[16:19]\nds_read_b128 a[48:51], v224 offset:40960\nv_mfma_f32_16x16x32_bf16 v[20:23], a[132:135], a[120:123], v[20:23]\n" \
    "v_mfma_f32_16x16x32_bf16 v[24:27], a[128:131], a[124:127], v[24:27]\nds_read_b128 a[52:55], v224 offset:43008\nbuffer_load_dwordx4 v239, %[srdX2], s80 offen offset:3072 sc1 lds\n" \
    "v_mfma_f32_16x16x32_bf16 v[28:31], a[132:135], a[124:127], v[28:31]\nv_mfma_f32_16x16x32_bf16 v[32:35], a[136:139], a[96:99], v[32:35]\nds_read_b128 a[56:59], v224 offset:45056\n" \
    "v_mfma_f32_16x16x32_bf16 v[36:39], a[140:143], a[96:99], v[36:39]\nv_mfma_f32_16x16x32_bf16 v[40:43], a[136:139], a[100:103], v[40:43]\nds_read_b128 a[60:63], v224 offset:47104\n" \
    "v_mfma_f32_16x16x32_bf16 v[44:47], a[140:143], a[100:103], v[44:47]\ns_add_u32 m0, %[m0x20], 0x1000\ns_add_u32 s80, s83, 0xa000\nbuffer_load_dwordx4 v236, %[srdX2], s80 offen offset:0 sc1 lds\n" \
    "v_mfma_f32_16x16x32_bf16 v[48:51], a[136:139], a[104:107], v[48:51]\nds_read_b64_tr_b16 a[64:65], v228 offset:0\nv_mfma_f32_16x16x32_bf16 v[52:55], a[140:143], a[104:107], v[52:55]\n" \
    "ds_read_b64_tr_b16 a[66:67], v228 offset:2048\nv_mfma_f32_16x16x32_bf16 v[56:59], a[136:139], a[108:111], v[56:59]\nv_mfma_f32_16x16x32_bf16 v[60:63], a[140:143], a[108:111], v[60:63]\n" \
    "ds_read_b64_tr_b16 a[68:69], v229 offset:0\nbuffer_load_dwordx4 v237, %[srdX2], s80 offen offset:1024 sc1 lds\nv_mfma_f32_16x16x32_bf16 v[64:67], a[136:139], a[112:115], v[64:67]\n" \
    "ds_read_b64_tr_b16 a[70:71], v229 offset:2048\nv_mfma_f32_16x16x32_bf16 v[68:71], a[140:143], a[112:115], v[68:71]\nv_mfma_f32_16x16x32_bf16 v[72:75], a[136:139], a[116:119], v[72:75]\n" \
    "ds_read_b64_tr_b16 a[72:73], v230 offset:0\nv_mfma_f32_16x16x32_bf16 v[76:79], a[140:143], a[116:119], v[76:79]\nds_read_b64_tr_b16 a[74:75], v230 offset:2048\n" \
    "v_mfma_f32_16x16x32_bf16 v[80:83], a[136:139], a[120:123], v[80:83]\nbuffer_load_dwordx4 v238, %[srdX2], s80 offen offset:2048 sc1 lds\nv_mfma_f32_16x16x32_bf16 v[84:87], a[140:143], a[120:123], v[84:87]\n" \
    "ds_read_b64_tr_b16 a[76:77], v231 offset:0\nv_mfma_f32_16x16x32_bf16 v[88:91], a[136:139], a[124:127], v[88:91]\nds_read_b64_tr_b16 a[78:79], v231 offset:2048\n" \
    "v_mfma_f32_16x16x32_bf16 v[92:95], a[140:143], a[124:127], v[92:95]\nv_mfma_f32_16x16x32_bf16 v[96:99], a[144:147], a[96:99], v[96:99]\nds_read_b64_tr_b16 a[80:81], v232 offset:0\n" \
    "buffer_load_dwordx4 v239, %[srdX2], s80 offen offset:3072 sc1 lds\nv_mfma_f32_16x16x32_bf16 v[100:103], a[148:151], a[96:99], v[100:103]\nv_mfma_f32_16x16x32_bf16 v[104:107], a[144:147], a[100:103], v[104:107]\n" \
    "ds_read_b64_tr_b16 a[82:83], v232 offset:2048\nv_mfma_f32_16x16x32_bf16 v[108:111], a[148:151], a[100:103], v[108:111]\nds_read_b64_tr_b16 a[84:85], v233 offset:0\n" \
    "v_mfma_f32_16x16x32_bf16 v[112:115], a[144:147], a[104:107], v[112:115]\nv_mfma_f32_16x16x32_bf16 v[116:119], a[148:151], a[104:107], v[116:119]\nds_read_b64_tr_b16 a[86:87], v233 offset:2048\n" \
    "v_mfma_f32_16x16x32_bf16 v[120:123], a[144:147], a[108:111], v[120:123]\nds_read_b64_tr_b16 a[88:89], v234 offset:0\nv_mfma_f32_16x16x32_bf16 v[124:127], a[148:151], a[108:111], v[124:127]\n" \
    "v_mfma_f32_16x16x32_bf16 v[128:131], a[144:147], a[112:115], v[128:131]\nds_read_b64_tr_b16 a[90:91], v234 offset:2048\nv_mfma_f32_16x16x32_bf16 v[132:135], a[148:151], a[112:115], v[132:135]\n" \
    "ds_read_b64_tr_b16 a[92:93], v235 offset:0\nv_mfma_f32_16x16x32_bf16 v[136:139], a[144:147], a[116:119], v[136:139]\nv_mfma_f32_16x16x32_bf16 v[140:143], a[148:151], a[116:119], v[140:143]\n" \
    "ds_read_b64_tr_b16 a[94:95], v235 offset:2048\nv_mfma_f32_16x16x32_bf16 v[144:147], a[144:147], a[120:123], v[144:147]\nv_mfma_f32_16x16x32_bf16 v[148:151], a[148:151], a[120:123], v[148:151]\n" \
    "v_mfma_f32_16x16x32_bf16 v[152:155], a[144:147], a[124:127], v[152:155]\nv_mfma_f32_16x16x32_bf16 v[156:159], a[148:151], a[124:127], v[156:159]\nv_mfma_f32_16x16x32_bf16 v[160:163], a[152:155], a[96:99], v[160:163]\n" \
    "v_mfma_f32_16x16x32_bf16 v[164:167], a[156:159], a[96:99], v[164:167]\nv_mfma_f32_16x16x32_bf16 v[168:171], a[152:155], a[100:103], v[168:171]\nv_mfma_f32_16x16x32_bf16 v[172:175], a[156:159], a[100:103], v[172:175]\n" \
    "v_mfma_f32_16x16x32_bf16 v[176:179], a[152:155], a[104:107], v[176:179]\nv_mfma_f32_16x16x32_bf16 v[180:183], a[156:159], a[104:107], v[180:183]\nv_mfma_f32_16x16x32_bf16 v[184:187], a[152:155], a[108:111], v[184:187]\n" \
    "v_mfma_f32_16x16x32_bf16 v[188:191], a[156:159], a[108:111], v[188:191]\nv_mfma_f32_16x16x32_bf16 v[192:195], a[152:155], a[112:115], v[192:195]\nv_mfma_f32_16x16x32_bf16 v[196:199], a[156:159], a[112:115], v[196:199]\n" \
    "v_mfma_f32_16x16x32_bf16 v[200:203], a[152:155], a[116:119], v[200:203]\nv_mfma_f32_16x16x32_bf16 v[204:207], a[156:159], a[116:119], v[204:207]\nv_mfma_f32_16x16x32_bf16 v[208:211], a[152:155], a[120:123], v[208:211]\n" \
    "v_mfma_f32_16x16x32_bf16 v[212:215], a[156:159], a[120:123], v[212:215]\nv_mfma_f32_16x16x32_bf16 v[216:219], a[152:155], a[124:127], v[216:219]\nv_mfma_f32_16x16x32_bf16 v[220:223], a[156:159], a[124:127], v[220:223]\n" \
    "s_waitcnt lgkmcnt(0)\nv_mfma_f32_16x16x32_bf16 a[0:3], a[64:67], a[32:35], a[0:3]\nds_read_b128 a[96:99], v225 offset:32768\nv_mfma_f32_16x16x32_bf16 a[4:7], a[68:71], a[32:35], a[4:7]\n" \
    "ds_read_b128 a[100:103], v225 offset:34816\ns_mov_b32 s83, %[sw2]\nv_mfma_f32_16x16x32_bf16 a[8:11], a[64:67], a[36:39], a[8:11]\ns_add_u32 %[sw2], %[sw2], 0x80000\n" \
    "v_mfma_f32_16x16x32_bf16 a[12:15], a[68:71], a[36:39], a[12:15]\nds_read_b128 a[104:107], v225 offset:36864\ns_add_u32 m0, %[m0w2], 0x10000\ns_add_u32 s81, s83, 0x0\n" \
    "buffer_load_dwordx4 v240, %[srdW2], s81 offen offset:0 lds\nv_mfma_f32_16x16x32_bf16 a[16:19], a[64:67], a[40:43], a[16:19]\nv_mfma_f32_16x16x32_bf16 a[20:23], a[68:71], a[40:43], a[20:23]\n" \
    "ds_read_b128 a[108:111], v225 offset:38912\nv_mfma_f32_16x16x32_bf16 a[24:27], a[64:67], a[44:47], a[24:27]\nv_mfma_f32_16x16x32_bf16 a[28:31], a[68:71], a[44:47], a[28:31]\nds_read_b128 a[112:115], v225 offset:40960\n" \
    "v_mfma_f32_16x16x32_bf16 v[0:3], a[64:67], a[48:51], v[0:3]\nv_mfma_f32_16x16x32_bf16 v[4:7], a[68:71], a[48:51], v[4:7]\nds_read_b128 a[116:119], v225 offset:43008\n" \
    "v_mfma_f32_16x16x32_bf16 v[8:11], a[64:67], a[52:55], v[8:11]\nv_mfma_f32_16x16x32_bf16 v[12:15], a[68:71], a[52:55], v[12:15]\nds_read_b128 a[120:123], v225 offset:45056\n" \
    "buffer_load_dwordx4 v241, %[srdW2], s81 offen offset:1024 lds\nv_mfma_f32_16x16x32_bf16 v[16:19], a[64:67], a[56:59], v[16:19]\nv_mfma_f32_16x16x32_bf16 v[20:23], a[68:71], a[56:59], v[20:23]\n" \
    "ds_read_b128 a[124:127], v225 offset:47104\nv_mfma_f32_16x16x32_bf16 v[24:27], a[64:67], a[60:63], v[24:27]\nv_mfma_f32_16x16x32_bf16 v[28:31], a[68:71], a[60:63], v[28:31]\n" \
    "ds_read_b64_tr_b16 a[128:129], v228 offset:16384\nv_mfma_f32_16x16x32_bf16 v[32:35], a[72:75], a[32:35], v[32:35]\nds_read_b64_tr_b16 a[130:131], v228 offset:18432\n" \
    "v_mfma_f32_16x16x32_bf16 v[36:39], a[76:79], a[32:35], v[36:39]\nv_mfma_f32_16x16x32_bf16 v[40:43], a[72:75], a[36:39], v[40:43]\nds_read_b64_tr_b16 a[132:133], v229 offset:16384\n" \
    "v_mfma_f32_16x16x32_bf16 v[44:47], a[76:79], a[36:39], v[44:47]\nbuffer_load_dwordx4 v242, %[srdW2], s81 offen offset:2048 lds\nv_mfma_f32_16x16x32_bf16 v[48:51], a[72:75], a[40:43], v[48:51]\n" \
    "ds_read_b64_tr_b16 a[134:135], v229 offset:18432\nv_mfma_f32_16x16x32_bf16 v[52:55], a[76:79], a[40:43], v[52:55]\nv_mfma_f32_16x16x32_bf16 v[56:59], a[72:75], a[44:47], v[56:59]\n" \
    "ds_read_b64_tr_b16 a[136:137], v230 offset:16384\nv_mfma_f32_16x16x32_bf16 v[60:63], a[76:79], a[44:47], v[60:63]\nv_mfma_f32_16x16x32_bf16 v[64:67], a[72:75], a[48:51], v[64:67]\n" \
    "ds_read_b64_tr_b16 a[138:139], v230 offset:18432\nv_mfma_f32_16x16x32_bf16 v[68:71], a[76:79], a[48:51], v[68:71]\nv_mfma_f32_16x16x32_bf16 v[72:75], a[72:75], a[52:55], v[72:75]\n" \
    "ds_read_b64_tr_b16 a[140:141], v231 offset:16384\nv_mfma_f32_16x16x32_bf16 v[76:79], a[76:79], a[52:55], v[76:79]\nv_mfma_f32_16x16x32_bf16 v[80:83], a[72:75], a[56:59], v[80:83]\n" \
    "ds_read_b64_tr_b16 a[142:143], v231 offset:18432\ns_add_u32 m0, %[m0w2], 0x11800\ns_add_u32 s81, s83, 0x20000\nbuffer_load_dwordx4 v240, %[srdW2], s81 offen offset:0 lds\n" \
    "v_mfma_f32_16x16x32_bf16 v[84:87], a[76:79], a[56:59], v[84:87]\nv_mfma_f32_16x16x32_bf16 v[88:91], a[72:75], a[60:63], v[88:91]\nds_read_b64_tr_b16 a[144:145], v232 offset:16384\n" \
    "v_mfma_f32_16x16x32_bf16 v[92:95], a[76:79], a[60:63], v[92:95]\nds_read_b64_tr_b16 a[146:147], v232 offset:18432\nv_mfma_f32_16x16x32_bf16 v[96:99], a[80:83], a[32:35], v[96:99]\n" \
    "v_mfma_f32_16x16x32_bf16 v[100:103], a[84:87], a[32:35], v[100:103]\nds_read_b64_tr_b16 a[148:149], v233 offset:16384\nv_mfma_f32_16x16x32_bf16 v[104:107], a[80:83], a[36:39], v[104:107]\n" \
    "v_mfma_f32_16x16x32_bf16 v[108:111], a[84:87], a[36:39], v[108:111]\nds_read_b64_tr_b16 a[150:151], v233 offset:18432\nv_mfma_f32_16x16x32_bf16 v[112:115], a[80:83], a[40:43], v[112:115]\n" \
    "buffer_load_dwordx4 v241, %[srdW2], s81 offen offset:1024 lds\nv_mfma_f32_16x16x32_bf16 v[116:119], a[84:87], a[40:43], v[116:119]\nds_read_b64_tr_b16 a[152:153], v234 offset:16384\n" \
    "v_mfma_f32_16x16x32_bf16 v[120:123], a[80:83], a[44:47], v[120:123]\nv_mfma_f32_16x16x32_bf16 v[124:127], a[84:87], a[44:47], v[124:127]\nds_read_b64_tr_b16 a[154:155], v234 offset:18432\n" \
    "v_mfma_f32_16x16x32_bf16 v[128:131], a[80:83], a[48:51], v[128:131]\nv_mfma_f32_16x16x32_bf16 v[132:135], a[84:87], a[48:51], v[132:135]\nds_read_b64_tr_b16 a[156:157], v235 offset:16384\n" \
    "v_mfma_f32_16x16x32_bf16 v[136:139], a[80:83], a[52:55], v[136:139]\nv_mfma_f32_16x16x32_bf16 v[140:143], a[84:87], a[52:55], v[140:143]\nds_read_b64_tr_b16 a[158:159], v235 offset:18432\n" \
    "v_mfma_f32_16x16x32_bf16 v[144:147], a[80:83], a[56:59], v[144:147]\nbuffer_load_dwordx4 v242, %[srdW2], s81 offen offset:2048 lds\nv_mfma_f32_16x16x32_bf16 v[148:151], a[84:87], a[56:59], v[148:151]\n" \
    "v_mfma_f32_16x16x32_bf16 v[152:155], a[80:83], a[60:63], v[152:155]\nv_mfma_f32_16x16x32_bf16 v[156:159], a[84:87], a[60:63], v[156:159]\nv_mfma_f32_16x16x32_bf16 v[160:163], a[88:91], a[32:35], v[160:163]\n" \
    "v_mfma_f32_16x16x32_bf16 v[164:167], a[92:95], a[32:35], v[164:167]\nv_mfma_f32_16x16x32_bf16 v[168:171], a[88:91], a[36:39], v[168:171]\nv_mfma_f32_16x16x32_bf16 v[172:175], a[92:95], a[36:39], v[172:175]\n" \
    "v_mfma_f32_16x16x32_bf16 v[176:179], a[88:91], a[40:43], v[176:179]\nv_mfma_f32_16x16x32_bf16 v[180:183], a[92:95], a[40:43], v[180:183]\nv_mfma_f32_16x16x32_bf16 v[184:187], a[88:91], a[44:47], v[184:187]\n" \
    "v_mfma_f32_16x16x32_bf16 v[188:191], a[92:95], a[44:47], v[188:191]\nv_mfma_f32_16x16x32_bf16 v[192:195], a[88:91], a[48:51], v[192:195]\nv_mfma_f32_16x16x32_bf16 v[196:199], a[92:95], a[48:51], v[196:199]\n" \
    "v_mfma_f32_16x16x32_bf16 v[200:203], a[88:91], a[52:55], v[200:203]\nv_mfma_f32_16x16x32_bf16 v[204:207], a[92:95], a[52:55], v[204:207]\nv_mfma_f32_16x16x32_bf16 v[208:211], a[88:91], a[56:59], v[208:211]\n" \
    "v_mfma_f32_16x16x32_bf16 v[212:215], a[92:95], a[56:59], v[212:215]\nv_mfma_f32_16x16x32_bf16 v[216:219], a[88:91], a[60:63], v[216:219]\nv_mfma_f32_16x16x32_bf16 v[220:223], a[92:95], a[60:63], v[220:223]\n" \
    "s_waitcnt vmcnt(6) lgkmcnt(0)\ns_barrier\nv_mfma_f32_16x16x32_bf16 a[0:3], a[128:131], a[96:99], a[0:3]\ns_mov_b32 s83, %[sx2]\nv_accvgpr_read_b32 v224, a239\nv_accvgpr_read_b32 v225, a240\n" \
    "v_mfma_f32_16x16x32_bf16 a[4:7], a[132:135], a[96:99], a[4:7]\nv_accvgpr_read_b32 v228, a243\nv_accvgpr_read_b32 v229, a244\ns_add_u32 m0, %[m0x20], 0x8000\ns_add_u32 s80, s83, 0x0\n" \
    "buffer_load_dwordx4 v236, %[srdX2], s80 offen offset:0 sc1 lds\nv_mfma_f32_16x16x32_bf16 a[8:11], a[128:131], a[100:103], a[8:11]\nv_accvgpr_read_b32 v230, a245\nv_accvgpr_read_b32 v231, a246\n" \
    "v_mfma_f32_16x16x32_bf16 a[12:15], a[132:135], a[100:103], a[12:15]\ns_add_u32 %[sx2], %[sx2], 0x80\nv_accvgpr_read_b32 v232, a247\nv_accvgpr_read_b32 v233, a248\n" \
    "v_mfma_f32_16x16x32_bf16 a[16:19], a[128:131], a[104:107], a[16:19]\nds_read_b128 a[32:35], v224 offset:0\nv_mfma_f32_16x16x32_bf16 a[20:23], a[132:135], a[104:107], a[20:23]\n" \
    "buffer_load_dwordx4 v237, %[srdX2], s80 offen offset:1024 sc1 lds\nv_mfma_f32_16x16x32_bf16 a[24:27], a[128:131], a[108:111], a[24:27]\nds_read_b128 a[36:39], v224 offset:2048\n" \
    "v_mfma_f32_16x16x32_bf16 a[28:31], a[132:135], a[108:111], a[28:31]\nv_mfma_f32_16x16x32_bf16 v[0:3], a[128:131], a[112:115], v[0:3]\nds_read_b128 a[40:43], v224 offset:4096\n" \
    "v_mfma_f32_16x16x32_bf16 v[4:7], a[132:135], a[112:115], v[4:7]\nv_mfma_f32_16x16x32_bf16 v[8:11], a[128:131], a[116:119], v[8:11]\nds_read_b128 a[44:47], v224 offset:6144\n" \
    "buffer_load_dwordx4 v238, %[srdX2], s80 offen offset:2048 sc1 lds\nv_mfma_f32_16x16x32_bf16 v[12:15], a[132:135], a[116:119], v[12:15]\nv_mfma_f32_16x16x32_bf16 v[16:19], a[128:131], a[120:123], v[16:19]\n" \
    "ds_read_b128 a[48:51], v224 offset:8192\nv_mfma_f32_16x16x32_bf16 v[20:23], a[132:135], a[120:123], v[20:23]\nv_mfma_f32_16x16x32_bf16 v[24:27], a[128:131], a[124:127], v[24:27]\n" \
    "ds_read_b128 a[52:55], v224 offset:10240\nbuffer_load_dwordx4 v239, %[srdX2], s80 offen offset:3072 sc1 lds\nv_mfma_f32_16x16x32_bf16 v[28:31], a[132:135], a[124:127], v[28:31]\nv_cvt_pk_bf16_f32 v0, v0, v1\n" \
    "v_cvt_pk_bf16_f32 v1, v2, v3\nv_cvt_pk_bf16_f32 v2, v4, v5\nv_mfma_f32_16x16x32_bf16 v[32:35], a[136:139], a[96:99], v[32:35]\nds_read_b128 a[56:59], v224 offset:12288\nv_cvt_pk_bf16_f32 v3, v6, v7\n" \
    "v_mfma_f32_16x16x32_bf16 v[36:39], a[140:143], a[96:99], v[36:39]\nv_cvt_pk_bf16_f32 v8, v8, v9\nv_cvt_pk_bf16_f32 v9, v10, v11\nv_cvt_pk_bf16_f32 v10, v12, v13\n" \
    "v_mfma_f32_16x16x32_bf16 v[40:43], a[136:139], a[100:103], v[40:43]\nds_read_b128 a[60:63], v224 offset:14336\nv_cvt_pk_bf16_f32 v11, v14, v15\nv_mfma_f32_16x16x32_bf16 v[44:47], a[140:143], a[100:103], v[44:47]\n" \
    "s_add_u32 m0, %[m0x20], 0x9000\ns_add_u32 s80, s83, 0xa000\nbuffer_load_dwordx4 v236, %[srdX2], s80 offen offset:0 sc1 lds\nv_cvt_pk_bf16_f32 v16, v16, v17\nv_cvt_pk_bf16_f32 v17, v18, v19\n" \
    "v_mfma_f32_16x16x32_bf16 v[48:51], a[136:139], a[104:107], v[48:51]\nds_read_b64_tr_b16 a[64:65], v228 offset:0\nv_cvt_pk_bf16_f32 v18, v20, v21\nv_cvt_pk_bf16_f32 v19, v22, v23\n" \
    "v_mfma_f32_16x16x32_bf16 v[52:55], a[140:143], a[104:107], v[52:55]\nv_cvt_pk_bf16_f32 v24, v24, v25\nv_cvt_pk_bf16_f32 v25, v26, v27\nv_cvt_pk_bf16_f32 v26, v28, v29\n" \
    "v_mfma_f32_16x16x32_bf16 v[56:59], a[136:139], a[108:111], v[56:59]\nds_read_b64_tr_b16 a[66:67], v228 offset:1536\nv_cvt_pk_bf16_f32 v27, v30, v31\nv_mfma_f32_16x16x32_bf16 v[60:63], a[140:143], a[108:111], v[60:63]\n" \
    "buffer_load_dwordx4 v237, %[srdX2], s80 offen offset:1024 sc1 lds\nv_cvt_pk_bf16_f32 v32, v32, v33\nv_cvt_pk_bf16_f32 v33, v34, v35\nv_mfma_f32_16x16x32_bf16 v[64:67], a[136:139], a[112:115], v[64:67]\n" \
    "ds_read_b64_tr_b16 a[68:69], v229 offset:0\nv_cvt_pk_bf16_f32 v34, v36, v37\nv_cvt_pk_bf16_f32 v35, v38, v39\nv_mfma_f32_16x16x32_bf16 v[68:71], a[140:143], a[112:115], v[68:71]\nv_cvt_pk_bf16_f32 v40, v40, v41\n" \
    "v_cvt_pk_bf16_f32 v41, v42, v43\nv_cvt_pk_bf16_f32 v42, v44, v45\nv_mfma_f32_16x16x32_bf16 v[72:75], a[136:139], a[116:119], v[72:75]\nds_read_b64_tr_b16 a[70:71], v229 offset:1536\nv_cvt_pk_bf16_f32 v43, v46, v47\n" \
    "v_mfma_f32_16x16x32_bf16 v[76:79], a[140:143], a[116:119], v[76:79]\nv_cvt_pk_bf16_f32 v48, v48, v49\nv_cvt_pk_bf16_f32 v49, v50, v51\nv_cvt_pk_bf16_f32 v50, v52, v53\n" \
    "v_mfma_f32_16x16x32_bf16 v[80:83], a[136:139], a[120:123], v[80:83]\nds_read_b64_tr_b16 a[72:73], v230 offset:0\nbuffer_load_dwordx4 v238, %[srdX2], s80 offen offset:2048 sc1 lds\nv_cvt_pk_bf16_f32 v51, v54, v55\n" \
    "v_mfma_f32_16x16x32_bf16 v[84:87], a[140:143], a[120:123], v[84:87]\nv_cvt_pk_bf16_f32 v56, v56, v57\nv_cvt_pk_bf16_f32 v57, v58, v59\nv_cvt_pk_bf16_f32 v58, v60, v61\n" \
    "v_mfma_f32_16x16x32_bf16 v[88:91], a[136:139], a[124:127], v[88:91]\nds_read_b64_tr_b16 a[74:75], v230 offset:1536\nv_cvt_pk_bf16_f32 v59, v62, v63\nv_mfma_f32_16x16x32_bf16 v[92:95], a[140:143], a[124:127], v[92:95]\n" \
    "v_cvt_pk_bf16_f32 v64, v64, v65\nv_cvt_pk_bf16_f32 v65, v66, v67\nv_cvt_pk_bf16_f32 v66, v68, v69\nv_mfma_f32_16x16x32_bf16 v[96:99], a[144:147], a[96:99], v[96:99]\nds_read_b64_tr_b16 a[76:77], v231 offset:0\n" \
    "buffer_load_dwordx4 v239, %[srdX2], s80 offen offset:3072 sc1 lds\nv_cvt_pk_bf16_f32 v67, v70, v71\nv_mfma_f32_16x16x32_bf16 v[100:103], a[148:151], a[96:99], v[100:103]\nv_cvt_pk_bf16_f32 v72, v72, v73\n" \
    "v_cvt_pk_bf16_f32 v73, v74, v75\nv_cvt_pk_bf16_f32 v74, v76, v77\nv_mfma_f32_16x16x32_bf16 v[104:107], a[144:147], a[100:103], v[104:107]\nds_read_b64_tr_b16 a[78:79], v231 offset:1536\nv_cvt_pk_bf16_f32 v75, v78, v79\n" \
    "v_mfma_f32_16x16x32_bf16 v[108:111], a[148:151], a[100:103], v[108:111]\nv_cvt_pk_bf16_f32 v80, v80, v81\nv_cvt_pk_bf16_f32 v81, v82, v83\nv_cvt_pk_bf16_f32 v82, v84, v85\n" \
    "v_mfma_f32_16x16x32_bf16 v[112:115], a[144:147], a[104:107], v[112:115]\nds_read_b64_tr_b16 a[80:81], v232 offset:0\nv_cvt_pk_bf16_f32 v83, v86, v87\n" \
    "v_mfma_f32_16x16x32_bf16 v[116:119], a[148:151], a[104:107], v[116:119]\nv_cvt_pk_bf16_f32 v88, v88, v89\nv_cvt_pk_bf16_f32 v89, v90, v91\nv_cvt_pk_bf16_f32 v90, v92, v93\n" \
    "v_mfma_f32_16x16x32_bf16 v[120:123], a[144:147], a[108:111], v[120:123]\nds_read_b64_tr_b16 a[82:83], v232 offset:1536\nv_cvt_pk_bf16_f32 v91, v94, v95\n" \
    "v_mfma_f32_16x16x32_bf16 v[124:127], a[148:151], a[108:111], v[124:127]\nv_cvt_pk_bf16_f32 v96, v96, v97\nv_cvt_pk_bf16_f32 v97, v98, v99\nv_cvt_pk_bf16_f32 v98, v100, v101\n" \
    "v_mfma_f32_16x16x32_bf16 v[128:131], a[144:147], a[112:115], v[128:131]\nds_read_b64_tr_b16 a[84:85], v233 offset:0\nv_cvt_pk_bf16_f32 v99, v102, v103\n" \
    "v_mfma_f32_16x16x32_bf16 v[132:135], a[148:151], a[112:115], v[132:135]\nv_cvt_pk_bf16_f32 v104, v104, v105\nv_cvt_pk_bf16_f32 v105, v106, v107\nv_cvt_pk_bf16_f32 v106, v108, v109\n" \
    "v_mfma_f32_16x16x32_bf16 v[136:139], a[144:147], a[116:119], v[136:139]\nds_read_b64_tr_b16 a[86:87], v233 offset:1536\nv_cvt_pk_bf16_f32 v107, v110, v111\n" \
    "v_mfma_f32_16x16x32_bf16 v[140:143], a[148:151], a[116:119], v[140:143]\nv_cvt_pk_bf16_f32 v112, v112, v113\nv_cvt_pk_bf16_f32 v113, v114, v115\nv_cvt_pk_bf16_f32 v114, v116, v117\n" \
    "v_mfma_f32_16x16x32_bf16 v[144:147], a[144:147], a[120:123], v[144:147]\nv_cvt_pk_bf16_f32 v115, v118, v119\nv_mfma_f32_16x16x32_bf16 v[148:151], a[148:151], a[120:123], v[148:151]\nv_cvt_pk_bf16_f32 v120, v120, v121\n" \
    "v_cvt_pk_bf16_f32 v121, v122, v123\nv_cvt_pk_bf16_f32 v122, v124, v125\nv_mfma_f32_16x16x32_bf16 v[152:155], a[144:147], a[124:127], v[152:155]\nv_cvt_pk_bf16_f32 v123, v126, v127\n" \
    "v_mfma_f32_16x16x32_bf16 v[156:159], a[148:151], a[124:127], v[156:159]\nv_cvt_pk_bf16_f32 v128, v128, v129\nv_cvt_pk_bf16_f32 v129, v130, v131\nv_cvt_pk_bf16_f32 v130, v132, v133\n" \
    "v_mfma_f32_16x16x32_bf16 v[160:163], a[152:155], a[96:99], v[160:163]\nv_cvt_pk_bf16_f32 v131, v134, v135\nv_mfma_f32_16x16x32_bf16 v[164:167], a[156:159], a[96:99], v[164:167]\nv_cvt_pk_bf16_f32 v136, v136, v137\n" \
    "v_cvt_pk_bf16_f32 v137, v138, v139\nv_cvt_pk_bf16_f32 v138, v140, v141\nv_mfma_f32_16x16x32_bf16 v[168:171], a[152:155], a[100:103], v[168:171]\nv_cvt_pk_bf16_f32 v139, v142, v143\n" \
    "v_mfma_f32_16x16x32_bf16 v[172:175], a[156:159], a[100:103], v[172:175]\nv_cvt_pk_bf16_f32 v144, v144, v145\nv_cvt_pk_bf16_f32 v145, v146, v147\nv_cvt_pk_bf16_f32 v146, v148, v149\n" \
    "v_mfma_f32_16x16x32_bf16 v[176:179], a[152:155], a[104:107], v[176:179]\nv_cvt_pk_bf16_f32 v147, v150, v151\nv_mfma_f32_16x16x32_bf16 v[180:183], a[156:159], a[104:107], v[180:183]\nv_cvt_pk_bf16_f32 v152, v152, v153\n" \
    "v_cvt_pk_bf16_f32 v153, v154, v155\nv_cvt_pk_bf16_f32 v154, v156, v157\nv_mfma_f32_16x16x32_bf16 v[184:187], a[152:155], a[108:111], v[184:187]\nv_cvt_pk_bf16_f32 v155, v158, v159\n" \
    "v_mfma_f32_16x16x32_bf16 v[188:191], a[156:159], a[108:111], v[188:191]\nv_cvt_pk_bf16_f32 v160, v160, v161\nv_cvt_pk_bf16_f32 v161, v162, v163\nv_cvt_pk_bf16_f32 v162, v164, v165\n" \
    "v_mfma_f32_16x16x32_bf16 v[192:195], a[152:155], a[112:115], v[192:195]\nv_cvt_pk_bf16_f32 v163, v166, v167\nv_mfma_f32_16x16x32_bf16 v[196:199], a[156:159], a[112:115], v[196:199]\nv_cvt_pk_bf16_f32 v168, v168, v169\n" \
    "v_cvt_pk_bf16_f32 v169, v170, v171\nv_cvt_pk_bf16_f32 v170, v172, v173\nv_mfma_f32_16x16x32_bf16 v[200:203], a[152:155], a[116:119], v[200:203]\nv_cvt_pk_bf16_f32 v171, v174, v175\n" \
    "v_mfma_f32_16x16x32_bf16 v[204:207], a[156:159], a[116:119], v[204:207]\nv_cvt_pk_bf16_f32 v176, v176, v177\nv_cvt_pk_bf16_f32 v177, v178, v179\nv_cvt_pk_bf16_f32 v178, v180, v181\n" \
    "v_mfma_f32_16x16x32_bf16 v[208:211], a[152:155], a[120:123], v[208:211]\nv_cvt_pk_bf16_f32 v179, v182, v183\nv_mfma_f32_16x16x32_bf16 v[212:215], a[156:159], a[120:123], v[212:215]\nv_cvt_pk_bf16_f32 v184, v184, v185\n" \
    "v_cvt_pk_bf16_f32 v185, v186, v187\nv_cvt_pk_bf16_f32 v186, v188, v189\nv_mfma_f32_16x16x32_bf16 v[216:219], a[152:155], a[124:127], v[216:219]\nv_cvt_pk_bf16_f32 v187, v190, v191\n" \
    "v_mfma_f32_16x16x32_bf16 v[220:223], a[156:159], a[124:127], v[220:223]\nv_cvt_pk_bf16_f32 v192, v192, v193\nv_cvt_pk_bf16_f32 v193, v194, v195\nv_cvt_pk_bf16_f32 v194, v196, v197\ns_waitcnt lgkmcnt(0)\n" \
    "v_mfma_f32_16x16x32_bf16 v[4:7], a[64:67], a[32:35], 0\nds_read_b128 a[96:99], v225 offset:0\nv_cvt_pk_bf16_f32 v195, v198, v199\nv_mfma_f32_16x16x32_bf16 a[160:163], a[68:71], a[32:35], 0\n" \
    "ds_read_b128 a[100:103], v225 offset:2048\ns_mov_b32 s83, %[sw2]\nv_cvt_pk_bf16_f32 v200, v200, v201\nv_cvt_pk_bf16_f32 v201, v202, v203\nv_mfma_f32_16x16x32_bf16 v[12:15], a[64:67], a[36:39], 0\n" \
    "s_add_u32 %[sw2], %[sw2], 0x80000\nv_cvt_pk_bf16_f32 v202, v204, v205\nv_cvt_pk_bf16_f32 v203, v206, v207\nv_mfma_f32_16x16x32_bf16 a[164:167], a[68:71], a[36:39], 0\nds_read_b128 a[104:107], v225 offset:4096\n" \
    "s_add_u32 m0, %[m0w2], 0x18000\ns_add_u32 s81, s83, 0x0\nbuffer_load_dwordx4 v240, %[srdW2], s81 offen offset:0 lds\nv_cvt_pk_bf16_f32 v208, v208, v209\nv_cvt_pk_bf16_f32 v209, v210, v211\n" \
    "v_mfma_f32_16x16x32_bf16 v[20:23], a[64:67], a[40:43], 0\nv_cvt_pk_bf16_f32 v210, v212, v213\nv_cvt_pk_bf16_f32 v211, v214, v215\nv_mfma_f32_16x16x32_bf16 a[168:171], a[68:71], a[40:43], 0\n" \
    "ds_read_b128 a[108:111], v225 offset:6144\nv_cvt_pk_bf16_f32 v216, v216, v217\nv_cvt_pk_bf16_f32 v217, v218, v219\nv_mfma_f32_16x16x32_bf16 v[28:31], a[64:67], a[44:47], 0\nds_read_b128 a[112:115], v225 offset:8192\n" \
    "v_cvt_pk_bf16_f32 v218, v220, v221\nv_cvt_pk_bf16_f32 v219, v222, v223\nv_mfma_f32_16x16x32_bf16 a[172:175], a[68:71], a[44:47], 0\nv_mfma_f32_16x16x32_bf16 v[36:39], a[64:67], a[48:51], 0\n" \
    "ds_read_b128 a[116:119], v225 offset:10240\nv_mfma_f32_16x16x32_bf16 a[176:179], a[68:71], a[48:51], 0\nbuffer_load_dwordx4 v241, %[srdW2], s81 offen offset:1024 lds\n" \
    "v_mfma_f32_16x16x32_bf16 v[44:47], a[64:67], a[52:55], 0\nds_read_b128 a[120:123], v225 offset:12288\nv_mfma_f32_16x16x32_bf16 a[180:183], a[68:71], a[52:55], 0\nds_read_b128 a[124:127], v225 offset:14336\n" \
    "v_mfma_f32_16x16x32_bf16 v[52:55], a[64:67], a[56:59], 0\nv_mfma_f32_16x16x32_bf16 a[184:187], a[68:71], a[56:59], 0\nds_read_b64_tr_b16 a[128:129], v228 offset:12288\n" \
    "v_mfma_f32_16x16x32_bf16 v[60:63], a[64:67], a[60:63], 0\nv_mfma_f32_16x16x32_bf16 a[188:191], a[68:71], a[60:63], 0\nds_read_b64_tr_b16 a[130:131], v228 offset:13824\n" \
    "buffer_load_dwordx4 v242, %[srdW2], s81 offen offset:2048 lds\nv_mfma_f32_16x16x32_bf16 v[68:71], a[72:75], a[32:35], 0\nv_mfma_f32_16x16x32_bf16 a[192:195], a[76:79], a[32:35], 0\n" \
    "ds_read_b64_tr_b16 a[132:133], v229 offset:12288\nv_mfma_f32_16x16x32_bf16 v[76:79], a[72:75], a[36:39], 0\nds_read_b64_tr_b16 a[134:135], v229 offset:13824\nv_mfma_f32_16x16x32_bf16 a[196:199], a[76:79], a[36:39], 0\n" \
    "v_mfma_f32_16x16x32_bf16 v[84:87], a[72:75], a[40:43], 0\nds_read_b64_tr_b16 a[136:137], v230 offset:12288\nv_mfma_f32_16x16x32_bf16 a[200:203], a[76:79], a[40:43], 0\ns_add_u32 m0, %[m0w2], 0x19800\n" \
    "s_add_u32 s81, s83, 0x20000\nbuffer_load_dwordx4 v240, %[srdW2], s81 offen offset:0 lds\nv_mfma_f32_16x16x32_bf16 v[92:95], a[72:75], a[44:47], 0\nds_read_b64_tr_b16 a[138:139], v230 offset:13824\n" \
    "v_mfma_f32_16x16x32_bf16 a[204:207], a[76:79], a[44:47], 0\nds_read_b64_tr_b16 a[140:141], v231 offset:12288\nv_mfma_f32_16x16x32_bf16 v[100:103], a[72:75], a[48:51], 0\n" \
    "v_mfma_f32_16x16x32_bf16 a[208:211], a[76:79], a[48:51], 0\nds_read_b64_tr_b16 a[142:143], v231 offset:13824\nv_mfma_f32_16x16x32_bf16 v[108:111], a[72:75], a[52:55], 0\n" \
    "v_mfma_f32_16x16x32_bf16 a[212:215], a[76:79], a[52:55], 0\nds_read_b64_tr_b16 a[144:145], v232 offset:12288\nbuffer_load_dwordx4 v241, %[srdW2], s81 offen offset:1024 lds\n" \
    "v_mfma_f32_16x16x32_bf16 v[116:119], a[72:75], a[56:59], 0\nds_read_b64_tr_b16 a[146:147], v232 offset:13824\nv_mfma_f32_16x16x32_bf16 a[216:219], a[76:79], a[56:59], 0\n" \
    "v_mfma_f32_16x16x32_bf16 v[124:127], a[72:75], a[60:63], 0\nds_read_b64_tr_b16 a[148:149], v233 offset:12288\nv_mfma_f32_16x16x32_bf16 a[220:223], a[76:79], a[60:63], 0\n" \
    "v_mfma_f32_16x16x32_bf16 v[132:135], a[80:83], a[32:35], 0\nds_read_b64_tr_b16 a[150:151], v233 offset:13824\nv_mfma_f32_16x16x32_bf16 a[224:227], a[84:87], a[32:35], 0\n" \
    "buffer_load_dwordx4 v242, %[srdW2], s81 offen offset:2048 lds\nv_mfma_f32_16x16x32_bf16 v[140:143], a[80:83], a[36:39], 0\nv_mfma_f32_16x16x32_bf16 a[228:231], a[84:87], a[36:39], 0\n" \
    "v_mfma_f32_16x16x32_bf16 v[148:151], a[80:83], a[40:43], 0\nv_mfma_f32_16x16x32_bf16 a[232:235], a[84:87], a[40:43], 0\nv_mfma_f32_16x16x32_bf16 v[156:159], a[80:83], a[44:47], 0\n" \
    "v_mfma_f32_16x16x32_bf16 a[236:239], a[84:87], a[44:47], 0\nv_mfma_f32_16x16x32_bf16 v[164:167], a[80:83], a[48:51], 0\nv_mfma_f32_16x16x32_bf16 a[240:243], a[84:87], a[48:51], 0\n" \
    "v_mfma_f32_16x16x32_bf16 v[172:175], a[80:83], a[52:55], 0\nv_mfma_f32_16x16x32_bf16 a[244:247], a[84:87], a[52:55], 0\nv_mfma_f32_16x16x32_bf16 v[180:183], a[80:83], a[56:59], 0\n" \
    "v_mfma_f32_16x16x32_bf16 a[248:251], a[84:87], a[56:59], 0\nv_mfma_f32_16x16x32_bf16 v[188:191], a[80:83], a[60:63], 0\nv_mfma_f32_16x16x32_bf16 a[252:255], a[84:87], a[60:63], 0\ns_waitcnt vmcnt(6) lgkmcnt(0)\n" \
    "s_barrier\nv_mfma_f32_16x16x32_bf16 v[4:7], a[128:131], a[96:99], v[4:7]\ns_mov_b32 s83, %[sx2]\nv_mfma_f32_16x16x32_bf16 a[160:163], a[132:135], a[96:99], a[160:163]\ns_add_u32 m0, %[m0x20], 0x0\n" \
    "s_add_u32 s80, s83, 0x0\nbuffer_load_dwordx4 v236, %[srdX2], s80 offen offset:0 sc1 lds\nv_permlane16_swap_b32_e32 v0, v2\nv_permlane16_swap_b32_e32 v1, v3\n" \
    "v_mfma_f32_16x16x32_bf16 v[12:15], a[128:131], a[100:103], v[12:15]\ns_add_u32 s82, %[so0], 0x4000\nbuffer_store_dwordx4 v[0:3], v244, %[srdO], s82 offen sc1\n" \
    "v_mfma_f32_16x16x32_bf16 a[164:167], a[132:135], a[100:103], a[164:167]\ns_add_u32 %[sx2], %[sx2], 0x80\nv_mfma_f32_16x16x32_bf16 v[20:23], a[128:131], a[104:107], v[20:23]\nv_add_u32_e32 v228, 0xffff0000, v228\n" \
    "ds_read_b128 a[32:35], v224 offset:32768\nbuffer_load_dwordx4 v237, %[srdX2], s80 offen offset:1024 sc1 lds\nv_mfma_f32_16x16x32_bf16 a[168:171], a[132:135], a[104:107], a[168:171]\nv_add_u32_e32 v229, 0xffff0000, v229\n" \
    "v_mfma_f32_16x16x32_bf16 v[28:31], a[128:131], a[108:111], v[28:31]\nv_add_u32_e32 v230, 0xffff0000, v230\nds_read_b128 a[36:39], v224 offset:34816\n" \
    "v_mfma_f32_16x16x32_bf16 a[172:175], a[132:135], a[108:111], a[172:175]\nv_add_u32_e32 v231, 0xffff0000, v231\nbuffer_load_dwordx4 v238, %[srdX2], s80 offen offset:2048 sc1 lds\n" \
    "v_mfma_f32_16x16x32_bf16 v[36:39], a[128:131], a[112:115], v[36:39]\nv_add_u32_e32 v232, 0xffff0000, v232\nds_read_b128 a[40:43], v224 offset:36864\n" \
    "v_mfma_f32_16x16x32_bf16 a[176:179], a[132:135], a[112:115], a[176:179]\nv_add_u32_e32 v233, 0xffff0000, v233\nv_mfma_f32_16x16x32_bf16 v[44:47], a[128:131], a[116:119], v[44:47]\n" \
    "ds_read_b128 a[44:47], v224 offset:38912\nv_mfma_f32_16x16x32_bf16 a[180:183], a[132:135], a[116:119], a[180:183]\nbuffer_load_dwordx4 v239, %[srdX2], s80 offen offset:3072 sc1 lds\n" \
    "v_mfma_f32_16x16x32_bf16 v[52:55], a[128:131], a[120:123], v[52:55]\nds_read_b128 a[48:51], v224 offset:40960\nv_permlane16_swap_b32_e32 v8, v10\nv_permlane16_swap_b32_e32 v9, v11\n" \
    "v_mfma_f32_16x16x32_bf16 a[184:187], a[132:135], a[120:123], a[184:187]\ns_add_u32 s82, %[so0], 0x5000\nbuffer_store_dwordx4 v[8:11], v244, %[srdO], s82 offen sc1\n" \
    "v_mfma_f32_16x16x32_bf16 v[60:63], a[128:131], a[124:127], v[60:63]\nds_read_b128 a[52:55], v224 offset:43008\ns_add_u32 m0, %[m0x20], 0x1000\ns_add_u32 s80, s83, 0xa000\n" \
    "buffer_load_dwordx4 v236, %[srdX2], s80 offen offset:0 sc1 lds\nv_mfma_f32_16x16x32_bf16 a[188:191], a[132:135], a[124:127], a[188:191]\nv_mfma_f32_16x16x32_bf16 v[68:71], a[136:139], a[96:99], v[68:71]\n" \
    "ds_read_b128 a[56:59], v224 offset:45056\nv_mfma_f32_16x16x32_bf16 a[192:195], a[140:143], a[96:99], a[192:195]\nbuffer_load_dwordx4 v237, %[srdX2], s80 offen offset:1024 sc1 lds\n" \
    "v_mfma_f32_16x16x32_bf16 v[76:79], a[136:139], a[100:103], v[76:79]\nds_read_b128 a[60:63], v224 offset:47104\nv_mfma_f32_16x16x32_bf16 a[196:199], a[140:143], a[100:103], a[196:199]\n" \
    "v_mfma_f32_16x16x32_bf16 v[84:87], a[136:139], a[104:107], v[84:87]\nds_read_b64_tr_b16 a[64:65], v228 offset:0\nv_mfma_f32_16x16x32_bf16 a[200:203], a[140:143], a[104:107], a[200:203]\n" \
    "ds_read_b64_tr_b16 a[66:67], v228 offset:1536\nbuffer_load_dwordx4 v238, %[srdX2], s80 offen offset:2048 sc1 lds\nv_mfma_f32_16x16x32_bf16 v[92:95], a[136:139], a[108:111], v[92:95]\n" \
    "ds_read_b64_tr_b16 a[68:69], v229 offset:0\nv_mfma_f32_16x16x32_bf16 a[204:207], a[140:143], a[108:111], a[204:207]\nds_read_b64_tr_b16 a[70:71], v229 offset:1536\nv_permlane16_swap_b32_e32 v16, v18\n" \
    "v_permlane16_swap_b32_e32 v17, v19\nv_mfma_f32_16x16x32_bf16 v[100:103], a[136:139], a[112:115], v[100:103]\nds_read_b64_tr_b16 a[72:73], v230 offset:0\nbuffer_load_dwordx4 v239, %[srdX2], s80 offen offset:3072 sc1 lds\n" \
    "s_add_u32 s82, %[so0], 0x6000\nbuffer_store_dwordx4 v[16:19], v244, %[srdO], s82 offen sc1\nv_mfma_f32_16x16x32_bf16 a[208:211], a[140:143], a[112:115], a[208:211]\nds_read_b64_tr_b16 a[74:75], v230 offset:1536\n" \
    "v_mfma_f32_16x16x32_bf16 v[108:111], a[136:139], a[116:119], v[108:111]\nv_mfma_f32_16x16x32_bf16 a[212:215], a[140:143], a[116:119], a[212:215]\nds_read_b64_tr_b16 a[76:77], v231 offset:0\n" \
    "v_mfma_f32_16x16x32_bf16 v[116:119], a[136:139], a[120:123], v[116:119]\nds_read_b64_tr_b16 a[78:79], v231 offset:1536\nv_mfma_f32_16x16x32_bf16 a[216:219], a[140:143], a[120:123], a[216:219]\n" \
    "ds_read_b64_tr_b16 a[80:81], v232 offset:0\nv_mfma_f32_16x16x32_bf16 v[124:127], a[136:139], a[124:127], v[124:127]\nds_read_b64_tr_b16 a[82:83], v232 offset:1536\n" \
    "v_mfma_f32_16x16x32_bf16 a[220:223], a[140:143], a[124:127], a[220:223]\nds_read_b64_tr_b16 a[84:85], v233 offset:0\nv_mfma_f32_16x16x32_bf16 v[132:135], a[144:147], a[96:99], v[132:135]\n" \
    "ds_read_b64_tr_b16 a[86:87], v233 offset:1536\nv_mfma_f32_16x16x32_bf16 a[224:227], a[148:151], a[96:99], a[224:227]\nv_mfma_f32_16x16x32_bf16 v[140:143], a[144:147], a[100:103], v[140:143]\n" \
    "v_permlane16_swap_b32_e32 v24, v26\nv_permlane16_swap_b32_e32 v25, v27\nv_mfma_f32_16x16x32_bf16 a[228:231], a[148:151], a[100:103], a[228:231]\ns_add_u32 s82, %[so0], 0x7000\n" \
    "buffer_store_dwordx4 v[24:27], v244, %[srdO], s82 offen sc1\nv_mfma_f32_16x16x32_bf16 v[148:151], a[144:147], a[104:107], v[148:151]\nv_mfma_f32_16x16x32_bf16 a[232:235], a[148:151], a[104:107], a[232:235]\n" \
    "v_mfma_f32_16x16x32_bf16 v[156:159], a[144:147], a[108:111], v[156:159]\nv_mfma_f32_16x16x32_bf16 a[236:239], a[148:151], a[108:111], a[236:239]\nv_mfma_f32_16x16x32_bf16 v[164:167], a[144:147], a[112:115], v[164:167]\n" \
    "v_mfma_f32_16x16x32_bf16 a[240:243], a[148:151], a[112:115], a[240:243]\nv_mfma_f32_16x16x32_bf16 v[172:175], a[144:147], a[116:119], v[172:175]\nv_mfma_f32_16x16x32_bf16 a[244:247], a[148:151], a[116:119], a[244:247]\n" \
    "v_mfma_f32_16x16x32_bf16 v[180:183], a[144:147], a[120:123], v[180:183]\nv_mfma_f32_16x16x32_bf16 a[248:251], a[148:151], a[120:123], a[248:251]\nv_mfma_f32_16x16x32_bf16 v[188:191], a[144:147], a[124:127], v[188:191]\n" \
    "v_mfma_f32_16x16x32_bf16 a[252:255], a[148:151], a[124:127], a[252:255]\ns_waitcnt lgkmcnt(0)\nv_mfma_f32_16x16x32_bf16 v[4:7], a[64:67], a[32:35], v[4:7]\nds_read_b128 a[96:99], v225 offset:32768\n" \
    "v_mfma_f32_16x16x32_bf16 a[160:163], a[68:71], a[32:35], a[160:163]\nds_read_b128 a[100:103], v225 offset:34816\ns_mov_b32 s83, %[sw2]\nv_permlane16_swap_b32_e32 v32, v34\nv_permlane16_swap_b32_e32 v33, v35\n" \
    "v_mfma_f32_16x16x32_bf16 v[12:15], a[64:67], a[36:39], v[12:15]\ns_add_u32 %[sw2], %[sw2], 0x80000\ns_add_u32 s82, %[so1], 0x0\nbuffer_store_dwordx4 v[32:35], v244, %[srdO], s82 offen sc1\n" \
    "v_mfma_f32_16x16x32_bf16 a[164:167], a[68:71], a[36:39], a[164:167]\nds_read_b128 a[104:107], v225 offset:36864\ns_add_u32 m0, %[m0w2], 0x20000\ns_add_u32 s81, s83, 0x0\n" \
    "buffer_load_dwordx4 v240, %[srdW2], s81 offen offset:0 lds\nv_mfma_f32_16x16x32_bf16 v[20:23], a[64:67], a[40:43], v[20:23]\nv_mfma_f32_16x16x32_bf16 a[168:171], a[68:71], a[40:43], a[168:171]\n" \
    "ds_read_b128 a[108:111], v225 offset:38912\nv_mfma_f32_16x16x32_bf16 v[28:31], a[64:67], a[44:47], v[28:31]\nds_read_b128 a[112:115], v225 offset:40960\n" \
    "v_mfma_f32_16x16x32_bf16 a[172:175], a[68:71], a[44:47], a[172:175]\nv_mfma_f32_16x16x32_bf16 v[36:39], a[64:67], a[48:51], v[36:39]\nds_read_b128 a[116:119], v225 offset:43008\n" \
    "v_mfma_f32_16x16x32_bf16 a[176:179], a[68:71], a[48:51], a[176:179]\nbuffer_load_dwordx4 v241, %[srdW2], s81 offen offset:1024 lds\nv_mfma_f32_16x16x32_bf16 v[44:47], a[64:67], a[52:55], v[44:47]\n" \
    "ds_read_b128 a[120:123], v225 offset:45056\nv_mfma_f32_16x16x32_bf16 a[180:183], a[68:71], a[52:55], a[180:183]\nds_read_b128 a[124:127], v225 offset:47104\n" \
    "v_mfma_f32_16x16x32_bf16 v[52:55], a[64:67], a[56:59], v[52:55]\nv_permlane16_swap_b32_e32 v40, v42\nv_permlane16_swap_b32_e32 v41, v43\nv_mfma_f32_16x16x32_bf16 a[184:187], a[68:71], a[56:59], a[184:187]\n" \
    "ds_read_b64_tr_b16 a[128:129], v228 offset:12288\ns_add_u32 s82, %[so1], 0x1000\nbuffer_store_dwordx4 v[40:43], v244, %[srdO], s82 offen sc1\nv_mfma_f32_16x16x32_bf16 v[60:63], a[64:67], a[60:63], v[60:63]\n" \
    "v_mfma_f32_16x16x32_bf16 a[188:191], a[68:71], a[60:63], a[188:191]\nds_read_b64_tr_b16 a[130:131], v228 offset:13824\nbuffer_load_dwordx4 v242, %[srdW2], s81 offen offset:2048 lds\n" \
    "v_mfma_f32_16x16x32_bf16 v[68:71], a[72:75], a[32:35], v[68:71]\nv_mfma_f32_16x16x32_bf16 a[192:195], a[76:79], a[32:35], a[192:195]\nds_read_b64_tr_b16 a[132:133], v229 offset:12288\n" \
    "v_mfma_f32_16x16x32_bf16 v[76:79], a[72:75], a[36:39], v[76:79]\nds_read_b64_tr_b16 a[134:135], v229 offset:13824\nv_mfma_f32_16x16x32_bf16 a[196:199], a[76:79], a[36:39], a[196:199]\n" \
    "v_mfma_f32_16x16x32_bf16 v[84:87], a[72:75], a[40:43], v[84:87]\nds_read_b64_tr_b16 a[136:137], v230 offset:12288\nv_mfma_f32_16x16x32_bf16 a[200:203], a[76:79], a[40:43], a[200:203]\ns_add_u32 m0, %[m0w2], 0x21800\n" \
    "s_add_u32 s81, s83, 0x20000\nbuffer_load_dwordx4 v240, %[srdW2], s81 offen offset:0 lds\nv_mfma_f32_16x16x32_bf16 v[92:95], a[72:75], a[44:47], v[92:95]\nds_read_b64_tr_b16 a[138:139], v230 offset:13824\n" \
    "v_mfma_f32_16x16x32_bf16 a[204:207], a[76:79], a[44:47], a[204:207]\nds_read_b64_tr_b16 a[140:141], v231 offset:12288\nv_permlane16_swap_b32_e32 v48, v50\nv_permlane16_swap_b32_e32 v49, v51\n" \
    "v_mfma_f32_16x16x32_bf16 v[100:103], a[72:75], a[48:51], v[100:103]\ns_add_u32 s82, %[so1], 0x2000\nbuffer_store_dwordx4 v[48:51], v244, %[srdO], s82 offen sc1\n" \
    "v_mfma_f32_16x16x32_bf16 a[208:211], a[76:79], a[48:51], a[208:211]\nds_read_b64_tr_b16 a[142:143], v231 offset:13824\nv_mfma_f32_16x16x32_bf16 v[108:111], a[72:75], a[52:55], v[108:111]\n" \
    "v_mfma_f32_16x16x32_bf16 a[212:215], a[76:79], a[52:55], a[212:215]\nds_read_b64_tr_b16 a[144:145], v232 offset:12288\nbuffer_load_dwordx4 v241, %[srdW2], s81 offen offset:1024 lds\n" \
    "v_mfma_f32_16x16x32_bf16 v[116:119], a[72:75], a[56:59], v[116:119]\nds_read_b64_tr_b16 a[146:147], v232 offset:13824\nv_mfma_f32_16x16x32_bf16 a[216:219], a[76:79], a[56:59], a[216:219]\n" \
    "v_mfma_f32_16x16x32_bf16 v[124:127], a[72:75], a[60:63], v[124:127]\nds_read_b64_tr_b16 a[148:149], v233 offset:12288\nv_mfma_f32_16x16x32_bf16 a[220:223], a[76:79], a[60:63], a[220:223]\n" \
    "v_mfma_f32_16x16x32_bf16 v[132:135], a[80:83], a[32:35], v[132:135]\nds_read_b64_tr_b16 a[150:151], v233 offset:13824\nv_mfma_f32_16x16x32_bf16 a[224:227], a[84:87], a[32:35], a[224:227]\n" \
    "buffer_load_dwordx4 v242, %[srdW2], s81 offen offset:2048 lds\nv_mfma_f32_16x16x32_bf16 v[140:143], a[80:83], a[36:39], v[140:143]\nv_permlane16_swap_b32_e32 v56, v58\nv_permlane16_swap_b32_e32 v57, v59\n" \
    "v_mfma_f32_16x16x32_bf16 a[228:231], a[84:87], a[36:39], a[228:231]\ns_add_u32 s82, %[so1], 0x3000\nbuffer_store_dwordx4 v[56:59], v244, %[srdO], s82 offen sc1\n" \
    "v_mfma_f32_16x16x32_bf16 v[148:151], a[80:83], a[40:43], v[148:151]\nv_mfma_f32_16x16x32_bf16 a[232:235], a[84:87], a[40:43], a[232:235]\nv_mfma_f32_16x16x32_bf16 v[156:159], a[80:83], a[44:47], v[156:159]\n" \
    "v_mfma_f32_16x16x32_bf16 a[236:239], a[84:87], a[44:47], a[236:239]\nv_mfma_f32_16x16x32_bf16 v[164:167], a[80:83], a[48:51], v[164:167]\nv_mfma_f32_16x16x32_bf16 a[240:243], a[84:87], a[48:51], a[240:243]\n" \
    "v_mfma_f32_16x16x32_bf16 v[172:175], a[80:83], a[52:55], v[172:175]\nv_mfma_f32_16x16x32_bf16 a[244:247], a[84:87], a[52:55], a[244:247]\nv_mfma_f32_16x16x32_bf16 v[180:183], a[80:83], a[56:59], v[180:183]\n" \
    "v_mfma_f32_16x16x32_bf16 a[248:251], a[84:87], a[56:59], a[248:251]\nv_mfma_f32_16x16x32_bf16 v[188:191], a[80:83], a[60:63], v[188:191]\nv_mfma_f32_16x16x32_bf16 a[252:255], a[84:87], a[60:63], a[252:255]\n" \
    "s_waitcnt vmcnt(12) lgkmcnt(0)\ns_barrier\nv_mfma_f32_16x16x32_bf16 v[4:7], a[128:131], a[96:99], v[4:7]\ns_mov_b32 s83, %[sx2]\nv_mfma_f32_16x16x32_bf16 a[160:163], a[132:135], a[96:99], a[160:163]\n" \
    "s_add_u32 m0, %[m0x20], 0x8000\ns_add_u32 s80, s83, 0x0\nbuffer_load_dwordx4 v236, %[srdX2], s80 offen offset:0 sc1 lds\nv_permlane16_swap_b32_e32 v64, v66\nv_permlane16_swap_b32_e32 v65, v67\n" \
    "v_mfma_f32_16x16x32_bf16 v[12:15], a[128:131], a[100:103], v[12:15]\ns_add_u32 s82, %[so1], 0x4000\nbuffer_store_dwordx4 v[64:67], v244, %[srdO], s82 offen sc1\n" \
    "v_mfma_f32_16x16x32_bf16 a[164:167], a[132:135], a[100:103], a[164:167]\ns_add_u32 %[sx2], %[sx2], 0x80\nv_mfma_f32_16x16x32_bf16 v[20:23], a[128:131], a[104:107], v[20:23]\nv_add_u32_e32 v228, 0x8000, v228\n" \
    "ds_read_b128 a[32:35], v224 offset:0\nbuffer_load_dwordx4 v237, %[srdX2], s80 offen offset:1024 sc1 lds\nv_mfma_f32_16x16x32_bf16 a[168:171], a[132:135], a[104:107], a[168:171]\nv_add_u32_e32 v229, 0x8000, v229\n" \
    "v_mfma_f32_16x16x32_bf16 v[28:31], a[128:131], a[108:111], v[28:31]\nv_add_u32_e32 v230, 0x8000, v230\nds_read_b128 a[36:39], v224 offset:2048\nv_mfma_f32_16x16x32_bf16 a[172:175], a[132:135], a[108:111], a[172:175]\n" \
    "v_add_u32_e32 v231, 0x8000, v231\nbuffer_load_dwordx4 v238, %[srdX2], s80 offen offset:2048 sc1 lds\nv_mfma_f32_16x16x32_bf16 v[36:39], a[128:131], a[112:115], v[36:39]\nv_add_u32_e32 v232, 0x8000, v232\n" \
    "ds_read_b128 a[40:43], v224 offset:4096\nv_mfma_f32_16x16x32_bf16 a[176:179], a[132:135], a[112:115], a[176:179]\nv_add_u32_e32 v233, 0x8000, v233\nv_mfma_f32_16x16x32_bf16 v[44:47], a[128:131], a[116:119], v[44:47]\n" \
    "ds_read_b128 a[44:47], v224 offset:6144\nv_mfma_f32_16x16x32_bf16 a[180:183], a[132:135], a[116:119], a[180:183]\nbuffer_load_dwordx4 v239, %[srdX2], s80 offen offset:3072 sc1 lds\n" \
    "v_mfma_f32_16x16x32_bf16 v[52:55], a[128:131], a[120:123], v[52:55]\nds_read_b128 a[48:51], v224 offset:8192\nv_permlane16_swap_b32_e32 v72, v74\nv_permlane16_swap_b32_e32 v73, v75\n" \
    "v_mfma_f32_16x16x32_bf16 a[184:187], a[132:135], a[120:123], a[184:187]\ns_add_u32 s82, %[so1], 0x5000\nbuffer_store_dwordx4 v[72:75], v244, %[srdO], s82 offen sc1\n" \
    "v_mfma_f32_16x16x32_bf16 v[60:63], a[128:131], a[124:127], v[60:63]\nds_read_b128 a[52:55], v224 offset:10240\ns_add_u32 m0, %[m0x20], 0x9000\ns_add_u32 s80, s83, 0xa000\n" \
    "buffer_load_dwordx4 v236, %[srdX2], s80 offen offset:0 sc1 lds\nv_mfma_f32_16x16x32_bf16 a[188:191], a[132:135], a[124:127], a[188:191]\nv_mfma_f32_16x16x32_bf16 v[68:71], a[136:139], a[96:99], v[68:71]\n" \
    "ds_read_b128 a[56:59], v224 offset:12288\nv_mfma_f32_16x16x32_bf16 a[192:195], a[140:143], a[96:99], a[192:195]\nbuffer_load_dwordx4 v237, %[srdX2], s80 offen offset:1024 sc1 lds\n" \
    "v_mfma_f32_16x16x32_bf16 v[76:79], a[136:139], a[100:103], v[76:79]\nds_read_b128 a[60:63], v224 offset:14336\nv_mfma_f32_16x16x32_bf16 a[196:199], a[140:143], a[100:103], a[196:199]\n" \
    "v_mfma_f32_16x16x32_bf16 v[84:87], a[136:139], a[104:107], v[84:87]\nds_read_b64_tr_b16 a[64:65], v228 offset:0\nv_mfma_f32_16x16x32_bf16 a[200:203], a[140:143], a[104:107], a[200:203]\n" \
    "ds_read_b64_tr_b16 a[66:67], v228 offset:1536\nbuffer_load_dwordx4 v238, %[srdX2], s80 offen offset:2048 sc1 lds\nv_mfma_f32_16x16x32_bf16 v[92:95], a[136:139], a[108:111], v[92:95]\n" \
    "ds_read_b64_tr_b16 a[68:69], v229 offset:0\nv_mfma_f32_16x16x32_bf16 a[204:207], a[140:143], a[108:111], a[204:207]\nds_read_b64_tr_b16 a[70:71], v229 offset:1536\nv_permlane16_swap_b32_e32 v80, v82\n" \
    "v_permlane16_swap_b32_e32 v81, v83\nv_mfma_f32_16x16x32_bf16 v[100:103], a[136:139], a[112:115], v[100:103]\nds_read_b64_tr_b16 a[72:73], v230 offset:0\nbuffer_load_dwordx4 v239, %[srdX2], s80 offen offset:3072 sc1 lds\n" \
    "s_add_u32 s82, %[so1], 0x6000\nbuffer_store_dwordx4 v[80:83], v244, %[srdO], s82 offen sc1\nv_mfma_f32_16x16x32_bf16 a[208:211], a[140:143], a[112:115], a[208:211]\nds_read_b64_tr_b16 a[74:75], v230 offset:1536\n" \
    "v_mfma_f32_16x16x32_bf16 v[108:111], a[136:139], a[116:119], v[108:111]\nv_mfma_f32_16x16x32_bf16 a[212:215], a[140:143], a[116:119], a[212:215]\nds_read_b64_tr_b16 a[76:77], v231 offset:0\n" \
    "v_mfma_f32_16x16x32_bf16 v[116:119], a[136:139], a[120:123], v[116:119]\nds_read_b64_tr_b16 a[78:79], v231 offset:1536\nv_mfma_f32_16x16x32_bf16 a[216:219], a[140:143], a[120:123], a[216:219]\n" \
    "ds_read_b64_tr_b16 a[80:81], v232 offset:0\nv_mfma_f32_16x16x32_bf16 v[124:127], a[136:139], a[124:127], v[124:127]\nds_read_b64_tr_b16 a[82:83], v232 offset:1536\n" \
    "v_mfma_f32_16x16x32_bf16 a[220:223], a[140:143], a[124:127], a[220:223]\nds_read_b64_tr_b16 a[84:85], v233 offset:0\nv_mfma_f32_16x16x32_bf16 v[132:135], a[144:147], a[96:99], v[132:135]\n" \
    "ds_read_b64_tr_b16 a[86:87], v233 offset:1536\nv_mfma_f32_16x16x32_bf16 a[224:227], a[148:151], a[96:99], a[224:227]\nv_mfma_f32_16x16x32_bf16 v[140:143], a[144:147], a[100:103], v[140:143]\n" \
    "v_permlane16_swap_b32_e32 v88, v90\nv_permlane16_swap_b32_e32 v89, v91\nv_mfma_f32_16x16x32_bf16 a[228:231], a[148:151], a[100:103], a[228:231]\ns_add_u32 s82, %[so1], 0x7000\n" \
    "buffer_store_dwordx4 v[88:91], v244, %[srdO], s82 offen sc1\nv_mfma_f32_16x16x32_bf16 v[148:151], a[144:147], a[104:107], v[148:151]\nv_mfma_f32_16x16x32_bf16 a[232:235], a[148:151], a[104:107], a[232:235]\n" \
    "v_mfma_f32_16x16x32_bf16 v[156:159], a[144:147], a[108:111], v[156:159]\nv_mfma_f32_16x16x32_bf16 a[236:239], a[148:151], a[108:111], a[236:239]\nv_mfma_f32_16x16x32_bf16 v[164:167], a[144:147], a[112:115], v[164:167]\n" \
    "v_mfma_f32_16x16x32_bf16 a[240:243], a[148:151], a[112:115], a[240:243]\nv_mfma_f32_16x16x32_bf16 v[172:175], a[144:147], a[116:119], v[172:175]\nv_mfma_f32_16x16x32_bf16 a[244:247], a[148:151], a[116:119], a[244:247]\n" \
    "v_mfma_f32_16x16x32_bf16 v[180:183], a[144:147], a[120:123], v[180:183]\nv_mfma_f32_16x16x32_bf16 a[248:251], a[148:151], a[120:123], a[248:251]\nv_mfma_f32_16x16x32_bf16 v[188:191], a[144:147], a[124:127], v[188:191]\n" \
    "v_mfma_f32_16x16x32_bf16 a[252:255], a[148:151], a[124:127], a[252:255]\ns_waitcnt lgkmcnt(0)\nv_mfma_f32_16x16x32_bf16 v[4:7], a[64:67], a[32:35], v[4:7]\nds_read_b128 a[96:99], v225 offset:0\n" \
    "v_mfma_f32_16x16x32_bf16 a[160:163], a[68:71], a[32:35], a[160:163]\nds_read_b128 a[100:103], v225 offset:2048\ns_mov_b32 s83, %[sw2]\nv_permlane16_swap_b32_e32 v96, v98\nv_permlane16_swap_b32_e32 v97, v99\n" \
    "v_mfma_f32_16x16x32_bf16 v[12:15], a[64:67], a[36:39], v[12:15]\ns_add_u32 %[sw2], %[sw2], 0x80000\ns_add_u32 s82, %[so2], 0x0\nbuffer_store_dwordx4 v[96:99], v244, %[srdO], s82 offen sc1\n" \
    "v_mfma_f32_16x16x32_bf16 a[164:167], a[68:71], a[36:39], a[164:167]\nds_read_b128 a[104:107], v225 offset:4096\ns_add_u32 m0, %[m0w2], 0x10000\ns_add_u32 s81, s83, 0x0\n" \
    "buffer_load_dwordx4 v240, %[srdW2], s81 offen offset:0 lds\nv_mfma_f32_16x16x32_bf16 v[20:23], a[64:67], a[40:43], v[20:23]\nv_mfma_f32_16x16x32_bf16 a[168:171], a[68:71], a[40:43], a[168:171]\n" \
    "ds_read_b128 a[108:111], v225 offset:6144\nv_mfma_f32_16x16x32_bf16 v[28:31], a[64:67], a[44:47], v[28:31]\nds_read_b128 a[112:115], v225 offset:8192\nv_mfma_f32_16x16x32_bf16 a[172:175], a[68:71], a[44:47], a[172:175]\n" \
    "v_mfma_f32_16x16x32_bf16 v[36:39], a[64:67], a[48:51], v[36:39]\nds_read_b128 a[116:119], v225 offset:10240\nv_mfma_f32_16x16x32_bf16 a[176:179], a[68:71], a[48:51], a[176:179]\n" \
    "buffer_load_dwordx4 v241, %[srdW2], s81 offen offset:1024 lds\nv_mfma_f32_16x16x32_bf16 v[44:47], a[64:67], a[52:55], v[44:47]\nds_read_b128 a[120:123], v225 offset:12288\n" \
    "v_mfma_f32_16x16x32_bf16 a[180:183], a[68:71], a[52:55], a[180:183]\nds_read_b128 a[124:127], v225 offset:14336\nv_mfma_f32_16x16x32_bf16 v[52:55], a[64:67], a[56:59], v[52:55]\nv_permlane16_swap_b32_e32 v104, v106\n" \
    "v_permlane16_swap_b32_e32 v105, v107\nv_mfma_f32_16x16x32_bf16 a[184:187], a[68:71], a[56:59], a[184:187]\nds_read_b64_tr_b16 a[128:129], v228 offset:12288\ns_add_u32 s82, %[so2], 0x1000\n" \
    "buffer_store_dwordx4 v[104:107], v244, %[srdO], s82 offen sc1\nv_mfma_f32_16x16x32_bf16 v[60:63], a[64:67], a[60:63], v[60:63]\nv_mfma_f32_16x16x32_bf16 a[188:191], a[68:71], a[60:63], a[188:191]\n" \
    "ds_read_b64_tr_b16 a[130:131], v228 offset:13824\nbuffer_load_dwordx4 v242, %[srdW2], s81 offen offset:2048 lds\nv_mfma_f32_16x16x32_bf16 v[68:71], a[72:75], a[32:35], v[68:71]\n" \
    "v_mfma_f32_16x16x32_bf16 a[192:195], a[76:79], a[32:35], a[192:195]\nds_read_b64_tr_b16 a[132:133], v229 offset:12288\nv_mfma_f32_16x16x32_bf16 v[76:79], a[72:75], a[36:39], v[76:79]\n" \
    "ds_read_b64_tr_b16 a[134:135], v229 offset:13824\nv_mfma_f32_16x16x32_bf16 a[196:199], a[76:79], a[36:39], a[196:199]\nv_mfma_f32_16x16x32_bf16 v[84:87], a[72:75], a[40:43], v[84:87]\n" \
    "ds_read_b64_tr_b16 a[136:137], v230 offset:12288\nv_mfma_f32_16x16x32_bf16 a[200:203], a[76:79], a[40:43], a[200:203]\ns_add_u32 m0, %[m0w2], 0x11800\ns_add_u32 s81, s83, 0x20000\n" \
    "buffer_load_dwordx4 v240, %[srdW2], s81 offen offset:0 lds\nv_mfma_f32_16x16x32_bf16 v[92:95], a[72:75], a[44:47], v[92:95]\nds_read_b64_tr_b16 a[138:139], v230 offset:13824\n" \
    "v_mfma_f32_16x16x32_bf16 a[204:207], a[76:79], a[44:47], a[204:207]\nds_read_b64_tr_b16 a[140:141], v231 offset:12288\nv_permlane16_swap_b32_e32 v112, v114\nv_permlane16_swap_b32_e32 v113, v115\n" \
    "v_mfma_f32_16x16x32_bf16 v[100:103], a[72:75], a[48:51], v[100:103]\ns_add_u32 s82, %[so2], 0x2000\nbuffer_store_dwordx4 v[112:115], v244, %[srdO], s82 offen sc1\n" \
    "v_mfma_f32_16x16x32_bf16 a[208:211], a[76:79], a[48:51], a[208:211]\nds_read_b64_tr_b16 a[142:143], v231 offset:13824\nv_mfma_f32_16x16x32_bf16 v[108:111], a[72:75], a[52:55], v[108:111]\n" \
    "v_mfma_f32_16x16x32_bf16 a[212:215], a[76:79], a[52:55], a[212:215]\nds_read_b64_tr_b16 a[144:145], v232 offset:12288\nbuffer_load_dwordx4 v241, %[srdW2], s81 offen offset:1024 lds\n" \
    "v_mfma_f32_16x16x32_bf16 v[116:119], a[72:75], a[56:59], v[116:119]\nds_read_b64_tr_b16 a[146:147], v232 offset:13824\nv_mfma_f32_16x16x32_bf16 a[216:219], a[76:79], a[56:59], a[216:219]\n" \
    "v_mfma_f32_16x16x32_bf16 v[124:127], a[72:75], a[60:63], v[124:127]\nds_read_b64_tr_b16 a[148:149], v233 offset:12288\nv_mfma_f32_16x16x32_bf16 a[220:223], a[76:79], a[60:63], a[220:223]\n" \
    "v_mfma_f32_16x16x32_bf16 v[132:135], a[80:83], a[32:35], v[132:135]\nds_read_b64_tr_b16 a[150:151], v233 offset:13824\nv_mfma_f32_16x16x32_bf16 a[224:227], a[84:87], a[32:35], a[224:227]\n" \
    "buffer_load_dwordx4 v242, %[srdW2], s81 offen offset:2048 lds\nv_mfma_f32_16x16x32_bf16 v[140:143], a[80:83], a[36:39], v[140:143]\nv_permlane16_swap_b32_e32 v120, v122\nv_permlane16_swap_b32_e32 v121, v123\n" \
    "v_mfma_f32_16x16x32_bf16 a[228:231], a[84:87], a[36:39], a[228:231]\ns_add_u32 s82, %[so2], 0x3000\nbuffer_store_dwordx4 v[120:123], v244, %[srdO], s82 offen sc1\n" \
    "v_mfma_f32_16x16x32_bf16 v[148:151], a[80:83], a[40:43], v[148:151]\nv_mfma_f32_16x16x32_bf16 a[232:235], a[84:87], a[40:43], a[232:235]\nv_mfma_f32_16x16x32_bf16 v[156:159], a[80:83], a[44:47], v[156:159]\n" \
    "v_mfma_f32_16x16x32_bf16 a[236:239], a[84:87], a[44:47], a[236:239]\nv_mfma_f32_16x16x32_bf16 v[164:167], a[80:83], a[48:51], v[164:167]\nv_mfma_f32_16x16x32_bf16 a[240:243], a[84:87], a[48:51], a[240:243]\n" \
    "v_mfma_f32_16x16x32_bf16 v[172:175], a[80:83], a[52:55], v[172:175]\nv_mfma_f32_16x16x32_bf16 a[244:247], a[84:87], a[52:55], a[244:247]\nv_mfma_f32_16x16x32_bf16 v[180:183], a[80:83], a[56:59], v[180:183]\n" \
    "v_mfma_f32_16x16x32_bf16 a[248:251], a[84:87], a[56:59], a[248:251]\nv_mfma_f32_16x16x32_bf16 v[188:191], a[80:83], a[60:63], v[188:191]\nv_mfma_f32_16x16x32_bf16 a[252:255], a[84:87], a[60:63], a[252:255]\n" \
    "s_waitcnt vmcnt(12) lgkmcnt(0)\ns_barrier\nv_mfma_f32_16x16x32_bf16 v[4:7], a[128:131], a[96:99], v[4:7]\ns_mov_b32 s83, %[sx2]\nv_mfma_f32_16x16x32_bf16 a[160:163], a[132:135], a[96:99], a[160:163]\n" \
    "s_add_u32 m0, %[m0x20], 0x0\ns_add_u32 s80, s83, 0x0\nbuffer_load_dwordx4 v236, %[srdX2], s80 offen offset:0 sc1 lds\nv_permlane16_swap_b32_e32 v128, v130\nv_permlane16_swap_b32_e32 v129, v131\n" \
    "v_mfma_f32_16x16x32_bf16 v[12:15], a[128:131], a[100:103], v[12:15]\ns_add_u32 s82, %[so2], 0x4000\nbuffer_store_dwordx4 v[128:131], v244, %[srdO], s82 offen sc1\n" \
    "v_mfma_f32_16x16x32_bf16 a[164:167], a[132:135], a[100:103], a[164:167]\ns_add_u32 %[sx2], %[sx2], 0x80\nv_mfma_f32_16x16x32_bf16 v[20:23], a[128:131], a[104:107], v[20:23]\nv_add_u32_e32 v228, 0x8000, v228\n" \
    "ds_read_b128 a[32:35], v224 offset:32768\nbuffer_load_dwordx4 v237, %[srdX2], s80 offen offset:1024 sc1 lds\nv_mfma_f32_16x16x32_bf16 a[168:171], a[132:135], a[104:107], a[168:171]\nv_add_u32_e32 v229, 0x8000, v229\n" \
    "v_mfma_f32_16x16x32_bf16 v[28:31], a[128:131], a[108:111], v[28:31]\nv_add_u32_e32 v230, 0x8000, v230\nds_read_b128 a[36:39], v224 offset:34816\nv_mfma_f32_16x16x32_bf16 a[172:175], a[132:135], a[108:111], a[172:175]\n" \
    "v_add_u32_e32 v231, 0x8000, v231\nbuffer_load_dwordx4 v238, %[srdX2], s80 offen offset:2048 sc1 lds\nv_mfma_f32_16x16x32_bf16 v[36:39], a[128:131], a[112:115], v[36:39]\nv_add_u32_e32 v232, 0x8000, v232\n" \
    "ds_read_b128 a[40:43], v224 offset:36864\nv_mfma_f32_16x16x32_bf16 a[176:179], a[132:135], a[112:115], a[176:179]\nv_add_u32_e32 v233, 0x8000, v233\nv_mfma_f32_16x16x32_bf16 v[44:47], a[128:131], a[116:119], v[44:47]\n" \
    "ds_read_b128 a[44:47], v224 offset:38912\nv_mfma_f32_16x16x32_bf16 a[180:183], a[132:135], a[116:119], a[180:183]\nbuffer_load_dwordx4 v239, %[srdX2], s80 offen offset:3072 sc1 lds\n" \
    "v_mfma_f32_16x16x32_bf16 v[52:55], a[128:131], a[120:123], v[52:55]\nds_read_b128 a[48:51], v224 offset:40960\nv_permlane16_swap_b32_e32 v136, v138\nv_permlane16_swap_b32_e32 v137, v139\n" \
    "v_mfma_f32_16x16x32_bf16 a[184:187], a[132:135], a[120:123], a[184:187]\ns_add_u32 s82, %[so2], 0x5000\nbuffer_store_dwordx4 v[136:139], v244, %[srdO], s82 offen sc1\n" \
    "v_mfma_f32_16x16x32_bf16 v[60:63], a[128:131], a[124:127], v[60:63]\nds_read_b128 a[52:55], v224 offset:43008\ns_add_u32 m0, %[m0x20], 0x1000\ns_add_u32 s80, s83, 0xa000\n" \
    "buffer_load_dwordx4 v236, %[srdX2], s80 offen offset:0 sc1 lds\nv_mfma_f32_16x16x32_bf16 a[188:191], a[132:135], a[124:127], a[188:191]\nv_mfma_f32_16x16x32_bf16 v[68:71], a[136:139], a[96:99], v[68:71]\n" \
    "ds_read_b128 a[56:59], v224 offset:45056\nv_mfma_f32_16x16x32_bf16 a[192:195], a[140:143], a[96:99], a[192:195]\nbuffer_load_dwordx4 v237, %[srdX2], s80 offen offset:1024 sc1 lds\n" \
    "v_mfma_f32_16x16x32_bf16 v[76:79], a[136:139], a[100:103], v[76:79]\nds_read_b128 a[60:63], v224 offset:47104\nv_mfma_f32_16x16x32_bf16 a[196:199], a[140:143], a[100:103], a[196:199]\n" \
    "v_mfma_f32_16x16x32_bf16 v[84:87], a[136:139], a[104:107], v[84:87]\nds_read_b64_tr_b16 a[64:65], v228 offset:0\nv_mfma_f32_16x16x32_bf16 a[200:203], a[140:143], a[104:107], a[200:203]\n" \
    "ds_read_b64_tr_b16 a[66:67], v228 offset:1536\nbuffer_load_dwordx4 v238, %[srdX2], s80 offen offset:2048 sc1 lds\nv_mfma_f32_16x16x32_bf16 v[92:95], a[136:139], a[108:111], v[92:95]\n" \
    "ds_read_b64_tr_b16 a[68:69], v229 offset:0\nv_mfma_f32_16x16x32_bf16 a[204:207], a[140:143], a[108:111], a[204:207]\nds_read_b64_tr_b16 a[70:71], v229 offset:1536\nv_permlane16_swap_b32_e32 v144, v146\n" \
    "v_permlane16_swap_b32_e32 v145, v147\nv_mfma_f32_16x16x32_bf16 v[100:103], a[136:139], a[112:115], v[100:103]\nds_read_b64_tr_b16 a[72:73], v230 offset:0\n" \
    "buffer_load_dwordx4 v239, %[srdX2], s80 offen offset:3072 sc1 lds\ns_add_u32 s82, %[so2], 0x6000\nbuffer_store_dwordx4 v[144:147], v244, %[srdO], s82 offen sc1\n" \
    "v_mfma_f32_16x16x32_bf16 a[208:211], a[140:143], a[112:115], a[208:211]\nds_read_b64_tr_b16 a[74:75], v230 offset:1536\nv_mfma_f32_16x16x32_bf16 v[108:111], a[136:139], a[116:119], v[108:111]\n" \
    "v_mfma_f32_16x16x32_bf16 a[212:215], a[140:143], a[116:119], a[212:215]\nds_read_b64_tr_b16 a[76:77], v231 offset:0\nv_mfma_f32_16x16x32_bf16 v[116:119], a[136:139], a[120:123], v[116:119]\n" \
    "ds_read_b64_tr_b16 a[78:79], v231 offset:1536\nv_mfma_f32_16x16x32_bf16 a[216:219], a[140:143], a[120:123], a[216:219]\nds_read_b64_tr_b16 a[80:81], v232 offset:0\n" \
    "v_mfma_f32_16x16x32_bf16 v[124:127], a[136:139], a[124:127], v[124:127]\nds_read_b64_tr_b16 a[82:83], v232 offset:1536\nv_mfma_f32_16x16x32_bf16 a[220:223], a[140:143], a[124:127], a[220:223]\n" \
    "ds_read_b64_tr_b16 a[84:85], v233 offset:0\nv_mfma_f32_16x16x32_bf16 v[132:135], a[144:147], a[96:99], v[132:135]\nds_read_b64_tr_b16 a[86:87], v233 offset:1536\n" \
    "v_mfma_f32_16x16x32_bf16 a[224:227], a[148:151], a[96:99], a[224:227]\nv_mfma_f32_16x16x32_bf16 v[140:143], a[144:147], a[100:103], v[140:143]\nv_permlane16_swap_b32_e32 v152, v154\nv_permlane16_swap_b32_e32 v153, v155\n" \
    "v_mfma_f32_16x16x32_bf16 a[228:231], a[148:151], a[100:103], a[228:231]\ns_add_u32 s82, %[so2], 0x7000\nbuffer_store_dwordx4 v[152:155], v244, %[srdO], s82 offen sc1\n" \
    "v_mfma_f32_16x16x32_bf16 v[148:151], a[144:147], a[104:107], v[148:151]\nv_mfma_f32_16x16x32_bf16 a[232:235], a[148:151], a[104:107], a[232:235]\nv_mfma_f32_16x16x32_bf16 v[156:159], a[144:147], a[108:111], v[156:159]\n" \
    "v_mfma_f32_16x16x32_bf16 a[236:239], a[148:151], a[108:111], a[236:239]\nv_mfma_f32_16x16x32_bf16 v[164:167], a[144:147], a[112:115], v[164:167]\nv_mfma_f32_16x16x32_bf16 a[240:243], a[148:151], a[112:115], a[240:243]\n" \
    "v_mfma_f32_16x16x32_bf16 v[172:175], a[144:147], a[116:119], v[172:175]\nv_mfma_f32_16x16x32_bf16 a[244:247], a[148:151], a[116:119], a[244:247]\nv_mfma_f32_16x16x32_bf16 v[180:183], a[144:147], a[120:123], v[180:183]\n" \
    "v_mfma_f32_16x16x32_bf16 a[248:251], a[148:151], a[120:123], a[248:251]\nv_mfma_f32_16x16x32_bf16 v[188:191], a[144:147], a[124:127], v[188:191]\nv_mfma_f32_16x16x32_bf16 a[252:255], a[148:151], a[124:127], a[252:255]\n" \
    "s_waitcnt lgkmcnt(0)\nv_mfma_f32_16x16x32_bf16 v[4:7], a[64:67], a[32:35], v[4:7]\nds_read_b128 a[96:99], v225 offset:32768\nv_mfma_f32_16x16x32_bf16 a[160:163], a[68:71], a[32:35], a[160:163]\n" \
    "ds_read_b128 a[100:103], v225 offset:34816\ns_mov_b32 s83, %[sw2]\nv_permlane16_swap_b32_e32 v160, v162\nv_permlane16_swap_b32_e32 v161, v163\nv_mfma_f32_16x16x32_bf16 v[12:15], a[64:67], a[36:39], v[12:15]\n" \
    "s_add_u32 %[sw2], %[sw2], 0x80000\ns_add_u32 s82, %[so3], 0x0\nbuffer_store_dwordx4 v[160:163], v244, %[srdO], s82 offen sc1\nv_mfma_f32_16x16x32_bf16 a[164:167], a[68:71], a[36:39], a[164:167]\n" \
    "ds_read_b128 a[104:107], v225 offset:36864\ns_add_u32 m0, %[m0w2], 0x18000\ns_add_u32 s81, s83, 0x0\nbuffer_load_dwordx4 v240, %[srdW2], s81 offen offset:0 lds\n" \
    "v_mfma_f32_16x16x32_bf16 v[20:23], a[64:67], a[40:43], v[20:23]\nv_mfma_f32_16x16x32_bf16 a[168:171], a[68:71], a[40:43], a[168:171]\nds_read_b128 a[108:111], v225 offset:38912\n" \
    "v_mfma_f32_16x16x32_bf16 v[28:31], a[64:67], a[44:47], v[28:31]\nds_read_b128 a[112:115], v225 offset:40960\nv_mfma_f32_16x16x32_bf16 a[172:175], a[68:71], a[44:47], a[172:175]\n" \
    "v_mfma_f32_16x16x32_bf16 v[36:39], a[64:67], a[48:51], v[36:39]\nds_read_b128 a[116:119], v225 offset:43008\nv_mfma_f32_16x16x32_bf16 a[176:179], a[68:71], a[48:51], a[176:179]\n" \
    "buffer_load_dwordx4 v241, %[srdW2], s81 offen offset:1024 lds\nv_mfma_f32_16x16x32_bf16 v[44:47], a[64:67], a[52:55], v[44:47]\nds_read_b128 a[120:123], v225 offset:45056\n" \
    "v_mfma_f32_16x16x32_bf16 a[180:183], a[68:71], a[52:55], a[180:183]\nds_read_b128 a[124:127], v225 offset:47104\nv_mfma_f32_16x16x32_bf16 v[52:55], a[64:67], a[56:59], v[52:55]\nv_permlane16_swap_b32_e32 v168, v170\n" \
    "v_permlane16_swap_b32_e32 v169, v171\nv_mfma_f32_16x16x32_bf16 a[184:187], a[68:71], a[56:59], a[184:187]\nds_read_b64_tr_b16 a[128:129], v228 offset:12288\ns_add_u32 s82, %[so3], 0x1000\n" \
    "buffer_store_dwordx4 v[168:171], v244, %[srdO], s82 offen sc1\nv_mfma_f32_16x16x32_bf16 v[60:63], a[64:67], a[60:63], v[60:63]\nv_mfma_f32_16x16x32_bf16 a[188:191], a[68:71], a[60:63], a[188:191]\n" \
    "ds_read_b64_tr_b16 a[130:131], v228 offset:13824\nbuffer_load_dwordx4 v242, %[srdW2], s81 offen offset:2048 lds\nv_mfma_f32_16x16x32_bf16 v[68:71], a[72:75], a[32:35], v[68:71]\n" \
    "v_mfma_f32_16x16x32_bf16 a[192:195], a[76:79], a[32:35], a[192:195]\nds_read_b64_tr_b16 a[132:133], v229 offset:12288\nv_mfma_f32_16x16x32_bf16 v[76:79], a[72:75], a[36:39], v[76:79]\n" \
    "ds_read_b64_tr_b16 a[134:135], v229 offset:13824\nv_mfma_f32_16x16x32_bf16 a[196:199], a[76:79], a[36:39], a[196:199]\nv_mfma_f32_16x16x32_bf16 v[84:87], a[72:75], a[40:43], v[84:87]\n" \
    "ds_read_b64_tr_b16 a[136:137], v230 offset:12288\nv_mfma_f32_16x16x32_bf16 a[200:203], a[76:79], a[40:43], a[200:203]\ns_add_u32 m0, %[m0w2], 0x19800\ns_add_u32 s81, s83, 0x20000\n" \
    "buffer_load_dwordx4 v240, %[srdW2], s81 offen offset:0 lds\nv_mfma_f32_16x16x32_bf16 v[92:95], a[72:75], a[44:47], v[92:95]\nds_read_b64_tr_b16 a[138:139], v230 offset:13824\n" \
    "v_mfma_f32_16x16x32_bf16 a[204:207], a[76:79], a[44:47], a[204:207]\nds_read_b64_tr_b16 a[140:141], v231 offset:12288\nv_permlane16_swap_b32_e32 v176, v178\nv_permlane16_swap_b32_e32 v177, v179\n" \
    "v_mfma_f32_16x16x32_bf16 v[100:103], a[72:75], a[48:51], v[100:103]\ns_add_u32 s82, %[so3], 0x2000\nbuffer_store_dwordx4 v[176:179], v244, %[srdO], s82 offen sc1\n" \
    "v_mfma_f32_16x16x32_bf16 a[208:211], a[76:79], a[48:51], a[208:211]\nds_read_b64_tr_b16 a[142:143], v231 offset:13824\nv_mfma_f32_16x16x32_bf16 v[108:111], a[72:75], a[52:55], v[108:111]\n" \
    "v_mfma_f32_16x16x32_bf16 a[212:215], a[76:79], a[52:55], a[212:215]\nds_read_b64_tr_b16 a[144:145], v232 offset:12288\nbuffer_load_dwordx4 v241, %[srdW2], s81 offen offset:1024 lds\n" \
    "v_mfma_f32_16x16x32_bf16 v[116:119], a[72:75], a[56:59], v[116:119]\nds_read_b64_tr_b16 a[146:147], v232 offset:13824\nv_mfma_f32_16x16x32_bf16 a[216:219], a[76:79], a[56:59], a[216:219]\n" \
    "v_mfma_f32_16x16x32_bf16 v[124:127], a[72:75], a[60:63], v[124:127]\nds_read_b64_tr_b16 a[148:149], v233 offset:12288\nv_mfma_f32_16x16x32_bf16 a[220:223], a[76:79], a[60:63], a[220:223]\n" \
    "v_mfma_f32_16x16x32_bf16 v[132:135], a[80:83], a[32:35], v[132:135]\nds_read_b64_tr_b16 a[150:151], v233 offset:13824\nv_mfma_f32_16x16x32_bf16 a[224:227], a[84:87], a[32:35], a[224:227]\n" \
    "buffer_load_dwordx4 v242, %[srdW2], s81 offen offset:2048 lds\nv_mfma_f32_16x16x32_bf16 v[140:143], a[80:83], a[36:39], v[140:143]\nv_permlane16_swap_b32_e32 v184, v186\nv_permlane16_swap_b32_e32 v185, v187\n" \
    "v_mfma_f32_16x16x32_bf16 a[228:231], a[84:87], a[36:39], a[228:231]\ns_add_u32 s82, %[so3], 0x3000\nbuffer_store_dwordx4 v[184:187], v244, %[srdO], s82 offen sc1\n" \
    "v_mfma_f32_16x16x32_bf16 v[148:151], a[80:83], a[40:43], v[148:151]\nv_mfma_f32_16x16x32_bf16 a[232:235], a[84:87], a[40:43], a[232:235]\nv_mfma_f32_16x16x32_bf16 v[156:159], a[80:83], a[44:47], v[156:159]\n" \
    "v_mfma_f32_16x16x32_bf16 a[236:239], a[84:87], a[44:47], a[236:239]\nv_mfma_f32_16x16x32_bf16 v[164:167], a[80:83], a[48:51], v[164:167]\nv_mfma_f32_16x16x32_bf16 a[240:243], a[84:87], a[48:51], a[240:243]\n" \
    "v_mfma_f32_16x16x32_bf16 v[172:175], a[80:83], a[52:55], v[172:175]\nv_mfma_f32_16x16x32_bf16 a[244:247], a[84:87], a[52:55], a[244:247]\nv_mfma_f32_16x16x32_bf16 v[180:183], a[80:83], a[56:59], v[180:183]\n" \
    "v_mfma_f32_16x16x32_bf16 a[248:251], a[84:87], a[56:59], a[248:251]\nv_mfma_f32_16x16x32_bf16 v[188:191], a[80:83], a[60:63], v[188:191]\nv_mfma_f32_16x16x32_bf16 a[252:255], a[84:87], a[60:63], a[252:255]\n" \
    "s_waitcnt vmcnt(12) lgkmcnt(0)\ns_barrier\nv_mfma_f32_16x16x32_bf16 v[4:7], a[128:131], a[96:99], v[4:7]\ns_mov_b32 s83, %[sx2]\nv_mfma_f32_16x16x32_bf16 a[160:163], a[132:135], a[96:99], a[160:163]\n" \
    "s_add_u32 m0, %[m0x20], 0x8000\ns_add_u32 s80, s83, 0x0\nbuffer_load_dwordx4 v236, %[srdX2], s80 offen offset:0 sc1 lds\nv_permlane16_swap_b32_e32 v192, v194\nv_permlane16_swap_b32_e32 v193, v195\n" \
    "v_mfma_f32_16x16x32_bf16 v[12:15], a[128:131], a[100:103], v[12:15]\ns_add_u32 s82, %[so3], 0x4000\nbuffer_store_dwordx4 v[192:195], v244, %[srdO], s82 offen sc1\n" \
    "v_mfma_f32_16x16x32_bf16 a[164:167], a[132:135], a[100:103], a[164:167]\ns_add_u32 %[sx2], %[sx2], 0x80\nv_mfma_f32_16x16x32_bf16 v[20:23], a[128:131], a[104:107], v[20:23]\nv_add_u32_e32 v228, 0xffff0000, v228\n" \
    "ds_read_b128 a[32:35], v224 offset:0\nbuffer_load_dwordx4 v237, %[srdX2], s80 offen offset:1024 sc1 lds\nv_mfma_f32_16x16x32_bf16 a[168:171], a[132:135], a[104:107], a[168:171]\nv_add_u32_e32 v229, 0xffff0000, v229\n" \
    "v_mfma_f32_16x16x32_bf16 v[28:31], a[128:131], a[108:111], v[28:31]\nv_add_u32_e32 v230, 0xffff0000, v230\nds_read_b128 a[36:39], v224 offset:2048\n" \
    "v_mfma_f32_16x16x32_bf16 a[172:175], a[132:135], a[108:111], a[172:175]\nv_add_u32_e32 v231, 0xffff0000, v231\nbuffer_load_dwordx4 v238, %[srdX2], s80 offen offset:2048 sc1 lds\n" \
    "v_mfma_f32_16x16x32_bf16 v[36:39], a[128:131], a[112:115], v[36:39]\nv_add_u32_e32 v232, 0xffff0000, v232\nds_read_b128 a[40:43], v224 offset:4096\n" \
    "v_mfma_f32_16x16x32_bf16 a[176:179], a[132:135], a[112:115], a[176:179]\nv_add_u32_e32 v233, 0xffff0000, v233\nv_mfma_f32_16x16x32_bf16 v[44:47], a[128:131], a[116:119], v[44:47]\n" \
    "ds_read_b128 a[44:47], v224 offset:6144\nv_mfma_f32_16x16x32_bf16 a[180:183], a[132:135], a[116:119], a[180:183]\nbuffer_load_dwordx4 v239, %[srdX2], s80 offen offset:3072 sc1 lds\n" \
    "v_mfma_f32_16x16x32_bf16 v[52:55], a[128:131], a[120:123], v[52:55]\nds_read_b128 a[48:51], v224 offset:8192\nv_permlane16_swap_b32_e32 v200, v202\nv_permlane16_swap_b32_e32 v201, v203\n" \
    "v_mfma_f32_16x16x32_bf16 a[184:187], a[132:135], a[120:123], a[184:187]\ns_add_u32 s82, %[so3], 0x5000\nbuffer_store_dwordx4 v[200:203], v244, %[srdO], s82 offen sc1\n" \
    "v_mfma_f32_16x16x32_bf16 v[60:63], a[128:131], a[124:127], v[60:63]\nds_read_b128 a[52:55], v224 offset:10240\ns_add_u32 m0, %[m0x20], 0x9000\ns_add_u32 s80, s83, 0xa000\n" \
    "buffer_load_dwordx4 v236, %[srdX2], s80 offen offset:0 sc1 lds\nv_mfma_f32_16x16x32_bf16 a[188:191], a[132:135], a[124:127], a[188:191]\nv_mfma_f32_16x16x32_bf16 v[68:71], a[136:139], a[96:99], v[68:71]\n" \
    "ds_read_b128 a[56:59], v224 offset:12288\nv_mfma_f32_16x16x32_bf16 a[192:195], a[140:143], a[96:99], a[192:195]\nbuffer_load_dwordx4 v237, %[srdX2], s80 offen offset:1024 sc1 lds\n" \
    "v_mfma_f32_16x16x32_bf16 v[76:79], a[136:139], a[100:103], v[76:79]\nds_read_b128 a[60:63], v224 offset:14336\nv_mfma_f32_16x16x32_bf16 a[196:199], a[140:143], a[100:103], a[196:199]\n" \
    "v_mfma_f32_16x16x32_bf16 v[84:87], a[136:139], a[104:107], v[84:87]\nds_read_b64_tr_b16 a[64:65], v228 offset:0\nv_mfma_f32_16x16x32_bf16 a[200:203], a[140:143], a[104:107], a[200:203]\n" \
    "ds_read_b64_tr_b16 a[66:67], v228 offset:1536\nbuffer_load_dwordx4 v238, %[srdX2], s80 offen offset:2048 sc1 lds\nv_mfma_f32_16x16x32_bf16 v[92:95], a[136:139], a[108:111], v[92:95]\n" \
    "ds_read_b64_tr_b16 a[68:69], v229 offset:0\nv_mfma_f32_16x16x32_bf16 a[204:207], a[140:143], a[108:111], a[204:207]\nds_read_b64_tr_b16 a[70:71], v229 offset:1536\nv_permlane16_swap_b32_e32 v208, v210\n" \
    "v_permlane16_swap_b32_e32 v209, v211\nv_mfma_f32_16x16x32_bf16 v[100:103], a[136:139], a[112:115], v[100:103]\nds_read_b64_tr_b16 a[72:73], v230 offset:0\n" \
    "buffer_load_dwordx4 v239, %[srdX2], s80 offen offset:3072 sc1 lds\ns_add_u32 s82, %[so3], 0x6000\nbuffer_store_dwordx4 v[208:211], v244, %[srdO], s82 offen sc1\n" \
    "v_mfma_f32_16x16x32_bf16 a[208:211], a[140:143], a[112:115], a[208:211]\nds_read_b64_tr_b16 a[74:75], v230 offset:1536\nv_mfma_f32_16x16x32_bf16 v[108:111], a[136:139], a[116:119], v[108:111]\n" \
    "v_mfma_f32_16x16x32_bf16 a[212:215], a[140:143], a[116:119], a[212:215]\nds_read_b64_tr_b16 a[76:77], v231 offset:0\nv_mfma_f32_16x16x32_bf16 v[116:119], a[136:139], a[120:123], v[116:119]\n" \
    "ds_read_b64_tr_b16 a[78:79], v231 offset:1536\nv_mfma_f32_16x16x32_bf16 a[216:219], a[140:143], a[120:123], a[216:219]\nds_read_b64_tr_b16 a[80:81], v232 offset:0\n" \
    "v_mfma_f32_16x16x32_bf16 v[124:127], a[136:139], a[124:127], v[124:127]\nds_read_b64_tr_b16 a[82:83], v232 offset:1536\nv_mfma_f32_16x16x32_bf16 a[220:223], a[140:143], a[124:127], a[220:223]\n" \
    "ds_read_b64_tr_b16 a[84:85], v233 offset:0\nv_mfma_f32_16x16x32_bf16 v[132:135], a[144:147], a[96:99], v[132:135]\nds_read_b64_tr_b16 a[86:87], v233 offset:1536\n" \
    "v_mfma_f32_16x16x32_bf16 a[224:227], a[148:151], a[96:99], a[224:227]\nv_mfma_f32_16x16x32_bf16 v[140:143], a[144:147], a[100:103], v[140:143]\nv_permlane16_swap_b32_e32 v216, v218\nv_permlane16_swap_b32_e32 v217, v219\n" \
    "v_mfma_f32_16x16x32_bf16 a[228:231], a[148:151], a[100:103], a[228:231]\ns_add_u32 s82, %[so3], 0x7000\nbuffer_store_dwordx4 v[216:219], v244, %[srdO], s82 offen sc1\n" \
    "v_mfma_f32_16x16x32_bf16 v[148:151], a[144:147], a[104:107], v[148:151]\nv_mfma_f32_16x16x32_bf16 a[232:235], a[148:151], a[104:107], a[232:235]\nv_mfma_f32_16x16x32_bf16 v[156:159], a[144:147], a[108:111], v[156:159]\n" \
    "v_mfma_f32_16x16x32_bf16 a[236:239], a[148:151], a[108:111], a[236:239]\nv_mfma_f32_16x16x32_bf16 v[164:167], a[144:147], a[112:115], v[164:167]\nv_mfma_f32_16x16x32_bf16 a[240:243], a[148:151], a[112:115], a[240:243]\n" \
    "v_mfma_f32_16x16x32_bf16 v[172:175], a[144:147], a[116:119], v[172:175]\nv_mfma_f32_16x16x32_bf16 a[244:247], a[148:151], a[116:119], a[244:247]\nv_mfma_f32_16x16x32_bf16 v[180:183], a[144:147], a[120:123], v[180:183]\n" \
    "v_mfma_f32_16x16x32_bf16 a[248:251], a[148:151], a[120:123], a[248:251]\nv_mfma_f32_16x16x32_bf16 v[188:191], a[144:147], a[124:127], v[188:191]\nv_mfma_f32_16x16x32_bf16 a[252:255], a[148:151], a[124:127], a[252:255]\n" \
    "s_waitcnt lgkmcnt(0)\nv_mfma_f32_16x16x32_bf16 v[4:7], a[64:67], a[32:35], v[4:7]\nds_read_b128 a[96:99], v225 offset:0\nv_mfma_f32_16x16x32_bf16 a[160:163], a[68:71], a[32:35], a[160:163]\n" \
    "ds_read_b128 a[100:103], v225 offset:2048\ns_mov_b32 s83, %[sw2]\nv_accvgpr_read_b32 v246, a0\nv_accvgpr_read_b32 v247, a1\nv_mfma_f32_16x16x32_bf16 v[12:15], a[64:67], a[36:39], v[12:15]\n" \
    "s_add_u32 %[sw2], %[sw2], 0x80000\nv_accvgpr_read_b32 v248, a2\nv_accvgpr_read_b32 v249, a3\nv_mfma_f32_16x16x32_bf16 a[164:167], a[68:71], a[36:39], a[164:167]\nds_read_b128 a[104:107], v225 offset:4096\n" \
    "s_add_u32 m0, %[m0w2], 0x20000\ns_add_u32 s81, s83, 0x0\nbuffer_load_dwordx4 v240, %[srdW2], s81 offen offset:0 lds\nv_accvgpr_read_b32 v250, a4\nv_accvgpr_read_b32 v251, a5\n" \
    "v_mfma_f32_16x16x32_bf16 v[20:23], a[64:67], a[40:43], v[20:23]\nv_accvgpr_read_b32 v252, a6\nv_accvgpr_read_b32 v253, a7\nv_mfma_f32_16x16x32_bf16 a[168:171], a[68:71], a[40:43], a[168:171]\n" \
    "ds_read_b128 a[108:111], v225 offset:6144\nv_cvt_pk_bf16_f32 v246, v246, v247\nv_cvt_pk_bf16_f32 v247, v248, v249\nv_mfma_f32_16x16x32_bf16 v[28:31], a[64:67], a[44:47], v[28:31]\n" \
    "ds_read_b128 a[112:115], v225 offset:8192\nv_cvt_pk_bf16_f32 v248, v250, v251\nv_cvt_pk_bf16_f32 v249, v252, v253\nv_mfma_f32_16x16x32_bf16 a[172:175], a[68:71], a[44:47], a[172:175]\ns_add_u32 s82, %[so0], 0x0\n" \
    "s_nop 0\nv_permlane16_swap_b32_e32 v246, v248\nv_permlane16_swap_b32_e32 v247, v249\nv_mfma_f32_16x16x32_bf16 v[36:39], a[64:67], a[48:51], v[36:39]\nds_read_b128 a[116:119], v225 offset:10240\n" \
    "buffer_store_dwordx4 v[246:249], v244, %[srdO], s82 offen sc1\ns_nop 1\nv_mfma_f32_16x16x32_bf16 a[176:179], a[68:71], a[48:51], a[176:179]\nbuffer_load_dwordx4 v241, %[srdW2], s81 offen offset:1024 lds\n" \
    "v_mfma_f32_16x16x32_bf16 v[44:47], a[64:67], a[52:55], v[44:47]\nds_read_b128 a[120:123], v225 offset:12288\nv_mfma_f32_16x16x32_bf16 a[180:183], a[68:71], a[52:55], a[180:183]\n" \
    "ds_read_b128 a[124:127], v225 offset:14336\nv_mfma_f32_16x16x32_bf16 v[52:55], a[64:67], a[56:59], v[52:55]\nv_accvgpr_read_b32 v246, a8\nv_accvgpr_read_b32 v247, a9\n" \
    "v_mfma_f32_16x16x32_bf16 a[184:187], a[68:71], a[56:59], a[184:187]\nds_read_b64_tr_b16 a[128:129], v228 offset:12288\nv_accvgpr_read_b32 v248, a10\nv_accvgpr_read_b32 v249, a11\n" \
    "v_mfma_f32_16x16x32_bf16 v[60:63], a[64:67], a[60:63], v[60:63]\nv_accvgpr_read_b32 v250, a12\nv_accvgpr_read_b32 v251, a13\nv_mfma_f32_16x16x32_bf16 a[188:191], a[68:71], a[60:63], a[188:191]\n" \
    "ds_read_b64_tr_b16 a[130:131], v228 offset:13824\nbuffer_load_dwordx4 v242, %[srdW2], s81 offen offset:2048 lds\nv_accvgpr_read_b32 v252, a14\nv_accvgpr_read_b32 v253, a15\n" \
    "v_mfma_f32_16x16x32_bf16 v[68:71], a[72:75], a[32:35], v[68:71]\nv_cvt_pk_bf16_f32 v246, v246, v247\nv_cvt_pk_bf16_f32 v247, v248, v249\nv_mfma_f32_16x16x32_bf16 a[192:195], a[76:79], a[32:35], a[192:195]\n" \
    "ds_read_b64_tr_b16 a[132:133], v229 offset:12288\nv_cvt_pk_bf16_f32 v248, v250, v251\nv_cvt_pk_bf16_f32 v249, v252, v253\nv_mfma_f32_16x16x32_bf16 v[76:79], a[72:75], a[36:39], v[76:79]\n" \
    "ds_read_b64_tr_b16 a[134:135], v229 offset:13824\ns_add_u32 s82, %[so0], 0x1000\ns_nop 0\nv_permlane16_swap_b32_e32 v246, v248\nv_permlane16_swap_b32_e32 v247, v249\n" \
    "v_mfma_f32_16x16x32_bf16 a[196:199], a[76:79], a[36:39], a[196:199]\nbuffer_store_dwordx4 v[246:249], v244, %[srdO], s82 offen sc1\ns_nop 1\nv_mfma_f32_16x16x32_bf16 v[84:87], a[72:75], a[40:43], v[84:87]\n" \
    "ds_read_b64_tr_b16 a[136:137], v230 offset:12288\nv_mfma_f32_16x16x32_bf16 a[200:203], a[76:79], a[40:43], a[200:203]\ns_add_u32 m0, %[m0w2], 0x21800\ns_add_u32 s81, s83, 0x20000\n" \
    "buffer_load_dwordx4 v240, %[srdW2], s81 offen offset:0 lds\nv_mfma_f32_16x16x32_bf16 v[92:95], a[72:75], a[44:47], v[92:95]\nds_read_b64_tr_b16 a[138:139], v230 offset:13824\n" \
    "v_mfma_f32_16x16x32_bf16 a[204:207], a[76:79], a[44:47], a[204:207]\nds_read_b64_tr_b16 a[140:141], v231 offset:12288\nv_accvgpr_read_b32 v246, a16\nv_accvgpr_read_b32 v247, a17\n" \
    "v_mfma_f32_16x16x32_bf16 v[100:103], a[72:75], a[48:51], v[100:103]\nv_accvgpr_read_b32 v248, a18\nv_accvgpr_read_b32 v249, a19\nv_mfma_f32_16x16x32_bf16 a[208:211], a[76:79], a[48:51], a[208:211]\n" \
    "ds_read_b64_tr_b16 a[142:143], v231 offset:13824\nv_accvgpr_read_b32 v250, a20\nv_accvgpr_read_b32 v251, a21\nv_mfma_f32_16x16x32_bf16 v[108:111], a[72:75], a[52:55], v[108:111]\nv_accvgpr_read_b32 v252, a22\n" \
    "v_accvgpr_read_b32 v253, a23\nv_mfma_f32_16x16x32_bf16 a[212:215], a[76:79], a[52:55], a[212:215]\nds_read_b64_tr_b16 a[144:145], v232 offset:12288\nbuffer_load_dwordx4 v241, %[srdW2], s81 offen offset:1024 lds\n" \
    "v_cvt_pk_bf16_f32 v246, v246, v247\nv_cvt_pk_bf16_f32 v247, v248, v249\nv_mfma_f32_16x16x32_bf16 v[116:119], a[72:75], a[56:59], v[116:119]\nds_read_b64_tr_b16 a[146:147], v232 offset:13824\n" \
    "v_cvt_pk_bf16_f32 v248, v250, v251\nv_cvt_pk_bf16_f32 v249, v252, v253\nv_mfma_f32_16x16x32_bf16 a[216:219], a[76:79], a[56:59], a[216:219]\ns_add_u32 s82, %[so0], 0x2000\ns_nop 0\nv_permlane16_swap_b32_e32 v246, v248\n" \
    "v_permlane16_swap_b32_e32 v247, v249\nv_mfma_f32_16x16x32_bf16 v[124:127], a[72:75], a[60:63], v[124:127]\nds_read_b64_tr_b16 a[148:149], v233 offset:12288\nbuffer_store_dwordx4 v[246:249], v244, %[srdO], s82 offen sc1\n" \
    "s_nop 1\nv_mfma_f32_16x16x32_bf16 a[220:223], a[76:79], a[60:63], a[220:223]\nv_mfma_f32_16x16x32_bf16 v[132:135], a[80:83], a[32:35], v[132:135]\nds_read_b64_tr_b16 a[150:151], v233 offset:13824\n" \
    "v_mfma_f32_16x16x32_bf16 a[224:227], a[84:87], a[32:35], a[224:227]\nbuffer_load_dwordx4 v242, %[srdW2], s81 offen offset:2048 lds\nv_mfma_f32_16x16x32_bf16 v[140:143], a[80:83], a[36:39], v[140:143]\n" \
    "v_accvgpr_read_b32 v246, a24\nv_accvgpr_read_b32 v247, a25\nv_mfma_f32_16x16x32_bf16 a[228:231], a[84:87], a[36:39], a[228:231]\nv_accvgpr_read_b32 v248, a26\nv_accvgpr_read_b32 v249, a27\n" \
    "v_mfma_f32_16x16x32_bf16 v[148:151], a[80:83], a[40:43], v[148:151]\nv_accvgpr_read_b32 v250, a28\nv_accvgpr_read_b32 v251, a29\nv_mfma_f32_16x16x32_bf16 a[232:235], a[84:87], a[40:43], a[232:235]\n" \
    "v_accvgpr_read_b32 v252, a30\nv_accvgpr_read_b32 v253, a31\nv_mfma_f32_16x16x32_bf16 v[156:159], a[80:83], a[44:47], v[156:159]\nv_cvt_pk_bf16_f32 v246, v246, v247\nv_cvt_pk_bf16_f32 v247, v248, v249\n" \
    "v_mfma_f32_16x16x32_bf16 a[236:239], a[84:87], a[44:47], a[236:239]\nv_cvt_pk_bf16_f32 v248, v250, v251\nv_cvt_pk_bf16_f32 v249, v252, v253\nv_mfma_f32_16x16x32_bf16 v[164:167], a[80:83], a[48:51], v[164:167]\n" \
    "s_add_u32 s82, %[so0], 0x3000\ns_nop 0\nv_permlane16_swap_b32_e32 v246, v248\nv_permlane16_swap_b32_e32 v247, v249\nv_mfma_f32_16x16x32_bf16 a[240:243], a[84:87], a[48:51], a[240:243]\n" \
    "buffer_store_dwordx4 v[246:249], v244, %[srdO], s82 offen sc1\ns_nop 1\nv_mfma_f32_16x16x32_bf16 v[172:175], a[80:83], a[52:55], v[172:175]\nv_mfma_f32_16x16x32_bf16 a[244:247], a[84:87], a[52:55], a[244:247]\n" \
    "v_mfma_f32_16x16x32_bf16 v[180:183], a[80:83], a[56:59], v[180:183]\nv_mfma_f32_16x16x32_bf16 a[248:251], a[84:87], a[56:59], a[248:251]\nv_mfma_f32_16x16x32_bf16 v[188:191], a[80:83], a[60:63], v[188:191]\n" \
    "v_mfma_f32_16x16x32_bf16 a[252:255], a[84:87], a[60:63], a[252:255]\ns_waitcnt vmcnt(12) lgkmcnt(0)\ns_barrier\ns_mov_b32 s84, 0x10000\ns_mov_b32 s85, 0x18000\ns_mov_b32 s86, 0x20000\ns_mov_b32 %[cnt], 6\nL_Q4l%=:\n" \
    "v_mfma_f32_16x16x32_bf16 v[4:7], a[128:131], a[96:99], v[4:7]\ns_cmp_gt_u32 %[cnt], 2\ns_cselect_b32 s83, %[sx2], 0x70000000\nv_mfma_f32_16x16x32_bf16 a[160:163], a[132:135], a[96:99], a[160:163]\n" \
    "s_sub_u32 s81, s85, s84\ns_mov_b32 s87, s84\ns_add_u32 m0, %[m0x20], 0x0\ns_add_u32 s80, s83, 0x0\nbuffer_load_dwordx4 v236, %[srdX2], s80 offen offset:0 sc1 lds\n" \
    "v_mfma_f32_16x16x32_bf16 v[12:15], a[128:131], a[100:103], v[12:15]\ns_mov_b32 s84, s85\ns_mov_b32 s85, s86\nv_mfma_f32_16x16x32_bf16 a[164:167], a[132:135], a[100:103], a[164:167]\ns_add_u32 %[sx2], %[sx2], 0x80\n" \
    "s_mov_b32 s86, s87\nv_mfma_f32_16x16x32_bf16 v[20:23], a[128:131], a[104:107], v[20:23]\nv_add_u32_e32 v228, s81, v228\nds_read_b128 a[32:35], v224 offset:32768\n" \
    "buffer_load_dwordx4 v237, %[srdX2], s80 offen offset:1024 sc1 lds\nv_mfma_f32_16x16x32_bf16 a[168:171], a[132:135], a[104:107], a[168:171]\nv_add_u32_e32 v229, s81, v229\n" \
    "v_mfma_f32_16x16x32_bf16 v[28:31], a[128:131], a[108:111], v[28:31]\nv_add_u32_e32 v230, s81, v230\nds_read_b128 a[36:39], v224 offset:34816\nv_mfma_f32_16x16x32_bf16 a[172:175], a[132:135], a[108:111], a[172:175]\n" \
    "v_add_u32_e32 v231, s81, v231\nbuffer_load_dwordx4 v238, %[srdX2], s80 offen offset:2048 sc1 lds\nv_mfma_f32_16x16x32_bf16 v[36:39], a[128:131], a[112:115], v[36:39]\nv_add_u32_e32 v232, s81, v232\n" \
    "ds_read_b128 a[40:43], v224 offset:36864\nv_mfma_f32_16x16x32_bf16 a[176:179], a[132:135], a[112:115], a[176:179]\nv_add_u32_e32 v233, s81, v233\nv_mfma_f32_16x16x32_bf16 v[44:47], a[128:131], a[116:119], v[44:47]\n" \
    "ds_read_b128 a[44:47], v224 offset:38912\ns_sub_u32 %[cnt], %[cnt], 1\nv_mfma_f32_16x16x32_bf16 a[180:183], a[132:135], a[116:119], a[180:183]\nbuffer_load_dwordx4 v239, %[srdX2], s80 offen offset:3072 sc1 lds\n" \
    "v_mfma_f32_16x16x32_bf16 v[52:55], a[128:131], a[120:123], v[52:55]\nds_read_b128 a[48:51], v224 offset:40960\nv_mfma_f32_16x16x32_bf16 a[184:187], a[132:135], a[120:123], a[184:187]\n" \
    "v_mfma_f32_16x16x32_bf16 v[60:63], a[128:131], a[124:127], v[60:63]\nds_read_b128 a[52:55], v224 offset:43008\ns_add_u32 m0, %[m0x20], 0x1000\ns_add_u32 s80, s83, 0xa000\n" \
    "buffer_load_dwordx4 v236, %[srdX2], s80 offen offset:0 sc1 lds\nv_mfma_f32_16x16x32_bf16 a[188:191], a[132:135], a[124:127], a[188:191]\nv_mfma_f32_16x16x32_bf16 v[68:71], a[136:139], a[96:99], v[68:71]\n" \
    "ds_read_b128 a[56:59], v224 offset:45056\nv_mfma_f32_16x16x32_bf16 a[192:195], a[140:143], a[96:99], a[192:195]\nbuffer_load_dwordx4 v237, %[srdX2], s80 offen offset:1024 sc1 lds\n" \
    "v_mfma_f32_16x16x32_bf16 v[76:79], a[136:139], a[100:103], v[76:79]\nds_read_b128 a[60:63], v224 offset:47104\nv_mfma_f32_16x16x32_bf16 a[196:199], a[140:143], a[100:103], a[196:199]\n" \
    "v_mfma_f32_16x16x32_bf16 v[84:87], a[136:139], a[104:107], v[84:87]\nds_read_b64_tr_b16 a[64:65], v228 offset:0\nv_mfma_f32_16x16x32_bf16 a[200:203], a[140:143], a[104:107], a[200:203]\n" \
    "ds_read_b64_tr_b16 a[66:67], v228 offset:1536\nbuffer_load_dwordx4 v238, %[srdX2], s80 offen offset:2048 sc1 lds\nv_mfma_f32_16x16x32_bf16 v[92:95], a[136:139], a[108:111], v[92:95]\n" \
    "ds_read_b64_tr_b16 a[68:69], v229 offset:0\nv_mfma_f32_16x16x32_bf16 a[204:207], a[140:143], a[108:111], a[204:207]\nds_read_b64_tr_b16 a[70:71], v229 offset:1536\n" \
    "v_mfma_f32_16x16x32_bf16 v[100:103], a[136:139], a[112:115], v[100:103]\nds_read_b64_tr_b16 a[72:73], v230 offset:0\nbuffer_load_dwordx4 v239, %[srdX2], s80 offen offset:3072 sc1 lds\n" \
    "v_mfma_f32_16x16x32_bf16 a[208:211], a[140:143], a[112:115], a[208:211]\nds_read_b64_tr_b16 a[74:75], v230 offset:1536\nv_mfma_f32_16x16x32_bf16 v[108:111], a[136:139], a[116:119], v[108:111]\n" \
    "v_mfma_f32_16x16x32_bf16 a[212:215], a[140:143], a[116:119], a[212:215]\nds_read_b64_tr_b16 a[76:77], v231 offset:0\nv_mfma_f32_16x16x32_bf16 v[116:119], a[136:139], a[120:123], v[116:119]\n" \
    "ds_read_b64_tr_b16 a[78:79], v231 offset:1536\nv_mfma_f32_16x16x32_bf16 a[216:219], a[140:143], a[120:123], a[216:219]\nds_read_b64_tr_b16 a[80:81], v232 offset:0\n" \
    "v_mfma_f32_16x16x32_bf16 v[124:127], a[136:139], a[124:127], v[124:127]\nds_read_b64_tr_b16 a[82:83], v232 offset:1536\nv_mfma_f32_16x16x32_bf16 a[220:223], a[140:143], a[124:127], a[220:223]\n" \
    "ds_read_b64_tr_b16 a[84:85], v233 offset:0\nv_mfma_f32_16x16x32_bf16 v[132:135], a[144:147], a[96:99], v[132:135]\nds_read_b64_tr_b16 a[86:87], v233 offset:1536\n" \
    "v_mfma_f32_16x16x32_bf16 a[224:227], a[148:151], a[96:99], a[224:227]\nv_mfma_f32_16x16x32_bf16 v[140:143], a[144:147], a[100:103], v[140:143]\nv_mfma_f32_16x16x32_bf16 a[228:231], a[148:151], a[100:103], a[228:231]\n" \
    "v_mfma_f32_16x16x32_bf16 v[148:151], a[144:147], a[104:107], v[148:151]\nv_mfma_f32_16x16x32_bf16 a[232:235], a[148:151], a[104:107], a[232:235]\nv_mfma_f32_16x16x32_bf16 v[156:159], a[144:147], a[108:111], v[156:159]\n" \
    "v_mfma_f32_16x16x32_bf16 a[236:239], a[148:151], a[108:111], a[236:239]\nv_mfma_f32_16x16x32_bf16 v[164:167], a[144:147], a[112:115], v[164:167]\nv_mfma_f32_16x16x32_bf16 a[240:243], a[148:151], a[112:115], a[240:243]\n" \
    "v_mfma_f32_16x16x32_bf16 v[172:175], a[144:147], a[116:119], v[172:175]\nv_mfma_f32_16x16x32_bf16 a[244:247], a[148:151], a[116:119], a[244:247]\nv_mfma_f32_16x16x32_bf16 v[180:183], a[144:147], a[120:123], v[180:183]\n" \
    "v_mfma_f32_16x16x32_bf16 a[248:251], a[148:151], a[120:123], a[248:251]\nv_mfma_f32_16x16x32_bf16 v[188:191], a[144:147], a[124:127], v[188:191]\nv_mfma_f32_16x16x32_bf16 a[252:255], a[148:151], a[124:127], a[252:255]\n" \
    "s_waitcnt lgkmcnt(0)\nv_mfma_f32_16x16x32_bf16 v[4:7], a[64:67], a[32:35], v[4:7]\nds_read_b128 a[96:99], v225 offset:32768\nv_mfma_f32_16x16x32_bf16 a[160:163], a[68:71], a[32:35], a[160:163]\n" \
    "ds_read_b128 a[100:103], v225 offset:34816\ns_cmp_gt_u32 %[cnt], 2\ns_cselect_b32 s83, %[sw2], 0x70000000\nv_mfma_f32_16x16x32_bf16 v[12:15], a[64:67], a[36:39], v[12:15]\ns_add_u32 %[sw2], %[sw2], 0x80000\n" \
    "v_mfma_f32_16x16x32_bf16 a[164:167], a[68:71], a[36:39], a[164:167]\nds_read_b128 a[104:107], v225 offset:36864\ns_add_u32 m0, %[m0w2], s86\ns_add_u32 s81, s83, 0x0\n" \
    "buffer_load_dwordx4 v240, %[srdW2], s81 offen offset:0 lds\nv_mfma_f32_16x16x32_bf16 v[20:23], a[64:67], a[40:43], v[20:23]\nv_mfma_f32_16x16x32_bf16 a[168:171], a[68:71], a[40:43], a[168:171]\n" \
    "ds_read_b128 a[108:111], v225 offset:38912\nv_mfma_f32_16x16x32_bf16 v[28:31], a[64:67], a[44:47], v[28:31]\nds_read_b128 a[112:115], v225 offset:40960\n" \
    "v_mfma_f32_16x16x32_bf16 a[172:175], a[68:71], a[44:47], a[172:175]\nv_mfma_f32_16x16x32_bf16 v[36:39], a[64:67], a[48:51], v[36:39]\nds_read_b128 a[116:119], v225 offset:43008\n" \
    "v_mfma_f32_16x16x32_bf16 a[176:179], a[68:71], a[48:51], a[176:179]\nbuffer_load_dwordx4 v241, %[srdW2], s81 offen offset:1024 lds\nv_mfma_f32_16x16x32_bf16 v[44:47], a[64:67], a[52:55], v[44:47]\n" \
    "ds_read_b128 a[120:123], v225 offset:45056\nv_mfma_f32_16x16x32_bf16 a[180:183], a[68:71], a[52:55], a[180:183]\nds_read_b128 a[124:127], v225 offset:47104\n" \
    "v_mfma_f32_16x16x32_bf16 v[52:55], a[64:67], a[56:59], v[52:55]\nv_mfma_f32_16x16x32_bf16 a[184:187], a[68:71], a[56:59], a[184:187]\nds_read_b64_tr_b16 a[128:129], v228 offset:12288\n" \
    "v_mfma_f32_16x16x32_bf16 v[60:63], a[64:67], a[60:63], v[60:63]\nv_mfma_f32_16x16x32_bf16 a[188:191], a[68:71], a[60:63], a[188:191]\nds_read_b64_tr_b16 a[130:131], v228 offset:13824\n" \
    "buffer_load_dwordx4 v242, %[srdW2], s81 offen offset:2048 lds\nv_mfma_f32_16x16x32_bf16 v[68:71], a[72:75], a[32:35], v[68:71]\nv_mfma_f32_16x16x32_bf16 a[192:195], a[76:79], a[32:35], a[192:195]\n" \
    "ds_read_b64_tr_b16 a[132:133], v229 offset:12288\nv_mfma_f32_16x16x32_bf16 v[76:79], a[72:75], a[36:39], v[76:79]\nds_read_b64_tr_b16 a[134:135], v229 offset:13824\n" \
    "v_mfma_f32_16x16x32_bf16 a[196:199], a[76:79], a[36:39], a[196:199]\nv_mfma_f32_16x16x32_bf16 v[84:87], a[72:75], a[40:43], v[84:87]\nds_read_b64_tr_b16 a[136:137], v230 offset:12288\n" \
    "v_mfma_f32_16x16x32_bf16 a[200:203], a[76:79], a[40:43], a[200:203]\ns_add_u32 m0, m0, 0x1800\ns_add_u32 s81, s83, 0x20000\nbuffer_load_dwordx4 v240, %[srdW2], s81 offen offset:0 lds\n" \
    "v_mfma_f32_16x16x32_bf16 v[92:95], a[72:75], a[44:47], v[92:95]\nds_read_b64_tr_b16 a[138:139], v230 offset:13824\nv_mfma_f32_16x16x32_bf16 a[204:207], a[76:79], a[44:47], a[204:207]\n" \
    "ds_read_b64_tr_b16 a[140:141], v231 offset:12288\nv_mfma_f32_16x16x32_bf16 v[100:103], a[72:75], a[48:51], v[100:103]\nv_mfma_f32_16x16x32_bf16 a[208:211], a[76:79], a[48:51], a[208:211]\n" \
    "ds_read_b64_tr_b16 a[142:143], v231 offset:13824\nv_mfma_f32_16x16x32_bf16 v[108:111], a[72:75], a[52:55], v[108:111]\nv_mfma_f32_16x16x32_bf16 a[212:215], a[76:79], a[52:55], a[212:215]\n" \
    "ds_read_b64_tr_b16 a[144:145], v232 offset:12288\nbuffer_load_dwordx4 v241, %[srdW2], s81 offen offset:1024 lds\nv_mfma_f32_16x16x32_bf16 v[116:119], a[72:75], a[56:59], v[116:119]\n" \
    "ds_read_b64_tr_b16 a[146:147], v232 offset:13824\nv_mfma_f32_16x16x32_bf16 a[216:219], a[76:79], a[56:59], a[216:219]\nv_mfma_f32_16x16x32_bf16 v[124:127], a[72:75], a[60:63], v[124:127]\n" \
    "ds_read_b64_tr_b16 a[148:149], v233 offset:12288\nv_mfma_f32_16x16x32_bf16 a[220:223], a[76:79], a[60:63], a[220:223]\nv_mfma_f32_16x16x32_bf16 v[132:135], a[80:83], a[32:35], v[132:135]\n" \
    "ds_read_b64_tr_b16 a[150:151], v233 offset:13824\nv_mfma_f32_16x16x32_bf16 a[224:227], a[84:87], a[32:35], a[224:227]\nbuffer_load_dwordx4 v242, %[srdW2], s81 offen offset:2048 lds\n" \
    "v_mfma_f32_16x16x32_bf16 v[140:143], a[80:83], a[36:39], v[140:143]\nv_mfma_f32_16x16x32_bf16 a[228:231], a[84:87], a[36:39], a[228:231]\nv_mfma_f32_16x16x32_bf16 v[148:151], a[80:83], a[40:43], v[148:151]\n" \
    "v_mfma_f32_16x16x32_bf16 a[232:235], a[84:87], a[40:43], a[232:235]\nv_mfma_f32_16x16x32_bf16 v[156:159], a[80:83], a[44:47], v[156:159]\nv_mfma_f32_16x16x32_bf16 a[236:239], a[84:87], a[44:47], a[236:239]\n" \
    "v_mfma_f32_16x16x32_bf16 v[164:167], a[80:83], a[48:51], v[164:167]\nv_mfma_f32_16x16x32_bf16 a[240:243], a[84:87], a[48:51], a[240:243]\nv_mfma_f32_16x16x32_bf16 v[172:175], a[80:83], a[52:55], v[172:175]\n" \
    "v_mfma_f32_16x16x32_bf16 a[244:247], a[84:87], a[52:55], a[244:247]\nv_mfma_f32_16x16x32_bf16 v[180:183], a[80:83], a[56:59], v[180:183]\nv_mfma_f32_16x16x32_bf16 a[248:251], a[84:87], a[56:59], a[248:251]\n" \
    "v_mfma_f32_16x16x32_bf16 v[188:191], a[80:83], a[60:63], v[188:191]\nv_mfma_f32_16x16x32_bf16 a[252:255], a[84:87], a[60:63], a[252:255]\ns_waitcnt vmcnt(6) lgkmcnt(0)\ns_barrier\ns_cmp_eq_u32 %[cnt], 1\n" \
    "s_cbranch_scc1 L_Q4x%=\nv_mfma_f32_16x16x32_bf16 v[4:7], a[128:131], a[96:99], v[4:7]\ns_cmp_gt_u32 %[cnt], 2\ns_cselect_b32 s83, %[sx2], 0x70000000\n" \
    "v_mfma_f32_16x16x32_bf16 a[160:163], a[132:135], a[96:99], a[160:163]\ns_sub_u32 s81, s85, s84\ns_mov_b32 s87, s84\ns_add_u32 m0, %[m0x20], 0x8000\ns_add_u32 s80, s83, 0x0\n" \
    "buffer_load_dwordx4 v236, %[srdX2], s80 offen offset:0 sc1 lds\nv_mfma_f32_16x16x32_bf16 v[12:15], a[128:131], a[100:103], v[12:15]\ns_mov_b32 s84, s85\ns_mov_b32 s85, s86\n" \
    "v_mfma_f32_16x16x32_bf16 a[164:167], a[132:135], a[100:103], a[164:167]\ns_add_u32 %[sx2], %[sx2], 0x80\ns_mov_b32 s86, s87\nv_mfma_f32_16x16x32_bf16 v[20:23], a[128:131], a[104:107], v[20:23]\n" \
    "v_add_u32_e32 v228, s81, v228\nds_read_b128 a[32:35], v224 offset:0\nbuffer_load_dwordx4 v237, %[srdX2], s80 offen offset:1024 sc1 lds\nv_mfma_f32_16x16x32_bf16 a[168:171], a[132:135], a[104:107], a[168:171]\n" \
    "v_add_u32_e32 v229, s81, v229\nv_mfma_f32_16x16x32_bf16 v[28:31], a[128:131], a[108:111], v[28:31]\nv_add_u32_e32 v230, s81, v230\nds_read_b128 a[36:39], v224 offset:2048\n" \
    "v_mfma_f32_16x16x32_bf16 a[172:175], a[132:135], a[108:111], a[172:175]\nv_add_u32_e32 v231, s81, v231\nbuffer_load_dwordx4 v238, %[srdX2], s80 offen offset:2048 sc1 lds\n" \
    "v_mfma_f32_16x16x32_bf16 v[36:39], a[128:131], a[112:115], v[36:39]\nv_add_u32_e32 v232, s81, v232\nds_read_b128 a[40:43], v224 offset:4096\nv_mfma_f32_16x16x32_bf16 a[176:179], a[132:135], a[112:115], a[176:179]\n" \
    "v_add_u32_e32 v233, s81, v233\nv_mfma_f32_16x16x32_bf16 v[44:47], a[128:131], a[116:119], v[44:47]\nds_read_b128 a[44:47], v224 offset:6144\ns_sub_u32 %[cnt], %[cnt], 1\n" \
    "v_mfma_f32_16x16x32_bf16 a[180:183], a[132:135], a[116:119], a[180:183]\nbuffer_load_dwordx4 v239, %[srdX2], s80 offen offset:3072 sc1 lds\nv_mfma_f32_16x16x32_bf16 v[52:55], a[128:131], a[120:123], v[52:55]\n" \
    "ds_read_b128 a[48:51], v224 offset:8192\nv_mfma_f32_16x16x32_bf16 a[184:187], a[132:135], a[120:123], a[184:187]\nv_mfma_f32_16x16x32_bf16 v[60:63], a[128:131], a[124:127], v[60:63]\n" \
    "ds_read_b128 a[52:55], v224 offset:10240\ns_add_u32 m0, %[m0x20], 0x9000\ns_add_u32 s80, s83, 0xa000\nbuffer_load_dwordx4 v236, %[srdX2], s80 offen offset:0 sc1 lds\n" \
    "v_mfma_f32_16x16x32_bf16 a[188:191], a[132:135], a[124:127], a[188:191]\nv_mfma_f32_16x16x32_bf16 v[68:71], a[136:139], a[96:99], v[68:71]\nds_read_b128 a[56:59], v224 offset:12288\n" \
    "v_mfma_f32_16x16x32_bf16 a[192:195], a[140:143], a[96:99], a[192:195]\nbuffer_load_dwordx4 v237, %[srdX2], s80 offen offset:1024 sc1 lds\nv_mfma_f32_16x16x32_bf16 v[76:79], a[136:139], a[100:103], v[76:79]\n" \
    "ds_read_b128 a[60:63], v224 offset:14336\nv_mfma_f32_16x16x32_bf16 a[196:199], a[140:143], a[100:103], a[196:199]\nv_mfma_f32_16x16x32_bf16 v[84:87], a[136:139], a[104:107], v[84:87]\n" \
    "ds_read_b64_tr_b16 a[64:65], v228 offset:0\nv_mfma_f32_16x16x32_bf16 a[200:203], a[140:143], a[104:107], a[200:203]\nds_read_b64_tr_b16 a[66:67], v228 offset:1536\n" \
    "buffer_load_dwordx4 v238, %[srdX2], s80 offen offset:2048 sc1 lds\nv_mfma_f32_16x16x32_bf16 v[92:95], a[136:139], a[108:111], v[92:95]\nds_read_b64_tr_b16 a[68:69], v229 offset:0\n" \
    "v_mfma_f32_16x16x32_bf16 a[204:207], a[140:143], a[108:111], a[204:207]\nds_read_b64_tr_b16 a[70:71], v229 offset:1536\nv_mfma_f32_16x16x32_bf16 v[100:103], a[136:139], a[112:115], v[100:103]\n" \
    "ds_read_b64_tr_b16 a[72:73], v230 offset:0\nbuffer_load_dwordx4 v239, %[srdX2], s80 offen offset:3072 sc1 lds\nv_mfma_f32_16x16x32_bf16 a[208:211], a[140:143], a[112:115], a[208:211]\n" \
    "ds_read_b64_tr_b16 a[74:75], v230 offset:1536\nv_mfma_f32_16x16x32_bf16 v[108:111], a[136:139], a[116:119], v[108:111]\nv_mfma_f32_16x16x32_bf16 a[212:215], a[140:143], a[116:119], a[212:215]\n" \
    "ds_read_b64_tr_b16 a[76:77], v231 offset:0\nv_mfma_f32_16x16x32_bf16 v[116:119], a[136:139], a[120:123], v[116:119]\nds_read_b64_tr_b16 a[78:79], v231 offset:1536\n" \
    "v_mfma_f32_16x16x32_bf16 a[216:219], a[140:143], a[120:123], a[216:219]\nds_read_b64_tr_b16 a[80:81], v232 offset:0\nv_mfma_f32_16x16x32_bf16 v[124:127], a[136:139], a[124:127], v[124:127]\n" \
    "ds_read_b64_tr_b16 a[82:83], v232 offset:1536\nv_mfma_f32_16x16x32_bf16 a[220:223], a[140:143], a[124:127], a[220:223]\nds_read_b64_tr_b16 a[84:85], v233 offset:0\n" \
    "v_mfma_f32_16x16x32_bf16 v[132:135], a[144:147], a[96:99], v[132:135]\nds_read_b64_tr_b16 a[86:87], v233 offset:1536\nv_mfma_f32_16x16x32_bf16 a[224:227], a[148:151], a[96:99], a[224:227]\n" \
    "v_mfma_f32_16x16x32_bf16 v[140:143], a[144:147], a[100:103], v[140:143]\nv_mfma_f32_16x16x32_bf16 a[228:231], a[148:151], a[100:103], a[228:231]\nv_mfma_f32_16x16x32_bf16 v[148:151], a[144:147], a[104:107], v[148:151]\n" \
    "v_mfma_f32_16x16x32_bf16 a[232:235], a[148:151], a[104:107], a[232:235]\nv_mfma_f32_16x16x32_bf16 v[156:159], a[144:147], a[108:111], v[156:159]\nv_mfma_f32_16x16x32_bf16 a[236:239], a[148:151], a[108:111], a[236:239]\n" \
    "v_mfma_f32_16x16x32_bf16 v[164:167], a[144:147], a[112:115], v[164:167]\nv_mfma_f32_16x16x32_bf16 a[240:243], a[148:151], a[112:115], a[240:243]\nv_mfma_f32_16x16x32_bf16 v[172:175], a[144:147], a[116:119], v[172:175]\n" \
    "v_mfma_f32_16x16x32_bf16 a[244:247], a[148:151], a[116:119], a[244:247]\nv_mfma_f32_16x16x32_bf16 v[180:183], a[144:147], a[120:123], v[180:183]\nv_mfma_f32_16x16x32_bf16 a[248:251], a[148:151], a[120:123], a[248:251]\n" \
    "v_mfma_f32_16x16x32_bf16 v[188:191], a[144:147], a[124:127], v[188:191]\nv_mfma_f32_16x16x32_bf16 a[252:255], a[148:151], a[124:127], a[252:255]\ns_waitcnt lgkmcnt(0)\n" \
    "v_mfma_f32_16x16x32_bf16 v[4:7], a[64:67], a[32:35], v[4:7]\nds_read_b128 a[96:99], v225 offset:0\nv_mfma_f32_16x16x32_bf16 a[160:163], a[68:71], a[32:35], a[160:163]\nds_read_b128 a[100:103], v225 offset:2048\n" \
    "s_cmp_gt_u32 %[cnt], 2\ns_cselect_b32 s83, %[sw2], 0x70000000\nv_mfma_f32_16x16x32_bf16 v[12:15], a[64:67], a[36:39], v[12:15]\ns_add_u32 %[sw2], %[sw2], 0x80000\n" \
    "v_mfma_f32_16x16x32_bf16 a[164:167], a[68:71], a[36:39], a[164:167]\nds_read_b128 a[104:107], v225 offset:4096\ns_add_u32 m0, %[m0w2], s86\ns_add_u32 s81, s83, 0x0\n" \
    "buffer_load_dwordx4 v240, %[srdW2], s81 offen offset:0 lds\nv_mfma_f32_16x16x32_bf16 v[20:23], a[64:67], a[40:43], v[20:23]\nv_mfma_f32_16x16x32_bf16 a[168:171], a[68:71], a[40:43], a[168:171]\n" \
    "ds_read_b128 a[108:111], v225 offset:6144\nv_mfma_f32_16x16x32_bf16 v[28:31], a[64:67], a[44:47], v[28:31]\nds_read_b128 a[112:115], v225 offset:8192\nv_mfma_f32_16x16x32_bf16 a[172:175], a[68:71], a[44:47], a[172:175]\n" \
    "v_mfma_f32_16x16x32_bf16 v[36:39], a[64:67], a[48:51], v[36:39]\nds_read_b128 a[116:119], v225 offset:10240\nv_mfma_f32_16x16x32_bf16 a[176:179], a[68:71], a[48:51], a[176:179]\n" \
    "buffer_load_dwordx4 v241, %[srdW2], s81 offen offset:1024 lds\nv_mfma_f32_16x16x32_bf16 v[44:47], a[64:67], a[52:55], v[44:47]\nds_read_b128 a[120:123], v225 offset:12288\n" \
    "v_mfma_f32_16x16x32_bf16 a[180:183], a[68:71], a[52:55], a[180:183]\nds_read_b128 a[124:127], v225 offset:14336\nv_mfma_f32_16x16x32_bf16 v[52:55], a[64:67], a[56:59], v[52:55]\n" \
    "v_mfma_f32_16x16x32_bf16 a[184:187], a[68:71], a[56:59], a[184:187]\nds_read_b64_tr_b16 a[128:129], v228 offset:12288\nv_mfma_f32_16x16x32_bf16 v[60:63], a[64:67], a[60:63], v[60:63]\n" \
    "v_mfma_f32_16x16x32_bf16 a[188:191], a[68:71], a[60:63], a[188:191]\nds_read_b64_tr_b16 a[130:131], v228 offset:13824\nbuffer_load_dwordx4 v242, %[srdW2], s81 offen offset:2048 lds\n" \
    "v_mfma_f32_16x16x32_bf16 v[68:71], a[72:75], a[32:35], v[68:71]\nv_mfma_f32_16x16x32_bf16 a[192:195], a[76:79], a[32:35], a[192:195]\nds_read_b64_tr_b16 a[132:133], v229 offset:12288\n" \
    "v_mfma_f32_16x16x32_bf16 v[76:79], a[72:75], a[36:39], v[76:79]\nds_read_b64_tr_b16 a[134:135], v229 offset:13824\nv_mfma_f32_16x16x32_bf16 a[196:199], a[76:79], a[36:39], a[196:199]\n" \
    "v_mfma_f32_16x16x32_bf16 v[84:87], a[72:75], a[40:43], v[84:87]\nds_read_b64_tr_b16 a[136:137], v230 offset:12288\nv_mfma_f32_16x16x32_bf16 a[200:203], a[76:79], a[40:43], a[200:203]\ns_add_u32 m0, m0, 0x1800\n" \
    "s_add_u32 s81, s83, 0x20000\nbuffer_load_dwordx4 v240, %[srdW2], s81 offen offset:0 lds\nv_mfma_f32_16x16x32_bf16 v[92:95], a[72:75], a[44:47], v[92:95]\nds_read_b64_tr_b16 a[138:139], v230 offset:13824\n" \
    "v_mfma_f32_16x16x32_bf16 a[204:207], a[76:79], a[44:47], a[204:207]\nds_read_b64_tr_b16 a[140:141], v231 offset:12288\nv_mfma_f32_16x16x32_bf16 v[100:103], a[72:75], a[48:51], v[100:103]\n" \
    "v_mfma_f32_16x16x32_bf16 a[208:211], a[76:79], a[48:51], a[208:211]\nds_read_b64_tr_b16 a[142:143], v231 offset:13824\nv_mfma_f32_16x16x32_bf16 v[108:111], a[72:75], a[52:55], v[108:111]\n" \
    "v_mfma_f32_16x16x32_bf16 a[212:215], a[76:79], a[52:55], a[212:215]\nds_read_b64_tr_b16 a[144:145], v232 offset:12288\nbuffer_load_dwordx4 v241, %[srdW2], s81 offen offset:1024 lds\n" \
    "v_mfma_f32_16x16x32_bf16 v[116:119], a[72:75], a[56:59], v[116:119]\nds_read_b64_tr_b16 a[146:147], v232 offset:13824\nv_mfma_f32_16x16x32_bf16 a[216:219], a[76:79], a[56:59], a[216:219]\n" \
    "v_mfma_f32_16x16x32_bf16 v[124:127], a[72:75], a[60:63], v[124:127]\nds_read_b64_tr_b16 a[148:149], v233 offset:12288\nv_mfma_f32_16x16x32_bf16 a[220:223], a[76:79], a[60:63], a[220:223]\n" \
    "v_mfma_f32_16x16x32_bf16 v[132:135], a[80:83], a[32:35], v[132:135]\nds_read_b64_tr_b16 a[150:151], v233 offset:13824\nv_mfma_f32_16x16x32_bf16 a[224:227], a[84:87], a[32:35], a[224:227]\n" \
    "buffer_load_dwordx4 v242, %[srdW2], s81 offen offset:2048 lds\nv_mfma_f32_16x16x32_bf16 v[140:143], a[80:83], a[36:39], v[140:143]\nv_mfma_f32_16x16x32_bf16 a[228:231], a[84:87], a[36:39], a[228:231]\n" \
    "v_mfma_f32_16x16x32_bf16 v[148:151], a[80:83], a[40:43], v[148:151]\nv_mfma_f32_16x16x32_bf16 a[232:235], a[84:87], a[40:43], a[232:235]\nv_mfma_f32_16x16x32_bf16 v[156:159], a[80:83], a[44:47], v[156:159]\n" \
    "v_mfma_f32_16x16x32_bf16 a[236:239], a[84:87], a[44:47], a[236:239]\nv_mfma_f32_16x16x32_bf16 v[164:167], a[80:83], a[48:51], v[164:167]\nv_mfma_f32_16x16x32_bf16 a[240:243], a[84:87], a[48:51], a[240:243]\n" \
    "v_mfma_f32_16x16x32_bf16 v[172:175], a[80:83], a[52:55], v[172:175]\nv_mfma_f32_16x16x32_bf16 a[244:247], a[84:87], a[52:55], a[244:247]\nv_mfma_f32_16x16x32_bf16 v[180:183], a[80:83], a[56:59], v[180:183]\n" \
    "v_mfma_f32_16x16x32_bf16 a[248:251], a[84:87], a[56:59], a[248:251]\nv_mfma_f32_16x16x32_bf16 v[188:191], a[80:83], a[60:63], v[188:191]\nv_mfma_f32_16x16x32_bf16 a[252:255], a[84:87], a[60:63], a[252:255]\n" \
    "s_waitcnt vmcnt(6) lgkmcnt(0)\ns_barrier\ns_branch L_Q4l%=\nL_Q4x%=:\nv_mfma_f32_16x16x32_bf16 v[4:7], a[128:131], a[96:99], v[4:7]\nv_mfma_f32_16x16x32_bf16 a[160:163], a[132:135], a[96:99], a[160:163]\n" \
    "v_mfma_f32_16x16x32_bf16 v[12:15], a[128:131], a[100:103], v[12:15]\nv_mfma_f32_16x16x32_bf16 a[164:167], a[132:135], a[100:103], a[164:167]\nv_mfma_f32_16x16x32_bf16 v[20:23], a[128:131], a[104:107], v[20:23]\n" \
    "v_mfma_f32_16x16x32_bf16 a[168:171], a[132:135], a[104:107], a[168:171]\nv_mfma_f32_16x16x32_bf16 v[28:31], a[128:131], a[108:111], v[28:31]\nv_mfma_f32_16x16x32_bf16 a[172:175], a[132:135], a[108:111], a[172:175]\n" \
    "v_accvgpr_read_b32 v246, a160\nv_accvgpr_read_b32 v247, a161\nv_mfma_f32_16x16x32_bf16 v[36:39], a[128:131], a[112:115], v[36:39]\nv_accvgpr_read_b32 v248, a162\nv_accvgpr_read_b32 v249, a163\n" \
    "v_mfma_f32_16x16x32_bf16 a[176:179], a[132:135], a[112:115], a[176:179]\nv_mul_f32_e32 v4, %[scl], v4\nv_mul_f32_e32 v5, %[scl], v5\nv_mfma_f32_16x16x32_bf16 v[44:47], a[128:131], a[116:119], v[44:47]\n" \
    "v_mul_f32_e32 v6, %[scl], v6\nv_mul_f32_e32 v7, %[scl], v7\nv_mfma_f32_16x16x32_bf16 a[180:183], a[132:135], a[116:119], a[180:183]\nv_mul_f32_e32 v246, %[scl], v246\nv_mul_f32_e32 v247, %[scl], v247\n" \
    "v_mfma_f32_16x16x32_bf16 v[52:55], a[128:131], a[120:123], v[52:55]\nv_mul_f32_e32 v248, %[scl], v248\nv_mul_f32_e32 v249, %[scl], v249\nv_mfma_f32_16x16x32_bf16 a[184:187], a[132:135], a[120:123], a[184:187]\n" \
    "v_cvt_pk_bf16_f32 v4, v4, v5\nv_cvt_pk_bf16_f32 v5, v6, v7\nv_mfma_f32_16x16x32_bf16 v[60:63], a[128:131], a[124:127], v[60:63]\nv_cvt_pk_bf16_f32 v6, v246, v247\nv_cvt_pk_bf16_f32 v7, v248, v249\n" \
    "v_mfma_f32_16x16x32_bf16 a[188:191], a[132:135], a[124:127], a[188:191]\ns_add_u32 s82, %[so20], 0x0\ns_nop 0\nv_permlane16_swap_b32_e32 v4, v6\nv_mfma_f32_16x16x32_bf16 v[68:71], a[136:139], a[96:99], v[68:71]\n" \
    "v_permlane16_swap_b32_e32 v5, v7\nbuffer_store_dwordx4 v[4:7], v244, %[srdO2], s82 offen sc1\nv_mfma_f32_16x16x32_bf16 a[192:195], a[140:143], a[96:99], a[192:195]\nv_accvgpr_read_b32 v250, a164\n" \
    "v_accvgpr_read_b32 v251, a165\nv_mfma_f32_16x16x32_bf16 v[76:79], a[136:139], a[100:103], v[76:79]\nv_accvgpr_read_b32 v252, a166\nv_accvgpr_read_b32 v253, a167\n" \
    "v_mfma_f32_16x16x32_bf16 a[196:199], a[140:143], a[100:103], a[196:199]\nv_mul_f32_e32 v12, %[scl], v12\nv_mul_f32_e32 v13, %[scl], v13\nv_mfma_f32_16x16x32_bf16 v[84:87], a[136:139], a[104:107], v[84:87]\n" \
    "v_mul_f32_e32 v14, %[scl], v14\nv_mul_f32_e32 v15, %[scl], v15\nv_mfma_f32_16x16x32_bf16 a[200:203], a[140:143], a[104:107], a[200:203]\nv_mul_f32_e32 v250, %[scl], v250\nv_mul_f32_e32 v251, %[scl], v251\n" \
    "v_mfma_f32_16x16x32_bf16 v[92:95], a[136:139], a[108:111], v[92:95]\nv_mul_f32_e32 v252, %[scl], v252\nv_mul_f32_e32 v253, %[scl], v253\nv_mfma_f32_16x16x32_bf16 a[204:207], a[140:143], a[108:111], a[204:207]\n" \
    "v_cvt_pk_bf16_f32 v12, v12, v13\nv_cvt_pk_bf16_f32 v13, v14, v15\nv_mfma_f32_16x16x32_bf16 v[100:103], a[136:139], a[112:115], v[100:103]\nv_cvt_pk_bf16_f32 v14, v250, v251\nv_cvt_pk_bf16_f32 v15, v252, v253\n" \
    "v_mfma_f32_16x16x32_bf16 a[208:211], a[140:143], a[112:115], a[208:211]\ns_add_u32 s82, %[so20], 0x1000\ns_nop 0\nv_permlane16_swap_b32_e32 v12, v14\n" \
    "v_mfma_f32_16x16x32_bf16 v[108:111], a[136:139], a[116:119], v[108:111]\nv_permlane16_swap_b32_e32 v13, v15\nbuffer_store_dwordx4 v[12:15], v244, %[srdO2], s82 offen sc1\n" \
    "v_mfma_f32_16x16x32_bf16 a[212:215], a[140:143], a[116:119], a[212:215]\nv_accvgpr_read_b32 v246, a168\nv_accvgpr_read_b32 v247, a169\nv_mfma_f32_16x16x32_bf16 v[116:119], a[136:139], a[120:123], v[116:119]\n" \
    "v_accvgpr_read_b32 v248, a170\nv_accvgpr_read_b32 v249, a171\nv_mfma_f32_16x16x32_bf16 a[216:219], a[140:143], a[120:123], a[216:219]\nv_mul_f32_e32 v20, %[scl], v20\nv_mul_f32_e32 v21, %[scl], v21\n" \
    "v_mfma_f32_16x16x32_bf16 v[124:127], a[136:139], a[124:127], v[124:127]\nv_mul_f32_e32 v22, %[scl], v22\nv_mul_f32_e32 v23, %[scl], v23\nv_mfma_f32_16x16x32_bf16 a[220:223], a[140:143], a[124:127], a[220:223]\n" \
    "v_mul_f32_e32 v246, %[scl], v246\nv_mul_f32_e32 v247, %[scl], v247\nv_mfma_f32_16x16x32_bf16 v[132:135], a[144:147], a[96:99], v[132:135]\nv_mul_f32_e32 v248, %[scl], v248\nv_mul_f32_e32 v249, %[scl], v249\n" \
    "v_mfma_f32_16x16x32_bf16 a[224:227], a[148:151], a[96:99], a[224:227]\nv_cvt_pk_bf16_f32 v20, v20, v21\nv_cvt_pk_bf16_f32 v21, v22, v23\nv_mfma_f32_16x16x32_bf16 v[140:143], a[144:147], a[100:103], v[140:143]\n" \
    "v_cvt_pk_bf16_f32 v22, v246, v247\nv_cvt_pk_bf16_f32 v23, v248, v249\nv_mfma_f32_16x16x32_bf16 a[228:231], a[148:151], a[100:103], a[228:231]\ns_add_u32 s82, %[so20], 0x2000\ns_nop 0\nv_permlane16_swap_b32_e32 v20, v22\n" \
    "v_mfma_f32_16x16x32_bf16 v[148:151], a[144:147], a[104:107], v[148:151]\nv_permlane16_swap_b32_e32 v21, v23\nbuffer_store_dwordx4 v[20:23], v244, %[srdO2], s82 offen sc1\n" \
    "v_mfma_f32_16x16x32_bf16 a[232:235], a[148:151], a[104:107], a[232:235]\nv_accvgpr_read_b32 v250, a172\nv_accvgpr_read_b32 v251, a173\nv_mfma_f32_16x16x32_bf16 v[156:159], a[144:147], a[108:111], v[156:159]\n" \
    "v_accvgpr_read_b32 v252, a174\nv_accvgpr_read_b32 v253, a175\nv_mfma_f32_16x16x32_bf16 a[236:239], a[148:151], a[108:111], a[236:239]\nv_mul_f32_e32 v28, %[scl], v28\nv_mul_f32_e32 v29, %[scl], v29\n" \
    "v_mfma_f32_16x16x32_bf16 v[164:167], a[144:147], a[112:115], v[164:167]\nv_mul_f32_e32 v30, %[scl], v30\nv_mul_f32_e32 v31, %[scl], v31\nv_mfma_f32_16x16x32_bf16 a[240:243], a[148:151], a[112:115], a[240:243]\n" \
    "v_mul_f32_e32 v250, %[scl], v250\nv_mul_f32_e32 v251, %[scl], v251\nv_mfma_f32_16x16x32_bf16 v[172:175], a[144:147], a[116:119], v[172:175]\nv_mul_f32_e32 v252, %[scl], v252\nv_mul_f32_e32 v253, %[scl], v253\n" \
    "v_mfma_f32_16x16x32_bf16 a[244:247], a[148:151], a[116:119], a[244:247]\nv_cvt_pk_bf16_f32 v28, v28, v29\nv_cvt_pk_bf16_f32 v29, v30, v31\nv_mfma_f32_16x16x32_bf16 v[180:183], a[144:147], a[120:123], v[180:183]\n" \
    "v_cvt_pk_bf16_f32 v30, v250, v251\nv_cvt_pk_bf16_f32 v31, v252, v253\nv_mfma_f32_16x16x32_bf16 a[248:251], a[148:151], a[120:123], a[248:251]\ns_add_u32 s82, %[so20], 0x3000\ns_nop 0\nv_permlane16_swap_b32_e32 v28, v30\n" \
    "v_mfma_f32_16x16x32_bf16 v[188:191], a[144:147], a[124:127], v[188:191]\nv_permlane16_swap_b32_e32 v29, v31\nbuffer_store_dwordx4 v[28:31], v244, %[srdO2], s82 offen sc1\n" \
    "v_mfma_f32_16x16x32_bf16 a[252:255], a[148:151], a[124:127], a[252:255]\nv_accvgpr_read_b32 v246, a176\nv_accvgpr_read_b32 v247, a177\ns_nop 7\ns_nop 3\nv_accvgpr_read_b32 v248, a178\nv_accvgpr_read_b32 v249, a179\n" \
    "v_mul_f32_e32 v36, %[scl], v36\nv_mul_f32_e32 v37, %[scl], v37\nv_mul_f32_e32 v38, %[scl], v38\nv_mul_f32_e32 v39, %[scl], v39\nv_mul_f32_e32 v246, %[scl], v246\nv_mul_f32_e32 v247, %[scl], v247\n" \
    "v_mul_f32_e32 v248, %[scl], v248\nv_mul_f32_e32 v249, %[scl], v249\nv_cvt_pk_bf16_f32 v36, v36, v37\nv_cvt_pk_bf16_f32 v37, v38, v39\nv_cvt_pk_bf16_f32 v38, v246, v247\nv_cvt_pk_bf16_f32 v39, v248, v249\n" \
    "s_add_u32 s82, %[so20], 0x4000\ns_nop 0\nv_permlane16_swap_b32_e32 v36, v38\nv_permlane16_swap_b32_e32 v37, v39\nbuffer_store_dwordx4 v[36:39], v244, %[srdO2], s82 offen sc1\nv_accvgpr_read_b32 v250, a180\n" \
    "v_accvgpr_read_b32 v251, a181\nv_accvgpr_read_b32 v252, a182\nv_accvgpr_read_b32 v253, a183\nv_mul_f32_e32 v44, %[scl], v44\nv_mul_f32_e32 v45, %[scl], v45\nv_mul_f32_e32 v46, %[scl], v46\n" \
    "v_mul_f32_e32 v47, %[scl], v47\nv_mul_f32_e32 v250, %[scl], v250\nv_mul_f32_e32 v251, %[scl], v251\nv_mul_f32_e32 v252, %[scl], v252\nv_mul_f32_e32 v253, %[scl], v253\nv_cvt_pk_bf16_f32 v44, v44, v45\n" \
    "v_cvt_pk_bf16_f32 v45, v46, v47\nv_cvt_pk_bf16_f32 v46, v250, v251\nv_cvt_pk_bf16_f32 v47, v252, v253\ns_add_u32 s82, %[so20], 0x5000\ns_nop 0\nv_permlane16_swap_b32_e32 v44, v46\nv_permlane16_swap_b32_e32 v45, v47\n" \
    "buffer_store_dwordx4 v[44:47], v244, %[srdO2], s82 offen sc1\nv_accvgpr_read_b32 v246, a184\nv_accvgpr_read_b32 v247, a185\nv_accvgpr_read_b32 v248, a186\nv_accvgpr_read_b32 v249, a187\nv_mul_f32_e32 v52, %[scl], v52\n" \
    "v_mul_f32_e32 v53, %[scl], v53\nv_mul_f32_e32 v54, %[scl], v54\nv_mul_f32_e32 v55, %[scl], v55\nv_mul_f32_e32 v246, %[scl], v246\nv_mul_f32_e32 v247, %[scl], v247\nv_mul_f32_e32 v248, %[scl], v248\n" \
    "v_mul_f32_e32 v249, %[scl], v249\nv_cvt_pk_bf16_f32 v52, v52, v53\nv_cvt_pk_bf16_f32 v53, v54, v55\nv_cvt_pk_bf16_f32 v54, v246, v247\nv_cvt_pk_bf16_f32 v55, v248, v249\ns_add_u32 s82, %[so20], 0x6000\ns_nop 0\n" \
    "v_permlane16_swap_b32_e32 v52, v54\nv_permlane16_swap_b32_e32 v53, v55\nbuffer_store_dwordx4 v[52:55], v244, %[srdO2], s82 offen sc1\nv_accvgpr_read_b32 v250, a188\nv_accvgpr_read_b32 v251, a189\n" \
    "v_accvgpr_read_b32 v252, a190\nv_accvgpr_read_b32 v253, a191\nv_mul_f32_e32 v60, %[scl], v60\nv_mul_f32_e32 v61, %[scl], v61\nv_mul_f32_e32 v62, %[scl], v62\nv_mul_f32_e32 v63, %[scl], v63\n" \
    "v_mul_f32_e32 v250, %[scl], v250\nv_mul_f32_e32 v251, %[scl], v251\nv_mul_f32_e32 v252, %[scl], v252\nv_mul_f32_e32 v253, %[scl], v253\nv_cvt_pk_bf16_f32 v60, v60, v61\nv_cvt_pk_bf16_f32 v61, v62, v63\n" \
    "v_cvt_pk_bf16_f32 v62, v250, v251\nv_cvt_pk_bf16_f32 v63, v252, v253\ns_add_u32 s82, %[so20], 0x7000\ns_nop 0\nv_permlane16_swap_b32_e32 v60, v62\nv_permlane16_swap_b32_e32 v61, v63\n" \
    "buffer_store_dwordx4 v[60:63], v244, %[srdO2], s82 offen sc1\nv_accvgpr_read_b32 v246, a192\nv_accvgpr_read_b32 v247, a193\nv_accvgpr_read_b32 v248, a194\nv_accvgpr_read_b32 v249, a195\nv_mul_f32_e32 v68, %[scl], v68\n" \
    "v_mul_f32_e32 v69, %[scl], v69\nv_mul_f32_e32 v70, %[scl], v70\nv_mul_f32_e32 v71, %[scl], v71\nv_mul_f32_e32 v246, %[scl], v246\nv_mul_f32_e32 v247, %[scl], v247\nv_mul_f32_e32 v248, %[scl], v248\n" \
    "v_mul_f32_e32 v249, %[scl], v249\nv_cvt_pk_bf16_f32 v68, v68, v69\nv_cvt_pk_bf16_f32 v69, v70, v71\nv_cvt_pk_bf16_f32 v70, v246, v247\nv_cvt_pk_bf16_f32 v71, v248, v249\ns_add_u32 s82, %[so21], 0x0\ns_nop 0\n" \
    "v_permlane16_swap_b32_e32 v68, v70\nv_permlane16_swap_b32_e32 v69, v71\nbuffer_store_dwordx4 v[68:71], v244, %[srdO2], s82 offen sc1\nv_accvgpr_read_b32 v250, a196\nv_accvgpr_read_b32 v251, a197\n" \
    "v_accvgpr_read_b32 v252, a198\nv_accvgpr_read_b32 v253, a199\nv_mul_f32_e32 v76, %[scl], v76\nv_mul_f32_e32 v77, %[scl], v77\nv_mul_f32_e32 v78, %[scl], v78\nv_mul_f32_e32 v79, %[scl], v79\n" \
    "v_mul_f32_e32 v250, %[scl], v250\nv_mul_f32_e32 v251, %[scl], v251\nv_mul_f32_e32 v252, %[scl], v252\nv_mul_f32_e32 v253, %[scl], v253\nv_cvt_pk_bf16_f32 v76, v76, v77\nv_cvt_pk_bf16_f32 v77, v78, v79\n" \
    "v_cvt_pk_bf16_f32 v78, v250, v251\nv_cvt_pk_bf16_f32 v79, v252, v253\ns_add_u32 s82, %[so21], 0x1000\ns_nop 0\nv_permlane16_swap_b32_e32 v76, v78\nv_permlane16_swap_b32_e32 v77, v79\n" \
    "buffer_store_dwordx4 v[76:79], v244, %[srdO2], s82 offen sc1\nv_accvgpr_read_b32 v246, a200\nv_accvgpr_read_b32 v247, a201\nv_accvgpr_read_b32 v248, a202\nv_accvgpr_read_b32 v249, a203\nv_mul_f32_e32 v84, %[scl], v84\n" \
    "v_mul_f32_e32 v85, %[scl], v85\nv_mul_f32_e32 v86, %[scl], v86\nv_mul_f32_e32 v87, %[scl], v87\nv_mul_f32_e32 v246, %[scl], v246\nv_mul_f32_e32 v247, %[scl], v247\nv_mul_f32_e32 v248, %[scl], v248\n" \
    "v_mul_f32_e32 v249, %[scl], v249\nv_cvt_pk_bf16_f32 v84, v84, v85\nv_cvt_pk_bf16_f32 v85, v86, v87\nv_cvt_pk_bf16_f32 v86, v246, v247\nv_cvt_pk_bf16_f32 v87, v248, v249\ns_add_u32 s82, %[so21], 0x2000\ns_nop 0\n" \
    "v_permlane16_swap_b32_e32 v84, v86\nv_permlane16_swap_b32_e32 v85, v87\nbuffer_store_dwordx4 v[84:87], v244, %[srdO2], s82 offen sc1\nv_accvgpr_read_b32 v250, a204\nv_accvgpr_read_b32 v251, a205\n" \
    "v_accvgpr_read_b32 v252, a206\nv_accvgpr_read_b32 v253, a207\nv_mul_f32_e32 v92, %[scl], v92\nv_mul_f32_e32 v93, %[scl], v93\nv_mul_f32_e32 v94, %[scl], v94\nv_mul_f32_e32 v95, %[scl], v95\n" \
    "v_mul_f32_e32 v250, %[scl], v250\nv_mul_f32_e32 v251, %[scl], v251\nv_mul_f32_e32 v252, %[scl], v252\nv_mul_f32_e32 v253, %[scl], v253\nv_cvt_pk_bf16_f32 v92, v92, v93\nv_cvt_pk_bf16_f32 v93, v94, v95\n" \
    "v_cvt_pk_bf16_f32 v94, v250, v251\nv_cvt_pk_bf16_f32 v95, v252, v253\ns_add_u32 s82, %[so21], 0x3000\ns_nop 0\nv_permlane16_swap_b32_e32 v92, v94\nv_permlane16_swap_b32_e32 v93, v95\n" \
    "buffer_store_dwordx4 v[92:95], v244, %[srdO2], s82 offen sc1\nv_accvgpr_read_b32 v246, a208\nv_accvgpr_read_b32 v247, a209\nv_accvgpr_read_b32 v248, a210\nv_accvgpr_read_b32 v249, a211\nv_mul_f32_e32 v100, %[scl], v100\n" \
    "v_mul_f32_e32 v101, %[scl], v101\nv_mul_f32_e32 v102, %[scl], v102\nv_mul_f32_e32 v103, %[scl], v103\nv_mul_f32_e32 v246, %[scl], v246\nv_mul_f32_e32 v247, %[scl], v247\nv_mul_f32_e32 v248, %[scl], v248\n" \
    "v_mul_f32_e32 v249, %[scl], v249\nv_cvt_pk_bf16_f32 v100, v100, v101\nv_cvt_pk_bf16_f32 v101, v102, v103\nv_cvt_pk_bf16_f32 v102, v246, v247\nv_cvt_pk_bf16_f32 v103, v248, v249\ns_add_u32 s82, %[so21], 0x4000\ns_nop 0\n" \
    "v_permlane16_swap_b32_e32 v100, v102\nv_permlane16_swap_b32_e32 v101, v103\nbuffer_store_dwordx4 v[100:103], v244, %[srdO2], s82 offen sc1\nv_accvgpr_read_b32 v250, a212\nv_accvgpr_read_b32 v251, a213\n" \
    "v_accvgpr_read_b32 v252, a214\nv_accvgpr_read_b32 v253, a215\nv_mul_f32_e32 v108, %[scl], v108\nv_mul_f32_e32 v109, %[scl], v109\nv_mul_f32_e32 v110, %[scl], v110\nv_mul_f32_e32 v111, %[scl], v111\n" \
    "v_mul_f32_e32 v250, %[scl], v250\nv_mul_f32_e32 v251, %[scl], v251\nv_mul_f32_e32 v252, %[scl], v252\nv_mul_f32_e32 v253, %[scl], v253\nv_cvt_pk_bf16_f32 v108, v108, v109\nv_cvt_pk_bf16_f32 v109, v110, v111\n" \
    "v_cvt_pk_bf16_f32 v110, v250, v251\nv_cvt_pk_bf16_f32 v111, v252, v253\ns_add_u32 s82, %[so21], 0x5000\ns_nop 0\nv_permlane16_swap_b32_e32 v108, v110\nv_permlane16_swap_b32_e32 v109, v111\n" \
    "buffer_store_dwordx4 v[108:111], v244, %[srdO2], s82 offen sc1\nv_accvgpr_read_b32 v246, a216\nv_accvgpr_read_b32 v247, a217\nv_accvgpr_read_b32 v248, a218\nv_accvgpr_read_b32 v249, a219\n" \
    "v_mul_f32_e32 v116, %[scl], v116\nv_mul_f32_e32 v117, %[scl], v117\nv_mul_f32_e32 v118, %[scl], v118\nv_mul_f32_e32 v119, %[scl], v119\nv_mul_f32_e32 v246, %[scl], v246\nv_mul_f32_e32 v247, %[scl], v247\n" \
    "v_mul_f32_e32 v248, %[scl], v248\nv_mul_f32_e32 v249, %[scl], v249\nv_cvt_pk_bf16_f32 v116, v116, v117\nv_cvt_pk_bf16_f32 v117, v118, v119\nv_cvt_pk_bf16_f32 v118, v246, v247\nv_cvt_pk_bf16_f32 v119, v248, v249\n" \
    "s_add_u32 s82, %[so21], 0x6000\ns_nop 0\nv_permlane16_swap_b32_e32 v116, v118\nv_permlane16_swap_b32_e32 v117, v119\nbuffer_store_dwordx4 v[116:119], v244, %[srdO2], s82 offen sc1\nv_accvgpr_read_b32 v250, a220\n" \
    "v_accvgpr_read_b32 v251, a221\nv_accvgpr_read_b32 v252, a222\nv_accvgpr_read_b32 v253, a223\nv_mul_f32_e32 v124, %[scl], v124\nv_mul_f32_e32 v125, %[scl], v125\nv_mul_f32_e32 v126, %[scl], v126\n" \
    "v_mul_f32_e32 v127, %[scl], v127\nv_mul_f32_e32 v250, %[scl], v250\nv_mul_f32_e32 v251, %[scl], v251\nv_mul_f32_e32 v252, %[scl], v252\nv_mul_f32_e32 v253, %[scl], v253\nv_cvt_pk_bf16_f32 v124, v124, v125\n" \
    "v_cvt_pk_bf16_f32 v125, v126, v127\nv_cvt_pk_bf16_f32 v126, v250, v251\nv_cvt_pk_bf16_f32 v127, v252, v253\ns_add_u32 s82, %[so21], 0x7000\ns_nop 0\nv_permlane16_swap_b32_e32 v124, v126\n" \
    "v_permlane16_swap_b32_e32 v125, v127\nbuffer_store_dwordx4 v[124:127], v244, %[srdO2], s82 offen sc1\nv_accvgpr_read_b32 v246, a224\nv_accvgpr_read_b32 v247, a225\nv_accvgpr_read_b32 v248, a226\n" \
    "v_accvgpr_read_b32 v249, a227\nv_mul_f32_e32 v132, %[scl], v132\nv_mul_f32_e32 v133, %[scl], v133\nv_mul_f32_e32 v134, %[scl], v134\nv_mul_f32_e32 v135, %[scl], v135\nv_mul_f32_e32 v246, %[scl], v246\n" \
    "v_mul_f32_e32 v247, %[scl], v247\nv_mul_f32_e32 v248, %[scl], v248\nv_mul_f32_e32 v249, %[scl], v249\nv_cvt_pk_bf16_f32 v132, v132, v133\nv_cvt_pk_bf16_f32 v133, v134, v135\nv_cvt_pk_bf16_f32 v134, v246, v247\n" \
    "v_cvt_pk_bf16_f32 v135, v248, v249\ns_add_u32 s82, %[so22], 0x0\ns_nop 0\nv_permlane16_swap_b32_e32 v132, v134\nv_permlane16_swap_b32_e32 v133, v135\nbuffer_store_dwordx4 v[132:135], v244, %[srdO2], s82 offen sc1\n" \
    "v_accvgpr_read_b32 v250, a228\nv_accvgpr_read_b32 v251, a229\nv_accvgpr_read_b32 v252, a230\nv_accvgpr_read_b32 v253, a231\nv_mul_f32_e32 v140, %[scl], v140\nv_mul_f32_e32 v141, %[scl], v141\n" \
    "v_mul_f32_e32 v142, %[scl], v142\nv_mul_f32_e32 v143, %[scl], v143\nv_mul_f32_e32 v250, %[scl], v250\nv_mul_f32_e32 v251, %[scl], v251\nv_mul_f32_e32 v252, %[scl], v252\nv_mul_f32_e32 v253, %[scl], v253\n" \
    "v_cvt_pk_bf16_f32 v140, v140, v141\nv_cvt_pk_bf16_f32 v141, v142, v143\nv_cvt_pk_bf16_f32 v142, v250, v251\nv_cvt_pk_bf16_f32 v143, v252, v253\ns_add_u32 s82, %[so22], 0x1000\ns_nop 0\n" \
    "v_permlane16_swap_b32_e32 v140, v142\nv_permlane16_swap_b32_e32 v141, v143\nbuffer_store_dwordx4 v[140:143], v244, %[srdO2], s82 offen sc1\nv_accvgpr_read_b32 v246, a232\nv_accvgpr_read_b32 v247, a233\n" \
    "v_accvgpr_read_b32 v248, a234\nv_accvgpr_read_b32 v249, a235\nv_mul_f32_e32 v148, %[scl], v148\nv_mul_f32_e32 v149, %[scl], v149\nv_mul_f32_e32 v150, %[scl], v150\nv_mul_f32_e32 v151, %[scl], v151\n" \
    "v_mul_f32_e32 v246, %[scl], v246\nv_mul_f32_e32 v247, %[scl], v247\nv_mul_f32_e32 v248, %[scl], v248\nv_mul_f32_e32 v249, %[scl], v249\nv_cvt_pk_bf16_f32 v148, v148, v149\nv_cvt_pk_bf16_f32 v149, v150, v151\n" \
    "v_cvt_pk_bf16_f32 v150, v246, v247\nv_cvt_pk_bf16_f32 v151, v248, v249\ns_add_u32 s82, %[so22], 0x2000\ns_nop 0\nv_permlane16_swap_b32_e32 v148, v150\nv_permlane16_swap_b32_e32 v149, v151\n" \
    "buffer_store_dwordx4 v[148:151], v244, %[srdO2], s82 offen sc1\nv_accvgpr_read_b32 v250, a236\nv_accvgpr_read_b32 v251, a237\nv_accvgpr_read_b32 v252, a238\nv_accvgpr_read_b32 v253, a239\n" \
    "v_mul_f32_e32 v156, %[scl], v156\nv_mul_f32_e32 v157, %[scl], v157\nv_mul_f32_e32 v158, %[scl], v158\nv_mul_f32_e32 v159, %[scl], v159\nv_mul_f32_e32 v250, %[scl], v250\nv_mul_f32_e32 v251, %[scl], v251\n" \
    "v_mul_f32_e32 v252, %[scl], v252\nv_mul_f32_e32 v253, %[scl], v253\nv_cvt_pk_bf16_f32 v156, v156, v157\nv_cvt_pk_bf16_f32 v157, v158, v159\nv_cvt_pk_bf16_f32 v158, v250, v251\nv_cvt_pk_bf16_f32 v159, v252, v253\n" \
    "s_add_u32 s82, %[so22], 0x3000\ns_nop 0\nv_permlane16_swap_b32_e32 v156, v158\nv_permlane16_swap_b32_e32 v157, v159\nbuffer_store_dwordx4 v[156:159], v244, %[srdO2], s82 offen sc1\nv_accvgpr_read_b32 v246, a240\n" \
    "v_accvgpr_read_b32 v247, a241\nv_accvgpr_read_b32 v248, a242\nv_accvgpr_read_b32 v249, a243\nv_mul_f32_e32 v164, %[scl], v164\nv_mul_f32_e32 v165, %[scl], v165\nv_mul_f32_e32 v166, %[scl], v166\n" \
    "v_mul_f32_e32 v167, %[scl], v167\nv_mul_f32_e32 v246, %[scl], v246\nv_mul_f32_e32 v247, %[scl], v247\nv_mul_f32_e32 v248, %[scl], v248\nv_mul_f32_e32 v249, %[scl], v249\nv_cvt_pk_bf16_f32 v164, v164, v165\n" \
    "v_cvt_pk_bf16_f32 v165, v166, v167\nv_cvt_pk_bf16_f32 v166, v246, v247\nv_cvt_pk_bf16_f32 v167, v248, v249\ns_add_u32 s82, %[so22], 0x4000\ns_nop 0\nv_permlane16_swap_b32_e32 v164, v166\n" \
    "v_permlane16_swap_b32_e32 v165, v167\nbuffer_store_dwordx4 v[164:167], v244, %[srdO2], s82 offen sc1\nv_accvgpr_read_b32 v250, a244\nv_accvgpr_read_b32 v251, a245\nv_accvgpr_read_b32 v252, a246\n" \
    "v_accvgpr_read_b32 v253, a247\nv_mul_f32_e32 v172, %[scl], v172\nv_mul_f32_e32 v173, %[scl], v173\nv_mul_f32_e32 v174, %[scl], v174\nv_mul_f32_e32 v175, %[scl], v175\nv_mul_f32_e32 v250, %[scl], v250\n" \
    "v_mul_f32_e32 v251, %[scl], v251\nv_mul_f32_e32 v252, %[scl], v252\nv_mul_f32_e32 v253, %[scl], v253\nv_cvt_pk_bf16_f32 v172, v172, v173\nv_cvt_pk_bf16_f32 v173, v174, v175\nv_cvt_pk_bf16_f32 v174, v250, v251\n" \
    "v_cvt_pk_bf16_f32 v175, v252, v253\ns_add_u32 s82, %[so22], 0x5000\ns_nop 0\nv_permlane16_swap_b32_e32 v172, v174\nv_permlane16_swap_b32_e32 v173, v175\nbuffer_store_dwordx4 v[172:175], v244, %[srdO2], s82 offen sc1\n" \
    "v_accvgpr_read_b32 v246, a248\nv_accvgpr_read_b32 v247, a249\nv_accvgpr_read_b32 v248, a250\nv_accvgpr_read_b32 v249, a251\nv_mul_f32_e32 v180, %[scl], v180\nv_mul_f32_e32 v181, %[scl], v181\n" \
    "v_mul_f32_e32 v182, %[scl], v182\nv_mul_f32_e32 v183, %[scl], v183\nv_mul_f32_e32 v246, %[scl], v246\nv_mul_f32_e32 v247, %[scl], v247\nv_mul_f32_e32 v248, %[scl], v248\nv_mul_f32_e32 v249, %[scl], v249\n" \
    "v_cvt_pk_bf16_f32 v180, v180, v181\nv_cvt_pk_bf16_f32 v181, v182, v183\nv_cvt_pk_bf16_f32 v182, v246, v247\nv_cvt_pk_bf16_f32 v183, v248, v249\ns_add_u32 s82, %[so22], 0x6000\ns_nop 0\n" \
    "v_permlane16_swap_b32_e32 v180, v182\nv_permlane16_swap_b32_e32 v181, v183\nbuffer_store_dwordx4 v[180:183], v244, %[srdO2], s82 offen sc1\nv_accvgpr_read_b32 v250, a252\nv_accvgpr_read_b32 v251, a253\n" \
    "v_accvgpr_read_b32 v252, a254\nv_accvgpr_read_b32 v253, a255\nv_mul_f32_e32 v188, %[scl], v188\nv_mul_f32_e32 v189, %[scl], v189\nv_mul_f32_e32 v190, %[scl], v190\nv_mul_f32_e32 v191, %[scl], v191\n" \
    "v_mul_f32_e32 v250, %[scl], v250\nv_mul_f32_e32 v251, %[scl], v251\nv_mul_f32_e32 v252, %[scl], v252\nv_mul_f32_e32 v253, %[scl], v253\nv_cvt_pk_bf16_f32 v188, v188, v189\nv_cvt_pk_bf16_f32 v189, v190, v191\n" \
    "v_cvt_pk_bf16_f32 v190, v250, v251\nv_cvt_pk_bf16_f32 v191, v252, v253\ns_add_u32 s82, %[so22], 0x7000\ns_nop 0\nv_permlane16_swap_b32_e32 v188, v190\nv_permlane16_swap_b32_e32 v189, v191\n" \
    "buffer_store_dwordx4 v[188:191], v244, %[srdO2], s82 offen sc1\ns_branch L_end%=\nL_fU%=:\nv_mfma_f32_16x16x32_bf16 a[0:3], a[64:67], a[32:35], a[0:3]\nv_accvgpr_read_b32 v240, a253\nv_accvgpr_read_b32 v241, a254\n" \
    "v_accvgpr_read_b32 v242, a255\nds_read_b128 a[96:99], v225 offset:0\nv_mfma_f32_16x16x32_bf16 a[4:7], a[68:71], a[32:35], a[4:7]\nds_read_b128 a[100:103], v225 offset:2048\ns_mov_b32 s83, %[sw2]\n" \
    "v_mfma_f32_16x16x32_bf16 a[8:11], a[64:67], a[36:39], a[8:11]\ns_add_u32 %[sw2], %[sw2], 0x36000\nv_mfma_f32_16x16x32_bf16 a[12:15], a[68:71], a[36:39], a[12:15]\nds_read_b128 a[104:107], v225 offset:4096\n" \
    "s_add_u32 m0, %[m0w2], 0x22000\ns_add_u32 s81, s83, 0x0\nbuffer_load_dwordx4 v240, %[srdW2], s81 offen offset:0 lds\nv_mfma_f32_16x16x32_bf16 a[16:19], a[64:67], a[40:43], a[16:19]\n" \
    "v_mfma_f32_16x16x32_bf16 a[20:23], a[68:71], a[40:43], a[20:23]\nds_read_b128 a[108:111], v225 offset:6144\nv_mfma_f32_16x16x32_bf16 a[24:27], a[64:67], a[44:47], a[24:27]\n" \
    "v_mfma_f32_16x16x32_bf16 a[28:31], a[68:71], a[44:47], a[28:31]\nds_read_b128 a[112:115], v225 offset:8192\nv_mfma_f32_16x16x32_bf16 v[0:3], a[64:67], a[48:51], v[0:3]\n" \
    "v_mfma_f32_16x16x32_bf16 v[4:7], a[68:71], a[48:51], v[4:7]\nds_read_b128 a[116:119], v225 offset:10240\nv_mfma_f32_16x16x32_bf16 v[8:11], a[64:67], a[52:55], v[8:11]\n" \
    "v_mfma_f32_16x16x32_bf16 v[12:15], a[68:71], a[52:55], v[12:15]\nds_read_b128 a[120:123], v225 offset:12288\nbuffer_load_dwordx4 v241, %[srdW2], s81 offen offset:1024 lds\n" \
    "v_mfma_f32_16x16x32_bf16 v[16:19], a[64:67], a[56:59], v[16:19]\nv_mfma_f32_16x16x32_bf16 v[20:23], a[68:71], a[56:59], v[20:23]\nds_read_b128 a[124:127], v225 offset:14336\n" \
    "v_mfma_f32_16x16x32_bf16 v[24:27], a[64:67], a[60:63], v[24:27]\nv_mfma_f32_16x16x32_bf16 v[28:31], a[68:71], a[60:63], v[28:31]\nds_read_b64_tr_b16 a[128:129], v228 offset:16384\n" \
    "v_mfma_f32_16x16x32_bf16 v[32:35], a[72:75], a[32:35], v[32:35]\nds_read_b64_tr_b16 a[130:131], v228 offset:18432\nv_mfma_f32_16x16x32_bf16 v[36:39], a[76:79], a[32:35], v[36:39]\n" \
    "v_mfma_f32_16x16x32_bf16 v[40:43], a[72:75], a[36:39], v[40:43]\nds_read_b64_tr_b16 a[132:133], v229 offset:16384\nv_mfma_f32_16x16x32_bf16 v[44:47], a[76:79], a[36:39], v[44:47]\n" \
    "buffer_load_dwordx4 v242, %[srdW2], s81 offen offset:2048 lds\nv_mfma_f32_16x16x32_bf16 v[48:51], a[72:75], a[40:43], v[48:51]\nds_read_b64_tr_b16 a[134:135], v229 offset:18432\n" \
    "v_mfma_f32_16x16x32_bf16 v[52:55], a[76:79], a[40:43], v[52:55]\nv_mfma_f32_16x16x32_bf16 v[56:59], a[72:75], a[44:47], v[56:59]\nds_read_b64_tr_b16 a[136:137], v230 offset:16384\n" \
    "v_mfma_f32_16x16x32_bf16 v[60:63], a[76:79], a[44:47], v[60:63]\nv_mfma_f32_16x16x32_bf16 v[64:67], a[72:75], a[48:51], v[64:67]\nds_read_b64_tr_b16 a[138:139], v230 offset:18432\n" \
    "v_mfma_f32_16x16x32_bf16 v[68:71], a[76:79], a[48:51], v[68:71]\nv_mfma_f32_16x16x32_bf16 v[72:75], a[72:75], a[52:55], v[72:75]\nds_read_b64_tr_b16 a[140:141], v231 offset:16384\n" \
    "v_mfma_f32_16x16x32_bf16 v[76:79], a[76:79], a[52:55], v[76:79]\nv_mfma_f32_16x16x32_bf16 v[80:83], a[72:75], a[56:59], v[80:83]\nds_read_b64_tr_b16 a[142:143], v231 offset:18432\ns_add_u32 m0, %[m0w2], 0x23800\n" \
    "s_add_u32 s81, s83, 0xd800\nbuffer_load_dwordx4 v240, %[srdW2], s81 offen offset:0 lds\nv_mfma_f32_16x16x32_bf16 v[84:87], a[76:79], a[56:59], v[84:87]\nv_mfma_f32_16x16x32_bf16 v[88:91], a[72:75], a[60:63], v[88:91]\n" \
    "ds_read_b64_tr_b16 a[144:145], v232 offset:16384\nv_mfma_f32_16x16x32_bf16 v[92:95], a[76:79], a[60:63], v[92:95]\nds_read_b64_tr_b16 a[146:147], v232 offset:18432\n" \
    "v_mfma_f32_16x16x32_bf16 v[96:99], a[80:83], a[32:35], v[96:99]\nv_mfma_f32_16x16x32_bf16 v[100:103], a[84:87], a[32:35], v[100:103]\nds_read_b64_tr_b16 a[148:149], v233 offset:16384\n" \
    "v_mfma_f32_16x16x32_bf16 v[104:107], a[80:83], a[36:39], v[104:107]\nv_mfma_f32_16x16x32_bf16 v[108:111], a[84:87], a[36:39], v[108:111]\nds_read_b64_tr_b16 a[150:151], v233 offset:18432\n" \
    "v_mfma_f32_16x16x32_bf16 v[112:115], a[80:83], a[40:43], v[112:115]\nbuffer_load_dwordx4 v241, %[srdW2], s81 offen offset:1024 lds\nv_mfma_f32_16x16x32_bf16 v[116:119], a[84:87], a[40:43], v[116:119]\n" \
    "ds_read_b64_tr_b16 a[152:153], v234 offset:16384\nv_mfma_f32_16x16x32_bf16 v[120:123], a[80:83], a[44:47], v[120:123]\nv_mfma_f32_16x16x32_bf16 v[124:127], a[84:87], a[44:47], v[124:127]\n" \
    "ds_read_b64_tr_b16 a[154:155], v234 offset:18432\nv_mfma_f32_16x16x32_bf16 v[128:131], a[80:83], a[48:51], v[128:131]\nv_mfma_f32_16x16x32_bf16 v[132:135], a[84:87], a[48:51], v[132:135]\n" \
    "ds_read_b64_tr_b16 a[156:157], v235 offset:16384\nv_mfma_f32_16x16x32_bf16 v[136:139], a[80:83], a[52:55], v[136:139]\nv_mfma_f32_16x16x32_bf16 v[140:143], a[84:87], a[52:55], v[140:143]\n" \
    "ds_read_b64_tr_b16 a[158:159], v235 offset:18432\nv_mfma_f32_16x16x32_bf16 v[144:147], a[80:83], a[56:59], v[144:147]\nbuffer_load_dwordx4 v242, %[srdW2], s81 offen offset:2048 lds\n" \
    "v_mfma_f32_16x16x32_bf16 v[148:151], a[84:87], a[56:59], v[148:151]\nv_mfma_f32_16x16x32_bf16 v[152:155], a[80:83], a[60:63], v[152:155]\nv_mfma_f32_16x16x32_bf16 v[156:159], a[84:87], a[60:63], v[156:159]\n" \
    "v_mfma_f32_16x16x32_bf16 v[160:163], a[88:91], a[32:35], v[160:163]\nv_mfma_f32_16x16x32_bf16 v[164:167], a[92:95], a[32:35], v[164:167]\nv_mfma_f32_16x16x32_bf16 v[168:171], a[88:91], a[36:39], v[168:171]\n" \
    "v_mfma_f32_16x16x32_bf16 v[172:175], a[92:95], a[36:39], v[172:175]\nv_mfma_f32_16x16x32_bf16 v[176:179], a[88:91], a[40:43], v[176:179]\nv_mfma_f32_16x16x32_bf16 v[180:183], a[92:95], a[40:43], v[180:183]\n" \
    "v_mfma_f32_16x16x32_bf16 v[184:187], a[88:91], a[44:47], v[184:187]\nv_mfma_f32_16x16x32_bf16 v[188:191], a[92:95], a[44:47], v[188:191]\nv_mfma_f32_16x16x32_bf16 v[192:195], a[88:91], a[48:51], v[192:195]\n" \
    "v_mfma_f32_16x16x32_bf16 v[196:199], a[92:95], a[48:51], v[196:199]\nv_mfma_f32_16x16x32_bf16 v[200:203], a[88:91], a[52:55], v[200:203]\nv_mfma_f32_16x16x32_bf16 v[204:207], a[92:95], a[52:55], v[204:207]\n" \
    "v_mfma_f32_16x16x32_bf16 v[208:211], a[88:91], a[56:59], v[208:211]\nv_mfma_f32_16x16x32_bf16 v[212:215], a[92:95], a[56:59], v[212:215]\nv_mfma_f32_16x16x32_bf16 v[216:219], a[88:91], a[60:63], v[216:219]\n" \
    "v_mfma_f32_16x16x32_bf16 v[220:223], a[92:95], a[60:63], v[220:223]\ns_waitcnt vmcnt(6) lgkmcnt(0)\ns_barrier\nv_mfma_f32_16x16x32_bf16 a[0:3], a[128:131], a[96:99], a[0:3]\nv_accvgpr_read_b32 v236, a249\n" \
    "s_mov_b32 s83, %[sx2]\nv_mfma_f32_16x16x32_bf16 a[4:7], a[132:135], a[96:99], a[4:7]\ns_mov_b32 m0, %[m0x20]\ns_add_u32 s80, s83, 0x0\nbuffer_load_dwordx4 v236, %[srdX2], s80 offen offset:0 sc1 lds\n" \
    "v_mfma_f32_16x16x32_bf16 a[8:11], a[128:131], a[100:103], a[8:11]\nv_mfma_f32_16x16x32_bf16 a[12:15], a[132:135], a[100:103], a[12:15]\ns_add_u32 %[sx2], %[sx2], 0x14000\n" \
    "v_mfma_f32_16x16x32_bf16 a[16:19], a[128:131], a[104:107], a[16:19]\nv_add_u32_e32 v228, 0x8000, v228\nds_read_b128 a[32:35], v224 offset:32768\nbuffer_load_dwordx4 v236, %[srdX2], s80 offen offset:1024 sc1 lds\n" \
    "v_mfma_f32_16x16x32_bf16 a[20:23], a[132:135], a[104:107], a[20:23]\nv_add_u32_e32 v229, 0x8000, v229\nv_mfma_f32_16x16x32_bf16 a[24:27], a[128:131], a[108:111], a[24:27]\nv_add_u32_e32 v230, 0x8000, v230\n" \
    "ds_read_b128 a[36:39], v224 offset:34816\nv_mfma_f32_16x16x32_bf16 a[28:31], a[132:135], a[108:111], a[28:31]\nv_add_u32_e32 v231, 0x8000, v231\nv_mfma_f32_16x16x32_bf16 v[0:3], a[128:131], a[112:115], v[0:3]\n" \
    "v_add_u32_e32 v232, 0x8000, v232\nds_read_b128 a[40:43], v224 offset:36864\nbuffer_load_dwordx4 v236, %[srdX2], s80 offen offset:2048 sc1 lds\nv_mfma_f32_16x16x32_bf16 v[4:7], a[132:135], a[112:115], v[4:7]\n" \
    "v_add_u32_e32 v233, 0x8000, v233\nv_mfma_f32_16x16x32_bf16 v[8:11], a[128:131], a[116:119], v[8:11]\nv_add_u32_e32 v234, 0x8000, v234\nds_read_b128 a[44:47], v224 offset:38912\n" \
    "v_mfma_f32_16x16x32_bf16 v[12:15], a[132:135], a[116:119], v[12:15]\nv_add_u32_e32 v235, 0x8000, v235\nbuffer_load_dwordx4 v236, %[srdX2], s80 offen offset:3072 sc1 lds\n" \
    "v_mfma_f32_16x16x32_bf16 v[16:19], a[128:131], a[120:123], v[16:19]\nds_read_b128 a[48:51], v224 offset:40960\nv_mfma_f32_16x16x32_bf16 v[20:23], a[132:135], a[120:123], v[20:23]\n" \
    "v_mfma_f32_16x16x32_bf16 v[24:27], a[128:131], a[124:127], v[24:27]\nds_read_b128 a[52:55], v224 offset:43008\nv_mfma_f32_16x16x32_bf16 v[28:31], a[132:135], a[124:127], v[28:31]\ns_add_u32 m0, m0, 0x1000\n" \
    "s_add_u32 s80, s80, 0x1000\nbuffer_load_dwordx4 v236, %[srdX2], s80 offen offset:0 sc1 lds\nv_mfma_f32_16x16x32_bf16 v[32:35], a[136:139], a[96:99], v[32:35]\nds_read_b128 a[56:59], v224 offset:45056\n" \
    "v_mfma_f32_16x16x32_bf16 v[36:39], a[140:143], a[96:99], v[36:39]\nv_mfma_f32_16x16x32_bf16 v[40:43], a[136:139], a[100:103], v[40:43]\nds_read_b128 a[60:63], v224 offset:47104\n" \
    "v_mfma_f32_16x16x32_bf16 v[44:47], a[140:143], a[100:103], v[44:47]\ns_mov_b32 m0, %[m0x21]\ns_add_u32 s80, s83, 0x2800\nbuffer_load_dwordx4 v236, %[srdX2], s80 offen offset:0 sc1 lds\n" \
    "v_mfma_f32_16x16x32_bf16 v[48:51], a[136:139], a[104:107], v[48:51]\nds_read_b64_tr_b16 a[64:65], v228 offset:0\nv_mfma_f32_16x16x32_bf16 v[52:55], a[140:143], a[104:107], v[52:55]\n" \
    "ds_read_b64_tr_b16 a[66:67], v228 offset:2048\nv_mfma_f32_16x16x32_bf16 v[56:59], a[136:139], a[108:111], v[56:59]\nbuffer_load_dwordx4 v236, %[srdX2], s80 offen offset:1024 sc1 lds\n" \
    "v_mfma_f32_16x16x32_bf16 v[60:63], a[140:143], a[108:111], v[60:63]\nds_read_b64_tr_b16 a[68:69], v229 offset:0\nv_mfma_f32_16x16x32_bf16 v[64:67], a[136:139], a[112:115], v[64:67]\n" \
    "ds_read_b64_tr_b16 a[70:71], v229 offset:2048\nv_mfma_f32_16x16x32_bf16 v[68:71], a[140:143], a[112:115], v[68:71]\nv_mfma_f32_16x16x32_bf16 v[72:75], a[136:139], a[116:119], v[72:75]\n" \
    "ds_read_b64_tr_b16 a[72:73], v230 offset:0\nbuffer_load_dwordx4 v236, %[srdX2], s80 offen offset:2048 sc1 lds\nv_mfma_f32_16x16x32_bf16 v[76:79], a[140:143], a[116:119], v[76:79]\n" \
    "ds_read_b64_tr_b16 a[74:75], v230 offset:2048\nv_mfma_f32_16x16x32_bf16 v[80:83], a[136:139], a[120:123], v[80:83]\nv_mfma_f32_16x16x32_bf16 v[84:87], a[140:143], a[120:123], v[84:87]\n" \
    "ds_read_b64_tr_b16 a[76:77], v231 offset:0\nbuffer_load_dwordx4 v236, %[srdX2], s80 offen offset:3072 sc1 lds\nv_mfma_f32_16x16x32_bf16 v[88:91], a[136:139], a[124:127], v[88:91]\n" \
    "ds_read_b64_tr_b16 a[78:79], v231 offset:2048\nv_mfma_f32_16x16x32_bf16 v[92:95], a[140:143], a[124:127], v[92:95]\nv_mfma_f32_16x16x32_bf16 v[96:99], a[144:147], a[96:99], v[96:99]\n" \
    "ds_read_b64_tr_b16 a[80:81], v232 offset:0\nv_mfma_f32_16x16x32_bf16 v[100:103], a[148:151], a[96:99], v[100:103]\ns_add_u32 m0, m0, 0x1000\ns_add_u32 s80, s80, 0x1000\n" \
    "buffer_load_dwordx4 v236, %[srdX2], s80 offen offset:0 sc1 lds\nv_mfma_f32_16x16x32_bf16 v[104:107], a[144:147], a[100:103], v[104:107]\nds_read_b64_tr_b16 a[82:83], v232 offset:2048\n" \
    "v_mfma_f32_16x16x32_bf16 v[108:111], a[148:151], a[100:103], v[108:111]\nds_read_b64_tr_b16 a[84:85], v233 offset:0\nv_mfma_f32_16x16x32_bf16 v[112:115], a[144:147], a[104:107], v[112:115]\n" \
    "v_mfma_f32_16x16x32_bf16 v[116:119], a[148:151], a[104:107], v[116:119]\nds_read_b64_tr_b16 a[86:87], v233 offset:2048\nv_mfma_f32_16x16x32_bf16 v[120:123], a[144:147], a[108:111], v[120:123]\n" \
    "ds_read_b64_tr_b16 a[88:89], v234 offset:0\nv_mfma_f32_16x16x32_bf16 v[124:127], a[148:151], a[108:111], v[124:127]\nv_mfma_f32_16x16x32_bf16 v[128:131], a[144:147], a[112:115], v[128:131]\n" \
    "ds_read_b64_tr_b16 a[90:91], v234 offset:2048\nv_mfma_f32_16x16x32_bf16 v[132:135], a[148:151], a[112:115], v[132:135]\nds_read_b64_tr_b16 a[92:93], v235 offset:0\n" \
    "v_mfma_f32_16x16x32_bf16 v[136:139], a[144:147], a[116:119], v[136:139]\nv_mfma_f32_16x16x32_bf16 v[140:143], a[148:151], a[116:119], v[140:143]\nds_read_b64_tr_b16 a[94:95], v235 offset:2048\n" \
    "v_mfma_f32_16x16x32_bf16 v[144:147], a[144:147], a[120:123], v[144:147]\nv_mfma_f32_16x16x32_bf16 v[148:151], a[148:151], a[120:123], v[148:151]\nv_mfma_f32_16x16x32_bf16 v[152:155], a[144:147], a[124:127], v[152:155]\n" \
    "v_mfma_f32_16x16x32_bf16 v[156:159], a[148:151], a[124:127], v[156:159]\nv_mfma_f32_16x16x32_bf16 v[160:163], a[152:155], a[96:99], v[160:163]\nv_mfma_f32_16x16x32_bf16 v[164:167], a[156:159], a[96:99], v[164:167]\n" \
    "v_mfma_f32_16x16x32_bf16 v[168:171], a[152:155], a[100:103], v[168:171]\nv_mfma_f32_16x16x32_bf16 v[172:175], a[156:159], a[100:103], v[172:175]\nv_mfma_f32_16x16x32_bf16 v[176:179], a[152:155], a[104:107], v[176:179]\n" \
    "v_mfma_f32_16x16x32_bf16 v[180:183], a[156:159], a[104:107], v[180:183]\nv_mfma_f32_16x16x32_bf16 v[184:187], a[152:155], a[108:111], v[184:187]\nv_mfma_f32_16x16x32_bf16 v[188:191], a[156:159], a[108:111], v[188:191]\n" \
    "v_mfma_f32_16x16x32_bf16 v[192:195], a[152:155], a[112:115], v[192:195]\nv_mfma_f32_16x16x32_bf16 v[196:199], a[156:159], a[112:115], v[196:199]\nv_mfma_f32_16x16x32_bf16 v[200:203], a[152:155], a[116:119], v[200:203]\n" \
    "v_mfma_f32_16x16x32_bf16 v[204:207], a[156:159], a[116:119], v[204:207]\nv_mfma_f32_16x16x32_bf16 v[208:211], a[152:155], a[120:123], v[208:211]\nv_mfma_f32_16x16x32_bf16 v[212:215], a[156:159], a[120:123], v[212:215]\n" \
    "v_mfma_f32_16x16x32_bf16 v[216:219], a[152:155], a[124:127], v[216:219]\nv_mfma_f32_16x16x32_bf16 v[220:223], a[156:159], a[124:127], v[220:223]\ns_waitcnt lgkmcnt(0)\n" \
    "v_mfma_f32_16x16x32_bf16 a[0:3], a[64:67], a[32:35], a[0:3]\nds_read_b128 a[96:99], v225 offset:32768\nv_mfma_f32_16x16x32_bf16 a[4:7], a[68:71], a[32:35], a[4:7]\nds_read_b128 a[100:103], v225 offset:34816\n" \
    "s_mov_b32 s83, %[sw2]\nv_mfma_f32_16x16x32_bf16 a[8:11], a[64:67], a[36:39], a[8:11]\ns_add_u32 %[sw2], %[sw2], 0x36000\nv_mfma_f32_16x16x32_bf16 a[12:15], a[68:71], a[36:39], a[12:15]\n" \
    "ds_read_b128 a[104:107], v225 offset:36864\ns_add_u32 m0, %[m0w2], 0x10000\ns_add_u32 s81, s83, 0x0\nbuffer_load_dwordx4 v240, %[srdW2], s81 offen offset:0 lds\n" \
    "v_mfma_f32_16x16x32_bf16 a[16:19], a[64:67], a[40:43], a[16:19]\nv_mfma_f32_16x16x32_bf16 a[20:23], a[68:71], a[40:43], a[20:23]\nds_read_b128 a[108:111], v225 offset:38912\n" \
    "v_mfma_f32_16x16x32_bf16 a[24:27], a[64:67], a[44:47], a[24:27]\nv_mfma_f32_16x16x32_bf16 a[28:31], a[68:71], a[44:47], a[28:31]\nds_read_b128 a[112:115], v225 offset:40960\n" \
    "v_mfma_f32_16x16x32_bf16 v[0:3], a[64:67], a[48:51], v[0:3]\nv_mfma_f32_16x16x32_bf16 v[4:7], a[68:71], a[48:51], v[4:7]\nds_read_b128 a[116:119], v225 offset:43008\n" \
    "v_mfma_f32_16x16x32_bf16 v[8:11], a[64:67], a[52:55], v[8:11]\nv_mfma_f32_16x16x32_bf16 v[12:15], a[68:71], a[52:55], v[12:15]\nds_read_b128 a[120:123], v225 offset:45056\n" \
    "buffer_load_dwordx4 v241, %[srdW2], s81 offen offset:1024 lds\nv_mfma_f32_16x16x32_bf16 v[16:19], a[64:67], a[56:59], v[16:19]\nv_mfma_f32_16x16x32_bf16 v[20:23], a[68:71], a[56:59], v[20:23]\n" \
    "ds_read_b128 a[124:127], v225 offset:47104\nv_mfma_f32_16x16x32_bf16 v[24:27], a[64:67], a[60:63], v[24:27]\nv_mfma_f32_16x16x32_bf16 v[28:31], a[68:71], a[60:63], v[28:31]\n" \
    "ds_read_b64_tr_b16 a[128:129], v228 offset:16384\nv_mfma_f32_16x16x32_bf16 v[32:35], a[72:75], a[32:35], v[32:35]\nds_read_b64_tr_b16 a[130:131], v228 offset:18432\n" \
    "v_mfma_f32_16x16x32_bf16 v[36:39], a[76:79], a[32:35], v[36:39]\nv_mfma_f32_16x16x32_bf16 v[40:43], a[72:75], a[36:39], v[40:43]\nds_read_b64_tr_b16 a[132:133], v229 offset:16384\n" \
    "v_mfma_f32_16x16x32_bf16 v[44:47], a[76:79], a[36:39], v[44:47]\nbuffer_load_dwordx4 v242, %[srdW2], s81 offen offset:2048 lds\nv_mfma_f32_16x16x32_bf16 v[48:51], a[72:75], a[40:43], v[48:51]\n" \
    "ds_read_b64_tr_b16 a[134:135], v229 offset:18432\nv_mfma_f32_16x16x32_bf16 v[52:55], a[76:79], a[40:43], v[52:55]\nv_mfma_f32_16x16x32_bf16 v[56:59], a[72:75], a[44:47], v[56:59]\n" \
    "ds_read_b64_tr_b16 a[136:137], v230 offset:16384\nv_mfma_f32_16x16x32_bf16 v[60:63], a[76:79], a[44:47], v[60:63]\nv_mfma_f32_16x16x32_bf16 v[64:67], a[72:75], a[48:51], v[64:67]\n" \
    "ds_read_b64_tr_b16 a[138:139], v230 offset:18432\nv_mfma_f32_16x16x32_bf16 v[68:71], a[76:79], a[48:51], v[68:71]\nv_mfma_f32_16x16x32_bf16 v[72:75], a[72:75], a[52:55], v[72:75]\n" \
    "ds_read_b64_tr_b16 a[140:141], v231 offset:16384\nv_mfma_f32_16x16x32_bf16 v[76:79], a[76:79], a[52:55], v[76:79]\nv_mfma_f32_16x16x32_bf16 v[80:83], a[72:75], a[56:59], v[80:83]\n" \
    "ds_read_b64_tr_b16 a[142:143], v231 offset:18432\ns_add_u32 m0, %[m0w2], 0x11800\ns_add_u32 s81, s83, 0xd800\nbuffer_load_dwordx4 v240, %[srdW2], s81 offen offset:0 lds\n" \
    "v_mfma_f32_16x16x32_bf16 v[84:87], a[76:79], a[56:59], v[84:87]\nv_mfma_f32_16x16x32_bf16 v[88:91], a[72:75], a[60:63], v[88:91]\nds_read_b64_tr_b16 a[144:145], v232 offset:16384\n" \
    "v_mfma_f32_16x16x32_bf16 v[92:95], a[76:79], a[60:63], v[92:95]\nds_read_b64_tr_b16 a[146:147], v232 offset:18432\nv_mfma_f32_16x16x32_bf16 v[96:99], a[80:83], a[32:35], v[96:99]\n" \
    "v_mfma_f32_16x16x32_bf16 v[100:103], a[84:87], a[32:35], v[100:103]\nds_read_b64_tr_b16 a[148:149], v233 offset:16384\nv_mfma_f32_16x16x32_bf16 v[104:107], a[80:83], a[36:39], v[104:107]\n" \
    "v_mfma_f32_16x16x32_bf16 v[108:111], a[84:87], a[36:39], v[108:111]\nds_read_b64_tr_b16 a[150:151], v233 offset:18432\nv_mfma_f32_16x16x32_bf16 v[112:115], a[80:83], a[40:43], v[112:115]\n" \
    "buffer_load_dwordx4 v241, %[srdW2], s81 offen offset:1024 lds\nv_mfma_f32_16x16x32_bf16 v[116:119], a[84:87], a[40:43], v[116:119]\nds_read_b64_tr_b16 a[152:153], v234 offset:16384\n" \
    "v_mfma_f32_16x16x32_bf16 v[120:123], a[80:83], a[44:47], v[120:123]\nv_mfma_f32_16x16x32_bf16 v[124:127], a[84:87], a[44:47], v[124:127]\nds_read_b64_tr_b16 a[154:155], v234 offset:18432\n" \
    "v_mfma_f32_16x16x32_bf16 v[128:131], a[80:83], a[48:51], v[128:131]\nv_mfma_f32_16x16x32_bf16 v[132:135], a[84:87], a[48:51], v[132:135]\nds_read_b64_tr_b16 a[156:157], v235 offset:16384\n" \
    "v_mfma_f32_16x16x32_bf16 v[136:139], a[80:83], a[52:55], v[136:139]\nv_mfma_f32_16x16x32_bf16 v[140:143], a[84:87], a[52:55], v[140:143]\nds_read_b64_tr_b16 a[158:159], v235 offset:18432\n" \
    "v_mfma_f32_16x16x32_bf16 v[144:147], a[80:83], a[56:59], v[144:147]\nbuffer_load_dwordx4 v242, %[srdW2], s81 offen offset:2048 lds\nv_mfma_f32_16x16x32_bf16 v[148:151], a[84:87], a[56:59], v[148:151]\n" \
    "v_mfma_f32_16x16x32_bf16 v[152:155], a[80:83], a[60:63], v[152:155]\nv_mfma_f32_16x16x32_bf16 v[156:159], a[84:87], a[60:63], v[156:159]\nv_mfma_f32_16x16x32_bf16 v[160:163], a[88:91], a[32:35], v[160:163]\n" \
    "v_mfma_f32_16x16x32_bf16 v[164:167], a[92:95], a[32:35], v[164:167]\nv_mfma_f32_16x16x32_bf16 v[168:171], a[88:91], a[36:39], v[168:171]\nv_mfma_f32_16x16x32_bf16 v[172:175], a[92:95], a[36:39], v[172:175]\n" \
    "v_mfma_f32_16x16x32_bf16 v[176:179], a[88:91], a[40:43], v[176:179]\nv_mfma_f32_16x16x32_bf16 v[180:183], a[92:95], a[40:43], v[180:183]\nv_mfma_f32_16x16x32_bf16 v[184:187], a[88:91], a[44:47], v[184:187]\n" \
    "v_mfma_f32_16x16x32_bf16 v[188:191], a[92:95], a[44:47], v[188:191]\nv_mfma_f32_16x16x32_bf16 v[192:195], a[88:91], a[48:51], v[192:195]\nv_mfma_f32_16x16x32_bf16 v[196:199], a[92:95], a[48:51], v[196:199]\n" \
    "v_mfma_f32_16x16x32_bf16 v[200:203], a[88:91], a[52:55], v[200:203]\nv_mfma_f32_16x16x32_bf16 v[204:207], a[92:95], a[52:55], v[204:207]\nv_mfma_f32_16x16x32_bf16 v[208:211], a[88:91], a[56:59], v[208:211]\n" \
    "v_mfma_f32_16x16x32_bf16 v[212:215], a[92:95], a[56:59], v[212:215]\nv_mfma_f32_16x16x32_bf16 v[216:219], a[88:91], a[60:63], v[216:219]\nv_mfma_f32_16x16x32_bf16 v[220:223], a[92:95], a[60:63], v[220:223]\n" \
    "s_waitcnt vmcnt(6) lgkmcnt(0)\ns_barrier\nv_mfma_f32_16x16x32_bf16 a[0:3], a[128:131], a[96:99], a[0:3]\ns_mov_b32 s83, %[sx2]\nv_accvgpr_read_b32 v224, a239\nv_accvgpr_read_b32 v225, a240\n" \
    "v_accvgpr_read_b32 v226, a241\nv_mfma_f32_16x16x32_bf16 a[4:7], a[132:135], a[96:99], a[4:7]\nv_accvgpr_read_b32 v227, a242\nv_accvgpr_read_b32 v228, a243\ns_mov_b32 m0, %[m0x22]\ns_add_u32 s80, s83, 0x0\n" \
    "buffer_load_dwordx4 v236, %[srdX2], s80 offen offset:0 sc1 lds\nv_mfma_f32_16x16x32_bf16 a[8:11], a[128:131], a[100:103], a[8:11]\nv_accvgpr_read_b32 v229, a244\nv_accvgpr_read_b32 v230, a245\n" \
    "v_accvgpr_read_b32 v231, a246\nv_mfma_f32_16x16x32_bf16 a[12:15], a[132:135], a[100:103], a[12:15]\ns_add_u32 %[sx2], %[sx2], 0x14000\nv_accvgpr_read_b32 v232, a247\nv_accvgpr_read_b32 v233, a248\n" \
    "v_mfma_f32_16x16x32_bf16 a[16:19], a[128:131], a[104:107], a[16:19]\nds_read_b128 a[32:35], v224 offset:0\nbuffer_load_dwordx4 v236, %[srdX2], s80 offen offset:1024 sc1 lds\n" \
    "v_mfma_f32_16x16x32_bf16 a[20:23], a[132:135], a[104:107], a[20:23]\nv_mfma_f32_16x16x32_bf16 a[24:27], a[128:131], a[108:111], a[24:27]\nds_read_b128 a[36:39], v224 offset:256\n" \
    "v_mfma_f32_16x16x32_bf16 a[28:31], a[132:135], a[108:111], a[28:31]\nv_accvgpr_read_b32 v246, a0\nv_accvgpr_read_b32 v247, a1\nv_accvgpr_read_b32 v248, a2\n" \
    "v_mfma_f32_16x16x32_bf16 v[0:3], a[128:131], a[112:115], v[0:3]\nds_read_b128 a[40:43], v224 offset:512\nbuffer_load_dwordx4 v236, %[srdX2], s80 offen offset:2048 sc1 lds\nv_accvgpr_read_b32 v249, a3\n" \
    "v_accvgpr_read_b32 v250, a4\nv_mfma_f32_16x16x32_bf16 v[4:7], a[132:135], a[112:115], v[4:7]\nv_accvgpr_read_b32 v251, a5\nv_accvgpr_read_b32 v252, a6\nv_accvgpr_read_b32 v253, a7\n" \
    "v_mfma_f32_16x16x32_bf16 v[8:11], a[128:131], a[116:119], v[8:11]\nds_read_b128 a[44:47], v224 offset:768\nv_cvt_pk_bf16_f32 v246, v246, v247\nv_cvt_pk_bf16_f32 v247, v248, v249\n" \
    "v_mfma_f32_16x16x32_bf16 v[12:15], a[132:135], a[116:119], v[12:15]\nbuffer_load_dwordx4 v236, %[srdX2], s80 offen offset:3072 sc1 lds\nv_cvt_pk_bf16_f32 v248, v250, v251\nv_cvt_pk_bf16_f32 v249, v252, v253\n" \
    "v_mfma_f32_16x16x32_bf16 v[16:19], a[128:131], a[120:123], v[16:19]\nds_read_b128 a[48:51], v224 offset:1024\ns_add_u32 s82, %[so0], 0x0\ns_nop 0\nv_permlane16_swap_b32_e32 v246, v248\n" \
    "v_permlane16_swap_b32_e32 v247, v249\nv_mfma_f32_16x16x32_bf16 v[20:23], a[132:135], a[120:123], v[20:23]\nbuffer_store_dwordx4 v[246:249], v244, %[srdO], s82 offen sc1\ns_nop 1\nv_accvgpr_read_b32 v246, a8\n" \
    "v_accvgpr_read_b32 v247, a9\nv_mfma_f32_16x16x32_bf16 v[24:27], a[128:131], a[124:127], v[24:27]\nds_read_b128 a[52:55], v224 offset:1280\nv_accvgpr_read_b32 v248, a10\nv_accvgpr_read_b32 v249, a11\n" \
    "v_mfma_f32_16x16x32_bf16 v[28:31], a[132:135], a[124:127], v[28:31]\ns_add_u32 m0, m0, 0x1000\ns_add_u32 s80, s80, 0x1000\nbuffer_load_dwordx4 v236, %[srdX2], s80 offen offset:0 sc1 lds\nv_accvgpr_read_b32 v250, a12\n" \
    "v_accvgpr_read_b32 v251, a13\nv_mfma_f32_16x16x32_bf16 v[32:35], a[136:139], a[96:99], v[32:35]\nds_read_b128 a[56:59], v224 offset:1536\nv_accvgpr_read_b32 v252, a14\nv_accvgpr_read_b32 v253, a15\n" \
    "v_mfma_f32_16x16x32_bf16 v[36:39], a[140:143], a[96:99], v[36:39]\nv_cvt_pk_bf16_f32 v246, v246, v247\nv_cvt_pk_bf16_f32 v247, v248, v249\nv_cvt_pk_bf16_f32 v248, v250, v251\n" \
    "v_mfma_f32_16x16x32_bf16 v[40:43], a[136:139], a[100:103], v[40:43]\nds_read_b128 a[60:63], v224 offset:1792\nv_cvt_pk_bf16_f32 v249, v252, v253\ns_add_u32 s82, %[so0], 0x1000\ns_nop 0\n" \
    "v_permlane16_swap_b32_e32 v246, v248\nv_mfma_f32_16x16x32_bf16 v[44:47], a[140:143], a[100:103], v[44:47]\ns_mov_b32 m0, %[m0x23]\ns_add_u32 s80, s83, 0x2800\n" \
    "buffer_load_dwordx4 v236, %[srdX2], s80 offen offset:0 sc1 lds\nv_permlane16_swap_b32_e32 v247, v249\nbuffer_store_dwordx4 v[246:249], v244, %[srdO], s82 offen sc1\n" \
    "v_mfma_f32_16x16x32_bf16 v[48:51], a[136:139], a[104:107], v[48:51]\nds_read_b128 a[64:67], v224 offset:2048\ns_nop 1\nv_accvgpr_read_b32 v246, a16\nv_accvgpr_read_b32 v247, a17\n" \
    "v_mfma_f32_16x16x32_bf16 v[52:55], a[140:143], a[104:107], v[52:55]\nv_accvgpr_read_b32 v248, a18\nv_accvgpr_read_b32 v249, a19\nv_accvgpr_read_b32 v250, a20\n" \
    "v_mfma_f32_16x16x32_bf16 v[56:59], a[136:139], a[108:111], v[56:59]\nds_read_b128 a[68:71], v224 offset:2304\nbuffer_load_dwordx4 v236, %[srdX2], s80 offen offset:1024 sc1 lds\nv_accvgpr_read_b32 v251, a21\n" \
    "v_accvgpr_read_b32 v252, a22\nv_mfma_f32_16x16x32_bf16 v[60:63], a[140:143], a[108:111], v[60:63]\nv_accvgpr_read_b32 v253, a23\nv_cvt_pk_bf16_f32 v246, v246, v247\nv_cvt_pk_bf16_f32 v247, v248, v249\n" \
    "v_mfma_f32_16x16x32_bf16 v[64:67], a[136:139], a[112:115], v[64:67]\nds_read_b64_tr_b16 a[72:73], v228 offset:0\nv_cvt_pk_bf16_f32 v248, v250, v251\nv_cvt_pk_bf16_f32 v249, v252, v253\n" \
    "v_mfma_f32_16x16x32_bf16 v[68:71], a[140:143], a[112:115], v[68:71]\nds_read_b64_tr_b16 a[74:75], v228 offset:1536\ns_add_u32 s82, %[so0], 0x2000\ns_nop 0\nv_permlane16_swap_b32_e32 v246, v248\n" \
    "v_permlane16_swap_b32_e32 v247, v249\nv_mfma_f32_16x16x32_bf16 v[72:75], a[136:139], a[116:119], v[72:75]\nbuffer_load_dwordx4 v236, %[srdX2], s80 offen offset:2048 sc1 lds\n" \
    "buffer_store_dwordx4 v[246:249], v244, %[srdO], s82 offen sc1\ns_nop 1\nv_accvgpr_read_b32 v246, a24\nv_mfma_f32_16x16x32_bf16 v[76:79], a[140:143], a[116:119], v[76:79]\nds_read_b64_tr_b16 a[76:77], v229 offset:0\n" \
    "v_accvgpr_read_b32 v247, a25\nv_accvgpr_read_b32 v248, a26\nv_mfma_f32_16x16x32_bf16 v[80:83], a[136:139], a[120:123], v[80:83]\nv_accvgpr_read_b32 v249, a27\nv_accvgpr_read_b32 v250, a28\nv_accvgpr_read_b32 v251, a29\n" \
    "v_mfma_f32_16x16x32_bf16 v[84:87], a[140:143], a[120:123], v[84:87]\nds_read_b64_tr_b16 a[78:79], v229 offset:1536\nbuffer_load_dwordx4 v236, %[srdX2], s80 offen offset:3072 sc1 lds\nv_accvgpr_read_b32 v252, a30\n" \
    "v_accvgpr_read_b32 v253, a31\nv_mfma_f32_16x16x32_bf16 v[88:91], a[136:139], a[124:127], v[88:91]\nv_cvt_pk_bf16_f32 v246, v246, v247\nv_cvt_pk_bf16_f32 v247, v248, v249\nv_cvt_pk_bf16_f32 v248, v250, v251\n" \
    "v_mfma_f32_16x16x32_bf16 v[92:95], a[140:143], a[124:127], v[92:95]\nds_read_b64_tr_b16 a[80:81], v230 offset:0\nv_cvt_pk_bf16_f32 v249, v252, v253\ns_add_u32 s82, %[so0], 0x3000\ns_nop 0\n" \
    "v_permlane16_swap_b32_e32 v246, v248\nv_mfma_f32_16x16x32_bf16 v[96:99], a[144:147], a[96:99], v[96:99]\nds_read_b64_tr_b16 a[82:83], v230 offset:1536\nv_permlane16_swap_b32_e32 v247, v249\n" \
    "buffer_store_dwordx4 v[246:249], v244, %[srdO], s82 offen sc1\nv_mfma_f32_16x16x32_bf16 v[100:103], a[148:151], a[96:99], v[100:103]\ns_add_u32 m0, m0, 0x1000\ns_add_u32 s80, s80, 0x1000\n" \
    "buffer_load_dwordx4 v236, %[srdX2], s80 offen offset:0 sc1 lds\ns_nop 1\nv_cvt_pk_bf16_f32 v0, v0, v1\nv_cvt_pk_bf16_f32 v1, v2, v3\nv_mfma_f32_16x16x32_bf16 v[104:107], a[144:147], a[100:103], v[104:107]\n" \
    "ds_read_b64_tr_b16 a[84:85], v231 offset:0\nv_cvt_pk_bf16_f32 v2, v4, v5\nv_cvt_pk_bf16_f32 v3, v6, v7\nv_mfma_f32_16x16x32_bf16 v[108:111], a[148:151], a[100:103], v[108:111]\nv_cvt_pk_bf16_f32 v8, v8, v9\n" \
    "v_cvt_pk_bf16_f32 v9, v10, v11\nv_cvt_pk_bf16_f32 v10, v12, v13\nv_mfma_f32_16x16x32_bf16 v[112:115], a[144:147], a[104:107], v[112:115]\nds_read_b64_tr_b16 a[86:87], v231 offset:1536\nv_cvt_pk_bf16_f32 v11, v14, v15\n" \
    "v_cvt_pk_bf16_f32 v16, v16, v17\nv_mfma_f32_16x16x32_bf16 v[116:119], a[148:151], a[104:107], v[116:119]\nv_cvt_pk_bf16_f32 v17, v18, v19\nv_cvt_pk_bf16_f32 v18, v20, v21\nv_cvt_pk_bf16_f32 v19, v22, v23\n" \
    "v_mfma_f32_16x16x32_bf16 v[120:123], a[144:147], a[108:111], v[120:123]\nds_read_b64_tr_b16 a[88:89], v232 offset:0\nv_cvt_pk_bf16_f32 v24, v24, v25\nv_cvt_pk_bf16_f32 v25, v26, v27\n" \
    "v_mfma_f32_16x16x32_bf16 v[124:127], a[148:151], a[108:111], v[124:127]\nds_read_b64_tr_b16 a[90:91], v232 offset:1536\nv_cvt_pk_bf16_f32 v26, v28, v29\nv_cvt_pk_bf16_f32 v27, v30, v31\n" \
    "v_mfma_f32_16x16x32_bf16 v[128:131], a[144:147], a[112:115], v[128:131]\nv_cvt_pk_bf16_f32 v32, v32, v33\nv_cvt_pk_bf16_f32 v33, v34, v35\nv_cvt_pk_bf16_f32 v34, v36, v37\n" \
    "v_mfma_f32_16x16x32_bf16 v[132:135], a[148:151], a[112:115], v[132:135]\nds_read_b64_tr_b16 a[92:93], v233 offset:0\nv_cvt_pk_bf16_f32 v35, v38, v39\nv_cvt_pk_bf16_f32 v40, v40, v41\n" \
    "v_mfma_f32_16x16x32_bf16 v[136:139], a[144:147], a[116:119], v[136:139]\nv_cvt_pk_bf16_f32 v41, v42, v43\nv_cvt_pk_bf16_f32 v42, v44, v45\nv_cvt_pk_bf16_f32 v43, v46, v47\n" \
    "v_mfma_f32_16x16x32_bf16 v[140:143], a[148:151], a[116:119], v[140:143]\nds_read_b64_tr_b16 a[94:95], v233 offset:1536\nv_cvt_pk_bf16_f32 v48, v48, v49\nv_cvt_pk_bf16_f32 v49, v50, v51\n" \
    "v_mfma_f32_16x16x32_bf16 v[144:147], a[144:147], a[120:123], v[144:147]\nv_cvt_pk_bf16_f32 v50, v52, v53\nv_cvt_pk_bf16_f32 v51, v54, v55\nv_cvt_pk_bf16_f32 v56, v56, v57\n" \
    "v_mfma_f32_16x16x32_bf16 v[148:151], a[148:151], a[120:123], v[148:151]\nv_cvt_pk_bf16_f32 v57, v58, v59\nv_cvt_pk_bf16_f32 v58, v60, v61\nv_cvt_pk_bf16_f32 v59, v62, v63\n" \
    "v_mfma_f32_16x16x32_bf16 v[152:155], a[144:147], a[124:127], v[152:155]\nv_cvt_pk_bf16_f32 v64, v64, v65\nv_cvt_pk_bf16_f32 v65, v66, v67\nv_cvt_pk_bf16_f32 v66, v68, v69\n" \
    "v_mfma_f32_16x16x32_bf16 v[156:159], a[148:151], a[124:127], v[156:159]\nv_cvt_pk_bf16_f32 v67, v70, v71\nv_cvt_pk_bf16_f32 v72, v72, v73\nv_cvt_pk_bf16_f32 v73, v74, v75\n" \
    "v_mfma_f32_16x16x32_bf16 v[160:163], a[152:155], a[96:99], v[160:163]\nv_cvt_pk_bf16_f32 v74, v76, v77\nv_cvt_pk_bf16_f32 v75, v78, v79\nv_cvt_pk_bf16_f32 v80, v80, v81\n" \
    "v_mfma_f32_16x16x32_bf16 v[164:167], a[156:159], a[96:99], v[164:167]\nv_cvt_pk_bf16_f32 v81, v82, v83\nv_cvt_pk_bf16_f32 v82, v84, v85\nv_cvt_pk_bf16_f32 v83, v86, v87\n" \
    "v_mfma_f32_16x16x32_bf16 v[168:171], a[152:155], a[100:103], v[168:171]\nv_cvt_pk_bf16_f32 v88, v88, v89\nv_cvt_pk_bf16_f32 v89, v90, v91\nv_cvt_pk_bf16_f32 v90, v92, v93\n" \
    "v_mfma_f32_16x16x32_bf16 v[172:175], a[156:159], a[100:103], v[172:175]\nv_cvt_pk_bf16_f32 v91, v94, v95\nv_cvt_pk_bf16_f32 v96, v96, v97\nv_cvt_pk_bf16_f32 v97, v98, v99\n" \
    "v_mfma_f32_16x16x32_bf16 v[176:179], a[152:155], a[104:107], v[176:179]\nv_cvt_pk_bf16_f32 v98, v100, v101\nv_cvt_pk_bf16_f32 v99, v102, v103\nv_cvt_pk_bf16_f32 v104, v104, v105\n" \
    "v_mfma_f32_16x16x32_bf16 v[180:183], a[156:159], a[104:107], v[180:183]\nv_cvt_pk_bf16_f32 v105, v106, v107\nv_cvt_pk_bf16_f32 v106, v108, v109\nv_cvt_pk_bf16_f32 v107, v110, v111\n" \
    "v_mfma_f32_16x16x32_bf16 v[184:187], a[152:155], a[108:111], v[184:187]\nv_cvt_pk_bf16_f32 v112, v112, v113\nv_cvt_pk_bf16_f32 v113, v114, v115\nv_cvt_pk_bf16_f32 v114, v116, v117\n" \
    "v_mfma_f32_16x16x32_bf16 v[188:191], a[156:159], a[108:111], v[188:191]\nv_cvt_pk_bf16_f32 v115, v118, v119\nv_cvt_pk_bf16_f32 v120, v120, v121\nv_cvt_pk_bf16_f32 v121, v122, v123\n" \
    "v_mfma_f32_16x16x32_bf16 v[192:195], a[152:155], a[112:115], v[192:195]\nv_cvt_pk_bf16_f32 v122, v124, v125\nv_cvt_pk_bf16_f32 v123, v126, v127\nv_cvt_pk_bf16_f32 v128, v128, v129\n" \
    "v_mfma_f32_16x16x32_bf16 v[196:199], a[156:159], a[112:115], v[196:199]\nv_cvt_pk_bf16_f32 v129, v130, v131\nv_cvt_pk_bf16_f32 v130, v132, v133\nv_cvt_pk_bf16_f32 v131, v134, v135\n" \
    "v_mfma_f32_16x16x32_bf16 v[200:203], a[152:155], a[116:119], v[200:203]\nv_cvt_pk_bf16_f32 v136, v136, v137\nv_cvt_pk_bf16_f32 v137, v138, v139\nv_cvt_pk_bf16_f32 v138, v140, v141\n" \
    "v_mfma_f32_16x16x32_bf16 v[204:207], a[156:159], a[116:119], v[204:207]\nv_cvt_pk_bf16_f32 v139, v142, v143\nv_cvt_pk_bf16_f32 v144, v144, v145\nv_cvt_pk_bf16_f32 v145, v146, v147\n" \
    "v_mfma_f32_16x16x32_bf16 v[208:211], a[152:155], a[120:123], v[208:211]\nv_cvt_pk_bf16_f32 v146, v148, v149\nv_cvt_pk_bf16_f32 v147, v150, v151\nv_cvt_pk_bf16_f32 v152, v152, v153\n" \
    "v_mfma_f32_16x16x32_bf16 v[212:215], a[156:159], a[120:123], v[212:215]\nv_cvt_pk_bf16_f32 v153, v154, v155\nv_cvt_pk_bf16_f32 v154, v156, v157\nv_cvt_pk_bf16_f32 v155, v158, v159\n" \
    "v_mfma_f32_16x16x32_bf16 v[216:219], a[152:155], a[124:127], v[216:219]\nv_cvt_pk_bf16_f32 v160, v160, v161\nv_cvt_pk_bf16_f32 v161, v162, v163\nv_cvt_pk_bf16_f32 v162, v164, v165\n" \
    "v_mfma_f32_16x16x32_bf16 v[220:223], a[156:159], a[124:127], v[220:223]\nv_cvt_pk_bf16_f32 v163, v166, v167\nv_cvt_pk_bf16_f32 v168, v168, v169\nv_cvt_pk_bf16_f32 v169, v170, v171\ns_waitcnt lgkmcnt(0)\n" \
    "v_mfma_f32_16x16x32_bf16 v[4:7], a[32:35], a[72:75], 0\nds_read_b128 a[96:99], v225 offset:0\nv_cvt_pk_bf16_f32 v170, v172, v173\nv_cvt_pk_bf16_f32 v171, v174, v175\n" \
    "v_mfma_f32_16x16x32_bf16 a[160:163], a[36:39], a[72:75], 0\nds_read_b128 a[100:103], v225 offset:256\ns_mov_b32 s83, %[sw2]\nv_cvt_pk_bf16_f32 v176, v176, v177\nv_cvt_pk_bf16_f32 v177, v178, v179\n" \
    "v_mfma_f32_16x16x32_bf16 v[12:15], a[32:35], a[76:79], 0\ns_add_u32 %[sw2], %[sw2], 0x36000\nv_cvt_pk_bf16_f32 v178, v180, v181\nv_cvt_pk_bf16_f32 v179, v182, v183\nv_cvt_pk_bf16_f32 v184, v184, v185\n" \
    "v_mfma_f32_16x16x32_bf16 a[164:167], a[36:39], a[76:79], 0\nds_read_b128 a[104:107], v225 offset:512\ns_add_u32 m0, %[m0w2], 0x17400\ns_add_u32 s81, s83, 0x0\nbuffer_load_dwordx4 v240, %[srdW2], s81 offen offset:0 lds\n" \
    "v_cvt_pk_bf16_f32 v185, v186, v187\nv_cvt_pk_bf16_f32 v186, v188, v189\nv_mfma_f32_16x16x32_bf16 v[20:23], a[32:35], a[80:83], 0\nv_cvt_pk_bf16_f32 v187, v190, v191\nv_cvt_pk_bf16_f32 v192, v192, v193\n" \
    "v_cvt_pk_bf16_f32 v193, v194, v195\nv_mfma_f32_16x16x32_bf16 a[168:171], a[36:39], a[80:83], 0\nds_read_b128 a[108:111], v225 offset:768\nv_cvt_pk_bf16_f32 v194, v196, v197\nv_cvt_pk_bf16_f32 v195, v198, v199\n" \
    "v_mfma_f32_16x16x32_bf16 v[28:31], a[32:35], a[84:87], 0\nv_cvt_pk_bf16_f32 v200, v200, v201\nv_cvt_pk_bf16_f32 v201, v202, v203\nv_cvt_pk_bf16_f32 v202, v204, v205\n" \
    "v_mfma_f32_16x16x32_bf16 a[172:175], a[36:39], a[84:87], 0\nds_read_b128 a[112:115], v225 offset:1024\nv_cvt_pk_bf16_f32 v203, v206, v207\nv_cvt_pk_bf16_f32 v208, v208, v209\n" \
    "v_mfma_f32_16x16x32_bf16 v[36:39], a[32:35], a[88:91], 0\nv_cvt_pk_bf16_f32 v209, v210, v211\nv_cvt_pk_bf16_f32 v210, v212, v213\nv_cvt_pk_bf16_f32 v211, v214, v215\n" \
    "v_mfma_f32_16x16x32_bf16 a[176:179], a[36:39], a[88:91], 0\nds_read_b128 a[116:119], v225 offset:1280\nv_cvt_pk_bf16_f32 v216, v216, v217\nv_cvt_pk_bf16_f32 v217, v218, v219\n" \
    "v_mfma_f32_16x16x32_bf16 v[44:47], a[32:35], a[92:95], 0\nbuffer_load_dwordx4 v241, %[srdW2], s81 offen offset:1024 lds\nv_cvt_pk_bf16_f32 v218, v220, v221\nv_cvt_pk_bf16_f32 v219, v222, v223\n" \
    "v_mfma_f32_16x16x32_bf16 a[180:183], a[36:39], a[92:95], 0\nds_read_b128 a[120:123], v225 offset:1536\nv_mfma_f32_16x16x32_bf16 v[52:55], a[40:43], a[72:75], 0\n" \
    "v_mfma_f32_16x16x32_bf16 a[184:187], a[44:47], a[72:75], 0\nds_read_b128 a[124:127], v225 offset:1792\nv_mfma_f32_16x16x32_bf16 v[60:63], a[40:43], a[76:79], 0\n" \
    "v_mfma_f32_16x16x32_bf16 a[188:191], a[44:47], a[76:79], 0\nds_read_b128 a[128:131], v225 offset:2048\nv_mfma_f32_16x16x32_bf16 v[68:71], a[40:43], a[80:83], 0\n" \
    "v_mfma_f32_16x16x32_bf16 a[192:195], a[44:47], a[80:83], 0\nds_read_b128 a[132:135], v225 offset:2304\nv_mfma_f32_16x16x32_bf16 v[76:79], a[40:43], a[84:87], 0\n" \
    "buffer_load_dwordx4 v242, %[srdW2], s81 offen offset:2048 lds\nv_mfma_f32_16x16x32_bf16 a[196:199], a[44:47], a[84:87], 0\nds_read_b64_tr_b16 a[136:137], v228 offset:12288\n" \
    "v_mfma_f32_16x16x32_bf16 v[84:87], a[40:43], a[88:91], 0\nv_mfma_f32_16x16x32_bf16 a[200:203], a[44:47], a[88:91], 0\nds_read_b64_tr_b16 a[138:139], v228 offset:13824\n" \
    "v_mfma_f32_16x16x32_bf16 v[92:95], a[40:43], a[92:95], 0\nds_read_b64_tr_b16 a[140:141], v229 offset:12288\nv_mfma_f32_16x16x32_bf16 a[204:207], a[44:47], a[92:95], 0\n" \
    "v_mfma_f32_16x16x32_bf16 v[100:103], a[48:51], a[72:75], 0\nds_read_b64_tr_b16 a[142:143], v229 offset:13824\nv_mfma_f32_16x16x32_bf16 a[208:211], a[52:55], a[72:75], 0\n" \
    "v_mfma_f32_16x16x32_bf16 v[108:111], a[48:51], a[76:79], 0\nds_read_b64_tr_b16 a[144:145], v230 offset:12288\ns_add_u32 m0, %[m0w2], 0x18c00\ns_add_u32 s81, s83, 0xd800\n" \
    "buffer_load_dwordx4 v240, %[srdW2], s81 offen offset:0 lds\nv_mfma_f32_16x16x32_bf16 a[212:215], a[52:55], a[76:79], 0\nv_mfma_f32_16x16x32_bf16 v[116:119], a[48:51], a[80:83], 0\n" \
    "ds_read_b64_tr_b16 a[146:147], v230 offset:13824\nv_mfma_f32_16x16x32_bf16 a[216:219], a[52:55], a[80:83], 0\nv_mfma_f32_16x16x32_bf16 v[124:127], a[48:51], a[84:87], 0\nds_read_b64_tr_b16 a[148:149], v231 offset:12288\n" \
    "v_mfma_f32_16x16x32_bf16 a[220:223], a[52:55], a[84:87], 0\nv_mfma_f32_16x16x32_bf16 v[132:135], a[48:51], a[88:91], 0\nds_read_b64_tr_b16 a[150:151], v231 offset:13824\n" \
    "v_mfma_f32_16x16x32_bf16 a[224:227], a[52:55], a[88:91], 0\nbuffer_load_dwordx4 v241, %[srdW2], s81 offen offset:1024 lds\nv_mfma_f32_16x16x32_bf16 v[140:143], a[48:51], a[92:95], 0\n" \
    "ds_read_b64_tr_b16 a[152:153], v232 offset:12288\nv_mfma_f32_16x16x32_bf16 a[228:231], a[52:55], a[92:95], 0\nv_mfma_f32_16x16x32_bf16 v[148:151], a[56:59], a[72:75], 0\nds_read_b64_tr_b16 a[154:155], v232 offset:13824\n" \
    "v_mfma_f32_16x16x32_bf16 a[232:235], a[60:63], a[72:75], 0\nv_mfma_f32_16x16x32_bf16 v[156:159], a[56:59], a[76:79], 0\nds_read_b64_tr_b16 a[156:157], v233 offset:12288\n" \
    "v_mfma_f32_16x16x32_bf16 a[236:239], a[60:63], a[76:79], 0\nv_mfma_f32_16x16x32_bf16 v[164:167], a[56:59], a[80:83], 0\nds_read_b64_tr_b16 a[158:159], v233 offset:13824\n" \
    "v_mfma_f32_16x16x32_bf16 a[240:243], a[60:63], a[80:83], 0\nbuffer_load_dwordx4 v242, %[srdW2], s81 offen offset:2048 lds\nv_mfma_f32_16x16x32_bf16 v[172:175], a[56:59], a[84:87], 0\n" \
    "v_mfma_f32_16x16x32_bf16 a[244:247], a[60:63], a[84:87], 0\nv_mfma_f32_16x16x32_bf16 v[180:183], a[56:59], a[88:91], 0\nv_mfma_f32_16x16x32_bf16 a[248:251], a[60:63], a[88:91], 0\n" \
    "v_mfma_f32_16x16x32_bf16 v[188:191], a[56:59], a[92:95], 0\nv_mfma_f32_16x16x32_bf16 a[252:255], a[60:63], a[92:95], 0\nv_mfma_f32_16x16x32_bf16 v[196:199], a[64:67], a[72:75], 0\n" \
    "v_mfma_f32_16x16x32_bf16 a[0:3], a[68:71], a[72:75], 0\nv_mfma_f32_16x16x32_bf16 v[204:207], a[64:67], a[76:79], 0\nv_mfma_f32_16x16x32_bf16 a[4:7], a[68:71], a[76:79], 0\n" \
    "v_mfma_f32_16x16x32_bf16 v[212:215], a[64:67], a[80:83], 0\nv_mfma_f32_16x16x32_bf16 a[8:11], a[68:71], a[80:83], 0\nv_mfma_f32_16x16x32_bf16 v[220:223], a[64:67], a[84:87], 0\n" \
    "v_mfma_f32_16x16x32_bf16 a[12:15], a[68:71], a[84:87], 0\nv_mfma_f32_16x16x32_bf16 a[24:27], a[64:67], a[88:91], 0\nv_mfma_f32_16x16x32_bf16 a[16:19], a[68:71], a[88:91], 0\n" \
    "v_mfma_f32_16x16x32_bf16 a[28:31], a[64:67], a[92:95], 0\nv_mfma_f32_16x16x32_bf16 a[20:23], a[68:71], a[92:95], 0\ns_waitcnt vmcnt(6) lgkmcnt(0)\ns_barrier\n" \
    "v_mfma_f32_16x16x32_bf16 v[4:7], a[96:99], a[136:139], v[4:7]\ns_mov_b32 s83, %[sx2]\nv_mfma_f32_16x16x32_bf16 a[160:163], a[100:103], a[136:139], a[160:163]\ns_mov_b32 m0, %[m0x20]\ns_add_u32 s80, s83, 0x0\n" \
    "buffer_load_dwordx4 v236, %[srdX2], s80 offen offset:0 sc1 lds\nv_permlane16_swap_b32_e32 v0, v2\nv_permlane16_swap_b32_e32 v1, v3\nv_mfma_f32_16x16x32_bf16 v[12:15], a[96:99], a[140:143], v[12:15]\n" \
    "s_add_u32 s82, %[so0], 0x4000\nbuffer_store_dwordx4 v[0:3], v244, %[srdO], s82 offen sc1\nv_mfma_f32_16x16x32_bf16 a[164:167], a[100:103], a[140:143], a[164:167]\ns_add_u32 %[sx2], %[sx2], 0x14000\n" \
    "v_mfma_f32_16x16x32_bf16 v[20:23], a[96:99], a[144:147], v[20:23]\nv_add_u32_e32 v228, 0xfffee000, v228\nds_read_b128 a[32:35], v226 offset:0\nbuffer_load_dwordx4 v236, %[srdX2], s80 offen offset:1024 sc1 lds\n" \
    "v_mfma_f32_16x16x32_bf16 a[168:171], a[100:103], a[144:147], a[168:171]\nv_add_u32_e32 v229, 0xfffee000, v229\nv_mfma_f32_16x16x32_bf16 v[28:31], a[96:99], a[148:151], v[28:31]\nv_add_u32_e32 v230, 0xfffee000, v230\n" \
    "ds_read_b128 a[36:39], v226 offset:256\nv_mfma_f32_16x16x32_bf16 a[172:175], a[100:103], a[148:151], a[172:175]\nv_add_u32_e32 v231, 0xfffee000, v231\nbuffer_load_dwordx4 v236, %[srdX2], s80 offen offset:2048 sc1 lds\n" \
    "v_mfma_f32_16x16x32_bf16 v[36:39], a[96:99], a[152:155], v[36:39]\nv_add_u32_e32 v232, 0xfffee000, v232\nds_read_b128 a[40:43], v226 offset:512\nv_mfma_f32_16x16x32_bf16 a[176:179], a[100:103], a[152:155], a[176:179]\n" \
    "v_add_u32_e32 v233, 0xfffee000, v233\nv_mfma_f32_16x16x32_bf16 v[44:47], a[96:99], a[156:159], v[44:47]\nds_read_b128 a[44:47], v226 offset:768\nbuffer_load_dwordx4 v236, %[srdX2], s80 offen offset:3072 sc1 lds\n" \
    "v_mfma_f32_16x16x32_bf16 a[180:183], a[100:103], a[156:159], a[180:183]\nv_mfma_f32_16x16x32_bf16 v[52:55], a[104:107], a[136:139], v[52:55]\nds_read_b128 a[48:51], v226 offset:1024\n" \
    "v_mfma_f32_16x16x32_bf16 a[184:187], a[108:111], a[136:139], a[184:187]\nv_mfma_f32_16x16x32_bf16 v[60:63], a[104:107], a[140:143], v[60:63]\nds_read_b128 a[52:55], v226 offset:1280\ns_add_u32 m0, m0, 0x1000\n" \
    "s_add_u32 s80, s80, 0x1000\nbuffer_load_dwordx4 v236, %[srdX2], s80 offen offset:0 sc1 lds\nv_mfma_f32_16x16x32_bf16 a[188:191], a[108:111], a[140:143], a[188:191]\n" \
    "v_mfma_f32_16x16x32_bf16 v[68:71], a[104:107], a[144:147], v[68:71]\nds_read_b128 a[56:59], v226 offset:1536\nv_mfma_f32_16x16x32_bf16 a[192:195], a[108:111], a[144:147], a[192:195]\ns_mov_b32 m0, %[m0x21]\n" \
    "s_add_u32 s80, s83, 0x2800\nbuffer_load_dwordx4 v236, %[srdX2], s80 offen offset:0 sc1 lds\nv_mfma_f32_16x16x32_bf16 v[76:79], a[104:107], a[148:151], v[76:79]\nds_read_b128 a[60:63], v226 offset:1792\n" \
    "v_mfma_f32_16x16x32_bf16 a[196:199], a[108:111], a[148:151], a[196:199]\nv_permlane16_swap_b32_e32 v8, v10\nv_permlane16_swap_b32_e32 v9, v11\nv_mfma_f32_16x16x32_bf16 v[84:87], a[104:107], a[152:155], v[84:87]\n" \
    "ds_read_b128 a[64:67], v226 offset:2048\nbuffer_load_dwordx4 v236, %[srdX2], s80 offen offset:1024 sc1 lds\ns_add_u32 s82, %[so0], 0x5000\nbuffer_store_dwordx4 v[8:11], v244, %[srdO], s82 offen sc1\n" \
    "v_mfma_f32_16x16x32_bf16 a[200:203], a[108:111], a[152:155], a[200:203]\nv_mfma_f32_16x16x32_bf16 v[92:95], a[104:107], a[156:159], v[92:95]\nds_read_b128 a[68:71], v226 offset:2304\n" \
    "v_mfma_f32_16x16x32_bf16 a[204:207], a[108:111], a[156:159], a[204:207]\nv_mfma_f32_16x16x32_bf16 v[100:103], a[112:115], a[136:139], v[100:103]\nds_read_b64_tr_b16 a[72:73], v228 offset:0\n" \
    "buffer_load_dwordx4 v236, %[srdX2], s80 offen offset:2048 sc1 lds\nv_mfma_f32_16x16x32_bf16 a[208:211], a[116:119], a[136:139], a[208:211]\nds_read_b64_tr_b16 a[74:75], v228 offset:1536\n" \
    "v_mfma_f32_16x16x32_bf16 v[108:111], a[112:115], a[140:143], v[108:111]\nv_mfma_f32_16x16x32_bf16 a[212:215], a[116:119], a[140:143], a[212:215]\nds_read_b64_tr_b16 a[76:77], v229 offset:0\n" \
    "buffer_load_dwordx4 v236, %[srdX2], s80 offen offset:3072 sc1 lds\nv_mfma_f32_16x16x32_bf16 v[116:119], a[112:115], a[144:147], v[116:119]\nds_read_b64_tr_b16 a[78:79], v229 offset:1536\n" \
    "v_mfma_f32_16x16x32_bf16 a[216:219], a[116:119], a[144:147], a[216:219]\nv_mfma_f32_16x16x32_bf16 v[124:127], a[112:115], a[148:151], v[124:127]\nds_read_b64_tr_b16 a[80:81], v230 offset:0\ns_add_u32 m0, m0, 0x1000\n" \
    "s_add_u32 s80, s80, 0x1000\nbuffer_load_dwordx4 v236, %[srdX2], s80 offen offset:0 sc1 lds\nv_mfma_f32_16x16x32_bf16 a[220:223], a[116:119], a[148:151], a[220:223]\nds_read_b64_tr_b16 a[82:83], v230 offset:1536\n" \
    "v_mfma_f32_16x16x32_bf16 v[132:135], a[112:115], a[152:155], v[132:135]\nv_mfma_f32_16x16x32_bf16 a[224:227], a[116:119], a[152:155], a[224:227]\nds_read_b64_tr_b16 a[84:85], v231 offset:0\n" \
    "v_mfma_f32_16x16x32_bf16 v[140:143], a[112:115], a[156:159], v[140:143]\nds_read_b64_tr_b16 a[86:87], v231 offset:1536\nv_mfma_f32_16x16x32_bf16 a[228:231], a[116:119], a[156:159], a[228:231]\n" \
    "v_mfma_f32_16x16x32_bf16 v[148:151], a[120:123], a[136:139], v[148:151]\nds_read_b64_tr_b16 a[88:89], v232 offset:0\nv_mfma_f32_16x16x32_bf16 a[232:235], a[124:127], a[136:139], a[232:235]\n" \
    "ds_read_b64_tr_b16 a[90:91], v232 offset:1536\nv_mfma_f32_16x16x32_bf16 v[156:159], a[120:123], a[140:143], v[156:159]\nv_permlane16_swap_b32_e32 v16, v18\nv_permlane16_swap_b32_e32 v17, v19\n" \
    "v_mfma_f32_16x16x32_bf16 a[236:239], a[124:127], a[140:143], a[236:239]\nds_read_b64_tr_b16 a[92:93], v233 offset:0\ns_add_u32 s82, %[so0], 0x6000\nbuffer_store_dwordx4 v[16:19], v244, %[srdO], s82 offen sc1\n" \
    "v_mfma_f32_16x16x32_bf16 v[164:167], a[120:123], a[144:147], v[164:167]\nds_read_b64_tr_b16 a[94:95], v233 offset:1536\nv_mfma_f32_16x16x32_bf16 a[240:243], a[124:127], a[144:147], a[240:243]\n" \
    "v_mfma_f32_16x16x32_bf16 v[172:175], a[120:123], a[148:151], v[172:175]\nv_mfma_f32_16x16x32_bf16 a[244:247], a[124:127], a[148:151], a[244:247]\nv_mfma_f32_16x16x32_bf16 v[180:183], a[120:123], a[152:155], v[180:183]\n" \
    "v_mfma_f32_16x16x32_bf16 a[248:251], a[124:127], a[152:155], a[248:251]\nv_mfma_f32_16x16x32_bf16 v[188:191], a[120:123], a[156:159], v[188:191]\nv_mfma_f32_16x16x32_bf16 a[252:255], a[124:127], a[156:159], a[252:255]\n" \
    "v_mfma_f32_16x16x32_bf16 v[196:199], a[128:131], a[136:139], v[196:199]\nv_mfma_f32_16x16x32_bf16 a[0:3], a[132:135], a[136:139], a[0:3]\nv_mfma_f32_16x16x32_bf16 v[204:207], a[128:131], a[140:143], v[204:207]\n" \
    "v_mfma_f32_16x16x32_bf16 a[4:7], a[132:135], a[140:143], a[4:7]\nv_mfma_f32_16x16x32_bf16 v[212:215], a[128:131], a[144:147], v[212:215]\nv_mfma_f32_16x16x32_bf16 a[8:11], a[132:135], a[144:147], a[8:11]\n" \
    "v_mfma_f32_16x16x32_bf16 v[220:223], a[128:131], a[148:151], v[220:223]\nv_mfma_f32_16x16x32_bf16 a[12:15], a[132:135], a[148:151], a[12:15]\nv_mfma_f32_16x16x32_bf16 a[24:27], a[128:131], a[152:155], a[24:27]\n" \
    "v_mfma_f32_16x16x32_bf16 a[16:19], a[132:135], a[152:155], a[16:19]\nv_mfma_f32_16x16x32_bf16 a[28:31], a[128:131], a[156:159], a[28:31]\nv_mfma_f32_16x16x32_bf16 a[20:23], a[132:135], a[156:159], a[20:23]\n" \
    "s_waitcnt lgkmcnt(0)\nv_mfma_f32_16x16x32_bf16 v[4:7], a[32:35], a[72:75], v[4:7]\nds_read_b128 a[96:99], v227 offset:0\nv_mfma_f32_16x16x32_bf16 a[160:163], a[36:39], a[72:75], a[160:163]\n" \
    "ds_read_b128 a[100:103], v227 offset:256\ns_mov_b32 s83, %[sw2]\nv_permlane16_swap_b32_e32 v24, v26\nv_permlane16_swap_b32_e32 v25, v27\nv_mfma_f32_16x16x32_bf16 v[12:15], a[32:35], a[76:79], v[12:15]\n" \
    "s_add_u32 %[sw2], %[sw2], 0x36000\ns_add_u32 s82, %[so0], 0x7000\nbuffer_store_dwordx4 v[24:27], v244, %[srdO], s82 offen sc1\nv_mfma_f32_16x16x32_bf16 a[164:167], a[36:39], a[76:79], a[164:167]\n" \
    "ds_read_b128 a[104:107], v227 offset:512\ns_add_u32 m0, %[m0w2], 0x22000\ns_add_u32 s81, s83, 0x0\nbuffer_load_dwordx4 v240, %[srdW2], s81 offen offset:0 lds\n" \
    "v_mfma_f32_16x16x32_bf16 v[20:23], a[32:35], a[80:83], v[20:23]\nv_mfma_f32_16x16x32_bf16 a[168:171], a[36:39], a[80:83], a[168:171]\nds_read_b128 a[108:111], v227 offset:768\n" \
    "v_mfma_f32_16x16x32_bf16 v[28:31], a[32:35], a[84:87], v[28:31]\nv_mfma_f32_16x16x32_bf16 a[172:175], a[36:39], a[84:87], a[172:175]\nds_read_b128 a[112:115], v227 offset:1024\n" \
    "v_mfma_f32_16x16x32_bf16 v[36:39], a[32:35], a[88:91], v[36:39]\nv_mfma_f32_16x16x32_bf16 a[176:179], a[36:39], a[88:91], a[176:179]\nds_read_b128 a[116:119], v227 offset:1280\n" \
    "v_mfma_f32_16x16x32_bf16 v[44:47], a[32:35], a[92:95], v[44:47]\nbuffer_load_dwordx4 v241, %[srdW2], s81 offen offset:1024 lds\nv_mfma_f32_16x16x32_bf16 a[180:183], a[36:39], a[92:95], a[180:183]\n" \
    "ds_read_b128 a[120:123], v227 offset:1536\nv_mfma_f32_16x16x32_bf16 v[52:55], a[40:43], a[72:75], v[52:55]\nv_mfma_f32_16x16x32_bf16 a[184:187], a[44:47], a[72:75], a[184:187]\nds_read_b128 a[124:127], v227 offset:1792\n" \
    "v_mfma_f32_16x16x32_bf16 v[60:63], a[40:43], a[76:79], v[60:63]\nv_mfma_f32_16x16x32_bf16 a[188:191], a[44:47], a[76:79], a[188:191]\nds_read_b128 a[128:131], v227 offset:2048\nv_permlane16_swap_b32_e32 v32, v34\n" \
    "v_permlane16_swap_b32_e32 v33, v35\nv_mfma_f32_16x16x32_bf16 v[68:71], a[40:43], a[80:83], v[68:71]\ns_add_u32 s82, %[so1], 0x0\nbuffer_store_dwordx4 v[32:35], v244, %[srdO], s82 offen sc1\n" \
    "v_mfma_f32_16x16x32_bf16 a[192:195], a[44:47], a[80:83], a[192:195]\nds_read_b128 a[132:135], v227 offset:2304\nv_mfma_f32_16x16x32_bf16 v[76:79], a[40:43], a[84:87], v[76:79]\n" \
    "buffer_load_dwordx4 v242, %[srdW2], s81 offen offset:2048 lds\nv_mfma_f32_16x16x32_bf16 a[196:199], a[44:47], a[84:87], a[196:199]\nds_read_b64_tr_b16 a[136:137], v228 offset:12288\n" \
    "v_mfma_f32_16x16x32_bf16 v[84:87], a[40:43], a[88:91], v[84:87]\nv_mfma_f32_16x16x32_bf16 a[200:203], a[44:47], a[88:91], a[200:203]\nds_read_b64_tr_b16 a[138:139], v228 offset:13824\n" \
    "v_mfma_f32_16x16x32_bf16 v[92:95], a[40:43], a[92:95], v[92:95]\nds_read_b64_tr_b16 a[140:141], v229 offset:12288\nv_mfma_f32_16x16x32_bf16 a[204:207], a[44:47], a[92:95], a[204:207]\n" \
    "v_mfma_f32_16x16x32_bf16 v[100:103], a[48:51], a[72:75], v[100:103]\nds_read_b64_tr_b16 a[142:143], v229 offset:13824\nv_mfma_f32_16x16x32_bf16 a[208:211], a[52:55], a[72:75], a[208:211]\n" \
    "v_mfma_f32_16x16x32_bf16 v[108:111], a[48:51], a[76:79], v[108:111]\nds_read_b64_tr_b16 a[144:145], v230 offset:12288\ns_add_u32 m0, %[m0w2], 0x23800\ns_add_u32 s81, s83, 0xd800\n" \
    "buffer_load_dwordx4 v240, %[srdW2], s81 offen offset:0 lds\nv_mfma_f32_16x16x32_bf16 a[212:215], a[52:55], a[76:79], a[212:215]\nv_mfma_f32_16x16x32_bf16 v[116:119], a[48:51], a[80:83], v[116:119]\n" \
    "ds_read_b64_tr_b16 a[146:147], v230 offset:13824\nv_mfma_f32_16x16x32_bf16 a[216:219], a[52:55], a[80:83], a[216:219]\nv_permlane16_swap_b32_e32 v40, v42\nv_permlane16_swap_b32_e32 v41, v43\n" \
    "v_mfma_f32_16x16x32_bf16 v[124:127], a[48:51], a[84:87], v[124:127]\nds_read_b64_tr_b16 a[148:149], v231 offset:12288\ns_add_u32 s82, %[so1], 0x1000\nbuffer_store_dwordx4 v[40:43], v244, %[srdO], s82 offen sc1\n" \
    "v_mfma_f32_16x16x32_bf16 a[220:223], a[52:55], a[84:87], a[220:223]\nv_mfma_f32_16x16x32_bf16 v[132:135], a[48:51], a[88:91], v[132:135]\nds_read_b64_tr_b16 a[150:151], v231 offset:13824\n" \
    "v_mfma_f32_16x16x32_bf16 a[224:227], a[52:55], a[88:91], a[224:227]\nbuffer_load_dwordx4 v241, %[srdW2], s81 offen offset:1024 lds\nv_mfma_f32_16x16x32_bf16 v[140:143], a[48:51], a[92:95], v[140:143]\n" \
    "ds_read_b64_tr_b16 a[152:153], v232 offset:12288\nv_mfma_f32_16x16x32_bf16 a[228:231], a[52:55], a[92:95], a[228:231]\nv_mfma_f32_16x16x32_bf16 v[148:151], a[56:59], a[72:75], v[148:151]\n" \
    "ds_read_b64_tr_b16 a[154:155], v232 offset:13824\nv_mfma_f32_16x16x32_bf16 a[232:235], a[60:63], a[72:75], a[232:235]\nv_mfma_f32_16x16x32_bf16 v[156:159], a[56:59], a[76:79], v[156:159]\n" \
    "ds_read_b64_tr_b16 a[156:157], v233 offset:12288\nv_mfma_f32_16x16x32_bf16 a[236:239], a[60:63], a[76:79], a[236:239]\nv_mfma_f32_16x16x32_bf16 v[164:167], a[56:59], a[80:83], v[164:167]\n" \
    "ds_read_b64_tr_b16 a[158:159], v233 offset:13824\nv_mfma_f32_16x16x32_bf16 a[240:243], a[60:63], a[80:83], a[240:243]\nbuffer_load_dwordx4 v242, %[srdW2], s81 offen offset:2048 lds\n" \
    "v_mfma_f32_16x16x32_bf16 v[172:175], a[56:59], a[84:87], v[172:175]\nv_mfma_f32_16x16x32_bf16 a[244:247], a[60:63], a[84:87], a[244:247]\nv_permlane16_swap_b32_e32 v48, v50\nv_permlane16_swap_b32_e32 v49, v51\n" \
    "v_mfma_f32_16x16x32_bf16 v[180:183], a[56:59], a[88:91], v[180:183]\ns_add_u32 s82, %[so1], 0x2000\nbuffer_store_dwordx4 v[48:51], v244, %[srdO], s82 offen sc1\n" \
    "v_mfma_f32_16x16x32_bf16 a[248:251], a[60:63], a[88:91], a[248:251]\nv_mfma_f32_16x16x32_bf16 v[188:191], a[56:59], a[92:95], v[188:191]\nv_mfma_f32_16x16x32_bf16 a[252:255], a[60:63], a[92:95], a[252:255]\n" \
    "v_mfma_f32_16x16x32_bf16 v[196:199], a[64:67], a[72:75], v[196:199]\nv_mfma_f32_16x16x32_bf16 a[0:3], a[68:71], a[72:75], a[0:3]\nv_mfma_f32_16x16x32_bf16 v[204:207], a[64:67], a[76:79], v[204:207]\n" \
    "v_mfma_f32_16x16x32_bf16 a[4:7], a[68:71], a[76:79], a[4:7]\nv_mfma_f32_16x16x32_bf16 v[212:215], a[64:67], a[80:83], v[212:215]\nv_mfma_f32_16x16x32_bf16 a[8:11], a[68:71], a[80:83], a[8:11]\n" \
    "v_mfma_f32_16x16x32_bf16 v[220:223], a[64:67], a[84:87], v[220:223]\nv_mfma_f32_16x16x32_bf16 a[12:15], a[68:71], a[84:87], a[12:15]\nv_mfma_f32_16x16x32_bf16 a[24:27], a[64:67], a[88:91], a[24:27]\n" \
    "v_mfma_f32_16x16x32_bf16 a[16:19], a[68:71], a[88:91], a[16:19]\nv_mfma_f32_16x16x32_bf16 a[28:31], a[64:67], a[92:95], a[28:31]\nv_mfma_f32_16x16x32_bf16 a[20:23], a[68:71], a[92:95], a[20:23]\n" \
    "s_waitcnt vmcnt(11) lgkmcnt(0)\ns_barrier\nv_mfma_f32_16x16x32_bf16 v[4:7], a[96:99], a[136:139], v[4:7]\ns_mov_b32 s83, %[sx2]\nv_mfma_f32_16x16x32_bf16 a[160:163], a[100:103], a[136:139], a[160:163]\n" \
    "s_mov_b32 m0, %[m0x22]\ns_add_u32 s80, s83, 0x0\nbuffer_load_dwordx4 v236, %[srdX2], s80 offen offset:0 sc1 lds\nv_permlane16_swap_b32_e32 v56, v58\nv_permlane16_swap_b32_e32 v57, v59\n" \
    "v_mfma_f32_16x16x32_bf16 v[12:15], a[96:99], a[140:143], v[12:15]\ns_add_u32 s82, %[so1], 0x3000\nbuffer_store_dwordx4 v[56:59], v244, %[srdO], s82 offen sc1\n" \
    "v_mfma_f32_16x16x32_bf16 a[164:167], a[100:103], a[140:143], a[164:167]\ns_add_u32 %[sx2], %[sx2], 0x14000\nv_mfma_f32_16x16x32_bf16 v[20:23], a[96:99], a[144:147], v[20:23]\nv_add_u32_e32 v228, 0x7400, v228\n" \
    "ds_read_b128 a[32:35], v224 offset:0\nbuffer_load_dwordx4 v236, %[srdX2], s80 offen offset:1024 sc1 lds\nv_mfma_f32_16x16x32_bf16 a[168:171], a[100:103], a[144:147], a[168:171]\nv_add_u32_e32 v229, 0x7400, v229\n" \
    "v_mfma_f32_16x16x32_bf16 v[28:31], a[96:99], a[148:151], v[28:31]\nv_add_u32_e32 v230, 0x7400, v230\nds_read_b128 a[36:39], v224 offset:256\nv_mfma_f32_16x16x32_bf16 a[172:175], a[100:103], a[148:151], a[172:175]\n" \
    "v_add_u32_e32 v231, 0x7400, v231\nbuffer_load_dwordx4 v236, %[srdX2], s80 offen offset:2048 sc1 lds\nv_mfma_f32_16x16x32_bf16 v[36:39], a[96:99], a[152:155], v[36:39]\nv_add_u32_e32 v232, 0x7400, v232\n" \
    "ds_read_b128 a[40:43], v224 offset:512\nv_mfma_f32_16x16x32_bf16 a[176:179], a[100:103], a[152:155], a[176:179]\nv_add_u32_e32 v233, 0x7400, v233\nv_mfma_f32_16x16x32_bf16 v[44:47], a[96:99], a[156:159], v[44:47]\n" \
    "ds_read_b128 a[44:47], v224 offset:768\nbuffer_load_dwordx4 v236, %[srdX2], s80 offen offset:3072 sc1 lds\nv_mfma_f32_16x16x32_bf16 a[180:183], a[100:103], a[156:159], a[180:183]\n" \
    "v_mfma_f32_16x16x32_bf16 v[52:55], a[104:107], a[136:139], v[52:55]\nds_read_b128 a[48:51], v224 offset:1024\nv_mfma_f32_16x16x32_bf16 a[184:187], a[108:111], a[136:139], a[184:187]\n" \
    "v_mfma_f32_16x16x32_bf16 v[60:63], a[104:107], a[140:143], v[60:63]\nds_read_b128 a[52:55], v224 offset:1280\ns_add_u32 m0, m0, 0x1000\ns_add_u32 s80, s80, 0x1000\n" \
    "buffer_load_dwordx4 v236, %[srdX2], s80 offen offset:0 sc1 lds\nv_mfma_f32_16x16x32_bf16 a[188:191], a[108:111], a[140:143], a[188:191]\nv_mfma_f32_16x16x32_bf16 v[68:71], a[104:107], a[144:147], v[68:71]\n" \
    "ds_read_b128 a[56:59], v224 offset:1536\nv_mfma_f32_16x16x32_bf16 a[192:195], a[108:111], a[144:147], a[192:195]\ns_mov_b32 m0, %[m0x23]\ns_add_u32 s80, s83, 0x2800\n" \
    "buffer_load_dwordx4 v236, %[srdX2], s80 offen offset:0 sc1 lds\nv_mfma_f32_16x16x32_bf16 v[76:79], a[104:107], a[148:151], v[76:79]\nds_read_b128 a[60:63], v224 offset:1792\n" \
    "v_mfma_f32_16x16x32_bf16 a[196:199], a[108:111], a[148:151], a[196:199]\nv_permlane16_swap_b32_e32 v64, v66\nv_permlane16_swap_b32_e32 v65, v67\nv_mfma_f32_16x16x32_bf16 v[84:87], a[104:107], a[152:155], v[84:87]\n" \
    "ds_read_b128 a[64:67], v224 offset:2048\nbuffer_load_dwordx4 v236, %[srdX2], s80 offen offset:1024 sc1 lds\ns_add_u32 s82, %[so1], 0x4000\nbuffer_store_dwordx4 v[64:67], v244, %[srdO], s82 offen sc1\n" \
    "v_mfma_f32_16x16x32_bf16 a[200:203], a[108:111], a[152:155], a[200:203]\nv_mfma_f32_16x16x32_bf16 v[92:95], a[104:107], a[156:159], v[92:95]\nds_read_b128 a[68:71], v224 offset:2304\n" \
    "v_mfma_f32_16x16x32_bf16 a[204:207], a[108:111], a[156:159], a[204:207]\nv_mfma_f32_16x16x32_bf16 v[100:103], a[112:115], a[136:139], v[100:103]\nds_read_b64_tr_b16 a[72:73], v228 offset:0\n" \
    "buffer_load_dwordx4 v236, %[srdX2], s80 offen offset:2048 sc1 lds\nv_mfma_f32_16x16x32_bf16 a[208:211], a[116:119], a[136:139], a[208:211]\nds_read_b64_tr_b16 a[74:75], v228 offset:1536\n" \
    "v_mfma_f32_16x16x32_bf16 v[108:111], a[112:115], a[140:143], v[108:111]\nv_mfma_f32_16x16x32_bf16 a[212:215], a[116:119], a[140:143], a[212:215]\nds_read_b64_tr_b16 a[76:77], v229 offset:0\n" \
    "buffer_load_dwordx4 v236, %[srdX2], s80 offen offset:3072 sc1 lds\nv_mfma_f32_16x16x32_bf16 v[116:119], a[112:115], a[144:147], v[116:119]\nds_read_b64_tr_b16 a[78:79], v229 offset:1536\n" \
    "v_mfma_f32_16x16x32_bf16 a[216:219], a[116:119], a[144:147], a[216:219]\nv_mfma_f32_16x16x32_bf16 v[124:127], a[112:115], a[148:151], v[124:127]\nds_read_b64_tr_b16 a[80:81], v230 offset:0\ns_add_u32 m0, m0, 0x1000\n" \
    "s_add_u32 s80, s80, 0x1000\nbuffer_load_dwordx4 v236, %[srdX2], s80 offen offset:0 sc1 lds\nv_mfma_f32_16x16x32_bf16 a[220:223], a[116:119], a[148:151], a[220:223]\nds_read_b64_tr_b16 a[82:83], v230 offset:1536\n" \
    "v_mfma_f32_16x16x32_bf16 v[132:135], a[112:115], a[152:155], v[132:135]\nv_mfma_f32_16x16x32_bf16 a[224:227], a[116:119], a[152:155], a[224:227]\nds_read_b64_tr_b16 a[84:85], v231 offset:0\n" \
    "v_mfma_f32_16x16x32_bf16 v[140:143], a[112:115], a[156:159], v[140:143]\nds_read_b64_tr_b16 a[86:87], v231 offset:1536\nv_mfma_f32_16x16x32_bf16 a[228:231], a[116:119], a[156:159], a[228:231]\n" \
    "v_mfma_f32_16x16x32_bf16 v[148:151], a[120:123], a[136:139], v[148:151]\nds_read_b64_tr_b16 a[88:89], v232 offset:0\nv_mfma_f32_16x16x32_bf16 a[232:235], a[124:127], a[136:139], a[232:235]\n" \
    "ds_read_b64_tr_b16 a[90:91], v232 offset:1536\nv_mfma_f32_16x16x32_bf16 v[156:159], a[120:123], a[140:143], v[156:159]\nv_permlane16_swap_b32_e32 v72, v74\nv_permlane16_swap_b32_e32 v73, v75\n" \
    "v_mfma_f32_16x16x32_bf16 a[236:239], a[124:127], a[140:143], a[236:239]\nds_read_b64_tr_b16 a[92:93], v233 offset:0\ns_add_u32 s82, %[so1], 0x5000\nbuffer_store_dwordx4 v[72:75], v244, %[srdO], s82 offen sc1\n" \
    "v_mfma_f32_16x16x32_bf16 v[164:167], a[120:123], a[144:147], v[164:167]\nds_read_b64_tr_b16 a[94:95], v233 offset:1536\nv_mfma_f32_16x16x32_bf16 a[240:243], a[124:127], a[144:147], a[240:243]\n" \
    "v_mfma_f32_16x16x32_bf16 v[172:175], a[120:123], a[148:151], v[172:175]\nv_mfma_f32_16x16x32_bf16 a[244:247], a[124:127], a[148:151], a[244:247]\nv_mfma_f32_16x16x32_bf16 v[180:183], a[120:123], a[152:155], v[180:183]\n" \
    "v_mfma_f32_16x16x32_bf16 a[248:251], a[124:127], a[152:155], a[248:251]\nv_mfma_f32_16x16x32_bf16 v[188:191], a[120:123], a[156:159], v[188:191]\nv_mfma_f32_16x16x32_bf16 a[252:255], a[124:127], a[156:159], a[252:255]\n" \
    "v_mfma_f32_16x16x32_bf16 v[196:199], a[128:131], a[136:139], v[196:199]\nv_mfma_f32_16x16x32_bf16 a[0:3], a[132:135], a[136:139], a[0:3]\nv_mfma_f32_16x16x32_bf16 v[204:207], a[128:131], a[140:143], v[204:207]\n" \
    "v_mfma_f32_16x16x32_bf16 a[4:7], a[132:135], a[140:143], a[4:7]\nv_mfma_f32_16x16x32_bf16 v[212:215], a[128:131], a[144:147], v[212:215]\nv_mfma_f32_16x16x32_bf16 a[8:11], a[132:135], a[144:147], a[8:11]\n" \
    "v_mfma_f32_16x16x32_bf16 v[220:223], a[128:131], a[148:151], v[220:223]\nv_mfma_f32_16x16x32_bf16 a[12:15], a[132:135], a[148:151], a[12:15]\nv_mfma_f32_16x16x32_bf16 a[24:27], a[128:131], a[152:155], a[24:27]\n" \
    "v_mfma_f32_16x16x32_bf16 a[16:19], a[132:135], a[152:155], a[16:19]\nv_mfma_f32_16x16x32_bf16 a[28:31], a[128:131], a[156:159], a[28:31]\nv_mfma_f32_16x16x32_bf16 a[20:23], a[132:135], a[156:159], a[20:23]\n" \
    "s_waitcnt lgkmcnt(0)\nv_mfma_f32_16x16x32_bf16 v[4:7], a[32:35], a[72:75], v[4:7]\nds_read_b128 a[96:99], v225 offset:0\nv_mfma_f32_16x16x32_bf16 a[160:163], a[36:39], a[72:75], a[160:163]\n" \
    "ds_read_b128 a[100:103], v225 offset:256\ns_mov_b32 s83, %[sw2]\nv_permlane16_swap_b32_e32 v80, v82\nv_permlane16_swap_b32_e32 v81, v83\nv_mfma_f32_16x16x32_bf16 v[12:15], a[32:35], a[76:79], v[12:15]\n" \
    "s_add_u32 %[sw2], %[sw2], 0x36000\ns_add_u32 s82, %[so1], 0x6000\nbuffer_store_dwordx4 v[80:83], v244, %[srdO], s82 offen sc1\nv_mfma_f32_16x16x32_bf16 a[164:167], a[36:39], a[76:79], a[164:167]\n" \
    "ds_read_b128 a[104:107], v225 offset:512\ns_add_u32 m0, %[m0w2], 0x10000\ns_add_u32 s81, s83, 0x0\nbuffer_load_dwordx4 v240, %[srdW2], s81 offen offset:0 lds\n" \
    "v_mfma_f32_16x16x32_bf16 v[20:23], a[32:35], a[80:83], v[20:23]\nv_mfma_f32_16x16x32_bf16 a[168:171], a[36:39], a[80:83], a[168:171]\nds_read_b128 a[108:111], v225 offset:768\n" \
    "v_mfma_f32_16x16x32_bf16 v[28:31], a[32:35], a[84:87], v[28:31]\nv_mfma_f32_16x16x32_bf16 a[172:175], a[36:39], a[84:87], a[172:175]\nds_read_b128 a[112:115], v225 offset:1024\n" \
    "v_mfma_f32_16x16x32_bf16 v[36:39], a[32:35], a[88:91], v[36:39]\nv_mfma_f32_16x16x32_bf16 a[176:179], a[36:39], a[88:91], a[176:179]\nds_read_b128 a[116:119], v225 offset:1280\n" \
    "v_mfma_f32_16x16x32_bf16 v[44:47], a[32:35], a[92:95], v[44:47]\nbuffer_load_dwordx4 v241, %[srdW2], s81 offen offset:1024 lds\nv_mfma_f32_16x16x32_bf16 a[180:183], a[36:39], a[92:95], a[180:183]\n" \
    "ds_read_b128 a[120:123], v225 offset:1536\nv_mfma_f32_16x16x32_bf16 v[52:55], a[40:43], a[72:75], v[52:55]\nv_mfma_f32_16x16x32_bf16 a[184:187], a[44:47], a[72:75], a[184:187]\nds_read_b128 a[124:127], v225 offset:1792\n" \
    "v_mfma_f32_16x16x32_bf16 v[60:63], a[40:43], a[76:79], v[60:63]\nv_mfma_f32_16x16x32_bf16 a[188:191], a[44:47], a[76:79], a[188:191]\nds_read_b128 a[128:131], v225 offset:2048\nv_permlane16_swap_b32_e32 v88, v90\n" \
    "v_permlane16_swap_b32_e32 v89, v91\nv_mfma_f32_16x16x32_bf16 v[68:71], a[40:43], a[80:83], v[68:71]\ns_add_u32 s82, %[so1], 0x7000\nbuffer_store_dwordx4 v[88:91], v244, %[srdO], s82 offen sc1\n" \
    "v_mfma_f32_16x16x32_bf16 a[192:195], a[44:47], a[80:83], a[192:195]\nds_read_b128 a[132:135], v225 offset:2304\nv_mfma_f32_16x16x32_bf16 v[76:79], a[40:43], a[84:87], v[76:79]\n" \
    "buffer_load_dwordx4 v242, %[srdW2], s81 offen offset:2048 lds\nv_mfma_f32_16x16x32_bf16 a[196:199], a[44:47], a[84:87], a[196:199]\nds_read_b64_tr_b16 a[136:137], v228 offset:12288\n" \
    "v_mfma_f32_16x16x32_bf16 v[84:87], a[40:43], a[88:91], v[84:87]\nv_mfma_f32_16x16x32_bf16 a[200:203], a[44:47], a[88:91], a[200:203]\nds_read_b64_tr_b16 a[138:139], v228 offset:13824\n" \
    "v_mfma_f32_16x16x32_bf16 v[92:95], a[40:43], a[92:95], v[92:95]\nds_read_b64_tr_b16 a[140:141], v229 offset:12288\nv_mfma_f32_16x16x32_bf16 a[204:207], a[44:47], a[92:95], a[204:207]\n" \
    "v_mfma_f32_16x16x32_bf16 v[100:103], a[48:51], a[72:75], v[100:103]\nds_read_b64_tr_b16 a[142:143], v229 offset:13824\nv_mfma_f32_16x16x32_bf16 a[208:211], a[52:55], a[72:75], a[208:211]\n" \
    "v_mfma_f32_16x16x32_bf16 v[108:111], a[48:51], a[76:79], v[108:111]\nds_read_b64_tr_b16 a[144:145], v230 offset:12288\ns_add_u32 m0, %[m0w2], 0x11800\ns_add_u32 s81, s83, 0xd800\n" \
    "buffer_load_dwordx4 v240, %[srdW2], s81 offen offset:0 lds\nv_mfma_f32_16x16x32_bf16 a[212:215], a[52:55], a[76:79], a[212:215]\nv_mfma_f32_16x16x32_bf16 v[116:119], a[48:51], a[80:83], v[116:119]\n" \
    "ds_read_b64_tr_b16 a[146:147], v230 offset:13824\nv_mfma_f32_16x16x32_bf16 a[216:219], a[52:55], a[80:83], a[216:219]\nv_permlane16_swap_b32_e32 v96, v98\nv_permlane16_swap_b32_e32 v97, v99\n" \
    "v_mfma_f32_16x16x32_bf16 v[124:127], a[48:51], a[84:87], v[124:127]\nds_read_b64_tr_b16 a[148:149], v231 offset:12288\ns_add_u32 s82, %[so2], 0x0\nbuffer_store_dwordx4 v[96:99], v244, %[srdO], s82 offen sc1\n" \
    "v_mfma_f32_16x16x32_bf16 a[220:223], a[52:55], a[84:87], a[220:223]\nv_mfma_f32_16x16x32_bf16 v[132:135], a[48:51], a[88:91], v[132:135]\nds_read_b64_tr_b16 a[150:151], v231 offset:13824\n" \
    "v_mfma_f32_16x16x32_bf16 a[224:227], a[52:55], a[88:91], a[224:227]\nbuffer_load_dwordx4 v241, %[srdW2], s81 offen offset:1024 lds\nv_mfma_f32_16x16x32_bf16 v[140:143], a[48:51], a[92:95], v[140:143]\n" \
    "ds_read_b64_tr_b16 a[152:153], v232 offset:12288\nv_mfma_f32_16x16x32_bf16 a[228:231], a[52:55], a[92:95], a[228:231]\nv_mfma_f32_16x16x32_bf16 v[148:151], a[56:59], a[72:75], v[148:151]\n" \
    "ds_read_b64_tr_b16 a[154:155], v232 offset:13824\nv_mfma_f32_16x16x32_bf16 a[232:235], a[60:63], a[72:75], a[232:235]\nv_mfma_f32_16x16x32_bf16 v[156:159], a[56:59], a[76:79], v[156:159]\n" \
    "ds_read_b64_tr_b16 a[156:157], v233 offset:12288\nv_mfma_f32_16x16x32_bf16 a[236:239], a[60:63], a[76:79], a[236:239]\nv_mfma_f32_16x16x32_bf16 v[164:167], a[56:59], a[80:83], v[164:167]\n" \
    "ds_read_b64_tr_b16 a[158:159], v233 offset:13824\nv_mfma_f32_16x16x32_bf16 a[240:243], a[60:63], a[80:83], a[240:243]\nbuffer_load_dwordx4 v242, %[srdW2], s81 offen offset:2048 lds\n" \
    "v_mfma_f32_16x16x32_bf16 v[172:175], a[56:59], a[84:87], v[172:175]\nv_mfma_f32_16x16x32_bf16 a[244:247], a[60:63], a[84:87], a[244:247]\nv_permlane16_swap_b32_e32 v104, v106\nv_permlane16_swap_b32_e32 v105, v107\n" \
    "v_mfma_f32_16x16x32_bf16 v[180:183], a[56:59], a[88:91], v[180:183]\ns_add_u32 s82, %[so2], 0x1000\nbuffer_store_dwordx4 v[104:107], v244, %[srdO], s82 offen sc1\n" \
    "v_mfma_f32_16x16x32_bf16 a[248:251], a[60:63], a[88:91], a[248:251]\nv_mfma_f32_16x16x32_bf16 v[188:191], a[56:59], a[92:95], v[188:191]\nv_mfma_f32_16x16x32_bf16 a[252:255], a[60:63], a[92:95], a[252:255]\n" \
    "v_mfma_f32_16x16x32_bf16 v[196:199], a[64:67], a[72:75], v[196:199]\nv_mfma_f32_16x16x32_bf16 a[0:3], a[68:71], a[72:75], a[0:3]\nv_mfma_f32_16x16x32_bf16 v[204:207], a[64:67], a[76:79], v[204:207]\n" \
    "v_mfma_f32_16x16x32_bf16 a[4:7], a[68:71], a[76:79], a[4:7]\nv_mfma_f32_16x16x32_bf16 v[212:215], a[64:67], a[80:83], v[212:215]\nv_mfma_f32_16x16x32_bf16 a[8:11], a[68:71], a[80:83], a[8:11]\n" \
    "v_mfma_f32_16x16x32_bf16 v[220:223], a[64:67], a[84:87], v[220:223]\nv_mfma_f32_16x16x32_bf16 a[12:15], a[68:71], a[84:87], a[12:15]\nv_mfma_f32_16x16x32_bf16 a[24:27], a[64:67], a[88:91], a[24:27]\n" \
    "v_mfma_f32_16x16x32_bf16 a[16:19], a[68:71], a[88:91], a[16:19]\nv_mfma_f32_16x16x32_bf16 a[28:31], a[64:67], a[92:95], a[28:31]\nv_mfma_f32_16x16x32_bf16 a[20:23], a[68:71], a[92:95], a[20:23]\n" \
    "s_waitcnt vmcnt(11) lgkmcnt(0)\ns_barrier\nv_mfma_f32_16x16x32_bf16 v[4:7], a[96:99], a[136:139], v[4:7]\ns_mov_b32 s83, %[sx2]\nv_mfma_f32_16x16x32_bf16 a[160:163], a[100:103], a[136:139], a[160:163]\n" \
    "s_mov_b32 m0, %[m0x20]\ns_add_u32 s80, s83, 0x0\nbuffer_load_dwordx4 v236, %[srdX2], s80 offen offset:0 sc1 lds\nv_permlane16_swap_b32_e32 v112, v114\nv_permlane16_swap_b32_e32 v113, v115\n" \
    "v_mfma_f32_16x16x32_bf16 v[12:15], a[96:99], a[140:143], v[12:15]\ns_add_u32 s82, %[so2], 0x2000\nbuffer_store_dwordx4 v[112:115], v244, %[srdO], s82 offen sc1\n" \
    "v_mfma_f32_16x16x32_bf16 a[164:167], a[100:103], a[140:143], a[164:167]\ns_add_u32 %[sx2], %[sx2], 0x14000\nv_mfma_f32_16x16x32_bf16 v[20:23], a[96:99], a[144:147], v[20:23]\nv_add_u32_e32 v228, 0xac00, v228\n" \
    "ds_read_b128 a[32:35], v226 offset:0\nbuffer_load_dwordx4 v236, %[srdX2], s80 offen offset:1024 sc1 lds\nv_mfma_f32_16x16x32_bf16 a[168:171], a[100:103], a[144:147], a[168:171]\nv_add_u32_e32 v229, 0xac00, v229\n" \
    "v_mfma_f32_16x16x32_bf16 v[28:31], a[96:99], a[148:151], v[28:31]\nv_add_u32_e32 v230, 0xac00, v230\nds_read_b128 a[36:39], v226 offset:256\nv_mfma_f32_16x16x32_bf16 a[172:175], a[100:103], a[148:151], a[172:175]\n" \
    "v_add_u32_e32 v231, 0xac00, v231\nbuffer_load_dwordx4 v236, %[srdX2], s80 offen offset:2048 sc1 lds\nv_mfma_f32_16x16x32_bf16 v[36:39], a[96:99], a[152:155], v[36:39]\nv_add_u32_e32 v232, 0xac00, v232\n" \
    "ds_read_b128 a[40:43], v226 offset:512\nv_mfma_f32_16x16x32_bf16 a[176:179], a[100:103], a[152:155], a[176:179]\nv_add_u32_e32 v233, 0xac00, v233\nv_mfma_f32_16x16x32_bf16 v[44:47], a[96:99], a[156:159], v[44:47]\n" \
    "ds_read_b128 a[44:47], v226 offset:768\nbuffer_load_dwordx4 v236, %[srdX2], s80 offen offset:3072 sc1 lds\nv_mfma_f32_16x16x32_bf16 a[180:183], a[100:103], a[156:159], a[180:183]\n" \
    "v_mfma_f32_16x16x32_bf16 v[52:55], a[104:107], a[136:139], v[52:55]\nds_read_b128 a[48:51], v226 offset:1024\nv_mfma_f32_16x16x32_bf16 a[184:187], a[108:111], a[136:139], a[184:187]\n" \
    "v_mfma_f32_16x16x32_bf16 v[60:63], a[104:107], a[140:143], v[60:63]\nds_read_b128 a[52:55], v226 offset:1280\ns_add_u32 m0, m0, 0x1000\ns_add_u32 s80, s80, 0x1000\n" \
    "buffer_load_dwordx4 v236, %[srdX2], s80 offen offset:0 sc1 lds\nv_mfma_f32_16x16x32_bf16 a[188:191], a[108:111], a[140:143], a[188:191]\nv_mfma_f32_16x16x32_bf16 v[68:71], a[104:107], a[144:147], v[68:71]\n" \
    "ds_read_b128 a[56:59], v226 offset:1536\nv_mfma_f32_16x16x32_bf16 a[192:195], a[108:111], a[144:147], a[192:195]\ns_mov_b32 m0, %[m0x21]\ns_add_u32 s80, s83, 0x2800\n" \
    "buffer_load_dwordx4 v236, %[srdX2], s80 offen offset:0 sc1 lds\nv_mfma_f32_16x16x32_bf16 v[76:79], a[104:107], a[148:151], v[76:79]\nds_read_b128 a[60:63], v226 offset:1792\n" \
    "v_mfma_f32_16x16x32_bf16 a[196:199], a[108:111], a[148:151], a[196:199]\nv_permlane16_swap_b32_e32 v120, v122\nv_permlane16_swap_b32_e32 v121, v123\nv_mfma_f32_16x16x32_bf16 v[84:87], a[104:107], a[152:155], v[84:87]\n" \
    "ds_read_b128 a[64:67], v226 offset:2048\nbuffer_load_dwordx4 v236, %[srdX2], s80 offen offset:1024 sc1 lds\ns_add_u32 s82, %[so2], 0x3000\nbuffer_store_dwordx4 v[120:123], v244, %[srdO], s82 offen sc1\n" \
    "v_mfma_f32_16x16x32_bf16 a[200:203], a[108:111], a[152:155], a[200:203]\nv_mfma_f32_16x16x32_bf16 v[92:95], a[104:107], a[156:159], v[92:95]\nds_read_b128 a[68:71], v226 offset:2304\n" \
    "v_mfma_f32_16x16x32_bf16 a[204:207], a[108:111], a[156:159], a[204:207]\nv_mfma_f32_16x16x32_bf16 v[100:103], a[112:115], a[136:139], v[100:103]\nds_read_b64_tr_b16 a[72:73], v228 offset:0\n" \
    "buffer_load_dwordx4 v236, %[srdX2], s80 offen offset:2048 sc1 lds\nv_mfma_f32_16x16x32_bf16 a[208:211], a[116:119], a[136:139], a[208:211]\nds_read_b64_tr_b16 a[74:75], v228 offset:1536\n" \
    "v_mfma_f32_16x16x32_bf16 v[108:111], a[112:115], a[140:143], v[108:111]\nv_mfma_f32_16x16x32_bf16 a[212:215], a[116:119], a[140:143], a[212:215]\nds_read_b64_tr_b16 a[76:77], v229 offset:0\n" \
    "buffer_load_dwordx4 v236, %[srdX2], s80 offen offset:3072 sc1 lds\nv_mfma_f32_16x16x32_bf16 v[116:119], a[112:115], a[144:147], v[116:119]\nds_read_b64_tr_b16 a[78:79], v229 offset:1536\n" \
    "v_mfma_f32_16x16x32_bf16 a[216:219], a[116:119], a[144:147], a[216:219]\nv_mfma_f32_16x16x32_bf16 v[124:127], a[112:115], a[148:151], v[124:127]\nds_read_b64_tr_b16 a[80:81], v230 offset:0\ns_add_u32 m0, m0, 0x1000\n" \
    "s_add_u32 s80, s80, 0x1000\nbuffer_load_dwordx4 v236, %[srdX2], s80 offen offset:0 sc1 lds\nv_mfma_f32_16x16x32_bf16 a[220:223], a[116:119], a[148:151], a[220:223]\nds_read_b64_tr_b16 a[82:83], v230 offset:1536\n" \
    "v_mfma_f32_16x16x32_bf16 v[132:135], a[112:115], a[152:155], v[132:135]\nv_mfma_f32_16x16x32_bf16 a[224:227], a[116:119], a[152:155], a[224:227]\nds_read_b64_tr_b16 a[84:85], v231 offset:0\n" \
    "v_mfma_f32_16x16x32_bf16 v[140:143], a[112:115], a[156:159], v[140:143]\nds_read_b64_tr_b16 a[86:87], v231 offset:1536\nv_mfma_f32_16x16x32_bf16 a[228:231], a[116:119], a[156:159], a[228:231]\n" \
    "v_mfma_f32_16x16x32_bf16 v[148:151], a[120:123], a[136:139], v[148:151]\nds_read_b64_tr_b16 a[88:89], v232 offset:0\nv_mfma_f32_16x16x32_bf16 a[232:235], a[124:127], a[136:139], a[232:235]\n" \
    "ds_read_b64_tr_b16 a[90:91], v232 offset:1536\nv_mfma_f32_16x16x32_bf16 v[156:159], a[120:123], a[140:143], v[156:159]\nv_permlane16_swap_b32_e32 v128, v130\nv_permlane16_swap_b32_e32 v129, v131\n" \
    "v_mfma_f32_16x16x32_bf16 a[236:239], a[124:127], a[140:143], a[236:239]\nds_read_b64_tr_b16 a[92:93], v233 offset:0\ns_add_u32 s82, %[so2], 0x4000\nbuffer_store_dwordx4 v[128:131], v244, %[srdO], s82 offen sc1\n" \
    "v_mfma_f32_16x16x32_bf16 v[164:167], a[120:123], a[144:147], v[164:167]\nds_read_b64_tr_b16 a[94:95], v233 offset:1536\nv_mfma_f32_16x16x32_bf16 a[240:243], a[124:127], a[144:147], a[240:243]\n" \
    "v_mfma_f32_16x16x32_bf16 v[172:175], a[120:123], a[148:151], v[172:175]\nv_mfma_f32_16x16x32_bf16 a[244:247], a[124:127], a[148:151], a[244:247]\nv_mfma_f32_16x16x32_bf16 v[180:183], a[120:123], a[152:155], v[180:183]\n" \
    "v_mfma_f32_16x16x32_bf16 a[248:251], a[124:127], a[152:155], a[248:251]\nv_mfma_f32_16x16x32_bf16 v[188:191], a[120:123], a[156:159], v[188:191]\nv_mfma_f32_16x16x32_bf16 a[252:255], a[124:127], a[156:159], a[252:255]\n" \
    "v_mfma_f32_16x16x32_bf16 v[196:199], a[128:131], a[136:139], v[196:199]\nv_mfma_f32_16x16x32_bf16 a[0:3], a[132:135], a[136:139], a[0:3]\nv_mfma_f32_16x16x32_bf16 v[204:207], a[128:131], a[140:143], v[204:207]\n" \
    "v_mfma_f32_16x16x32_bf16 a[4:7], a[132:135], a[140:143], a[4:7]\nv_mfma_f32_16x16x32_bf16 v[212:215], a[128:131], a[144:147], v[212:215]\nv_mfma_f32_16x16x32_bf16 a[8:11], a[132:135], a[144:147], a[8:11]\n" \
    "v_mfma_f32_16x16x32_bf16 v[220:223], a[128:131], a[148:151], v[220:223]\nv_mfma_f32_16x16x32_bf16 a[12:15], a[132:135], a[148:151], a[12:15]\nv_mfma_f32_16x16x32_bf16 a[24:27], a[128:131], a[152:155], a[24:27]\n" \
    "v_mfma_f32_16x16x32_bf16 a[16:19], a[132:135], a[152:155], a[16:19]\nv_mfma_f32_16x16x32_bf16 a[28:31], a[128:131], a[156:159], a[28:31]\nv_mfma_f32_16x16x32_bf16 a[20:23], a[132:135], a[156:159], a[20:23]\n" \
    "s_waitcnt lgkmcnt(0)\nv_mfma_f32_16x16x32_bf16 v[4:7], a[32:35], a[72:75], v[4:7]\nds_read_b128 a[96:99], v227 offset:0\nv_mfma_f32_16x16x32_bf16 a[160:163], a[36:39], a[72:75], a[160:163]\n" \
    "ds_read_b128 a[100:103], v227 offset:256\ns_mov_b32 s83, %[sw2]\nv_permlane16_swap_b32_e32 v136, v138\nv_permlane16_swap_b32_e32 v137, v139\nv_mfma_f32_16x16x32_bf16 v[12:15], a[32:35], a[76:79], v[12:15]\n" \
    "s_add_u32 %[sw2], %[sw2], 0x36000\ns_add_u32 s82, %[so2], 0x5000\nbuffer_store_dwordx4 v[136:139], v244, %[srdO], s82 offen sc1\nv_mfma_f32_16x16x32_bf16 a[164:167], a[36:39], a[76:79], a[164:167]\n" \
    "ds_read_b128 a[104:107], v227 offset:512\ns_add_u32 m0, %[m0w2], 0x17400\ns_add_u32 s81, s83, 0x0\nbuffer_load_dwordx4 v240, %[srdW2], s81 offen offset:0 lds\n" \
    "v_mfma_f32_16x16x32_bf16 v[20:23], a[32:35], a[80:83], v[20:23]\nv_mfma_f32_16x16x32_bf16 a[168:171], a[36:39], a[80:83], a[168:171]\nds_read_b128 a[108:111], v227 offset:768\n" \
    "v_mfma_f32_16x16x32_bf16 v[28:31], a[32:35], a[84:87], v[28:31]\nv_mfma_f32_16x16x32_bf16 a[172:175], a[36:39], a[84:87], a[172:175]\nds_read_b128 a[112:115], v227 offset:1024\n" \
    "v_mfma_f32_16x16x32_bf16 v[36:39], a[32:35], a[88:91], v[36:39]\nv_mfma_f32_16x16x32_bf16 a[176:179], a[36:39], a[88:91], a[176:179]\nds_read_b128 a[116:119], v227 offset:1280\n" \
    "v_mfma_f32_16x16x32_bf16 v[44:47], a[32:35], a[92:95], v[44:47]\nbuffer_load_dwordx4 v241, %[srdW2], s81 offen offset:1024 lds\nv_mfma_f32_16x16x32_bf16 a[180:183], a[36:39], a[92:95], a[180:183]\n" \
    "ds_read_b128 a[120:123], v227 offset:1536\nv_mfma_f32_16x16x32_bf16 v[52:55], a[40:43], a[72:75], v[52:55]\nv_mfma_f32_16x16x32_bf16 a[184:187], a[44:47], a[72:75], a[184:187]\nds_read_b128 a[124:127], v227 offset:1792\n" \
    "v_mfma_f32_16x16x32_bf16 v[60:63], a[40:43], a[76:79], v[60:63]\nv_mfma_f32_16x16x32_bf16 a[188:191], a[44:47], a[76:79], a[188:191]\nds_read_b128 a[128:131], v227 offset:2048\nv_permlane16_swap_b32_e32 v144, v146\n" \
    "v_permlane16_swap_b32_e32 v145, v147\nv_mfma_f32_16x16x32_bf16 v[68:71], a[40:43], a[80:83], v[68:71]\ns_add_u32 s82, %[so2], 0x6000\nbuffer_store_dwordx4 v[144:147], v244, %[srdO], s82 offen sc1\n" \
    "v_mfma_f32_16x16x32_bf16 a[192:195], a[44:47], a[80:83], a[192:195]\nds_read_b128 a[132:135], v227 offset:2304\nv_mfma_f32_16x16x32_bf16 v[76:79], a[40:43], a[84:87], v[76:79]\n" \
    "buffer_load_dwordx4 v242, %[srdW2], s81 offen offset:2048 lds\nv_mfma_f32_16x16x32_bf16 a[196:199], a[44:47], a[84:87], a[196:199]\nds_read_b64_tr_b16 a[136:137], v228 offset:12288\n" \
    "v_mfma_f32_16x16x32_bf16 v[84:87], a[40:43], a[88:91], v[84:87]\nv_mfma_f32_16x16x32_bf16 a[200:203], a[44:47], a[88:91], a[200:203]\nds_read_b64_tr_b16 a[138:139], v228 offset:13824\n" \
    "v_mfma_f32_16x16x32_bf16 v[92:95], a[40:43], a[92:95], v[92:95]\nds_read_b64_tr_b16 a[140:141], v229 offset:12288\nv_mfma_f32_16x16x32_bf16 a[204:207], a[44:47], a[92:95], a[204:207]\n" \
    "v_mfma_f32_16x16x32_bf16 v[100:103], a[48:51], a[72:75], v[100:103]\nds_read_b64_tr_b16 a[142:143], v229 offset:13824\nv_mfma_f32_16x16x32_bf16 a[208:211], a[52:55], a[72:75], a[208:211]\n" \
    "v_mfma_f32_16x16x32_bf16 v[108:111], a[48:51], a[76:79], v[108:111]\nds_read_b64_tr_b16 a[144:145], v230 offset:12288\ns_add_u32 m0, %[m0w2], 0x18c00\ns_add_u32 s81, s83, 0xd800\n" \
    "buffer_load_dwordx4 v240, %[srdW2], s81 offen offset:0 lds\nv_mfma_f32_16x16x32_bf16 a[212:215], a[52:55], a[76:79], a[212:215]\nv_mfma_f32_16x16x32_bf16 v[116:119], a[48:51], a[80:83], v[116:119]\n" \
    "ds_read_b64_tr_b16 a[146:147], v230 offset:13824\nv_mfma_f32_16x16x32_bf16 a[216:219], a[52:55], a[80:83], a[216:219]\nv_permlane16_swap_b32_e32 v152, v154\nv_permlane16_swap_b32_e32 v153, v155\n" \
    "v_mfma_f32_16x16x32_bf16 v[124:127], a[48:51], a[84:87], v[124:127]\nds_read_b64_tr_b16 a[148:149], v231 offset:12288\ns_add_u32 s82, %[so2], 0x7000\nbuffer_store_dwordx4 v[152:155], v244, %[srdO], s82 offen sc1\n" \
    "v_mfma_f32_16x16x32_bf16 a[220:223], a[52:55], a[84:87], a[220:223]\nv_mfma_f32_16x16x32_bf16 v[132:135], a[48:51], a[88:91], v[132:135]\nds_read_b64_tr_b16 a[150:151], v231 offset:13824\n" \
    "v_mfma_f32_16x16x32_bf16 a[224:227], a[52:55], a[88:91], a[224:227]\nbuffer_load_dwordx4 v241, %[srdW2], s81 offen offset:1024 lds\nv_mfma_f32_16x16x32_bf16 v[140:143], a[48:51], a[92:95], v[140:143]\n" \
    "ds_read_b64_tr_b16 a[152:153], v232 offset:12288\nv_mfma_f32_16x16x32_bf16 a[228:231], a[52:55], a[92:95], a[228:231]\nv_mfma_f32_16x16x32_bf16 v[148:151], a[56:59], a[72:75], v[148:151]\n" \
    "ds_read_b64_tr_b16 a[154:155], v232 offset:13824\nv_mfma_f32_16x16x32_bf16 a[232:235], a[60:63], a[72:75], a[232:235]\nv_mfma_f32_16x16x32_bf16 v[156:159], a[56:59], a[76:79], v[156:159]\n" \
    "ds_read_b64_tr_b16 a[156:157], v233 offset:12288\nv_mfma_f32_16x16x32_bf16 a[236:239], a[60:63], a[76:79], a[236:239]\nv_mfma_f32_16x16x32_bf16 v[164:167], a[56:59], a[80:83], v[164:167]\n" \
    "ds_read_b64_tr_b16 a[158:159], v233 offset:13824\nv_mfma_f32_16x16x32_bf16 a[240:243], a[60:63], a[80:83], a[240:243]\nbuffer_load_dwordx4 v242, %[srdW2], s81 offen offset:2048 lds\n" \
    "v_mfma_f32_16x16x32_bf16 v[172:175], a[56:59], a[84:87], v[172:175]\nv_mfma_f32_16x16x32_bf16 a[244:247], a[60:63], a[84:87], a[244:247]\nv_permlane16_swap_b32_e32 v160, v162\nv_permlane16_swap_b32_e32 v161, v163\n" \
    "v_mfma_f32_16x16x32_bf16 v[180:183], a[56:59], a[88:91], v[180:183]\ns_add_u32 s82, %[so3], 0x0\nbuffer_store_dwordx4 v[160:163], v244, %[srdO], s82 offen sc1\n" \
    "v_mfma_f32_16x16x32_bf16 a[248:251], a[60:63], a[88:91], a[248:251]\nv_mfma_f32_16x16x32_bf16 v[188:191], a[56:59], a[92:95], v[188:191]\nv_mfma_f32_16x16x32_bf16 a[252:255], a[60:63], a[92:95], a[252:255]\n" \
    "v_mfma_f32_16x16x32_bf16 v[196:199], a[64:67], a[72:75], v[196:199]\nv_mfma_f32_16x16x32_bf16 a[0:3], a[68:71], a[72:75], a[0:3]\nv_mfma_f32_16x16x32_bf16 v[204:207], a[64:67], a[76:79], v[204:207]\n" \
    "v_mfma_f32_16x16x32_bf16 a[4:7], a[68:71], a[76:79], a[4:7]\nv_mfma_f32_16x16x32_bf16 v[212:215], a[64:67], a[80:83], v[212:215]\nv_mfma_f32_16x16x32_bf16 a[8:11], a[68:71], a[80:83], a[8:11]\n" \
    "v_mfma_f32_16x16x32_bf16 v[220:223], a[64:67], a[84:87], v[220:223]\nv_mfma_f32_16x16x32_bf16 a[12:15], a[68:71], a[84:87], a[12:15]\nv_mfma_f32_16x16x32_bf16 a[24:27], a[64:67], a[88:91], a[24:27]\n" \
    "v_mfma_f32_16x16x32_bf16 a[16:19], a[68:71], a[88:91], a[16:19]\nv_mfma_f32_16x16x32_bf16 a[28:31], a[64:67], a[92:95], a[28:31]\nv_mfma_f32_16x16x32_bf16 a[20:23], a[68:71], a[92:95], a[20:23]\n" \
    "s_waitcnt vmcnt(11) lgkmcnt(0)\ns_barrier\nv_mfma_f32_16x16x32_bf16 v[4:7], a[96:99], a[136:139], v[4:7]\ns_mov_b32 s83, %[sx2]\nv_mfma_f32_16x16x32_bf16 a[160:163], a[100:103], a[136:139], a[160:163]\n" \
    "s_mov_b32 m0, %[m0x22]\ns_add_u32 s80, s83, 0x0\nbuffer_load_dwordx4 v236, %[srdX2], s80 offen offset:0 sc1 lds\nv_permlane16_swap_b32_e32 v168, v170\nv_permlane16_swap_b32_e32 v169, v171\n" \
    "v_mfma_f32_16x16x32_bf16 v[12:15], a[96:99], a[140:143], v[12:15]\ns_add_u32 s82, %[so3], 0x1000\nbuffer_store_dwordx4 v[168:171], v244, %[srdO], s82 offen sc1\n" \
    "v_mfma_f32_16x16x32_bf16 a[164:167], a[100:103], a[140:143], a[164:167]\ns_add_u32 %[sx2], %[sx2], 0x14000\nv_mfma_f32_16x16x32_bf16 v[20:23], a[96:99], a[144:147], v[20:23]\nv_add_u32_e32 v228, 0xfffee000, v228\n" \
    "ds_read_b128 a[32:35], v224 offset:0\nbuffer_load_dwordx4 v236, %[srdX2], s80 offen offset:1024 sc1 lds\nv_mfma_f32_16x16x32_bf16 a[168:171], a[100:103], a[144:147], a[168:171]\nv_add_u32_e32 v229, 0xfffee000, v229\n" \
    "v_mfma_f32_16x16x32_bf16 v[28:31], a[96:99], a[148:151], v[28:31]\nv_add_u32_e32 v230, 0xfffee000, v230\nds_read_b128 a[36:39], v224 offset:256\nv_mfma_f32_16x16x32_bf16 a[172:175], a[100:103], a[148:151], a[172:175]\n" \
    "v_add_u32_e32 v231, 0xfffee000, v231\nbuffer_load_dwordx4 v236, %[srdX2], s80 offen offset:2048 sc1 lds\nv_mfma_f32_16x16x32_bf16 v[36:39], a[96:99], a[152:155], v[36:39]\nv_add_u32_e32 v232, 0xfffee000, v232\n" \
    "ds_read_b128 a[40:43], v224 offset:512\nv_mfma_f32_16x16x32_bf16 a[176:179], a[100:103], a[152:155], a[176:179]\nv_add_u32_e32 v233, 0xfffee000, v233\nv_mfma_f32_16x16x32_bf16 v[44:47], a[96:99], a[156:159], v[44:47]\n" \
    "ds_read_b128 a[44:47], v224 offset:768\nbuffer_load_dwordx4 v236, %[srdX2], s80 offen offset:3072 sc1 lds\nv_mfma_f32_16x16x32_bf16 a[180:183], a[100:103], a[156:159], a[180:183]\n" \
    "v_mfma_f32_16x16x32_bf16 v[52:55], a[104:107], a[136:139], v[52:55]\nds_read_b128 a[48:51], v224 offset:1024\nv_mfma_f32_16x16x32_bf16 a[184:187], a[108:111], a[136:139], a[184:187]\n" \
    "v_mfma_f32_16x16x32_bf16 v[60:63], a[104:107], a[140:143], v[60:63]\nds_read_b128 a[52:55], v224 offset:1280\ns_add_u32 m0, m0, 0x1000\ns_add_u32 s80, s80, 0x1000\n" \
    "buffer_load_dwordx4 v236, %[srdX2], s80 offen offset:0 sc1 lds\nv_mfma_f32_16x16x32_bf16 a[188:191], a[108:111], a[140:143], a[188:191]\nv_mfma_f32_16x16x32_bf16 v[68:71], a[104:107], a[144:147], v[68:71]\n" \
    "ds_read_b128 a[56:59], v224 offset:1536\nv_mfma_f32_16x16x32_bf16 a[192:195], a[108:111], a[144:147], a[192:195]\ns_mov_b32 m0, %[m0x23]\ns_add_u32 s80, s83, 0x2800\n" \
    "buffer_load_dwordx4 v236, %[srdX2], s80 offen offset:0 sc1 lds\nv_mfma_f32_16x16x32_bf16 v[76:79], a[104:107], a[148:151], v[76:79]\nds_read_b128 a[60:63], v224 offset:1792\n" \
    "v_mfma_f32_16x16x32_bf16 a[196:199], a[108:111], a[148:151], a[196:199]\nv_permlane16_swap_b32_e32 v176, v178\nv_permlane16_swap_b32_e32 v177, v179\nv_mfma_f32_16x16x32_bf16 v[84:87], a[104:107], a[152:155], v[84:87]\n" \
    "ds_read_b128 a[64:67], v224 offset:2048\nbuffer_load_dwordx4 v236, %[srdX2], s80 offen offset:1024 sc1 lds\ns_add_u32 s82, %[so3], 0x2000\nbuffer_store_dwordx4 v[176:179], v244, %[srdO], s82 offen sc1\n" \
    "v_mfma_f32_16x16x32_bf16 a[200:203], a[108:111], a[152:155], a[200:203]\nv_mfma_f32_16x16x32_bf16 v[92:95], a[104:107], a[156:159], v[92:95]\nds_read_b128 a[68:71], v224 offset:2304\n" \
    "v_mfma_f32_16x16x32_bf16 a[204:207], a[108:111], a[156:159], a[204:207]\nv_mfma_f32_16x16x32_bf16 v[100:103], a[112:115], a[136:139], v[100:103]\nds_read_b64_tr_b16 a[72:73], v228 offset:0\n" \
    "buffer_load_dwordx4 v236, %[srdX2], s80 offen offset:2048 sc1 lds\nv_mfma_f32_16x16x32_bf16 a[208:211], a[116:119], a[136:139], a[208:211]\nds_read_b64_tr_b16 a[74:75], v228 offset:1536\n" \
    "v_mfma_f32_16x16x32_bf16 v[108:111], a[112:115], a[140:143], v[108:111]\nv_mfma_f32_16x16x32_bf16 a[212:215], a[116:119], a[140:143], a[212:215]\nds_read_b64_tr_b16 a[76:77], v229 offset:0\n" \
    "buffer_load_dwordx4 v236, %[srdX2], s80 offen offset:3072 sc1 lds\nv_mfma_f32_16x16x32_bf16 v[116:119], a[112:115], a[144:147], v[116:119]\nds_read_b64_tr_b16 a[78:79], v229 offset:1536\n" \
    "v_mfma_f32_16x16x32_bf16 a[216:219], a[116:119], a[144:147], a[216:219]\nv_mfma_f32_16x16x32_bf16 v[124:127], a[112:115], a[148:151], v[124:127]\nds_read_b64_tr_b16 a[80:81], v230 offset:0\ns_add_u32 m0, m0, 0x1000\n" \
    "s_add_u32 s80, s80, 0x1000\nbuffer_load_dwordx4 v236, %[srdX2], s80 offen offset:0 sc1 lds\nv_mfma_f32_16x16x32_bf16 a[220:223], a[116:119], a[148:151], a[220:223]\nds_read_b64_tr_b16 a[82:83], v230 offset:1536\n" \
    "v_mfma_f32_16x16x32_bf16 v[132:135], a[112:115], a[152:155], v[132:135]\nv_mfma_f32_16x16x32_bf16 a[224:227], a[116:119], a[152:155], a[224:227]\nds_read_b64_tr_b16 a[84:85], v231 offset:0\n" \
    "v_mfma_f32_16x16x32_bf16 v[140:143], a[112:115], a[156:159], v[140:143]\nds_read_b64_tr_b16 a[86:87], v231 offset:1536\nv_mfma_f32_16x16x32_bf16 a[228:231], a[116:119], a[156:159], a[228:231]\n" \
    "v_mfma_f32_16x16x32_bf16 v[148:151], a[120:123], a[136:139], v[148:151]\nds_read_b64_tr_b16 a[88:89], v232 offset:0\nv_mfma_f32_16x16x32_bf16 a[232:235], a[124:127], a[136:139], a[232:235]\n" \
    "ds_read_b64_tr_b16 a[90:91], v232 offset:1536\nv_mfma_f32_16x16x32_bf16 v[156:159], a[120:123], a[140:143], v[156:159]\nv_permlane16_swap_b32_e32 v184, v186\nv_permlane16_swap_b32_e32 v185, v187\n" \
    "v_mfma_f32_16x16x32_bf16 a[236:239], a[124:127], a[140:143], a[236:239]\nds_read_b64_tr_b16 a[92:93], v233 offset:0\ns_add_u32 s82, %[so3], 0x3000\nbuffer_store_dwordx4 v[184:187], v244, %[srdO], s82 offen sc1\n" \
    "v_mfma_f32_16x16x32_bf16 v[164:167], a[120:123], a[144:147], v[164:167]\nds_read_b64_tr_b16 a[94:95], v233 offset:1536\nv_mfma_f32_16x16x32_bf16 a[240:243], a[124:127], a[144:147], a[240:243]\n" \
    "v_mfma_f32_16x16x32_bf16 v[172:175], a[120:123], a[148:151], v[172:175]\nv_mfma_f32_16x16x32_bf16 a[244:247], a[124:127], a[148:151], a[244:247]\nv_mfma_f32_16x16x32_bf16 v[180:183], a[120:123], a[152:155], v[180:183]\n" \
    "v_mfma_f32_16x16x32_bf16 a[248:251], a[124:127], a[152:155], a[248:251]\nv_mfma_f32_16x16x32_bf16 v[188:191], a[120:123], a[156:159], v[188:191]\nv_mfma_f32_16x16x32_bf16 a[252:255], a[124:127], a[156:159], a[252:255]\n" \
    "v_mfma_f32_16x16x32_bf16 v[196:199], a[128:131], a[136:139], v[196:199]\nv_mfma_f32_16x16x32_bf16 a[0:3], a[132:135], a[136:139], a[0:3]\nv_mfma_f32_16x16x32_bf16 v[204:207], a[128:131], a[140:143], v[204:207]\n" \
    "v_mfma_f32_16x16x32_bf16 a[4:7], a[132:135], a[140:143], a[4:7]\nv_mfma_f32_16x16x32_bf16 v[212:215], a[128:131], a[144:147], v[212:215]\nv_mfma_f32_16x16x32_bf16 a[8:11], a[132:135], a[144:147], a[8:11]\n" \
    "v_mfma_f32_16x16x32_bf16 v[220:223], a[128:131], a[148:151], v[220:223]\nv_mfma_f32_16x16x32_bf16 a[12:15], a[132:135], a[148:151], a[12:15]\nv_mfma_f32_16x16x32_bf16 a[24:27], a[128:131], a[152:155], a[24:27]\n" \
    "v_mfma_f32_16x16x32_bf16 a[16:19], a[132:135], a[152:155], a[16:19]\nv_mfma_f32_16x16x32_bf16 a[28:31], a[128:131], a[156:159], a[28:31]\nv_mfma_f32_16x16x32_bf16 a[20:23], a[132:135], a[156:159], a[20:23]\n" \
    "s_waitcnt lgkmcnt(0)\nv_mfma_f32_16x16x32_bf16 v[4:7], a[32:35], a[72:75], v[4:7]\nds_read_b128 a[96:99], v225 offset:0\nv_mfma_f32_16x16x32_bf16 a[160:163], a[36:39], a[72:75], a[160:163]\n" \
    "ds_read_b128 a[100:103], v225 offset:256\ns_mov_b32 s83, %[sw2]\nv_permlane16_swap_b32_e32 v192, v194\nv_permlane16_swap_b32_e32 v193, v195\nv_mfma_f32_16x16x32_bf16 v[12:15], a[32:35], a[76:79], v[12:15]\n" \
    "s_add_u32 %[sw2], %[sw2], 0x36000\ns_add_u32 s82, %[so3], 0x4000\nbuffer_store_dwordx4 v[192:195], v244, %[srdO], s82 offen sc1\nv_mfma_f32_16x16x32_bf16 a[164:167], a[36:39], a[76:79], a[164:167]\n" \
    "ds_read_b128 a[104:107], v225 offset:512\ns_add_u32 m0, %[m0w2], 0x22000\ns_add_u32 s81, s83, 0x0\nbuffer_load_dwordx4 v240, %[srdW2], s81 offen offset:0 lds\n" \
    "v_mfma_f32_16x16x32_bf16 v[20:23], a[32:35], a[80:83], v[20:23]\nv_mfma_f32_16x16x32_bf16 a[168:171], a[36:39], a[80:83], a[168:171]\nds_read_b128 a[108:111], v225 offset:768\n" \
    "v_mfma_f32_16x16x32_bf16 v[28:31], a[32:35], a[84:87], v[28:31]\nv_mfma_f32_16x16x32_bf16 a[172:175], a[36:39], a[84:87], a[172:175]\nds_read_b128 a[112:115], v225 offset:1024\n" \
    "v_mfma_f32_16x16x32_bf16 v[36:39], a[32:35], a[88:91], v[36:39]\nv_mfma_f32_16x16x32_bf16 a[176:179], a[36:39], a[88:91], a[176:179]\nds_read_b128 a[116:119], v225 offset:1280\n" \
    "v_mfma_f32_16x16x32_bf16 v[44:47], a[32:35], a[92:95], v[44:47]\nbuffer_load_dwordx4 v241, %[srdW2], s81 offen offset:1024 lds\nv_mfma_f32_16x16x32_bf16 a[180:183], a[36:39], a[92:95], a[180:183]\n" \
    "ds_read_b128 a[120:123], v225 offset:1536\nv_mfma_f32_16x16x32_bf16 v[52:55], a[40:43], a[72:75], v[52:55]\nv_mfma_f32_16x16x32_bf16 a[184:187], a[44:47], a[72:75], a[184:187]\nds_read_b128 a[124:127], v225 offset:1792\n" \
    "v_mfma_f32_16x16x32_bf16 v[60:63], a[40:43], a[76:79], v[60:63]\nv_mfma_f32_16x16x32_bf16 a[188:191], a[44:47], a[76:79], a[188:191]\nds_read_b128 a[128:131], v225 offset:2048\nv_permlane16_swap_b32_e32 v200, v202\n" \
    "v_permlane16_swap_b32_e32 v201, v203\nv_mfma_f32_16x16x32_bf16 v[68:71], a[40:43], a[80:83], v[68:71]\ns_add_u32 s82, %[so3], 0x5000\nbuffer_store_dwordx4 v[200:203], v244, %[srdO], s82 offen sc1\n" \
    "v_mfma_f32_16x16x32_bf16 a[192:195], a[44:47], a[80:83], a[192:195]\nds_read_b128 a[132:135], v225 offset:2304\nv_mfma_f32_16x16x32_bf16 v[76:79], a[40:43], a[84:87], v[76:79]\n" \
    "buffer_load_dwordx4 v242, %[srdW2], s81 offen offset:2048 lds\nv_mfma_f32_16x16x32_bf16 a[196:199], a[44:47], a[84:87], a[196:199]\nds_read_b64_tr_b16 a[136:137], v228 offset:12288\n" \
    "v_mfma_f32_16x16x32_bf16 v[84:87], a[40:43], a[88:91], v[84:87]\nv_mfma_f32_16x16x32_bf16 a[200:203], a[44:47], a[88:91], a[200:203]\nds_read_b64_tr_b16 a[138:139], v228 offset:13824\n" \
    "v_mfma_f32_16x16x32_bf16 v[92:95], a[40:43], a[92:95], v[92:95]\nds_read_b64_tr_b16 a[140:141], v229 offset:12288\nv_mfma_f32_16x16x32_bf16 a[204:207], a[44:47], a[92:95], a[204:207]\n" \
    "v_mfma_f32_16x16x32_bf16 v[100:103], a[48:51], a[72:75], v[100:103]\nds_read_b64_tr_b16 a[142:143], v229 offset:13824\nv_mfma_f32_16x16x32_bf16 a[208:211], a[52:55], a[72:75], a[208:211]\n" \
    "v_mfma_f32_16x16x32_bf16 v[108:111], a[48:51], a[76:79], v[108:111]\nds_read_b64_tr_b16 a[144:145], v230 offset:12288\ns_add_u32 m0, %[m0w2], 0x23800\ns_add_u32 s81, s83, 0xd800\n" \
    "buffer_load_dwordx4 v240, %[srdW2], s81 offen offset:0 lds\nv_mfma_f32_16x16x32_bf16 a[212:215], a[52:55], a[76:79], a[212:215]\nv_mfma_f32_16x16x32_bf16 v[116:119], a[48:51], a[80:83], v[116:119]\n" \
    "ds_read_b64_tr_b16 a[146:147], v230 offset:13824\nv_mfma_f32_16x16x32_bf16 a[216:219], a[52:55], a[80:83], a[216:219]\nv_permlane16_swap_b32_e32 v208, v210\nv_permlane16_swap_b32_e32 v209, v211\n" \
    "v_mfma_f32_16x16x32_bf16 v[124:127], a[48:51], a[84:87], v[124:127]\nds_read_b64_tr_b16 a[148:149], v231 offset:12288\ns_add_u32 s82, %[so3], 0x6000\nbuffer_store_dwordx4 v[208:211], v244, %[srdO], s82 offen sc1\n" \
    "v_mfma_f32_16x16x32_bf16 a[220:223], a[52:55], a[84:87], a[220:223]\nv_mfma_f32_16x16x32_bf16 v[132:135], a[48:51], a[88:91], v[132:135]\nds_read_b64_tr_b16 a[150:151], v231 offset:13824\n" \
    "v_mfma_f32_16x16x32_bf16 a[224:227], a[52:55], a[88:91], a[224:227]\nbuffer_load_dwordx4 v241, %[srdW2], s81 offen offset:1024 lds\nv_mfma_f32_16x16x32_bf16 v[140:143], a[48:51], a[92:95], v[140:143]\n" \
    "ds_read_b64_tr_b16 a[152:153], v232 offset:12288\nv_mfma_f32_16x16x32_bf16 a[228:231], a[52:55], a[92:95], a[228:231]\nv_mfma_f32_16x16x32_bf16 v[148:151], a[56:59], a[72:75], v[148:151]\n" \
    "ds_read_b64_tr_b16 a[154:155], v232 offset:13824\nv_mfma_f32_16x16x32_bf16 a[232:235], a[60:63], a[72:75], a[232:235]\nv_mfma_f32_16x16x32_bf16 v[156:159], a[56:59], a[76:79], v[156:159]\n" \
    "ds_read_b64_tr_b16 a[156:157], v233 offset:12288\nv_mfma_f32_16x16x32_bf16 a[236:239], a[60:63], a[76:79], a[236:239]\nv_mfma_f32_16x16x32_bf16 v[164:167], a[56:59], a[80:83], v[164:167]\n" \
    "ds_read_b64_tr_b16 a[158:159], v233 offset:13824\nv_mfma_f32_16x16x32_bf16 a[240:243], a[60:63], a[80:83], a[240:243]\nbuffer_load_dwordx4 v242, %[srdW2], s81 offen offset:2048 lds\n" \
    "v_mfma_f32_16x16x32_bf16 v[172:175], a[56:59], a[84:87], v[172:175]\nv_mfma_f32_16x16x32_bf16 a[244:247], a[60:63], a[84:87], a[244:247]\nv_permlane16_swap_b32_e32 v216, v218\nv_permlane16_swap_b32_e32 v217, v219\n" \
    "v_mfma_f32_16x16x32_bf16 v[180:183], a[56:59], a[88:91], v[180:183]\ns_add_u32 s82, %[so3], 0x7000\nbuffer_store_dwordx4 v[216:219], v244, %[srdO], s82 offen sc1\n" \
    "v_mfma_f32_16x16x32_bf16 a[248:251], a[60:63], a[88:91], a[248:251]\nv_mfma_f32_16x16x32_bf16 v[188:191], a[56:59], a[92:95], v[188:191]\nv_mfma_f32_16x16x32_bf16 a[252:255], a[60:63], a[92:95], a[252:255]\n" \
    "v_mfma_f32_16x16x32_bf16 v[196:199], a[64:67], a[72:75], v[196:199]\nv_mfma_f32_16x16x32_bf16 a[0:3], a[68:71], a[72:75], a[0:3]\nv_mfma_f32_16x16x32_bf16 v[204:207], a[64:67], a[76:79], v[204:207]\n" \
    "v_mfma_f32_16x16x32_bf16 a[4:7], a[68:71], a[76:79], a[4:7]\nv_mfma_f32_16x16x32_bf16 v[212:215], a[64:67], a[80:83], v[212:215]\nv_mfma_f32_16x16x32_bf16 a[8:11], a[68:71], a[80:83], a[8:11]\n" \
    "v_mfma_f32_16x16x32_bf16 v[220:223], a[64:67], a[84:87], v[220:223]\nv_mfma_f32_16x16x32_bf16 a[12:15], a[68:71], a[84:87], a[12:15]\nv_mfma_f32_16x16x32_bf16 a[24:27], a[64:67], a[88:91], a[24:27]\n" \
    "v_mfma_f32_16x16x32_bf16 a[16:19], a[68:71], a[88:91], a[16:19]\nv_mfma_f32_16x16x32_bf16 a[28:31], a[64:67], a[92:95], a[28:31]\nv_mfma_f32_16x16x32_bf16 a[20:23], a[68:71], a[92:95], a[20:23]\n" \
    "s_waitcnt vmcnt(11) lgkmcnt(0)\ns_barrier\ns_mov_b32 s84, 0x10000\ns_mov_b32 s85, 0x17400\ns_mov_b32 s86, 0x22000\ns_mov_b32 %[cnt], 4\nL_U4l%=:\nv_mfma_f32_16x16x32_bf16 v[4:7], a[96:99], a[136:139], v[4:7]\n" \
    "s_cmp_gt_u32 %[cnt], 2\ns_cselect_b32 s83, %[sx2], 0x70000000\nv_mfma_f32_16x16x32_bf16 a[160:163], a[100:103], a[136:139], a[160:163]\ns_sub_u32 s81, s85, s84\ns_mov_b32 s87, s84\ns_mov_b32 m0, %[m0x20]\n" \
    "s_add_u32 s80, s83, 0x0\nbuffer_load_dwordx4 v236, %[srdX2], s80 offen offset:0 sc1 lds\nv_mfma_f32_16x16x32_bf16 v[12:15], a[96:99], a[140:143], v[12:15]\ns_mov_b32 s84, s85\ns_mov_b32 s85, s86\n" \
    "v_mfma_f32_16x16x32_bf16 a[164:167], a[100:103], a[140:143], a[164:167]\ns_add_u32 %[sx2], %[sx2], 0x14000\ns_mov_b32 s86, s87\nv_mfma_f32_16x16x32_bf16 v[20:23], a[96:99], a[144:147], v[20:23]\n" \
    "v_add_u32_e32 v228, s81, v228\nds_read_b128 a[32:35], v226 offset:0\nbuffer_load_dwordx4 v236, %[srdX2], s80 offen offset:1024 sc1 lds\nv_mfma_f32_16x16x32_bf16 a[168:171], a[100:103], a[144:147], a[168:171]\n" \
    "v_add_u32_e32 v229, s81, v229\nv_mfma_f32_16x16x32_bf16 v[28:31], a[96:99], a[148:151], v[28:31]\nv_add_u32_e32 v230, s81, v230\nds_read_b128 a[36:39], v226 offset:256\n" \
    "v_mfma_f32_16x16x32_bf16 a[172:175], a[100:103], a[148:151], a[172:175]\nv_add_u32_e32 v231, s81, v231\nbuffer_load_dwordx4 v236, %[srdX2], s80 offen offset:2048 sc1 lds\n" \
    "v_mfma_f32_16x16x32_bf16 v[36:39], a[96:99], a[152:155], v[36:39]\nv_add_u32_e32 v232, s81, v232\nds_read_b128 a[40:43], v226 offset:512\nv_mfma_f32_16x16x32_bf16 a[176:179], a[100:103], a[152:155], a[176:179]\n" \
    "v_add_u32_e32 v233, s81, v233\nv_mfma_f32_16x16x32_bf16 v[44:47], a[96:99], a[156:159], v[44:47]\nds_read_b128 a[44:47], v226 offset:768\ns_sub_u32 %[cnt], %[cnt], 1\n" \
    "buffer_load_dwordx4 v236, %[srdX2], s80 offen offset:3072 sc1 lds\nv_mfma_f32_16x16x32_bf16 a[180:183], a[100:103], a[156:159], a[180:183]\nv_mfma_f32_16x16x32_bf16 v[52:55], a[104:107], a[136:139], v[52:55]\n" \
    "ds_read_b128 a[48:51], v226 offset:1024\nv_mfma_f32_16x16x32_bf16 a[184:187], a[108:111], a[136:139], a[184:187]\nv_mfma_f32_16x16x32_bf16 v[60:63], a[104:107], a[140:143], v[60:63]\n" \
    "ds_read_b128 a[52:55], v226 offset:1280\ns_add_u32 m0, m0, 0x1000\ns_add_u32 s80, s80, 0x1000\nbuffer_load_dwordx4 v236, %[srdX2], s80 offen offset:0 sc1 lds\n" \
    "v_mfma_f32_16x16x32_bf16 a[188:191], a[108:111], a[140:143], a[188:191]\nv_mfma_f32_16x16x32_bf16 v[68:71], a[104:107], a[144:147], v[68:71]\nds_read_b128 a[56:59], v226 offset:1536\n" \
    "v_mfma_f32_16x16x32_bf16 a[192:195], a[108:111], a[144:147], a[192:195]\ns_mov_b32 m0, %[m0x21]\ns_add_u32 s80, s83, 0x2800\nbuffer_load_dwordx4 v236, %[srdX2], s80 offen offset:0 sc1 lds\n" \
    "v_mfma_f32_16x16x32_bf16 v[76:79], a[104:107], a[148:151], v[76:79]\nds_read_b128 a[60:63], v226 offset:1792\nv_mfma_f32_16x16x32_bf16 a[196:199], a[108:111], a[148:151], a[196:199]\n" \
    "v_mfma_f32_16x16x32_bf16 v[84:87], a[104:107], a[152:155], v[84:87]\nds_read_b128 a[64:67], v226 offset:2048\nbuffer_load_dwordx4 v236, %[srdX2], s80 offen offset:1024 sc1 lds\n" \
    "v_mfma_f32_16x16x32_bf16 a[200:203], a[108:111], a[152:155], a[200:203]\nv_mfma_f32_16x16x32_bf16 v[92:95], a[104:107], a[156:159], v[92:95]\nds_read_b128 a[68:71], v226 offset:2304\n" \
    "v_mfma_f32_16x16x32_bf16 a[204:207], a[108:111], a[156:159], a[204:207]\nv_mfma_f32_16x16x32_bf16 v[100:103], a[112:115], a[136:139], v[100:103]\nds_read_b64_tr_b16 a[72:73], v228 offset:0\n" \
    "buffer_load_dwordx4 v236, %[srdX2], s80 offen offset:2048 sc1 lds\nv_mfma_f32_16x16x32_bf16 a[208:211], a[116:119], a[136:139], a[208:211]\nds_read_b64_tr_b16 a[74:75], v228 offset:1536\n" \
    "v_mfma_f32_16x16x32_bf16 v[108:111], a[112:115], a[140:143], v[108:111]\nv_mfma_f32_16x16x32_bf16 a[212:215], a[116:119], a[140:143], a[212:215]\nds_read_b64_tr_b16 a[76:77], v229 offset:0\n" \
    "buffer_load_dwordx4 v236, %[srdX2], s80 offen offset:3072 sc1 lds\nv_mfma_f32_16x16x32_bf16 v[116:119], a[112:115], a[144:147], v[116:119]\nds_read_b64_tr_b16 a[78:79], v229 offset:1536\n" \
    "v_mfma_f32_16x16x32_bf16 a[216:219], a[116:119], a[144:147], a[216:219]\nv_mfma_f32_16x16x32_bf16 v[124:127], a[112:115], a[148:151], v[124:127]\nds_read_b64_tr_b16 a[80:81], v230 offset:0\ns_add_u32 m0, m0, 0x1000\n" \
    "s_add_u32 s80, s80, 0x1000\nbuffer_load_dwordx4 v236, %[srdX2], s80 offen offset:0 sc1 lds\nv_mfma_f32_16x16x32_bf16 a[220:223], a[116:119], a[148:151], a[220:223]\nds_read_b64_tr_b16 a[82:83], v230 offset:1536\n" \
    "v_mfma_f32_16x16x32_bf16 v[132:135], a[112:115], a[152:155], v[132:135]\nv_mfma_f32_16x16x32_bf16 a[224:227], a[116:119], a[152:155], a[224:227]\nds_read_b64_tr_b16 a[84:85], v231 offset:0\n" \
    "v_mfma_f32_16x16x32_bf16 v[140:143], a[112:115], a[156:159], v[140:143]\nds_read_b64_tr_b16 a[86:87], v231 offset:1536\nv_mfma_f32_16x16x32_bf16 a[228:231], a[116:119], a[156:159], a[228:231]\n" \
    "v_mfma_f32_16x16x32_bf16 v[148:151], a[120:123], a[136:139], v[148:151]\nds_read_b64_tr_b16 a[88:89], v232 offset:0\nv_mfma_f32_16x16x32_bf16 a[232:235], a[124:127], a[136:139], a[232:235]\n" \
    "ds_read_b64_tr_b16 a[90:91], v232 offset:1536\nv_mfma_f32_16x16x32_bf16 v[156:159], a[120:123], a[140:143], v[156:159]\nv_mfma_f32_16x16x32_bf16 a[236:239], a[124:127], a[140:143], a[236:239]\n" \
    "ds_read_b64_tr_b16 a[92:93], v233 offset:0\nv_mfma_f32_16x16x32_bf16 v[164:167], a[120:123], a[144:147], v[164:167]\nds_read_b64_tr_b16 a[94:95], v233 offset:1536\n" \
    "v_mfma_f32_16x16x32_bf16 a[240:243], a[124:127], a[144:147], a[240:243]\nv_mfma_f32_16x16x32_bf16 v[172:175], a[120:123], a[148:151], v[172:175]\nv_mfma_f32_16x16x32_bf16 a[244:247], a[124:127], a[148:151], a[244:247]\n" \
    "v_mfma_f32_16x16x32_bf16 v[180:183], a[120:123], a[152:155], v[180:183]\nv_mfma_f32_16x16x32_bf16 a[248:251], a[124:127], a[152:155], a[248:251]\nv_mfma_f32_16x16x32_bf16 v[188:191], a[120:123], a[156:159], v[188:191]\n" \
    "v_mfma_f32_16x16x32_bf16 a[252:255], a[124:127], a[156:159], a[252:255]\nv_mfma_f32_16x16x32_bf16 v[196:199], a[128:131], a[136:139], v[196:199]\nv_mfma_f32_16x16x32_bf16 a[0:3], a[132:135], a[136:139], a[0:3]\n" \
    "v_mfma_f32_16x16x32_bf16 v[204:207], a[128:131], a[140:143], v[204:207]\nv_mfma_f32_16x16x32_bf16 a[4:7], a[132:135], a[140:143], a[4:7]\nv_mfma_f32_16x16x32_bf16 v[212:215], a[128:131], a[144:147], v[212:215]\n" \
    "v_mfma_f32_16x16x32_bf16 a[8:11], a[132:135], a[144:147], a[8:11]\nv_mfma_f32_16x16x32_bf16 v[220:223], a[128:131], a[148:151], v[220:223]\nv_mfma_f32_16x16x32_bf16 a[12:15], a[132:135], a[148:151], a[12:15]\n" \
    "v_mfma_f32_16x16x32_bf16 a[24:27], a[128:131], a[152:155], a[24:27]\nv_mfma_f32_16x16x32_bf16 a[16:19], a[132:135], a[152:155], a[16:19]\nv_mfma_f32_16x16x32_bf16 a[28:31], a[128:131], a[156:159], a[28:31]\n" \
    "v_mfma_f32_16x16x32_bf16 a[20:23], a[132:135], a[156:159], a[20:23]\ns_waitcnt lgkmcnt(0)\nv_mfma_f32_16x16x32_bf16 v[4:7], a[32:35], a[72:75], v[4:7]\nds_read_b128 a[96:99], v227 offset:0\n" \
    "v_mfma_f32_16x16x32_bf16 a[160:163], a[36:39], a[72:75], a[160:163]\nds_read_b128 a[100:103], v227 offset:256\ns_cmp_gt_u32 %[cnt], 2\ns_cselect_b32 s83, %[sw2], 0x70000000\n" \
    "v_mfma_f32_16x16x32_bf16 v[12:15], a[32:35], a[76:79], v[12:15]\ns_add_u32 %[sw2], %[sw2], 0x36000\nv_mfma_f32_16x16x32_bf16 a[164:167], a[36:39], a[76:79], a[164:167]\nds_read_b128 a[104:107], v227 offset:512\n" \
    "s_add_u32 m0, %[m0w2], s86\ns_add_u32 s81, s83, 0x0\nbuffer_load_dwordx4 v240, %[srdW2], s81 offen offset:0 lds\nv_mfma_f32_16x16x32_bf16 v[20:23], a[32:35], a[80:83], v[20:23]\n" \
    "v_mfma_f32_16x16x32_bf16 a[168:171], a[36:39], a[80:83], a[168:171]\nds_read_b128 a[108:111], v227 offset:768\nv_mfma_f32_16x16x32_bf16 v[28:31], a[32:35], a[84:87], v[28:31]\n" \
    "v_mfma_f32_16x16x32_bf16 a[172:175], a[36:39], a[84:87], a[172:175]\nds_read_b128 a[112:115], v227 offset:1024\nv_mfma_f32_16x16x32_bf16 v[36:39], a[32:35], a[88:91], v[36:39]\n" \
    "v_mfma_f32_16x16x32_bf16 a[176:179], a[36:39], a[88:91], a[176:179]\nds_read_b128 a[116:119], v227 offset:1280\nv_mfma_f32_16x16x32_bf16 v[44:47], a[32:35], a[92:95], v[44:47]\n" \
    "buffer_load_dwordx4 v241, %[srdW2], s81 offen offset:1024 lds\nv_mfma_f32_16x16x32_bf16 a[180:183], a[36:39], a[92:95], a[180:183]\nds_read_b128 a[120:123], v227 offset:1536\n" \
    "v_mfma_f32_16x16x32_bf16 v[52:55], a[40:43], a[72:75], v[52:55]\nv_mfma_f32_16x16x32_bf16 a[184:187], a[44:47], a[72:75], a[184:187]\nds_read_b128 a[124:127], v227 offset:1792\n" \
    "v_mfma_f32_16x16x32_bf16 v[60:63], a[40:43], a[76:79], v[60:63]\nv_mfma_f32_16x16x32_bf16 a[188:191], a[44:47], a[76:79], a[188:191]\nds_read_b128 a[128:131], v227 offset:2048\n" \
    "v_mfma_f32_16x16x32_bf16 v[68:71], a[40:43], a[80:83], v[68:71]\nv_mfma_f32_16x16x32_bf16 a[192:195], a[44:47], a[80:83], a[192:195]\nds_read_b128 a[132:135], v227 offset:2304\n" \
    "v_mfma_f32_16x16x32_bf16 v[76:79], a[40:43], a[84:87], v[76:79]\nbuffer_load_dwordx4 v242, %[srdW2], s81 offen offset:2048 lds\nv_mfma_f32_16x16x32_bf16 a[196:199], a[44:47], a[84:87], a[196:199]\n" \
    "ds_read_b64_tr_b16 a[136:137], v228 offset:12288\nv_mfma_f32_16x16x32_bf16 v[84:87], a[40:43], a[88:91], v[84:87]\nv_mfma_f32_16x16x32_bf16 a[200:203], a[44:47], a[88:91], a[200:203]\n" \
    "ds_read_b64_tr_b16 a[138:139], v228 offset:13824\nv_mfma_f32_16x16x32_bf16 v[92:95], a[40:43], a[92:95], v[92:95]\nds_read_b64_tr_b16 a[140:141], v229 offset:12288\n" \
    "v_mfma_f32_16x16x32_bf16 a[204:207], a[44:47], a[92:95], a[204:207]\nv_mfma_f32_16x16x32_bf16 v[100:103], a[48:51], a[72:75], v[100:103]\nds_read_b64_tr_b16 a[142:143], v229 offset:13824\n" \
    "v_mfma_f32_16x16x32_bf16 a[208:211], a[52:55], a[72:75], a[208:211]\nv_mfma_f32_16x16x32_bf16 v[108:111], a[48:51], a[76:79], v[108:111]\nds_read_b64_tr_b16 a[144:145], v230 offset:12288\ns_add_u32 m0, m0, 0x1800\n" \
    "s_add_u32 s81, s83, 0xd800\nbuffer_load_dwordx4 v240, %[srdW2], s81 offen offset:0 lds\nv_mfma_f32_16x16x32_bf16 a[212:215], a[52:55], a[76:79], a[212:215]\n" \
    "v_mfma_f32_16x16x32_bf16 v[116:119], a[48:51], a[80:83], v[116:119]\nds_read_b64_tr_b16 a[146:147], v230 offset:13824\nv_mfma_f32_16x16x32_bf16 a[216:219], a[52:55], a[80:83], a[216:219]\n" \
    "v_mfma_f32_16x16x32_bf16 v[124:127], a[48:51], a[84:87], v[124:127]\nds_read_b64_tr_b16 a[148:149], v231 offset:12288\nv_mfma_f32_16x16x32_bf16 a[220:223], a[52:55], a[84:87], a[220:223]\n" \
    "v_mfma_f32_16x16x32_bf16 v[132:135], a[48:51], a[88:91], v[132:135]\nds_read_b64_tr_b16 a[150:151], v231 offset:13824\nv_mfma_f32_16x16x32_bf16 a[224:227], a[52:55], a[88:91], a[224:227]\n" \
    "buffer_load_dwordx4 v241, %[srdW2], s81 offen offset:1024 lds\nv_mfma_f32_16x16x32_bf16 v[140:143], a[48:51], a[92:95], v[140:143]\nds_read_b64_tr_b16 a[152:153], v232 offset:12288\n" \
    "v_mfma_f32_16x16x32_bf16 a[228:231], a[52:55], a[92:95], a[228:231]\nv_mfma_f32_16x16x32_bf16 v[148:151], a[56:59], a[72:75], v[148:151]\nds_read_b64_tr_b16 a[154:155], v232 offset:13824\n" \
    "v_mfma_f32_16x16x32_bf16 a[232:235], a[60:63], a[72:75], a[232:235]\nv_mfma_f32_16x16x32_bf16 v[156:159], a[56:59], a[76:79], v[156:159]\nds_read_b64_tr_b16 a[156:157], v233 offset:12288\n" \
    "v_mfma_f32_16x16x32_bf16 a[236:239], a[60:63], a[76:79], a[236:239]\nv_mfma_f32_16x16x32_bf16 v[164:167], a[56:59], a[80:83], v[164:167]\nds_read_b64_tr_b16 a[158:159], v233 offset:13824\n" \
    "v_mfma_f32_16x16x32_bf16 a[240:243], a[60:63], a[80:83], a[240:243]\nbuffer_load_dwordx4 v242, %[srdW2], s81 offen offset:2048 lds\nv_mfma_f32_16x16x32_bf16 v[172:175], a[56:59], a[84:87], v[172:175]\n" \
    "v_mfma_f32_16x16x32_bf16 a[244:247], a[60:63], a[84:87], a[244:247]\nv_mfma_f32_16x16x32_bf16 v[180:183], a[56:59], a[88:91], v[180:183]\nv_mfma_f32_16x16x32_bf16 a[248:251], a[60:63], a[88:91], a[248:251]\n" \
    "v_mfma_f32_16x16x32_bf16 v[188:191], a[56:59], a[92:95], v[188:191]\nv_mfma_f32_16x16x32_bf16 a[252:255], a[60:63], a[92:95], a[252:255]\nv_mfma_f32_16x16x32_bf16 v[196:199], a[64:67], a[72:75], v[196:199]\n" \
    "v_mfma_f32_16x16x32_bf16 a[0:3], a[68:71], a[72:75], a[0:3]\nv_mfma_f32_16x16x32_bf16 v[204:207], a[64:67], a[76:79], v[204:207]\nv_mfma_f32_16x16x32_bf16 a[4:7], a[68:71], a[76:79], a[4:7]\n" \
    "v_mfma_f32_16x16x32_bf16 v[212:215], a[64:67], a[80:83], v[212:215]\nv_mfma_f32_16x16x32_bf16 a[8:11], a[68:71], a[80:83], a[8:11]\nv_mfma_f32_16x16x32_bf16 v[220:223], a[64:67], a[84:87], v[220:223]\n" \
    "v_mfma_f32_16x16x32_bf16 a[12:15], a[68:71], a[84:87], a[12:15]\nv_mfma_f32_16x16x32_bf16 a[24:27], a[64:67], a[88:91], a[24:27]\nv_mfma_f32_16x16x32_bf16 a[16:19], a[68:71], a[88:91], a[16:19]\n" \
    "v_mfma_f32_16x16x32_bf16 a[28:31], a[64:67], a[92:95], a[28:31]\nv_mfma_f32_16x16x32_bf16 a[20:23], a[68:71], a[92:95], a[20:23]\ns_waitcnt vmcnt(6) lgkmcnt(0)\ns_barrier\ns_cmp_eq_u32 %[cnt], 1\ns_cbranch_scc1 L_U4x%=\n" \
    "v_mfma_f32_16x16x32_bf16 v[4:7], a[96:99], a[136:139], v[4:7]\ns_cmp_gt_u32 %[cnt], 2\ns_cselect_b32 s83, %[sx2], 0x70000000\nv_mfma_f32_16x16x32_bf16 a[160:163], a[100:103], a[136:139], a[160:163]\n" \
    "s_sub_u32 s81, s85, s84\ns_mov_b32 s87, s84\ns_mov_b32 m0, %[m0x22]\ns_add_u32 s80, s83, 0x0\nbuffer_load_dwordx4 v236, %[srdX2], s80 offen offset:0 sc1 lds\n" \
    "v_mfma_f32_16x16x32_bf16 v[12:15], a[96:99], a[140:143], v[12:15]\ns_mov_b32 s84, s85\ns_mov_b32 s85, s86\nv_mfma_f32_16x16x32_bf16 a[164:167], a[100:103], a[140:143], a[164:167]\ns_add_u32 %[sx2], %[sx2], 0x14000\n" \
    "s_mov_b32 s86, s87\nv_mfma_f32_16x16x32_bf16 v[20:23], a[96:99], a[144:147], v[20:23]\nv_add_u32_e32 v228, s81, v228\nds_read_b128 a[32:35], v224 offset:0\n" \
    "buffer_load_dwordx4 v236, %[srdX2], s80 offen offset:1024 sc1 lds\nv_mfma_f32_16x16x32_bf16 a[168:171], a[100:103], a[144:147], a[168:171]\nv_add_u32_e32 v229, s81, v229\n" \
    "v_mfma_f32_16x16x32_bf16 v[28:31], a[96:99], a[148:151], v[28:31]\nv_add_u32_e32 v230, s81, v230\nds_read_b128 a[36:39], v224 offset:256\nv_mfma_f32_16x16x32_bf16 a[172:175], a[100:103], a[148:151], a[172:175]\n" \
    "v_add_u32_e32 v231, s81, v231\nbuffer_load_dwordx4 v236, %[srdX2], s80 offen offset:2048 sc1 lds\nv_mfma_f32_16x16x32_bf16 v[36:39], a[96:99], a[152:155], v[36:39]\nv_add_u32_e32 v232, s81, v232\n" \
    "ds_read_b128 a[40:43], v224 offset:512\nv_mfma_f32_16x16x32_bf16 a[176:179], a[100:103], a[152:155], a[176:179]\nv_add_u32_e32 v233, s81, v233\nv_mfma_f32_16x16x32_bf16 v[44:47], a[96:99], a[156:159], v[44:47]\n" \
    "ds_read_b128 a[44:47], v224 offset:768\ns_sub_u32 %[cnt], %[cnt], 1\nbuffer_load_dwordx4 v236, %[srdX2], s80 offen offset:3072 sc1 lds\nv_mfma_f32_16x16x32_bf16 a[180:183], a[100:103], a[156:159], a[180:183]\n" \
    "v_mfma_f32_16x16x32_bf16 v[52:55], a[104:107], a[136:139], v[52:55]\nds_read_b128 a[48:51], v224 offset:1024\nv_mfma_f32_16x16x32_bf16 a[184:187], a[108:111], a[136:139], a[184:187]\n" \
    "v_mfma_f32_16x16x32_bf16 v[60:63], a[104:107], a[140:143], v[60:63]\nds_read_b128 a[52:55], v224 offset:1280\ns_add_u32 m0, m0, 0x1000\ns_add_u32 s80, s80, 0x1000\n" \
    "buffer_load_dwordx4 v236, %[srdX2], s80 offen offset:0 sc1 lds\nv_mfma_f32_16x16x32_bf16 a[188:191], a[108:111], a[140:143], a[188:191]\nv_mfma_f32_16x16x32_bf16 v[68:71], a[104:107], a[144:147], v[68:71]\n" \
    "ds_read_b128 a[56:59], v224 offset:1536\nv_mfma_f32_16x16x32_bf16 a[192:195], a[108:111], a[144:147], a[192:195]\ns_mov_b32 m0, %[m0x23]\ns_add_u32 s80, s83, 0x2800\n" \
    "buffer_load_dwordx4 v236, %[srdX2], s80 offen offset:0 sc1 lds\nv_mfma_f32_16x16x32_bf16 v[76:79], a[104:107], a[148:151], v[76:79]\nds_read_b128 a[60:63], v224 offset:1792\n" \
    "v_mfma_f32_16x16x32_bf16 a[196:199], a[108:111], a[148:151], a[196:199]\nv_mfma_f32_16x16x32_bf16 v[84:87], a[104:107], a[152:155], v[84:87]\nds_read_b128 a[64:67], v224 offset:2048\n" \
    "buffer_load_dwordx4 v236, %[srdX2], s80 offen offset:1024 sc1 lds\nv_mfma_f32_16x16x32_bf16 a[200:203], a[108:111], a[152:155], a[200:203]\nv_mfma_f32_16x16x32_bf16 v[92:95], a[104:107], a[156:159], v[92:95]\n" \
    "ds_read_b128 a[68:71], v224 offset:2304\nv_mfma_f32_16x16x32_bf16 a[204:207], a[108:111], a[156:159], a[204:207]\nv_mfma_f32_16x16x32_bf16 v[100:103], a[112:115], a[136:139], v[100:103]\n" \
    "ds_read_b64_tr_b16 a[72:73], v228 offset:0\nbuffer_load_dwordx4 v236, %[srdX2], s80 offen offset:2048 sc1 lds\nv_mfma_f32_16x16x32_bf16 a[208:211], a[116:119], a[136:139], a[208:211]\n" \
    "ds_read_b64_tr_b16 a[74:75], v228 offset:1536\nv_mfma_f32_16x16x32_bf16 v[108:111], a[112:115], a[140:143], v[108:111]\nv_mfma_f32_16x16x32_bf16 a[212:215], a[116:119], a[140:143], a[212:215]\n" \
    "ds_read_b64_tr_b16 a[76:77], v229 offset:0\nbuffer_load_dwordx4 v236, %[srdX2], s80 offen offset:3072 sc1 lds\nv_mfma_f32_16x16x32_bf16 v[116:119], a[112:115], a[144:147], v[116:119]\n" \
    "ds_read_b64_tr_b16 a[78:79], v229 offset:1536\nv_mfma_f32_16x16x32_bf16 a[216:219], a[116:119], a[144:147], a[216:219]\nv_mfma_f32_16x16x32_bf16 v[124:127], a[112:115], a[148:151], v[124:127]\n" \
    "ds_read_b64_tr_b16 a[80:81], v230 offset:0\ns_add_u32 m0, m0, 0x1000\ns_add_u32 s80, s80, 0x1000\nbuffer_load_dwordx4 v236, %[srdX2], s80 offen offset:0 sc1 lds\n" \
    "v_mfma_f32_16x16x32_bf16 a[220:223], a[116:119], a[148:151], a[220:223]\nds_read_b64_tr_b16 a[82:83], v230 offset:1536\nv_mfma_f32_16x16x32_bf16 v[132:135], a[112:115], a[152:155], v[132:135]\n" \
    "v_mfma_f32_16x16x32_bf16 a[224:227], a[116:119], a[152:155], a[224:227]\nds_read_b64_tr_b16 a[84:85], v231 offset:0\nv_mfma_f32_16x16x32_bf16 v[140:143], a[112:115], a[156:159], v[140:143]\n" \
    "ds_read_b64_tr_b16 a[86:87], v231 offset:1536\nv_mfma_f32_16x16x32_bf16 a[228:231], a[116:119], a[156:159], a[228:231]\nv_mfma_f32_16x16x32_bf16 v[148:151], a[120:123], a[136:139], v[148:151]\n" \
    "ds_read_b64_tr_b16 a[88:89], v232 offset:0\nv_mfma_f32_16x16x32_bf16 a[232:235], a[124:127], a[136:139], a[232:235]\nds_read_b64_tr_b16 a[90:91], v232 offset:1536\n" \
    "v_mfma_f32_16x16x32_bf16 v[156:159], a[120:123], a[140:143], v[156:159]\nv_mfma_f32_16x16x32_bf16 a[236:239], a[124:127], a[140:143], a[236:239]\nds_read_b64_tr_b16 a[92:93], v233 offset:0\n" \
    "v_mfma_f32_16x16x32_bf16 v[164:167], a[120:123], a[144:147], v[164:167]\nds_read_b64_tr_b16 a[94:95], v233 offset:1536\nv_mfma_f32_16x16x32_bf16 a[240:243], a[124:127], a[144:147], a[240:243]\n" \
    "v_mfma_f32_16x16x32_bf16 v[172:175], a[120:123], a[148:151], v[172:175]\nv_mfma_f32_16x16x32_bf16 a[244:247], a[124:127], a[148:151], a[244:247]\nv_mfma_f32_16x16x32_bf16 v[180:183], a[120:123], a[152:155], v[180:183]\n" \
    "v_mfma_f32_16x16x32_bf16 a[248:251], a[124:127], a[152:155], a[248:251]\nv_mfma_f32_16x16x32_bf16 v[188:191], a[120:123], a[156:159], v[188:191]\nv_mfma_f32_16x16x32_bf16 a[252:255], a[124:127], a[156:159], a[252:255]\n" \
    "v_mfma_f32_16x16x32_bf16 v[196:199], a[128:131], a[136:139], v[196:199]\nv_mfma_f32_16x16x32_bf16 a[0:3], a[132:135], a[136:139], a[0:3]\nv_mfma_f32_16x16x32_bf16 v[204:207], a[128:131], a[140:143], v[204:207]\n" \
    "v_mfma_f32_16x16x32_bf16 a[4:7], a[132:135], a[140:143], a[4:7]\nv_mfma_f32_16x16x32_bf16 v[212:215], a[128:131], a[144:147], v[212:215]\nv_mfma_f32_16x16x32_bf16 a[8:11], a[132:135], a[144:147], a[8:11]\n" \
    "v_mfma_f32_16x16x32_bf16 v[220:223], a[128:131], a[148:151], v[220:223]\nv_mfma_f32_16x16x32_bf16 a[12:15], a[132:135], a[148:151], a[12:15]\nv_mfma_f32_16x16x32_bf16 a[24:27], a[128:131], a[152:155], a[24:27]\n" \
    "v_mfma_f32_16x16x32_bf16 a[16:19], a[132:135], a[152:155], a[16:19]\nv_mfma_f32_16x16x32_bf16 a[28:31], a[128:131], a[156:159], a[28:31]\nv_mfma_f32_16x16x32_bf16 a[20:23], a[132:135], a[156:159], a[20:23]\n" \
    "s_waitcnt lgkmcnt(0)\nv_mfma_f32_16x16x32_bf16 v[4:7], a[32:35], a[72:75], v[4:7]\nds_read_b128 a[96:99], v225 offset:0\nv_mfma_f32_16x16x32_bf16 a[160:163], a[36:39], a[72:75], a[160:163]\n" \
    "ds_read_b128 a[100:103], v225 offset:256\ns_cmp_gt_u32 %[cnt], 2\ns_cselect_b32 s83, %[sw2], 0x70000000\nv_mfma_f32_16x16x32_bf16 v[12:15], a[32:35], a[76:79], v[12:15]\ns_add_u32 %[sw2], %[sw2], 0x36000\n" \
    "v_mfma_f32_16x16x32_bf16 a[164:167], a[36:39], a[76:79], a[164:167]\nds_read_b128 a[104:107], v225 offset:512\ns_add_u32 m0, %[m0w2], s86\ns_add_u32 s81, s83, 0x0\n" \
    "buffer_load_dwordx4 v240, %[srdW2], s81 offen offset:0 lds\nv_mfma_f32_16x16x32_bf16 v[20:23], a[32:35], a[80:83], v[20:23]\nv_mfma_f32_16x16x32_bf16 a[168:171], a[36:39], a[80:83], a[168:171]\n" \
    "ds_read_b128 a[108:111], v225 offset:768\nv_mfma_f32_16x16x32_bf16 v[28:31], a[32:35], a[84:87], v[28:31]\nv_mfma_f32_16x16x32_bf16 a[172:175], a[36:39], a[84:87], a[172:175]\nds_read_b128 a[112:115], v225 offset:1024\n" \
    "v_mfma_f32_16x16x32_bf16 v[36:39], a[32:35], a[88:91], v[36:39]\nv_mfma_f32_16x16x32_bf16 a[176:179], a[36:39], a[88:91], a[176:179]\nds_read_b128 a[116:119], v225 offset:1280\n" \
    "v_mfma_f32_16x16x32_bf16 v[44:47], a[32:35], a[92:95], v[44:47]\nbuffer_load_dwordx4 v241, %[srdW2], s81 offen offset:1024 lds\nv_mfma_f32_16x16x32_bf16 a[180:183], a[36:39], a[92:95], a[180:183]\n" \
    "ds_read_b128 a[120:123], v225 offset:1536\nv_mfma_f32_16x16x32_bf16 v[52:55], a[40:43], a[72:75], v[52:55]\nv_mfma_f32_16x16x32_bf16 a[184:187], a[44:47], a[72:75], a[184:187]\nds_read_b128 a[124:127], v225 offset:1792\n" \
    "v_mfma_f32_16x16x32_bf16 v[60:63], a[40:43], a[76:79], v[60:63]\nv_mfma_f32_16x16x32_bf16 a[188:191], a[44:47], a[76:79], a[188:191]\nds_read_b128 a[128:131], v225 offset:2048\n" \
    "v_mfma_f32_16x16x32_bf16 v[68:71], a[40:43], a[80:83], v[68:71]\nv_mfma_f32_16x16x32_bf16 a[192:195], a[44:47], a[80:83], a[192:195]\nds_read_b128 a[132:135], v225 offset:2304\n" \
    "v_mfma_f32_16x16x32_bf16 v[76:79], a[40:43], a[84:87], v[76:79]\nbuffer_load_dwordx4 v242, %[srdW2], s81 offen offset:2048 lds\nv_mfma_f32_16x16x32_bf16 a[196:199], a[44:47], a[84:87], a[196:199]\n" \
    "ds_read_b64_tr_b16 a[136:137], v228 offset:12288\nv_mfma_f32_16x16x32_bf16 v[84:87], a[40:43], a[88:91], v[84:87]\nv_mfma_f32_16x16x32_bf16 a[200:203], a[44:47], a[88:91], a[200:203]\n" \
    "ds_read_b64_tr_b16 a[138:139], v228 offset:13824\nv_mfma_f32_16x16x32_bf16 v[92:95], a[40:43], a[92:95], v[92:95]\nds_read_b64_tr_b16 a[140:141], v229 offset:12288\n" \
    "v_mfma_f32_16x16x32_bf16 a[204:207], a[44:47], a[92:95], a[204:207]\nv_mfma_f32_16x16x32_bf16 v[100:103], a[48:51], a[72:75], v[100:103]\nds_read_b64_tr_b16 a[142:143], v229 offset:13824\n" \
    "v_mfma_f32_16x16x32_bf16 a[208:211], a[52:55], a[72:75], a[208:211]\nv_mfma_f32_16x16x32_bf16 v[108:111], a[48:51], a[76:79], v[108:111]\nds_read_b64_tr_b16 a[144:145], v230 offset:12288\ns_add_u32 m0, m0, 0x1800\n" \
    "s_add_u32 s81, s83, 0xd800\nbuffer_load_dwordx4 v240, %[srdW2], s81 offen offset:0 lds\nv_mfma_f32_16x16x32_bf16 a[212:215], a[52:55], a[76:79], a[212:215]\n" \
    "v_mfma_f32_16x16x32_bf16 v[116:119], a[48:51], a[80:83], v[116:119]\nds_read_b64_tr_b16 a[146:147], v230 offset:13824\nv_mfma_f32_16x16x32_bf16 a[216:219], a[52:55], a[80:83], a[216:219]\n" \
    "v_mfma_f32_16x16x32_bf16 v[124:127], a[48:51], a[84:87], v[124:127]\nds_read_b64_tr_b16 a[148:149], v231 offset:12288\nv_mfma_f32_16x16x32_bf16 a[220:223], a[52:55], a[84:87], a[220:223]\n" \
    "v_mfma_f32_16x16x32_bf16 v[132:135], a[48:51], a[88:91], v[132:135]\nds_read_b64_tr_b16 a[150:151], v231 offset:13824\nv_mfma_f32_16x16x32_bf16 a[224:227], a[52:55], a[88:91], a[224:227]\n" \
    "buffer_load_dwordx4 v241, %[srdW2], s81 offen offset:1024 lds\nv_mfma_f32_16x16x32_bf16 v[140:143], a[48:51], a[92:95], v[140:143]\nds_read_b64_tr_b16 a[152:153], v232 offset:12288\n" \
    "v_mfma_f32_16x16x32_bf16 a[228:231], a[52:55], a[92:95], a[228:231]\nv_mfma_f32_16x16x32_bf16 v[148:151], a[56:59], a[72:75], v[148:151]\nds_read_b64_tr_b16 a[154:155], v232 offset:13824\n" \
    "v_mfma_f32_16x16x32_bf16 a[232:235], a[60:63], a[72:75], a[232:235]\nv_mfma_f32_16x16x32_bf16 v[156:159], a[56:59], a[76:79], v[156:159]\nds_read_b64_tr_b16 a[156:157], v233 offset:12288\n" \
    "v_mfma_f32_16x16x32_bf16 a[236:239], a[60:63], a[76:79], a[236:239]\nv_mfma_f32_16x16x32_bf16 v[164:167], a[56:59], a[80:83], v[164:167]\nds_read_b64_tr_b16 a[158:159], v233 offset:13824\n" \
    "v_mfma_f32_16x16x32_bf16 a[240:243], a[60:63], a[80:83], a[240:243]\nbuffer_load_dwordx4 v242, %[srdW2], s81 offen offset:2048 lds\nv_mfma_f32_16x16x32_bf16 v[172:175], a[56:59], a[84:87], v[172:175]\n" \
    "v_mfma_f32_16x16x32_bf16 a[244:247], a[60:63], a[84:87], a[244:247]\nv_mfma_f32_16x16x32_bf16 v[180:183], a[56:59], a[88:91], v[180:183]\nv_mfma_f32_16x16x32_bf16 a[248:251], a[60:63], a[88:91], a[248:251]\n" \
    "v_mfma_f32_16x16x32_bf16 v[188:191], a[56:59], a[92:95], v[188:191]\nv_mfma_f32_16x16x32_bf16 a[252:255], a[60:63], a[92:95], a[252:255]\nv_mfma_f32_16x16x32_bf16 v[196:199], a[64:67], a[72:75], v[196:199]\n" \
    "v_mfma_f32_16x16x32_bf16 a[0:3], a[68:71], a[72:75], a[0:3]\nv_mfma_f32_16x16x32_bf16 v[204:207], a[64:67], a[76:79], v[204:207]\nv_mfma_f32_16x16x32_bf16 a[4:7], a[68:71], a[76:79], a[4:7]\n" \
    "v_mfma_f32_16x16x32_bf16 v[212:215], a[64:67], a[80:83], v[212:215]\nv_mfma_f32_16x16x32_bf16 a[8:11], a[68:71], a[80:83], a[8:11]\nv_mfma_f32_16x16x32_bf16 v[220:223], a[64:67], a[84:87], v[220:223]\n" \
    "v_mfma_f32_16x16x32_bf16 a[12:15], a[68:71], a[84:87], a[12:15]\nv_mfma_f32_16x16x32_bf16 a[24:27], a[64:67], a[88:91], a[24:27]\nv_mfma_f32_16x16x32_bf16 a[16:19], a[68:71], a[88:91], a[16:19]\n" \
    "v_mfma_f32_16x16x32_bf16 a[28:31], a[64:67], a[92:95], a[28:31]\nv_mfma_f32_16x16x32_bf16 a[20:23], a[68:71], a[92:95], a[20:23]\ns_waitcnt vmcnt(6) lgkmcnt(0)\ns_barrier\ns_branch L_U4l%=\nL_U4x%=:\n" \
    "v_mfma_f32_16x16x32_bf16 v[4:7], a[96:99], a[136:139], v[4:7]\nv_mfma_f32_16x16x32_bf16 a[160:163], a[100:103], a[136:139], a[160:163]\nv_mfma_f32_16x16x32_bf16 v[12:15], a[96:99], a[140:143], v[12:15]\n" \
    "v_mfma_f32_16x16x32_bf16 a[164:167], a[100:103], a[140:143], a[164:167]\nv_mfma_f32_16x16x32_bf16 v[20:23], a[96:99], a[144:147], v[20:23]\nv_mfma_f32_16x16x32_bf16 a[168:171], a[100:103], a[144:147], a[168:171]\n" \
    "v_mfma_f32_16x16x32_bf16 v[28:31], a[96:99], a[148:151], v[28:31]\nv_mfma_f32_16x16x32_bf16 a[172:175], a[100:103], a[148:151], a[172:175]\nv_accvgpr_read_b32 v246, a160\nv_accvgpr_read_b32 v247, a161\n" \
    "v_mfma_f32_16x16x32_bf16 v[36:39], a[96:99], a[152:155], v[36:39]\nv_accvgpr_read_b32 v248, a162\nv_accvgpr_read_b32 v249, a163\nv_mfma_f32_16x16x32_bf16 a[176:179], a[100:103], a[152:155], a[176:179]\n" \
    "v_cvt_pk_bf16_f32 v4, v4, v5\nv_cvt_pk_bf16_f32 v5, v6, v7\nv_mfma_f32_16x16x32_bf16 v[44:47], a[96:99], a[156:159], v[44:47]\nv_cvt_pk_bf16_f32 v6, v246, v247\nv_cvt_pk_bf16_f32 v7, v248, v249\n" \
    "v_mfma_f32_16x16x32_bf16 a[180:183], a[100:103], a[156:159], a[180:183]\ns_add_u32 s82, %[so20], 0x0\ns_nop 0\nv_permlane16_swap_b32_e32 v4, v6\nv_mfma_f32_16x16x32_bf16 v[52:55], a[104:107], a[136:139], v[52:55]\n" \
    "v_permlane16_swap_b32_e32 v5, v7\nbuffer_store_dwordx4 v[4:7], v244, %[srdO2], s82 offen sc1\nv_mfma_f32_16x16x32_bf16 a[184:187], a[108:111], a[136:139], a[184:187]\nv_accvgpr_read_b32 v250, a164\n" \
    "v_accvgpr_read_b32 v251, a165\nv_mfma_f32_16x16x32_bf16 v[60:63], a[104:107], a[140:143], v[60:63]\nv_accvgpr_read_b32 v252, a166\nv_accvgpr_read_b32 v253, a167\n" \
    "v_mfma_f32_16x16x32_bf16 a[188:191], a[108:111], a[140:143], a[188:191]\nv_cvt_pk_bf16_f32 v12, v12, v13\nv_cvt_pk_bf16_f32 v13, v14, v15\nv_mfma_f32_16x16x32_bf16 v[68:71], a[104:107], a[144:147], v[68:71]\n" \
    "v_cvt_pk_bf16_f32 v14, v250, v251\nv_cvt_pk_bf16_f32 v15, v252, v253\nv_mfma_f32_16x16x32_bf16 a[192:195], a[108:111], a[144:147], a[192:195]\ns_add_u32 s82, %[so20], 0x5000\ns_nop 0\nv_permlane16_swap_b32_e32 v12, v14\n" \
    "v_mfma_f32_16x16x32_bf16 v[76:79], a[104:107], a[148:151], v[76:79]\nv_permlane16_swap_b32_e32 v13, v15\nbuffer_store_dwordx4 v[12:15], v244, %[srdO2], s82 offen sc1\n" \
    "v_mfma_f32_16x16x32_bf16 a[196:199], a[108:111], a[148:151], a[196:199]\nv_accvgpr_read_b32 v246, a168\nv_accvgpr_read_b32 v247, a169\nv_mfma_f32_16x16x32_bf16 v[84:87], a[104:107], a[152:155], v[84:87]\n" \
    "v_accvgpr_read_b32 v248, a170\nv_accvgpr_read_b32 v249, a171\nv_mfma_f32_16x16x32_bf16 a[200:203], a[108:111], a[152:155], a[200:203]\nv_cvt_pk_bf16_f32 v20, v20, v21\nv_cvt_pk_bf16_f32 v21, v22, v23\n" \
    "v_mfma_f32_16x16x32_bf16 v[92:95], a[104:107], a[156:159], v[92:95]\nv_cvt_pk_bf16_f32 v22, v246, v247\nv_cvt_pk_bf16_f32 v23, v248, v249\nv_mfma_f32_16x16x32_bf16 a[204:207], a[108:111], a[156:159], a[204:207]\n" \
    "s_add_u32 s82, %[so20], 0xa000\ns_nop 0\nv_permlane16_swap_b32_e32 v20, v22\nv_mfma_f32_16x16x32_bf16 v[100:103], a[112:115], a[136:139], v[100:103]\nv_permlane16_swap_b32_e32 v21, v23\n" \
    "buffer_store_dwordx4 v[20:23], v244, %[srdO2], s82 offen sc1\nv_mfma_f32_16x16x32_bf16 a[208:211], a[116:119], a[136:139], a[208:211]\nv_accvgpr_read_b32 v250, a172\nv_accvgpr_read_b32 v251, a173\n" \
    "v_mfma_f32_16x16x32_bf16 v[108:111], a[112:115], a[140:143], v[108:111]\nv_accvgpr_read_b32 v252, a174\nv_accvgpr_read_b32 v253, a175\nv_mfma_f32_16x16x32_bf16 a[212:215], a[116:119], a[140:143], a[212:215]\n" \
    "v_cvt_pk_bf16_f32 v28, v28, v29\nv_cvt_pk_bf16_f32 v29, v30, v31\nv_mfma_f32_16x16x32_bf16 v[116:119], a[112:115], a[144:147], v[116:119]\nv_cvt_pk_bf16_f32 v30, v250, v251\nv_cvt_pk_bf16_f32 v31, v252, v253\n" \
    "v_mfma_f32_16x16x32_bf16 a[216:219], a[116:119], a[144:147], a[216:219]\ns_add_u32 s82, %[so20], 0xf000\ns_nop 0\nv_permlane16_swap_b32_e32 v28, v30\n" \
    "v_mfma_f32_16x16x32_bf16 v[124:127], a[112:115], a[148:151], v[124:127]\nv_permlane16_swap_b32_e32 v29, v31\nbuffer_store_dwordx4 v[28:31], v244, %[srdO2], s82 offen sc1\n" \
    "v_mfma_f32_16x16x32_bf16 a[220:223], a[116:119], a[148:151], a[220:223]\nv_accvgpr_read_b32 v246, a176\nv_accvgpr_read_b32 v247, a177\nv_mfma_f32_16x16x32_bf16 v[132:135], a[112:115], a[152:155], v[132:135]\n" \
    "v_accvgpr_read_b32 v248, a178\nv_accvgpr_read_b32 v249, a179\nv_mfma_f32_16x16x32_bf16 a[224:227], a[116:119], a[152:155], a[224:227]\nv_cvt_pk_bf16_f32 v36, v36, v37\nv_cvt_pk_bf16_f32 v37, v38, v39\n" \
    "v_mfma_f32_16x16x32_bf16 v[140:143], a[112:115], a[156:159], v[140:143]\nv_cvt_pk_bf16_f32 v38, v246, v247\nv_cvt_pk_bf16_f32 v39, v248, v249\nv_mfma_f32_16x16x32_bf16 a[228:231], a[116:119], a[156:159], a[228:231]\n" \
    "s_add_u32 s82, %[so20], 0x14000\ns_nop 0\nv_permlane16_swap_b32_e32 v36, v38\nv_mfma_f32_16x16x32_bf16 v[148:151], a[120:123], a[136:139], v[148:151]\nv_permlane16_swap_b32_e32 v37, v39\n" \
    "buffer_store_dwordx4 v[36:39], v244, %[srdO2], s82 offen sc1\nv_mfma_f32_16x16x32_bf16 a[232:235], a[124:127], a[136:139], a[232:235]\nv_accvgpr_read_b32 v250, a180\nv_accvgpr_read_b32 v251, a181\n" \
    "v_mfma_f32_16x16x32_bf16 v[156:159], a[120:123], a[140:143], v[156:159]\nv_accvgpr_read_b32 v252, a182\nv_accvgpr_read_b32 v253, a183\nv_mfma_f32_16x16x32_bf16 a[236:239], a[124:127], a[140:143], a[236:239]\n" \
    "v_cvt_pk_bf16_f32 v44, v44, v45\nv_cvt_pk_bf16_f32 v45, v46, v47\nv_mfma_f32_16x16x32_bf16 v[164:167], a[120:123], a[144:147], v[164:167]\nv_cvt_pk_bf16_f32 v46, v250, v251\nv_cvt_pk_bf16_f32 v47, v252, v253\n" \
    "v_mfma_f32_16x16x32_bf16 a[240:243], a[124:127], a[144:147], a[240:243]\ns_add_u32 s82, %[so20], 0x19000\ns_nop 0\nv_permlane16_swap_b32_e32 v44, v46\n" \
    "v_mfma_f32_16x16x32_bf16 v[172:175], a[120:123], a[148:151], v[172:175]\nv_permlane16_swap_b32_e32 v45, v47\nbuffer_store_dwordx4 v[44:47], v244, %[srdO2], s82 offen sc1\n" \
    "v_mfma_f32_16x16x32_bf16 a[244:247], a[124:127], a[148:151], a[244:247]\nv_accvgpr_read_b32 v246, a184\nv_accvgpr_read_b32 v247, a185\nv_mfma_f32_16x16x32_bf16 v[180:183], a[120:123], a[152:155], v[180:183]\n" \
    "v_accvgpr_read_b32 v248, a186\nv_accvgpr_read_b32 v249, a187\nv_mfma_f32_16x16x32_bf16 a[248:251], a[124:127], a[152:155], a[248:251]\nv_cvt_pk_bf16_f32 v52, v52, v53\nv_cvt_pk_bf16_f32 v53, v54, v55\n" \
    "v_mfma_f32_16x16x32_bf16 v[188:191], a[120:123], a[156:159], v[188:191]\nv_cvt_pk_bf16_f32 v54, v246, v247\nv_cvt_pk_bf16_f32 v55, v248, v249\nv_mfma_f32_16x16x32_bf16 a[252:255], a[124:127], a[156:159], a[252:255]\n" \
    "s_add_u32 s82, %[so20], 0x400\ns_nop 0\nv_permlane16_swap_b32_e32 v52, v54\nv_mfma_f32_16x16x32_bf16 v[196:199], a[128:131], a[136:139], v[196:199]\nv_permlane16_swap_b32_e32 v53, v55\n" \
    "buffer_store_dwordx4 v[52:55], v244, %[srdO2], s82 offen sc1\nv_mfma_f32_16x16x32_bf16 a[0:3], a[132:135], a[136:139], a[0:3]\nv_accvgpr_read_b32 v250, a188\nv_accvgpr_read_b32 v251, a189\n" \
    "v_mfma_f32_16x16x32_bf16 v[204:207], a[128:131], a[140:143], v[204:207]\nv_accvgpr_read_b32 v252, a190\nv_accvgpr_read_b32 v253, a191\nv_mfma_f32_16x16x32_bf16 a[4:7], a[132:135], a[140:143], a[4:7]\n" \
    "v_cvt_pk_bf16_f32 v60, v60, v61\nv_cvt_pk_bf16_f32 v61, v62, v63\nv_mfma_f32_16x16x32_bf16 v[212:215], a[128:131], a[144:147], v[212:215]\nv_cvt_pk_bf16_f32 v62, v250, v251\nv_cvt_pk_bf16_f32 v63, v252, v253\n" \
    "v_mfma_f32_16x16x32_bf16 a[8:11], a[132:135], a[144:147], a[8:11]\ns_add_u32 s82, %[so20], 0x5400\ns_nop 0\nv_permlane16_swap_b32_e32 v60, v62\nv_mfma_f32_16x16x32_bf16 v[220:223], a[128:131], a[148:151], v[220:223]\n" \
    "v_permlane16_swap_b32_e32 v61, v63\nbuffer_store_dwordx4 v[60:63], v244, %[srdO2], s82 offen sc1\nv_mfma_f32_16x16x32_bf16 a[12:15], a[132:135], a[148:151], a[12:15]\nv_accvgpr_read_b32 v246, a192\n" \
    "v_accvgpr_read_b32 v247, a193\nv_mfma_f32_16x16x32_bf16 a[24:27], a[128:131], a[152:155], a[24:27]\nv_accvgpr_read_b32 v248, a194\nv_accvgpr_read_b32 v249, a195\n" \
    "v_mfma_f32_16x16x32_bf16 a[16:19], a[132:135], a[152:155], a[16:19]\nv_cvt_pk_bf16_f32 v68, v68, v69\nv_cvt_pk_bf16_f32 v69, v70, v71\nv_mfma_f32_16x16x32_bf16 a[28:31], a[128:131], a[156:159], a[28:31]\n" \
    "v_cvt_pk_bf16_f32 v70, v246, v247\nv_cvt_pk_bf16_f32 v71, v248, v249\nv_mfma_f32_16x16x32_bf16 a[20:23], a[132:135], a[156:159], a[20:23]\ns_add_u32 s82, %[so20], 0xa400\ns_nop 0\nv_permlane16_swap_b32_e32 v68, v70\n" \
    "s_nop 7\ns_nop 3\nv_permlane16_swap_b32_e32 v69, v71\nbuffer_store_dwordx4 v[68:71], v244, %[srdO2], s82 offen sc1\nv_accvgpr_read_b32 v250, a196\nv_accvgpr_read_b32 v251, a197\nv_accvgpr_read_b32 v252, a198\n" \
    "v_accvgpr_read_b32 v253, a199\nv_cvt_pk_bf16_f32 v76, v76, v77\nv_cvt_pk_bf16_f32 v77, v78, v79\nv_cvt_pk_bf16_f32 v78, v250, v251\nv_cvt_pk_bf16_f32 v79, v252, v253\ns_add_u32 s82, %[so20], 0xf400\ns_nop 0\n" \
    "v_permlane16_swap_b32_e32 v76, v78\nv_permlane16_swap_b32_e32 v77, v79\nbuffer_store_dwordx4 v[76:79], v244, %[srdO2], s82 offen sc1\nv_accvgpr_read_b32 v246, a200\nv_accvgpr_read_b32 v247, a201\n" \
    "v_accvgpr_read_b32 v248, a202\nv_accvgpr_read_b32 v249, a203\nv_cvt_pk_bf16_f32 v84, v84, v85\nv_cvt_pk_bf16_f32 v85, v86, v87\nv_cvt_pk_bf16_f32 v86, v246, v247\nv_cvt_pk_bf16_f32 v87, v248, v249\n" \
    "s_add_u32 s82, %[so20], 0x14400\ns_nop 0\nv_permlane16_swap_b32_e32 v84, v86\nv_permlane16_swap_b32_e32 v85, v87\nbuffer_store_dwordx4 v[84:87], v244, %[srdO2], s82 offen sc1\nv_accvgpr_read_b32 v250, a204\n" \
    "v_accvgpr_read_b32 v251, a205\nv_accvgpr_read_b32 v252, a206\nv_accvgpr_read_b32 v253, a207\nv_cvt_pk_bf16_f32 v92, v92, v93\nv_cvt_pk_bf16_f32 v93, v94, v95\nv_cvt_pk_bf16_f32 v94, v250, v251\n" \
    "v_cvt_pk_bf16_f32 v95, v252, v253\ns_add_u32 s82, %[so20], 0x19400\ns_nop 0\nv_permlane16_swap_b32_e32 v92, v94\nv_permlane16_swap_b32_e32 v93, v95\nbuffer_store_dwordx4 v[92:95], v244, %[srdO2], s82 offen sc1\n" \
    "v_accvgpr_read_b32 v246, a208\nv_accvgpr_read_b32 v247, a209\nv_accvgpr_read_b32 v248, a210\nv_accvgpr_read_b32 v249, a211\nv_cvt_pk_bf16_f32 v100, v100, v101\nv_cvt_pk_bf16_f32 v101, v102, v103\n" \
    "v_cvt_pk_bf16_f32 v102, v246, v247\nv_cvt_pk_bf16_f32 v103, v248, v249\ns_add_u32 s82, %[so20], 0x800\ns_nop 0\nv_permlane16_swap_b32_e32 v100, v102\nv_permlane16_swap_b32_e32 v101, v103\n" \
    "buffer_store_dwordx4 v[100:103], v244, %[srdO2], s82 offen sc1\nv_accvgpr_read_b32 v250, a212\nv_accvgpr_read_b32 v251, a213\nv_accvgpr_read_b32 v252, a214\nv_accvgpr_read_b32 v253, a215\n" \
    "v_cvt_pk_bf16_f32 v108, v108, v109\nv_cvt_pk_bf16_f32 v109, v110, v111\nv_cvt_pk_bf16_f32 v110, v250, v251\nv_cvt_pk_bf16_f32 v111, v252, v253\ns_add_u32 s82, %[so20], 0x5800\ns_nop 0\n" \
    "v_permlane16_swap_b32_e32 v108, v110\nv_permlane16_swap_b32_e32 v109, v111\nbuffer_store_dwordx4 v[108:111], v244, %[srdO2], s82 offen sc1\nv_accvgpr_read_b32 v246, a216\nv_accvgpr_read_b32 v247, a217\n" \
    "v_accvgpr_read_b32 v248, a218\nv_accvgpr_read_b32 v249, a219\nv_cvt_pk_bf16_f32 v116, v116, v117\nv_cvt_pk_bf16_f32 v117, v118, v119\nv_cvt_pk_bf16_f32 v118, v246, v247\nv_cvt_pk_bf16_f32 v119, v248, v249\n" \
    "s_add_u32 s82, %[so20], 0xa800\ns_nop 0\nv_permlane16_swap_b32_e32 v116, v118\nv_permlane16_swap_b32_e32 v117, v119\nbuffer_store_dwordx4 v[116:119], v244, %[srdO2], s82 offen sc1\nv_accvgpr_read_b32 v250, a220\n" \
    "v_accvgpr_read_b32 v251, a221\nv_accvgpr_read_b32 v252, a222\nv_accvgpr_read_b32 v253, a223\nv_cvt_pk_bf16_f32 v124, v124, v125\nv_cvt_pk_bf16_f32 v125, v126, v127\nv_cvt_pk_bf16_f32 v126, v250, v251\n" \
    "v_cvt_pk_bf16_f32 v127, v252, v253\ns_add_u32 s82, %[so20], 0xf800\ns_nop 0\nv_permlane16_swap_b32_e32 v124, v126\nv_permlane16_swap_b32_e32 v125, v127\nbuffer_store_dwordx4 v[124:127], v244, %[srdO2], s82 offen sc1\n" \
    "v_accvgpr_read_b32 v246, a224\nv_accvgpr_read_b32 v247, a225\nv_accvgpr_read_b32 v248, a226\nv_accvgpr_read_b32 v249, a227\nv_cvt_pk_bf16_f32 v132, v132, v133\nv_cvt_pk_bf16_f32 v133, v134, v135\n" \
    "v_cvt_pk_bf16_f32 v134, v246, v247\nv_cvt_pk_bf16_f32 v135, v248, v249\ns_add_u32 s82, %[so20], 0x14800\ns_nop 0\nv_permlane16_swap_b32_e32 v132, v134\nv_permlane16_swap_b32_e32 v133, v135\n" \
    "buffer_store_dwordx4 v[132:135], v244, %[srdO2], s82 offen sc1\nv_accvgpr_read_b32 v250, a228\nv_accvgpr_read_b32 v251, a229\nv_accvgpr_read_b32 v252, a230\nv_accvgpr_read_b32 v253, a231\n" \
    "v_cvt_pk_bf16_f32 v140, v140, v141\nv_cvt_pk_bf16_f32 v141, v142, v143\nv_cvt_pk_bf16_f32 v142, v250, v251\nv_cvt_pk_bf16_f32 v143, v252, v253\ns_add_u32 s82, %[so20], 0x19800\ns_nop 0\n" \
    "v_permlane16_swap_b32_e32 v140, v142\nv_permlane16_swap_b32_e32 v141, v143\nbuffer_store_dwordx4 v[140:143], v244, %[srdO2], s82 offen sc1\nv_accvgpr_read_b32 v246, a232\nv_accvgpr_read_b32 v247, a233\n" \
    "v_accvgpr_read_b32 v248, a234\nv_accvgpr_read_b32 v249, a235\nv_cvt_pk_bf16_f32 v148, v148, v149\nv_cvt_pk_bf16_f32 v149, v150, v151\nv_cvt_pk_bf16_f32 v150, v246, v247\nv_cvt_pk_bf16_f32 v151, v248, v249\n" \
    "s_add_u32 s82, %[so20], 0xc00\ns_nop 0\nv_permlane16_swap_b32_e32 v148, v150\nv_permlane16_swap_b32_e32 v149, v151\nbuffer_store_dwordx4 v[148:151], v244, %[srdO2], s82 offen sc1\nv_accvgpr_read_b32 v250, a236\n" \
    "v_accvgpr_read_b32 v251, a237\nv_accvgpr_read_b32 v252, a238\nv_accvgpr_read_b32 v253, a239\nv_cvt_pk_bf16_f32 v156, v156, v157\nv_cvt_pk_bf16_f32 v157, v158, v159\nv_cvt_pk_bf16_f32 v158, v250, v251\n" \
    "v_cvt_pk_bf16_f32 v159, v252, v253\ns_add_u32 s82, %[so20], 0x5c00\ns_nop 0\nv_permlane16_swap_b32_e32 v156, v158\nv_permlane16_swap_b32_e32 v157, v159\nbuffer_store_dwordx4 v[156:159], v244, %[srdO2], s82 offen sc1\n" \
    "v_accvgpr_read_b32 v246, a240\nv_accvgpr_read_b32 v247, a241\nv_accvgpr_read_b32 v248, a242\nv_accvgpr_read_b32 v249, a243\nv_cvt_pk_bf16_f32 v164, v164, v165\nv_cvt_pk_bf16_f32 v165, v166, v167\n" \
    "v_cvt_pk_bf16_f32 v166, v246, v247\nv_cvt_pk_bf16_f32 v167, v248, v249\ns_add_u32 s82, %[so20], 0xac00\ns_nop 0\nv_permlane16_swap_b32_e32 v164, v166\nv_permlane16_swap_b32_e32 v165, v167\n" \
    "buffer_store_dwordx4 v[164:167], v244, %[srdO2], s82 offen sc1\nv_accvgpr_read_b32 v250, a244\nv_accvgpr_read_b32 v251, a245\nv_accvgpr_read_b32 v252, a246\nv_accvgpr_read_b32 v253, a247\n" \
    "v_cvt_pk_bf16_f32 v172, v172, v173\nv_cvt_pk_bf16_f32 v173, v174, v175\nv_cvt_pk_bf16_f32 v174, v250, v251\nv_cvt_pk_bf16_f32 v175, v252, v253\ns_add_u32 s82, %[so20], 0xfc00\ns_nop 0\n" \
    "v_permlane16_swap_b32_e32 v172, v174\nv_permlane16_swap_b32_e32 v173, v175\nbuffer_store_dwordx4 v[172:175], v244, %[srdO2], s82 offen sc1\nv_accvgpr_read_b32 v246, a248\nv_accvgpr_read_b32 v247, a249\n" \
    "v_accvgpr_read_b32 v248, a250\nv_accvgpr_read_b32 v249, a251\nv_cvt_pk_bf16_f32 v180, v180, v181\nv_cvt_pk_bf16_f32 v181, v182, v183\nv_cvt_pk_bf16_f32 v182, v246, v247\nv_cvt_pk_bf16_f32 v183, v248, v249\n" \
    "s_add_u32 s82, %[so20], 0x14c00\ns_nop 0\nv_permlane16_swap_b32_e32 v180, v182\nv_permlane16_swap_b32_e32 v181, v183\nbuffer_store_dwordx4 v[180:183], v244, %[srdO2], s82 offen sc1\nv_accvgpr_read_b32 v250, a252\n" \
    "v_accvgpr_read_b32 v251, a253\nv_accvgpr_read_b32 v252, a254\nv_accvgpr_read_b32 v253, a255\nv_cvt_pk_bf16_f32 v188, v188, v189\nv_cvt_pk_bf16_f32 v189, v190, v191\nv_cvt_pk_bf16_f32 v190, v250, v251\n" \
    "v_cvt_pk_bf16_f32 v191, v252, v253\ns_add_u32 s82, %[so20], 0x19c00\ns_nop 0\nv_permlane16_swap_b32_e32 v188, v190\nv_permlane16_swap_b32_e32 v189, v191\nbuffer_store_dwordx4 v[188:191], v244, %[srdO2], s82 offen sc1\n" \
    "v_accvgpr_read_b32 v246, a0\nv_accvgpr_read_b32 v247, a1\nv_accvgpr_read_b32 v248, a2\nv_accvgpr_read_b32 v249, a3\nv_cvt_pk_bf16_f32 v196, v196, v197\nv_cvt_pk_bf16_f32 v197, v198, v199\n" \
    "v_cvt_pk_bf16_f32 v198, v246, v247\nv_cvt_pk_bf16_f32 v199, v248, v249\ns_add_u32 s82, %[so20], 0x1000\ns_nop 0\nv_permlane16_swap_b32_e32 v196, v198\nv_permlane16_swap_b32_e32 v197, v199\n" \
    "buffer_store_dwordx4 v[196:199], v244, %[srdO2], s82 offen sc1\nv_accvgpr_read_b32 v250, a4\nv_accvgpr_read_b32 v251, a5\nv_accvgpr_read_b32 v252, a6\nv_accvgpr_read_b32 v253, a7\nv_cvt_pk_bf16_f32 v204, v204, v205\n" \
    "v_cvt_pk_bf16_f32 v205, v206, v207\nv_cvt_pk_bf16_f32 v206, v250, v251\nv_cvt_pk_bf16_f32 v207, v252, v253\ns_add_u32 s82, %[so20], 0x6000\ns_nop 0\nv_permlane16_swap_b32_e32 v204, v206\n" \
    "v_permlane16_swap_b32_e32 v205, v207\nbuffer_store_dwordx4 v[204:207], v244, %[srdO2], s82 offen sc1\nv_accvgpr_read_b32 v246, a8\nv_accvgpr_read_b32 v247, a9\nv_accvgpr_read_b32 v248, a10\nv_accvgpr_read_b32 v249, a11\n" \
    "v_cvt_pk_bf16_f32 v212, v212, v213\nv_cvt_pk_bf16_f32 v213, v214, v215\nv_cvt_pk_bf16_f32 v214, v246, v247\nv_cvt_pk_bf16_f32 v215, v248, v249\ns_add_u32 s82, %[so20], 0xb000\ns_nop 0\n" \
    "v_permlane16_swap_b32_e32 v212, v214\nv_permlane16_swap_b32_e32 v213, v215\nbuffer_store_dwordx4 v[212:215], v244, %[srdO2], s82 offen sc1\nv_accvgpr_read_b32 v250, a12\nv_accvgpr_read_b32 v251, a13\n" \
    "v_accvgpr_read_b32 v252, a14\nv_accvgpr_read_b32 v253, a15\nv_cvt_pk_bf16_f32 v220, v220, v221\nv_cvt_pk_bf16_f32 v221, v222, v223\nv_cvt_pk_bf16_f32 v222, v250, v251\nv_cvt_pk_bf16_f32 v223, v252, v253\n" \
    "s_add_u32 s82, %[so20], 0x10000\ns_nop 0\nv_permlane16_swap_b32_e32 v220, v222\nv_permlane16_swap_b32_e32 v221, v223\nbuffer_store_dwordx4 v[220:223], v244, %[srdO2], s82 offen sc1\nv_accvgpr_read_b32 v246, a24\n" \
    "v_accvgpr_read_b32 v247, a25\nv_accvgpr_read_b32 v248, a26\nv_accvgpr_read_b32 v249, a27\nv_accvgpr_read_b32 v250, a16\nv_accvgpr_read_b32 v251, a17\nv_accvgpr_read_b32 v252, a18\nv_accvgpr_read_b32 v253, a19\n" \
    "v_cvt_pk_bf16_f32 v246, v246, v247\nv_cvt_pk_bf16_f32 v247, v248, v249\nv_cvt_pk_bf16_f32 v248, v250, v251\nv_cvt_pk_bf16_f32 v249, v252, v253\ns_add_u32 s82, %[so20], 0x15000\ns_nop 0\n" \
    "v_permlane16_swap_b32_e32 v246, v248\nv_permlane16_swap_b32_e32 v247, v249\nbuffer_store_dwordx4 v[246:249], v244, %[srdO2], s82 offen sc1\ns_nop 1\nv_accvgpr_read_b32 v246, a28\nv_accvgpr_read_b32 v247, a29\n" \
    "v_accvgpr_read_b32 v248, a30\nv_accvgpr_read_b32 v249, a31\nv_accvgpr_read_b32 v250, a20\nv_accvgpr_read_b32 v251, a21\nv_accvgpr_read_b32 v252, a22\nv_accvgpr_read_b32 v253, a23\nv_cvt_pk_bf16_f32 v246, v246, v247\n" \
    "v_cvt_pk_bf16_f32 v247, v248, v249\nv_cvt_pk_bf16_f32 v248, v250, v251\nv_cvt_pk_bf16_f32 v249, v252, v253\ns_add_u32 s82, %[so20], 0x1a000\ns_nop 0\nv_permlane16_swap_b32_e32 v246, v248\n" \
    "v_permlane16_swap_b32_e32 v247, v249\nbuffer_store_dwordx4 v[246:249], v244, %[srdO2], s82 offen sc1\ns_nop 1\nL_end%=:\ns_waitcnt vmcnt(0) lgkmcnt(0)\n"
#define G10_ALONE_Q \
    "s_add_u32 m0, %[m0w2], 0x20000\ns_add_u32 s81, %[sw2], 0x0\nbuffer_load_dwordx4 v240, %[srdW2], s81 offen offset:0 lds\nbuffer_load_dwordx4 v241, %[srdW2], s81 offen offset:1024 lds\n" \
    "buffer_load_dwordx4 v242, %[srdW2], s81 offen offset:2048 lds\ns_add_u32 m0, %[m0w2], 0x21800\ns_add_u32 s81, %[sw2], 0x20000\nbuffer_load_dwordx4 v240, %[srdW2], s81 offen offset:0 lds\n" \
    "buffer_load_dwordx4 v241, %[srdW2], s81 offen offset:1024 lds\nbuffer_load_dwordx4 v242, %[srdW2], s81 offen offset:2048 lds\ns_add_u32 %[sw2], %[sw2], 0x80000\ns_add_u32 m0, %[m0x20], 0x0\ns_add_u32 s80, %[sx2], 0x0\n" \
    "buffer_load_dwordx4 v236, %[srdX2], s80 offen offset:0 sc1 lds\nbuffer_load_dwordx4 v237, %[srdX2], s80 offen offset:1024 sc1 lds\nbuffer_load_dwordx4 v238, %[srdX2], s80 offen offset:2048 sc1 lds\n" \
    "buffer_load_dwordx4 v239, %[srdX2], s80 offen offset:3072 sc1 lds\ns_add_u32 m0, %[m0x20], 0x1000\ns_add_u32 s80, %[sx2], 0xa000\nbuffer_load_dwordx4 v236, %[srdX2], s80 offen offset:0 sc1 lds\n" \
    "buffer_load_dwordx4 v237, %[srdX2], s80 offen offset:1024 sc1 lds\nbuffer_load_dwordx4 v238, %[srdX2], s80 offen offset:2048 sc1 lds\nbuffer_load_dwordx4 v239, %[srdX2], s80 offen offset:3072 sc1 lds\n" \
    "s_add_u32 %[sx2], %[sx2], 0x80\ns_add_u32 m0, %[m0w2], 0x10000\ns_add_u32 s81, %[sw2], 0x0\nbuffer_load_dwordx4 v240, %[srdW2], s81 offen offset:0 lds\nbuffer_load_dwordx4 v241, %[srdW2], s81 offen offset:1024 lds\n" \
    "buffer_load_dwordx4 v242, %[srdW2], s81 offen offset:2048 lds\ns_add_u32 m0, %[m0w2], 0x11800\ns_add_u32 s81, %[sw2], 0x20000\nbuffer_load_dwordx4 v240, %[srdW2], s81 offen offset:0 lds\n" \
    "buffer_load_dwordx4 v241, %[srdW2], s81 offen offset:1024 lds\nbuffer_load_dwordx4 v242, %[srdW2], s81 offen offset:2048 lds\ns_add_u32 %[sw2], %[sw2], 0x80000\ns_add_u32 m0, %[m0x20], 0x8000\n" \
    "s_add_u32 s80, %[sx2], 0x0\nbuffer_load_dwordx4 v236, %[srdX2], s80 offen offset:0 sc1 lds\nbuffer_load_dwordx4 v237, %[srdX2], s80 offen offset:1024 sc1 lds\n" \
    "buffer_load_dwordx4 v238, %[srdX2], s80 offen offset:2048 sc1 lds\nbuffer_load_dwordx4 v239, %[srdX2], s80 offen offset:3072 sc1 lds\ns_add_u32 m0, %[m0x20], 0x9000\ns_add_u32 s80, %[sx2], 0xa000\n" \
    "buffer_load_dwordx4 v236, %[srdX2], s80 offen offset:0 sc1 lds\nbuffer_load_dwordx4 v237, %[srdX2], s80 offen offset:1024 sc1 lds\nbuffer_load_dwordx4 v238, %[srdX2], s80 offen offset:2048 sc1 lds\n" \
    "buffer_load_dwordx4 v239, %[srdX2], s80 offen offset:3072 sc1 lds\ns_add_u32 %[sx2], %[sx2], 0x80\ns_waitcnt vmcnt(14)\ns_barrier\nds_read_b128 a[32:35], v224 offset:0\nds_read_b128 a[36:39], v224 offset:2048\n" \
    "ds_read_b128 a[40:43], v224 offset:4096\nds_read_b128 a[44:47], v224 offset:6144\nds_read_b128 a[48:51], v224 offset:8192\nds_read_b128 a[52:55], v224 offset:10240\nds_read_b128 a[56:59], v224 offset:12288\n" \
    "ds_read_b128 a[60:63], v224 offset:14336\nds_read_b64_tr_b16 a[64:65], v228 offset:0\nds_read_b64_tr_b16 a[66:67], v228 offset:1536\nds_read_b64_tr_b16 a[68:69], v229 offset:0\n" \
    "ds_read_b64_tr_b16 a[70:71], v229 offset:1536\nds_read_b64_tr_b16 a[72:73], v230 offset:0\nds_read_b64_tr_b16 a[74:75], v230 offset:1536\nds_read_b64_tr_b16 a[76:77], v231 offset:0\n" \
    "ds_read_b64_tr_b16 a[78:79], v231 offset:1536\nds_read_b64_tr_b16 a[80:81], v232 offset:0\nds_read_b64_tr_b16 a[82:83], v232 offset:1536\nds_read_b64_tr_b16 a[84:85], v233 offset:0\n" \
    "ds_read_b64_tr_b16 a[86:87], v233 offset:1536\ns_waitcnt lgkmcnt(0)\nv_mfma_f32_16x16x32_bf16 v[4:7], a[64:67], a[32:35], 0\nds_read_b128 a[96:99], v225 offset:0\n" \
    "v_mfma_f32_16x16x32_bf16 a[160:163], a[68:71], a[32:35], 0\nds_read_b128 a[100:103], v225 offset:2048\ns_mov_b32 s83, %[sw2]\nv_mfma_f32_16x16x32_bf16 v[12:15], a[64:67], a[36:39], 0\ns_add_u32 %[sw2], %[sw2], 0x80000\n" \
    "v_mfma_f32_16x16x32_bf16 a[164:167], a[68:71], a[36:39], 0\nds_read_b128 a[104:107], v225 offset:4096\ns_add_u32 m0, %[m0w2], 0x18000\ns_add_u32 s81, s83, 0x0\nbuffer_load_dwordx4 v240, %[srdW2], s81 offen offset:0 lds\n" \
    "v_mfma_f32_16x16x32_bf16 v[20:23], a[64:67], a[40:43], 0\nv_mfma_f32_16x16x32_bf16 a[168:171], a[68:71], a[40:43], 0\nds_read_b128 a[108:111], v225 offset:6144\nv_mfma_f32_16x16x32_bf16 v[28:31], a[64:67], a[44:47], 0\n" \
    "ds_read_b128 a[112:115], v225 offset:8192\nv_mfma_f32_16x16x32_bf16 a[172:175], a[68:71], a[44:47], 0\nv_mfma_f32_16x16x32_bf16 v[36:39], a[64:67], a[48:51], 0\nds_read_b128 a[116:119], v225 offset:10240\n" \
    "v_mfma_f32_16x16x32_bf16 a[176:179], a[68:71], a[48:51], 0\nbuffer_load_dwordx4 v241, %[srdW2], s81 offen offset:1024 lds\nv_mfma_f32_16x16x32_bf16 v[44:47], a[64:67], a[52:55], 0\n" \
    "ds_read_b128 a[120:123], v225 offset:12288\nv_mfma_f32_16x16x32_bf16 a[180:183], a[68:71], a[52:55], 0\nds_read_b128 a[124:127], v225 offset:14336\nv_mfma_f32_16x16x32_bf16 v[52:55], a[64:67], a[56:59], 0\n" \
    "v_mfma_f32_16x16x32_bf16 a[184:187], a[68:71], a[56:59], 0\nds_read_b64_tr_b16 a[128:129], v228 offset:12288\nv_mfma_f32_16x16x32_bf16 v[60:63], a[64:67], a[60:63], 0\n" \
    "v_mfma_f32_16x16x32_bf16 a[188:191], a[68:71], a[60:63], 0\nds_read_b64_tr_b16 a[130:131], v228 offset:13824\nbuffer_load_dwordx4 v242, %[srdW2], s81 offen offset:2048 lds\n" \
    "v_mfma_f32_16x16x32_bf16 v[68:71], a[72:75], a[32:35], 0\nv_mfma_f32_16x16x32_bf16 a[192:195], a[76:79], a[32:35], 0\nds_read_b64_tr_b16 a[132:133], v229 offset:12288\n" \
    "v_mfma_f32_16x16x32_bf16 v[76:79], a[72:75], a[36:39], 0\nds_read_b64_tr_b16 a[134:135], v229 offset:13824\nv_mfma_f32_16x16x32_bf16 a[196:199], a[76:79], a[36:39], 0\n" \
    "v_mfma_f32_16x16x32_bf16 v[84:87], a[72:75], a[40:43], 0\nds_read_b64_tr_b16 a[136:137], v230 offset:12288\nv_mfma_f32_16x16x32_bf16 a[200:203], a[76:79], a[40:43], 0\ns_add_u32 m0, %[m0w2], 0x19800\n" \
    "s_add_u32 s81, s83, 0x20000\nbuffer_load_dwordx4 v240, %[srdW2], s81 offen offset:0 lds\nv_mfma_f32_16x16x32_bf16 v[92:95], a[72:75], a[44:47], 0\nds_read_b64_tr_b16 a[138:139], v230 offset:13824\n" \
    "v_mfma_f32_16x16x32_bf16 a[204:207], a[76:79], a[44:47], 0\nds_read_b64_tr_b16 a[140:141], v231 offset:12288\nv_mfma_f32_16x16x32_bf16 v[100:103], a[72:75], a[48:51], 0\n" \
    "v_mfma_f32_16x16x32_bf16 a[208:211], a[76:79], a[48:51], 0\nds_read_b64_tr_b16 a[142:143], v231 offset:13824\nv_mfma_f32_16x16x32_bf16 v[108:111], a[72:75], a[52:55], 0\n" \
    "v_mfma_f32_16x16x32_bf16 a[212:215], a[76:79], a[52:55], 0\nds_read_b64_tr_b16 a[144:145], v232 offset:12288\nbuffer_load_dwordx4 v241, %[srdW2], s81 offen offset:1024 lds\n" \
    "v_mfma_f32_16x16x32_bf16 v[116:119], a[72:75], a[56:59], 0\nds_read_b64_tr_b16 a[146:147], v232 offset:13824\nv_mfma_f32_16x16x32_bf16 a[216:219], a[76:79], a[56:59], 0\n" \
    "v_mfma_f32_16x16x32_bf16 v[124:127], a[72:75], a[60:63], 0\nds_read_b64_tr_b16 a[148:149], v233 offset:12288\nv_mfma_f32_16x16x32_bf16 a[220:223], a[76:79], a[60:63], 0\n" \
    "v_mfma_f32_16x16x32_bf16 v[132:135], a[80:83], a[32:35], 0\nds_read_b64_tr_b16 a[150:151], v233 offset:13824\nv_mfma_f32_16x16x32_bf16 a[224:227], a[84:87], a[32:35], 0\n" \
    "buffer_load_dwordx4 v242, %[srdW2], s81 offen offset:2048 lds\nv_mfma_f32_16x16x32_bf16 v[140:143], a[80:83], a[36:39], 0\nv_mfma_f32_16x16x32_bf16 a[228:231], a[84:87], a[36:39], 0\n" \
    "v_mfma_f32_16x16x32_bf16 v[148:151], a[80:83], a[40:43], 0\nv_mfma_f32_16x16x32_bf16 a[232:235], a[84:87], a[40:43], 0\nv_mfma_f32_16x16x32_bf16 v[156:159], a[80:83], a[44:47], 0\n" \
    "v_mfma_f32_16x16x32_bf16 a[236:239], a[84:87], a[44:47], 0\nv_mfma_f32_16x16x32_bf16 v[164:167], a[80:83], a[48:51], 0\nv_mfma_f32_16x16x32_bf16 a[240:243], a[84:87], a[48:51], 0\n" \
    "v_mfma_f32_16x16x32_bf16 v[172:175], a[80:83], a[52:55], 0\nv_mfma_f32_16x16x32_bf16 a[244:247], a[84:87], a[52:55], 0\nv_mfma_f32_16x16x32_bf16 v[180:183], a[80:83], a[56:59], 0\n" \
    "v_mfma_f32_16x16x32_bf16 a[248:251], a[84:87], a[56:59], 0\nv_mfma_f32_16x16x32_bf16 v[188:191], a[80:83], a[60:63], 0\nv_mfma_f32_16x16x32_bf16 a[252:255], a[84:87], a[60:63], 0\ns_waitcnt vmcnt(6) lgkmcnt(0)\n" \
    "s_barrier\ns_mov_b32 s84, 0x20000\ns_mov_b32 s85, 0x10000\ns_mov_b32 s86, 0x18000\ns_mov_b32 %[cnt], 10\nL_Q0l%=:\nv_mfma_f32_16x16x32_bf16 v[4:7], a[128:131], a[96:99], v[4:7]\ns_cmp_gt_u32 %[cnt], 2\n" \
    "s_cselect_b32 s83, %[sx2], 0x70000000\nv_mfma_f32_16x16x32_bf16 a[160:163], a[132:135], a[96:99], a[160:163]\ns_sub_u32 s81, s85, s84\ns_mov_b32 s87, s84\ns_add_u32 m0, %[m0x20], 0x0\ns_add_u32 s80, s83, 0x0\n" \
    "buffer_load_dwordx4 v236, %[srdX2], s80 offen offset:0 sc1 lds\nv_mfma_f32_16x16x32_bf16 v[12:15], a[128:131], a[100:103], v[12:15]\ns_mov_b32 s84, s85\ns_mov_b32 s85, s86\n" \
    "v_mfma_f32_16x16x32_bf16 a[164:167], a[132:135], a[100:103], a[164:167]\ns_add_u32 %[sx2], %[sx2], 0x80\ns_mov_b32 s86, s87\nv_mfma_f32_16x16x32_bf16 v[20:23], a[128:131], a[104:107], v[20:23]\n" \
    "v_add_u32_e32 v228, s81, v228\nds_read_b128 a[32:35], v224 offset:32768\nbuffer_load_dwordx4 v237, %[srdX2], s80 offen offset:1024 sc1 lds\nv_mfma_f32_16x16x32_bf16 a[168:171], a[132:135], a[104:107], a[168:171]\n" \
    "v_add_u32_e32 v229, s81, v229\nv_mfma_f32_16x16x32_bf16 v[28:31], a[128:131], a[108:111], v[28:31]\nv_add_u32_e32 v230, s81, v230\nds_read_b128 a[36:39], v224 offset:34816\n" \
    "v_mfma_f32_16x16x32_bf16 a[172:175], a[132:135], a[108:111], a[172:175]\nv_add_u32_e32 v231, s81, v231\nbuffer_load_dwordx4 v238, %[srdX2], s80 offen offset:2048 sc1 lds\n" \
    "v_mfma_f32_16x16x32_bf16 v[36:39], a[128:131], a[112:115], v[36:39]\nv_add_u32_e32 v232, s81, v232\nds_read_b128 a[40:43], v224 offset:36864\nv_mfma_f32_16x16x32_bf16 a[176:179], a[132:135], a[112:115], a[176:179]\n" \
    "v_add_u32_e32 v233, s81, v233\nv_mfma_f32_16x16x32_bf16 v[44:47], a[128:131], a[116:119], v[44:47]\nds_read_b128 a[44:47], v224 offset:38912\ns_sub_u32 %[cnt], %[cnt], 1\n" \
    "v_mfma_f32_16x16x32_bf16 a[180:183], a[132:135], a[116:119], a[180:183]\nbuffer_load_dwordx4 v239, %[srdX2], s80 offen offset:3072 sc1 lds\nv_mfma_f32_16x16x32_bf16 v[52:55], a[128:131], a[120:123], v[52:55]\n" \
    "ds_read_b128 a[48:51], v224 offset:40960\nv_mfma_f32_16x16x32_bf16 a[184:187], a[132:135], a[120:123], a[184:187]\nv_mfma_f32_16x16x32_bf16 v[60:63], a[128:131], a[124:127], v[60:63]\n" \
    "ds_read_b128 a[52:55], v224 offset:43008\ns_add_u32 m0, %[m0x20], 0x1000\ns_add_u32 s80, s83, 0xa000\nbuffer_load_dwordx4 v236, %[srdX2], s80 offen offset:0 sc1 lds\n" \
    "v_mfma_f32_16x16x32_bf16 a[188:191], a[132:135], a[124:127], a[188:191]\nv_mfma_f32_16x16x32_bf16 v[68:71], a[136:139], a[96:99], v[68:71]\nds_read_b128 a[56:59], v224 offset:45056\n" \
    "v_mfma_f32_16x16x32_bf16 a[192:195], a[140:143], a[96:99], a[192:195]\nbuffer_load_dwordx4 v237, %[srdX2], s80 offen offset:1024 sc1 lds\nv_mfma_f32_16x16x32_bf16 v[76:79], a[136:139], a[100:103], v[76:79]\n" \
    "ds_read_b128 a[60:63], v224 offset:47104\nv_mfma_f32_16x16x32_bf16 a[196:199], a[140:143], a[100:103], a[196:199]\nv_mfma_f32_16x16x32_bf16 v[84:87], a[136:139], a[104:107], v[84:87]\n" \
    "ds_read_b64_tr_b16 a[64:65], v228 offset:0\nv_mfma_f32_16x16x32_bf16 a[200:203], a[140:143], a[104:107], a[200:203]\nds_read_b64_tr_b16 a[66:67], v228 offset:1536\n" \
    "buffer_load_dwordx4 v238, %[srdX2], s80 offen offset:2048 sc1 lds\nv_mfma_f32_16x16x32_bf16 v[92:95], a[136:139], a[108:111], v[92:95]\nds_read_b64_tr_b16 a[68:69], v229 offset:0\n" \
    "v_mfma_f32_16x16x32_bf16 a[204:207], a[140:143], a[108:111], a[204:207]\nds_read_b64_tr_b16 a[70:71], v229 offset:1536\nv_mfma_f32_16x16x32_bf16 v[100:103], a[136:139], a[112:115], v[100:103]\n" \
    "ds_read_b64_tr_b16 a[72:73], v230 offset:0\nbuffer_load_dwordx4 v239, %[srdX2], s80 offen offset:3072 sc1 lds\nv_mfma_f32_16x16x32_bf16 a[208:211], a[140:143], a[112:115], a[208:211]\n" \
    "ds_read_b64_tr_b16 a[74:75], v230 offset:1536\nv_mfma_f32_16x16x32_bf16 v[108:111], a[136:139], a[116:119], v[108:111]\nv_mfma_f32_16x16x32_bf16 a[212:215], a[140:143], a[116:119], a[212:215]\n" \
    "ds_read_b64_tr_b16 a[76:77], v231 offset:0\nv_mfma_f32_16x16x32_bf16 v[116:119], a[136:139], a[120:123], v[116:119]\nds_read_b64_tr_b16 a[78:79], v231 offset:1536\n" \
    "v_mfma_f32_16x16x32_bf16 a[216:219], a[140:143], a[120:123], a[216:219]\nds_read_b64_tr_b16 a[80:81], v232 offset:0\nv_mfma_f32_16x16x32_bf16 v[124:127], a[136:139], a[124:127], v[124:127]\n" \
    "ds_read_b64_tr_b16 a[82:83], v232 offset:1536\nv_mfma_f32_16x16x32_bf16 a[220:223], a[140:143], a[124:127], a[220:223]\nds_read_b64_tr_b16 a[84:85], v233 offset:0\n" \
    "v_mfma_f32_16x16x32_bf16 v[132:135], a[144:147], a[96:99], v[132:135]\nds_read_b64_tr_b16 a[86:87], v233 offset:1536\nv_mfma_f32_16x16x32_bf16 a[224:227], a[148:151], a[96:99], a[224:227]\n" \
    "v_mfma_f32_16x16x32_bf16 v[140:143], a[144:147], a[100:103], v[140:143]\nv_mfma_f32_16x16x32_bf16 a[228:231], a[148:151], a[100:103], a[228:231]\nv_mfma_f32_16x16x32_bf16 v[148:151], a[144:147], a[104:107], v[148:151]\n" \
    "v_mfma_f32_16x16x32_bf16 a[232:235], a[148:151], a[104:107], a[232:235]\nv_mfma_f32_16x16x32_bf16 v[156:159], a[144:147], a[108:111], v[156:159]\nv_mfma_f32_16x16x32_bf16 a[236:239], a[148:151], a[108:111], a[236:239]\n" \
    "v_mfma_f32_16x16x32_bf16 v[164:167], a[144:147], a[112:115], v[164:167]\nv_mfma_f32_16x16x32_bf16 a[240:243], a[148:151], a[112:115], a[240:243]\nv_mfma_f32_16x16x32_bf16 v[172:175], a[144:147], a[116:119], v[172:175]\n" \
    "v_mfma_f32_16x16x32_bf16 a[244:247], a[148:151], a[116:119], a[244:247]\nv_mfma_f32_16x16x32_bf16 v[180:183], a[144:147], a[120:123], v[180:183]\nv_mfma_f32_16x16x32_bf16 a[248:251], a[148:151], a[120:123], a[248:251]\n" \
    "v_mfma_f32_16x16x32_bf16 v[188:191], a[144:147], a[124:127], v[188:191]\nv_mfma_f32_16x16x32_bf16 a[252:255], a[148:151], a[124:127], a[252:255]\ns_waitcnt lgkmcnt(0)\n" \
    "v_mfma_f32_16x16x32_bf16 v[4:7], a[64:67], a[32:35], v[4:7]\nds_read_b128 a[96:99], v225 offset:32768\nv_mfma_f32_16x16x32_bf16 a[160:163], a[68:71], a[32:35], a[160:163]\nds_read_b128 a[100:103], v225 offset:34816\n" \
    "s_cmp_gt_u32 %[cnt], 2\ns_cselect_b32 s83, %[sw2], 0x70000000\nv_mfma_f32_16x16x32_bf16 v[12:15], a[64:67], a[36:39], v[12:15]\ns_add_u32 %[sw2], %[sw2], 0x80000\n" \
    "v_mfma_f32_16x16x32_bf16 a[164:167], a[68:71], a[36:39], a[164:167]\nds_read_b128 a[104:107], v225 offset:36864\ns_add_u32 m0, %[m0w2], s86\ns_add_u32 s81, s83, 0x0\n" \
    "buffer_load_dwordx4 v240, %[srdW2], s81 offen offset:0 lds\nv_mfma_f32_16x16x32_bf16 v[20:23], a[64:67], a[40:43], v[20:23]\nv_mfma_f32_16x16x32_bf16 a[168:171], a[68:71], a[40:43], a[168:171]\n" \
    "ds_read_b128 a[108:111], v225 offset:38912\nv_mfma_f32_16x16x32_bf16 v[28:31], a[64:67], a[44:47], v[28:31]\nds_read_b128 a[112:115], v225 offset:40960\n" \
    "v_mfma_f32_16x16x32_bf16 a[172:175], a[68:71], a[44:47], a[172:175]\nv_mfma_f32_16x16x32_bf16 v[36:39], a[64:67], a[48:51], v[36:39]\nds_read_b128 a[116:119], v225 offset:43008\n" \
    "v_mfma_f32_16x16x32_bf16 a[176:179], a[68:71], a[48:51], a[176:179]\nbuffer_load_dwordx4 v241, %[srdW2], s81 offen offset:1024 lds\nv_mfma_f32_16x16x32_bf16 v[44:47], a[64:67], a[52:55], v[44:47]\n" \
    "ds_read_b128 a[120:123], v225 offset:45056\nv_mfma_f32_16x16x32_bf16 a[180:183], a[68:71], a[52:55], a[180:183]\nds_read_b128 a[124:127], v225 offset:47104\n" \
    "v_mfma_f32_16x16x32_bf16 v[52:55], a[64:67], a[56:59], v[52:55]\nv_mfma_f32_16x16x32_bf16 a[184:187], a[68:71], a[56:59], a[184:187]\nds_read_b64_tr_b16 a[128:129], v228 offset:12288\n" \
    "v_mfma_f32_16x16x32_bf16 v[60:63], a[64:67], a[60:63], v[60:63]\nv_mfma_f32_16x16x32_bf16 a[188:191], a[68:71], a[60:63], a[188:191]\nds_read_b64_tr_b16 a[130:131], v228 offset:13824\n" \
    "buffer_load_dwordx4 v242, %[srdW2], s81 offen offset:2048 lds\nv_mfma_f32_16x16x32_bf16 v[68:71], a[72:75], a[32:35], v[68:71]\nv_mfma_f32_16x16x32_bf16 a[192:195], a[76:79], a[32:35], a[192:195]\n" \
    "ds_read_b64_tr_b16 a[132:133], v229 offset:12288\nv_mfma_f32_16x16x32_bf16 v[76:79], a[72:75], a[36:39], v[76:79]\nds_read_b64_tr_b16 a[134:135], v229 offset:13824\n" \
    "v_mfma_f32_16x16x32_bf16 a[196:199], a[76:79], a[36:39], a[196:199]\nv_mfma_f32_16x16x32_bf16 v[84:87], a[72:75], a[40:43], v[84:87]\nds_read_b64_tr_b16 a[136:137], v230 offset:12288\n" \
    "v_mfma_f32_16x16x32_bf16 a[200:203], a[76:79], a[40:43], a[200:203]\ns_add_u32 m0, m0, 0x1800\ns_add_u32 s81, s83, 0x20000\nbuffer_load_dwordx4 v240, %[srdW2], s81 offen offset:0 lds\n" \
    "v_mfma_f32_16x16x32_bf16 v[92:95], a[72:75], a[44:47], v[92:95]\nds_read_b64_tr_b16 a[138:139], v230 offset:13824\nv_mfma_f32_16x16x32_bf16 a[204:207], a[76:79], a[44:47], a[204:207]\n" \
    "ds_read_b64_tr_b16 a[140:141], v231 offset:12288\nv_mfma_f32_16x16x32_bf16 v[100:103], a[72:75], a[48:51], v[100:103]\nv_mfma_f32_16x16x32_bf16 a[208:211], a[76:79], a[48:51], a[208:211]\n" \
    "ds_read_b64_tr_b16 a[142:143], v231 offset:13824\nv_mfma_f32_16x16x32_bf16 v[108:111], a[72:75], a[52:55], v[108:111]\nv_mfma_f32_16x16x32_bf16 a[212:215], a[76:79], a[52:55], a[212:215]\n" \
    "ds_read_b64_tr_b16 a[144:145], v232 offset:12288\nbuffer_load_dwordx4 v241, %[srdW2], s81 offen offset:1024 lds\nv_mfma_f32_16x16x32_bf16 v[116:119], a[72:75], a[56:59], v[116:119]\n" \
    "ds_read_b64_tr_b16 a[146:147], v232 offset:13824\nv_mfma_f32_16x16x32_bf16 a[216:219], a[76:79], a[56:59], a[216:219]\nv_mfma_f32_16x16x32_bf16 v[124:127], a[72:75], a[60:63], v[124:127]\n" \
    "ds_read_b64_tr_b16 a[148:149], v233 offset:12288\nv_mfma_f32_16x16x32_bf16 a[220:223], a[76:79], a[60:63], a[220:223]\nv_mfma_f32_16x16x32_bf16 v[132:135], a[80:83], a[32:35], v[132:135]\n" \
    "ds_read_b64_tr_b16 a[150:151], v233 offset:13824\nv_mfma_f32_16x16x32_bf16 a[224:227], a[84:87], a[32:35], a[224:227]\nbuffer_load_dwordx4 v242, %[srdW2], s81 offen offset:2048 lds\n" \
    "v_mfma_f32_16x16x32_bf16 v[140:143], a[80:83], a[36:39], v[140:143]\nv_mfma_f32_16x16x32_bf16 a[228:231], a[84:87], a[36:39], a[228:231]\nv_mfma_f32_16x16x32_bf16 v[148:151], a[80:83], a[40:43], v[148:151]\n" \
    "v_mfma_f32_16x16x32_bf16 a[232:235], a[84:87], a[40:43], a[232:235]\nv_mfma_f32_16x16x32_bf16 v[156:159], a[80:83], a[44:47], v[156:159]\nv_mfma_f32_16x16x32_bf16 a[236:239], a[84:87], a[44:47], a[236:239]\n" \
    "v_mfma_f32_16x16x32_bf16 v[164:167], a[80:83], a[48:51], v[164:167]\nv_mfma_f32_16x16x32_bf16 a[240:243], a[84:87], a[48:51], a[240:243]\nv_mfma_f32_16x16x32_bf16 v[172:175], a[80:83], a[52:55], v[172:175]\n" \
    "v_mfma_f32_16x16x32_bf16 a[244:247], a[84:87], a[52:55], a[244:247]\nv_mfma_f32_16x16x32_bf16 v[180:183], a[80:83], a[56:59], v[180:183]\nv_mfma_f32_16x16x32_bf16 a[248:251], a[84:87], a[56:59], a[248:251]\n" \
    "v_mfma_f32_16x16x32_bf16 v[188:191], a[80:83], a[60:63], v[188:191]\nv_mfma_f32_16x16x32_bf16 a[252:255], a[84:87], a[60:63], a[252:255]\ns_waitcnt vmcnt(6) lgkmcnt(0)\ns_barrier\ns_cmp_eq_u32 %[cnt], 1\n" \
    "s_cbranch_scc1 L_Q0x%=\nv_mfma_f32_16x16x32_bf16 v[4:7], a[128:131], a[96:99], v[4:7]\ns_cmp_gt_u32 %[cnt], 2\ns_cselect_b32 s83, %[sx2], 0x70000000\n" \
    "v_mfma_f32_16x16x32_bf16 a[160:163], a[132:135], a[96:99], a[160:163]\ns_sub_u32 s81, s85, s84\ns_mov_b32 s87, s84\ns_add_u32 m0, %[m0x20], 0x8000\ns_add_u32 s80, s83, 0x0\n" \
    "buffer_load_dwordx4 v236, %[srdX2], s80 offen offset:0 sc1 lds\nv_mfma_f32_16x16x32_bf16 v[12:15], a[128:131], a[100:103], v[12:15]\ns_mov_b32 s84, s85\ns_mov_b32 s85, s86\n" \
    "v_mfma_f32_16x16x32_bf16 a[164:167], a[132:135], a[100:103], a[164:167]\ns_add_u32 %[sx2], %[sx2], 0x80\ns_mov_b32 s86, s87\nv_mfma_f32_16x16x32_bf16 v[20:23], a[128:131], a[104:107], v[20:23]\n" \
    "v_add_u32_e32 v228, s81, v228\nds_read_b128 a[32:35], v224 offset:0\nbuffer_load_dwordx4 v237, %[srdX2], s80 offen offset:1024 sc1 lds\nv_mfma_f32_16x16x32_bf16 a[168:171], a[132:135], a[104:107], a[168:171]\n" \
    "v_add_u32_e32 v229, s81, v229\nv_mfma_f32_16x16x32_bf16 v[28:31], a[128:131], a[108:111], v[28:31]\nv_add_u32_e32 v230, s81, v230\nds_read_b128 a[36:39], v224 offset:2048\n" \
    "v_mfma_f32_16x16x32_bf16 a[172:175], a[132:135], a[108:111], a[172:175]\nv_add_u32_e32 v231, s81, v231\nbuffer_load_dwordx4 v238, %[srdX2], s80 offen offset:2048 sc1 lds\n" \
    "v_mfma_f32_16x16x32_bf16 v[36:39], a[128:131], a[112:115], v[36:39]\nv_add_u32_e32 v232, s81, v232\nds_read_b128 a[40:43], v224 offset:4096\nv_mfma_f32_16x16x32_bf16 a[176:179], a[132:135], a[112:115], a[176:179]\n" \
    "v_add_u32_e32 v233, s81, v233\nv_mfma_f32_16x16x32_bf16 v[44:47], a[128:131], a[116:119], v[44:47]\nds_read_b128 a[44:47], v224 offset:6144\ns_sub_u32 %[cnt], %[cnt], 1\n" \
    "v_mfma_f32_16x16x32_bf16 a[180:183], a[132:135], a[116:119], a[180:183]\nbuffer_load_dwordx4 v239, %[srdX2], s80 offen offset:3072 sc1 lds\nv_mfma_f32_16x16x32_bf16 v[52:55], a[128:131], a[120:123], v[52:55]\n" \
    "ds_read_b128 a[48:51], v224 offset:8192\nv_mfma_f32_16x16x32_bf16 a[184:187], a[132:135], a[120:123], a[184:187]\nv_mfma_f32_16x16x32_bf16 v[60:63], a[128:131], a[124:127], v[60:63]\n" \
    "ds_read_b128 a[52:55], v224 offset:10240\ns_add_u32 m0, %[m0x20], 0x9000\ns_add_u32 s80, s83, 0xa000\nbuffer_load_dwordx4 v236, %[srdX2], s80 offen offset:0 sc1 lds\n" \
    "v_mfma_f32_16x16x32_bf16 a[188:191], a[132:135], a[124:127], a[188:191]\nv_mfma_f32_16x16x32_bf16 v[68:71], a[136:139], a[96:99], v[68:71]\nds_read_b128 a[56:59], v224 offset:12288\n" \
    "v_mfma_f32_16x16x32_bf16 a[192:195], a[140:143], a[96:99], a[192:195]\nbuffer_load_dwordx4 v237, %[srdX2], s80 offen offset:1024 sc1 lds\nv_mfma_f32_16x16x32_bf16 v[76:79], a[136:139], a[100:103], v[76:79]\n" \
    "ds_read_b128 a[60:63], v224 offset:14336\nv_mfma_f32_16x16x32_bf16 a[196:199], a[140:143], a[100:103], a[196:199]\nv_mfma_f32_16x16x32_bf16 v[84:87], a[136:139], a[104:107], v[84:87]\n" \
    "ds_read_b64_tr_b16 a[64:65], v228 offset:0\nv_mfma_f32_16x16x32_bf16 a[200:203], a[140:143], a[104:107], a[200:203]\nds_read_b64_tr_b16 a[66:67], v228 offset:1536\n" \
    "buffer_load_dwordx4 v238, %[srdX2], s80 offen offset:2048 sc1 lds\nv_mfma_f32_16x16x32_bf16 v[92:95], a[136:139], a[108:111], v[92:95]\nds_read_b64_tr_b16 a[68:69], v229 offset:0\n" \
    "v_mfma_f32_16x16x32_bf16 a[204:207], a[140:143], a[108:111], a[204:207]\nds_read_b64_tr_b16 a[70:71], v229 offset:1536\nv_mfma_f32_16x16x32_bf16 v[100:103], a[136:139], a[112:115], v[100:103]\n" \
    "ds_read_b64_tr_b16 a[72:73], v230 offset:0\nbuffer_load_dwordx4 v239, %[srdX2], s80 offen offset:3072 sc1 lds\nv_mfma_f32_16x16x32_bf16 a[208:211], a[140:143], a[112:115], a[208:211]\n" \
    "ds_read_b64_tr_b16 a[74:75], v230 offset:1536\nv_mfma_f32_16x16x32_bf16 v[108:111], a[136:139], a[116:119], v[108:111]\nv_mfma_f32_16x16x32_bf16 a[212:215], a[140:143], a[116:119], a[212:215]\n" \
    "ds_read_b64_tr_b16 a[76:77], v231 offset:0\nv_mfma_f32_16x16x32_bf16 v[116:119], a[136:139], a[120:123], v[116:119]\nds_read_b64_tr_b16 a[78:79], v231 offset:1536\n" \
    "v_mfma_f32_16x16x32_bf16 a[216:219], a[140:143], a[120:123], a[216:219]\nds_read_b64_tr_b16 a[80:81], v232 offset:0\nv_mfma_f32_16x16x32_bf16 v[124:127], a[136:139], a[124:127], v[124:127]\n" \
    "ds_read_b64_tr_b16 a[82:83], v232 offset:1536\nv_mfma_f32_16x16x32_bf16 a[220:223], a[140:143], a[124:127], a[220:223]\nds_read_b64_tr_b16 a[84:85], v233 offset:0\n" \
    "v_mfma_f32_16x16x32_bf16 v[132:135], a[144:147], a[96:99], v[132:135]\nds_read_b64_tr_b16 a[86:87], v233 offset:1536\nv_mfma_f32_16x16x32_bf16 a[224:227], a[148:151], a[96:99], a[224:227]\n" \
    "v_mfma_f32_16x16x32_bf16 v[140:143], a[144:147], a[100:103], v[140:143]\nv_mfma_f32_16x16x32_bf16 a[228:231], a[148:151], a[100:103], a[228:231]\nv_mfma_f32_16x16x32_bf16 v[148:151], a[144:147], a[104:107], v[148:151]\n" \
    "v_mfma_f32_16x16x32_bf16 a[232:235], a[148:151], a[104:107], a[232:235]\nv_mfma_f32_16x16x32_bf16 v[156:159], a[144:147], a[108:111], v[156:159]\nv_mfma_f32_16x16x32_bf16 a[236:239], a[148:151], a[108:111], a[236:239]\n" \
    "v_mfma_f32_16x16x32_bf16 v[164:167], a[144:147], a[112:115], v[164:167]\nv_mfma_f32_16x16x32_bf16 a[240:243], a[148:151], a[112:115], a[240:243]\nv_mfma_f32_16x16x32_bf16 v[172:175], a[144:147], a[116:119], v[172:175]\n" \
    "v_mfma_f32_16x16x32_bf16 a[244:247], a[148:151], a[116:119], a[244:247]\nv_mfma_f32_16x16x32_bf16 v[180:183], a[144:147], a[120:123], v[180:183]\nv_mfma_f32_16x16x32_bf16 a[248:251], a[148:151], a[120:123], a[248:251]\n" \
    "v_mfma_f32_16x16x32_bf16 v[188:191], a[144:147], a[124:127], v[188:191]\nv_mfma_f32_16x16x32_bf16 a[252:255], a[148:151], a[124:127], a[252:255]\ns_waitcnt lgkmcnt(0)\n" \
    "v_mfma_f32_16x16x32_bf16 v[4:7], a[64:67], a[32:35], v[4:7]\nds_read_b128 a[96:99], v225 offset:0\nv_mfma_f32_16x16x32_bf16 a[160:163], a[68:71], a[32:35], a[160:163]\nds_read_b128 a[100:103], v225 offset:2048\n" \
    "s_cmp_gt_u32 %[cnt], 2\ns_cselect_b32 s83, %[sw2], 0x70000000\nv_mfma_f32_16x16x32_bf16 v[12:15], a[64:67], a[36:39], v[12:15]\ns_add_u32 %[sw2], %[sw2], 0x80000\n" \
    "v_mfma_f32_16x16x32_bf16 a[164:167], a[68:71], a[36:39], a[164:167]\nds_read_b128 a[104:107], v225 offset:4096\ns_add_u32 m0, %[m0w2], s86\ns_add_u32 s81, s83, 0x0\n" \
    "buffer_load_dwordx4 v240, %[srdW2], s81 offen offset:0 lds\nv_mfma_f32_16x16x32_bf16 v[20:23], a[64:67], a[40:43], v[20:23]\nv_mfma_f32_16x16x32_bf16 a[168:171], a[68:71], a[40:43], a[168:171]\n" \
    "ds_read_b128 a[108:111], v225 offset:6144\nv_mfma_f32_16x16x32_bf16 v[28:31], a[64:67], a[44:47], v[28:31]\nds_read_b128 a[112:115], v225 offset:8192\nv_mfma_f32_16x16x32_bf16 a[172:175], a[68:71], a[44:47], a[172:175]\n" \
    "v_mfma_f32_16x16x32_bf16 v[36:39], a[64:67], a[48:51], v[36:39]\nds_read_b128 a[116:119], v225 offset:10240\nv_mfma_f32_16x16x32_bf16 a[176:179], a[68:71], a[48:51], a[176:179]\n" \
    "buffer_load_dwordx4 v241, %[srdW2], s81 offen offset:1024 lds\nv_mfma_f32_16x16x32_bf16 v[44:47], a[64:67], a[52:55], v[44:47]\nds_read_b128 a[120:123], v225 offset:12288\n" \
    "v_mfma_f32_16x16x32_bf16 a[180:183], a[68:71], a[52:55], a[180:183]\nds_read_b128 a[124:127], v225 offset:14336\nv_mfma_f32_16x16x32_bf16 v[52:55], a[64:67], a[56:59], v[52:55]\n" \
    "v_mfma_f32_16x16x32_bf16 a[184:187], a[68:71], a[56:59], a[184:187]\nds_read_b64_tr_b16 a[128:129], v228 offset:12288\nv_mfma_f32_16x16x32_bf16 v[60:63], a[64:67], a[60:63], v[60:63]\n" \
    "v_mfma_f32_16x16x32_bf16 a[188:191], a[68:71], a[60:63], a[188:191]\nds_read_b64_tr_b16 a[130:131], v228 offset:13824\nbuffer_load_dwordx4 v242, %[srdW2], s81 offen offset:2048 lds\n" \
    "v_mfma_f32_16x16x32_bf16 v[68:71], a[72:75], a[32:35], v[68:71]\nv_mfma_f32_16x16x32_bf16 a[192:195], a[76:79], a[32:35], a[192:195]\nds_read_b64_tr_b16 a[132:133], v229 offset:12288\n" \
    "v_mfma_f32_16x16x32_bf16 v[76:79], a[72:75], a[36:39], v[76:79]\nds_read_b64_tr_b16 a[134:135], v229 offset:13824\nv_mfma_f32_16x16x32_bf16 a[196:199], a[76:79], a[36:39], a[196:199]\n" \
    "v_mfma_f32_16x16x32_bf16 v[84:87], a[72:75], a[40:43], v[84:87]\nds_read_b64_tr_b16 a[136:137], v230 offset:12288\nv_mfma_f32_16x16x32_bf16 a[200:203], a[76:79], a[40:43], a[200:203]\ns_add_u32 m0, m0, 0x1800\n" \
    "s_add_u32 s81, s83, 0x20000\nbuffer_load_dwordx4 v240, %[srdW2], s81 offen offset:0 lds\nv_mfma_f32_16x16x32_bf16 v[92:95], a[72:75], a[44:47], v[92:95]\nds_read_b64_tr_b16 a[138:139], v230 offset:13824\n" \
    "v_mfma_f32_16x16x32_bf16 a[204:207], a[76:79], a[44:47], a[204:207]\nds_read_b64_tr_b16 a[140:141], v231 offset:12288\nv_mfma_f32_16x16x32_bf16 v[100:103], a[72:75], a[48:51], v[100:103]\n" \
    "v_mfma_f32_16x16x32_bf16 a[208:211], a[76:79], a[48:51], a[208:211]\nds_read_b64_tr_b16 a[142:143], v231 offset:13824\nv_mfma_f32_16x16x32_bf16 v[108:111], a[72:75], a[52:55], v[108:111]\n" \
    "v_mfma_f32_16x16x32_bf16 a[212:215], a[76:79], a[52:55], a[212:215]\nds_read_b64_tr_b16 a[144:145], v232 offset:12288\nbuffer_load_dwordx4 v241, %[srdW2], s81 offen offset:1024 lds\n" \
    "v_mfma_f32_16x16x32_bf16 v[116:119], a[72:75], a[56:59], v[116:119]\nds_read_b64_tr_b16 a[146:147], v232 offset:13824\nv_mfma_f32_16x16x32_bf16 a[216:219], a[76:79], a[56:59], a[216:219]\n" \
    "v_mfma_f32_16x16x32_bf16 v[124:127], a[72:75], a[60:63], v[124:127]\nds_read_b64_tr_b16 a[148:149], v233 offset:12288\nv_mfma_f32_16x16x32_bf16 a[220:223], a[76:79], a[60:63], a[220:223]\n" \
    "v_mfma_f32_16x16x32_bf16 v[132:135], a[80:83], a[32:35], v[132:135]\nds_read_b64_tr_b16 a[150:151], v233 offset:13824\nv_mfma_f32_16x16x32_bf16 a[224:227], a[84:87], a[32:35], a[224:227]\n" \
    "buffer_load_dwordx4 v242, %[srdW2], s81 offen offset:2048 lds\nv_mfma_f32_16x16x32_bf16 v[140:143], a[80:83], a[36:39], v[140:143]\nv_mfma_f32_16x16x32_bf16 a[228:231], a[84:87], a[36:39], a[228:231]\n" \
    "v_mfma_f32_16x16x32_bf16 v[148:151], a[80:83], a[40:43], v[148:151]\nv_mfma_f32_16x16x32_bf16 a[232:235], a[84:87], a[40:43], a[232:235]\nv_mfma_f32_16x16x32_bf16 v[156:159], a[80:83], a[44:47], v[156:159]\n" \
    "v_mfma_f32_16x16x32_bf16 a[236:239], a[84:87], a[44:47], a[236:239]\nv_mfma_f32_16x16x32_bf16 v[164:167], a[80:83], a[48:51], v[164:167]\nv_mfma_f32_16x16x32_bf16 a[240:243], a[84:87], a[48:51], a[240:243]\n" \
    "v_mfma_f32_16x16x32_bf16 v[172:175], a[80:83], a[52:55], v[172:175]\nv_mfma_f32_16x16x32_bf16 a[244:247], a[84:87], a[52:55], a[244:247]\nv_mfma_f32_16x16x32_bf16 v[180:183], a[80:83], a[56:59], v[180:183]\n" \
    "v_mfma_f32_16x16x32_bf16 a[248:251], a[84:87], a[56:59], a[248:251]\nv_mfma_f32_16x16x32_bf16 v[188:191], a[80:83], a[60:63], v[188:191]\nv_mfma_f32_16x16x32_bf16 a[252:255], a[84:87], a[60:63], a[252:255]\n" \
    "s_waitcnt vmcnt(6) lgkmcnt(0)\ns_barrier\ns_branch L_Q0l%=\nL_Q0x%=:\nv_mfma_f32_16x16x32_bf16 v[4:7], a[128:131], a[96:99], v[4:7]\nv_mfma_f32_16x16x32_bf16 a[160:163], a[132:135], a[96:99], a[160:163]\n" \
    "v_mfma_f32_16x16x32_bf16 v[12:15], a[128:131], a[100:103], v[12:15]\nv_mfma_f32_16x16x32_bf16 a[164:167], a[132:135], a[100:103], a[164:167]\nv_mfma_f32_16x16x32_bf16 v[20:23], a[128:131], a[104:107], v[20:23]\n" \
    "v_mfma_f32_16x16x32_bf16 a[168:171], a[132:135], a[104:107], a[168:171]\nv_mfma_f32_16x16x32_bf16 v[28:31], a[128:131], a[108:111], v[28:31]\nv_mfma_f32_16x16x32_bf16 a[172:175], a[132:135], a[108:111], a[172:175]\n" \
    "v_accvgpr_read_b32 v246, a160\nv_accvgpr_read_b32 v247, a161\nv_mfma_f32_16x16x32_bf16 v[36:39], a[128:131], a[112:115], v[36:39]\nv_accvgpr_read_b32 v248, a162\nv_accvgpr_read_b32 v249, a163\n" \
    "v_mfma_f32_16x16x32_bf16 a[176:179], a[132:135], a[112:115], a[176:179]\nv_mul_f32_e32 v4, %[scl], v4\nv_mul_f32_e32 v5, %[scl], v5\nv_mfma_f32_16x16x32_bf16 v[44:47], a[128:131], a[116:119], v[44:47]\n" \
    "v_mul_f32_e32 v6, %[scl], v6\nv_mul_f32_e32 v7, %[scl], v7\nv_mfma_f32_16x16x32_bf16 a[180:183], a[132:135], a[116:119], a[180:183]\nv_mul_f32_e32 v246, %[scl], v246\nv_mul_f32_e32 v247, %[scl], v247\n" \
    "v_mfma_f32_16x16x32_bf16 v[52:55], a[128:131], a[120:123], v[52:55]\nv_mul_f32_e32 v248, %[scl], v248\nv_mul_f32_e32 v249, %[scl], v249\nv_mfma_f32_16x16x32_bf16 a[184:187], a[132:135], a[120:123], a[184:187]\n" \
    "v_cvt_pk_bf16_f32 v4, v4, v5\nv_cvt_pk_bf16_f32 v5, v6, v7\nv_mfma_f32_16x16x32_bf16 v[60:63], a[128:131], a[124:127], v[60:63]\nv_cvt_pk_bf16_f32 v6, v246, v247\nv_cvt_pk_bf16_f32 v7, v248, v249\n" \
    "v_mfma_f32_16x16x32_bf16 a[188:191], a[132:135], a[124:127], a[188:191]\ns_add_u32 s82, %[so20], 0x0\ns_nop 0\nv_permlane16_swap_b32_e32 v4, v6\nv_mfma_f32_16x16x32_bf16 v[68:71], a[136:139], a[96:99], v[68:71]\n" \
    "v_permlane16_swap_b32_e32 v5, v7\nbuffer_store_dwordx4 v[4:7], v244, %[srdO2], s82 offen sc1\nv_mfma_f32_16x16x32_bf16 a[192:195], a[140:143], a[96:99], a[192:195]\nv_accvgpr_read_b32 v250, a164\n" \
    "v_accvgpr_read_b32 v251, a165\nv_mfma_f32_16x16x32_bf16 v[76:79], a[136:139], a[100:103], v[76:79]\nv_accvgpr_read_b32 v252, a166\nv_accvgpr_read_b32 v253, a167\n" \
    "v_mfma_f32_16x16x32_bf16 a[196:199], a[140:143], a[100:103], a[196:199]\nv_mul_f32_e32 v12, %[scl], v12\nv_mul_f32_e32 v13, %[scl], v13\nv_mfma_f32_16x16x32_bf16 v[84:87], a[136:139], a[104:107], v[84:87]\n" \
    "v_mul_f32_e32 v14, %[scl], v14\nv_mul_f32_e32 v15, %[scl], v15\nv_mfma_f32_16x16x32_bf16 a[200:203], a[140:143], a[104:107], a[200:203]\nv_mul_f32_e32 v250, %[scl], v250\nv_mul_f32_e32 v251, %[scl], v251\n" \
    "v_mfma_f32_16x16x32_bf16 v[92:95], a[136:139], a[108:111], v[92:95]\nv_mul_f32_e32 v252, %[scl], v252\nv_mul_f32_e32 v253, %[scl], v253\nv_mfma_f32_16x16x32_bf16 a[204:207], a[140:143], a[108:111], a[204:207]\n" \
    "v_cvt_pk_bf16_f32 v12, v12, v13\nv_cvt_pk_bf16_f32 v13, v14, v15\nv_mfma_f32_16x16x32_bf16 v[100:103], a[136:139], a[112:115], v[100:103]\nv_cvt_pk_bf16_f32 v14, v250, v251\nv_cvt_pk_bf16_f32 v15, v252, v253\n" \
    "v_mfma_f32_16x16x32_bf16 a[208:211], a[140:143], a[112:115], a[208:211]\ns_add_u32 s82, %[so20], 0x1000\ns_nop 0\nv_permlane16_swap_b32_e32 v12, v14\n" \
    "v_mfma_f32_16x16x32_bf16 v[108:111], a[136:139], a[116:119], v[108:111]\nv_permlane16_swap_b32_e32 v13, v15\nbuffer_store_dwordx4 v[12:15], v244, %[srdO2], s82 offen sc1\n" \
    "v_mfma_f32_16x16x32_bf16 a[212:215], a[140:143], a[116:119], a[212:215]\nv_accvgpr_read_b32 v246, a168\nv_accvgpr_read_b32 v247, a169\nv_mfma_f32_16x16x32_bf16 v[116:119], a[136:139], a[120:123], v[116:119]\n" \
    "v_accvgpr_read_b32 v248, a170\nv_accvgpr_read_b32 v249, a171\nv_mfma_f32_16x16x32_bf16 a[216:219], a[140:143], a[120:123], a[216:219]\nv_mul_f32_e32 v20, %[scl], v20\nv_mul_f32_e32 v21, %[scl], v21\n" \
    "v_mfma_f32_16x16x32_bf16 v[124:127], a[136:139], a[124:127], v[124:127]\nv_mul_f32_e32 v22, %[scl], v22\nv_mul_f32_e32 v23, %[scl], v23\nv_mfma_f32_16x16x32_bf16 a[220:223], a[140:143], a[124:127], a[220:223]\n" \
    "v_mul_f32_e32 v246, %[scl], v246\nv_mul_f32_e32 v247, %[scl], v247\nv_mfma_f32_16x16x32_bf16 v[132:135], a[144:147], a[96:99], v[132:135]\nv_mul_f32_e32 v248, %[scl], v248\nv_mul_f32_e32 v249, %[scl], v249\n" \
    "v_mfma_f32_16x16x32_bf16 a[224:227], a[148:151], a[96:99], a[224:227]\nv_cvt_pk_bf16_f32 v20, v20, v21\nv_cvt_pk_bf16_f32 v21, v22, v23\nv_mfma_f32_16x16x32_bf16 v[140:143], a[144:147], a[100:103], v[140:143]\n" \
    "v_cvt_pk_bf16_f32 v22, v246, v247\nv_cvt_pk_bf16_f32 v23, v248, v249\nv_mfma_f32_16x16x32_bf16 a[228:231], a[148:151], a[100:103], a[228:231]\ns_add_u32 s82, %[so20], 0x2000\ns_nop 0\nv_permlane16_swap_b32_e32 v20, v22\n" \
    "v_mfma_f32_16x16x32_bf16 v[148:151], a[144:147], a[104:107], v[148:151]\nv_permlane16_swap_b32_e32 v21, v23\nbuffer_store_dwordx4 v[20:23], v244, %[srdO2], s82 offen sc1\n" \
    "v_mfma_f32_16x16x32_bf16 a[232:235], a[148:151], a[104:107], a[232:235]\nv_accvgpr_read_b32 v250, a172\nv_accvgpr_read_b32 v251, a173\nv_mfma_f32_16x16x32_bf16 v[156:159], a[144:147], a[108:111], v[156:159]\n" \
    "v_accvgpr_read_b32 v252, a174\nv_accvgpr_read_b32 v253, a175\nv_mfma_f32_16x16x32_bf16 a[236:239], a[148:151], a[108:111], a[236:239]\nv_mul_f32_e32 v28, %[scl], v28\nv_mul_f32_e32 v29, %[scl], v29\n" \
    "v_mfma_f32_16x16x32_bf16 v[164:167], a[144:147], a[112:115], v[164:167]\nv_mul_f32_e32 v30, %[scl], v30\nv_mul_f32_e32 v31, %[scl], v31\nv_mfma_f32_16x16x32_bf16 a[240:243], a[148:151], a[112:115], a[240:243]\n" \
    "v_mul_f32_e32 v250, %[scl], v250\nv_mul_f32_e32 v251, %[scl], v251\nv_mfma_f32_16x16x32_bf16 v[172:175], a[144:147], a[116:119], v[172:175]\nv_mul_f32_e32 v252, %[scl], v252\nv_mul_f32_e32 v253, %[scl], v253\n" \
    "v_mfma_f32_16x16x32_bf16 a[244:247], a[148:151], a[116:119], a[244:247]\nv_cvt_pk_bf16_f32 v28, v28, v29\nv_cvt_pk_bf16_f32 v29, v30, v31\nv_mfma_f32_16x16x32_bf16 v[180:183], a[144:147], a[120:123], v[180:183]\n" \
    "v_cvt_pk_bf16_f32 v30, v250, v251\nv_cvt_pk_bf16_f32 v31, v252, v253\nv_mfma_f32_16x16x32_bf16 a[248:251], a[148:151], a[120:123], a[248:251]\ns_add_u32 s82, %[so20], 0x3000\ns_nop 0\nv_permlane16_swap_b32_e32 v28, v30\n" \
    "v_mfma_f32_16x16x32_bf16 v[188:191], a[144:147], a[124:127], v[188:191]\nv_permlane16_swap_b32_e32 v29, v31\nbuffer_store_dwordx4 v[28:31], v244, %[srdO2], s82 offen sc1\n" \
    "v_mfma_f32_16x16x32_bf16 a[252:255], a[148:151], a[124:127], a[252:255]\nv_accvgpr_read_b32 v246, a176\nv_accvgpr_read_b32 v247, a177\ns_nop 7\ns_nop 3\nv_accvgpr_read_b32 v248, a178\nv_accvgpr_read_b32 v249, a179\n" \
    "v_mul_f32_e32 v36, %[scl], v36\nv_mul_f32_e32 v37, %[scl], v37\nv_mul_f32_e32 v38, %[scl], v38\nv_mul_f32_e32 v39, %[scl], v39\nv_mul_f32_e32 v246, %[scl], v246\nv_mul_f32_e32 v247, %[scl], v247\n" \
    "v_mul_f32_e32 v248, %[scl], v248\nv_mul_f32_e32 v249, %[scl], v249\nv_cvt_pk_bf16_f32 v36, v36, v37\nv_cvt_pk_bf16_f32 v37, v38, v39\nv_cvt_pk_bf16_f32 v38, v246, v247\nv_cvt_pk_bf16_f32 v39, v248, v249\n" \
    "s_add_u32 s82, %[so20], 0x4000\ns_nop 0\nv_permlane16_swap_b32_e32 v36, v38\nv_permlane16_swap_b32_e32 v37, v39\nbuffer_store_dwordx4 v[36:39], v244, %[srdO2], s82 offen sc1\nv_accvgpr_read_b32 v250, a180\n" \
    "v_accvgpr_read_b32 v251, a181\nv_accvgpr_read_b32 v252, a182\nv_accvgpr_read_b32 v253, a183\nv_mul_f32_e32 v44, %[scl], v44\nv_mul_f32_e32 v45, %[scl], v45\nv_mul_f32_e32 v46, %[scl], v46\n" \
    "v_mul_f32_e32 v47, %[scl], v47\nv_mul_f32_e32 v250, %[scl], v250\nv_mul_f32_e32 v251, %[scl], v251\nv_mul_f32_e32 v252, %[scl], v252\nv_mul_f32_e32 v253, %[scl], v253\nv_cvt_pk_bf16_f32 v44, v44, v45\n" \
    "v_cvt_pk_bf16_f32 v45, v46, v47\nv_cvt_pk_bf16_f32 v46, v250, v251\nv_cvt_pk_bf16_f32 v47, v252, v253\ns_add_u32 s82, %[so20], 0x5000\ns_nop 0\nv_permlane16_swap_b32_e32 v44, v46\nv_permlane16_swap_b32_e32 v45, v47\n" \
    "buffer_store_dwordx4 v[44:47], v244, %[srdO2], s82 offen sc1\nv_accvgpr_read_b32 v246, a184\nv_accvgpr_read_b32 v247, a185\nv_accvgpr_read_b32 v248, a186\nv_accvgpr_read_b32 v249, a187\nv_mul_f32_e32 v52, %[scl], v52\n" \
    "v_mul_f32_e32 v53, %[scl], v53\nv_mul_f32_e32 v54, %[scl], v54\nv_mul_f32_e32 v55, %[scl], v55\nv_mul_f32_e32 v246, %[scl], v246\nv_mul_f32_e32 v247, %[scl], v247\nv_mul_f32_e32 v248, %[scl], v248\n" \
    "v_mul_f32_e32 v249, %[scl], v249\nv_cvt_pk_bf16_f32 v52, v52, v53\nv_cvt_pk_bf16_f32 v53, v54, v55\nv_cvt_pk_bf16_f32 v54, v246, v247\nv_cvt_pk_bf16_f32 v55, v248, v249\ns_add_u32 s82, %[so20], 0x6000\ns_nop 0\n" \
    "v_permlane16_swap_b32_e32 v52, v54\nv_permlane16_swap_b32_e32 v53, v55\nbuffer_store_dwordx4 v[52:55], v244, %[srdO2], s82 offen sc1\nv_accvgpr_read_b32 v250, a188\nv_accvgpr_read_b32 v251, a189\n" \
    "v_accvgpr_read_b32 v252, a190\nv_accvgpr_read_b32 v253, a191\nv_mul_f32_e32 v60, %[scl], v60\nv_mul_f32_e32 v61, %[scl], v61\nv_mul_f32_e32 v62, %[scl], v62\nv_mul_f32_e32 v63, %[scl], v63\n" \
    "v_mul_f32_e32 v250, %[scl], v250\nv_mul_f32_e32 v251, %[scl], v251\nv_mul_f32_e32 v252, %[scl], v252\nv_mul_f32_e32 v253, %[scl], v253\nv_cvt_pk_bf16_f32 v60, v60, v61\nv_cvt_pk_bf16_f32 v61, v62, v63\n" \
    "v_cvt_pk_bf16_f32 v62, v250, v251\nv_cvt_pk_bf16_f32 v63, v252, v253\ns_add_u32 s82, %[so20], 0x7000\ns_nop 0\nv_permlane16_swap_b32_e32 v60, v62\nv_permlane16_swap_b32_e32 v61, v63\n" \
    "buffer_store_dwordx4 v[60:63], v244, %[srdO2], s82 offen sc1\nv_accvgpr_read_b32 v246, a192\nv_accvgpr_read_b32 v247, a193\nv_accvgpr_read_b32 v248, a194\nv_accvgpr_read_b32 v249, a195\nv_mul_f32_e32 v68, %[scl], v68\n" \
    "v_mul_f32_e32 v69, %[scl], v69\nv_mul_f32_e32 v70, %[scl], v70\nv_mul_f32_e32 v71, %[scl], v71\nv_mul_f32_e32 v246, %[scl], v246\nv_mul_f32_e32 v247, %[scl], v247\nv_mul_f32_e32 v248, %[scl], v248\n" \
    "v_mul_f32_e32 v249, %[scl], v249\nv_cvt_pk_bf16_f32 v68, v68, v69\nv_cvt_pk_bf16_f32 v69, v70, v71\nv_cvt_pk_bf16_f32 v70, v246, v247\nv_cvt_pk_bf16_f32 v71, v248, v249\ns_add_u32 s82, %[so21], 0x0\ns_nop 0\n" \
    "v_permlane16_swap_b32_e32 v68, v70\nv_permlane16_swap_b32_e32 v69, v71\nbuffer_store_dwordx4 v[68:71], v244, %[srdO2], s82 offen sc1\nv_accvgpr_read_b32 v250, a196\nv_accvgpr_read_b32 v251, a197\n" \
    "v_accvgpr_read_b32 v252, a198\nv_accvgpr_read_b32 v253, a199\nv_mul_f32_e32 v76, %[scl], v76\nv_mul_f32_e32 v77, %[scl], v77\nv_mul_f32_e32 v78, %[scl], v78\nv_mul_f32_e32 v79, %[scl], v79\n" \
    "v_mul_f32_e32 v250, %[scl], v250\nv_mul_f32_e32 v251, %[scl], v251\nv_mul_f32_e32 v252, %[scl], v252\nv_mul_f32_e32 v253, %[scl], v253\nv_cvt_pk_bf16_f32 v76, v76, v77\nv_cvt_pk_bf16_f32 v77, v78, v79\n" \
    "v_cvt_pk_bf16_f32 v78, v250, v251\nv_cvt_pk_bf16_f32 v79, v252, v253\ns_add_u32 s82, %[so21], 0x1000\ns_nop 0\nv_permlane16_swap_b32_e32 v76, v78\nv_permlane16_swap_b32_e32 v77, v79\n" \
    "buffer_store_dwordx4 v[76:79], v244, %[srdO2], s82 offen sc1\nv_accvgpr_read_b32 v246, a200\nv_accvgpr_read_b32 v247, a201\nv_accvgpr_read_b32 v248, a202\nv_accvgpr_read_b32 v249, a203\nv_mul_f32_e32 v84, %[scl], v84\n" \
    "v_mul_f32_e32 v85, %[scl], v85\nv_mul_f32_e32 v86, %[scl], v86\nv_mul_f32_e32 v87, %[scl], v87\nv_mul_f32_e32 v246, %[scl], v246\nv_mul_f32_e32 v247, %[scl], v247\nv_mul_f32_e32 v248, %[scl], v248\n" \
    "v_mul_f32_e32 v249, %[scl], v249\nv_cvt_pk_bf16_f32 v84, v84, v85\nv_cvt_pk_bf16_f32 v85, v86, v87\nv_cvt_pk_bf16_f32 v86, v246, v247\nv_cvt_pk_bf16_f32 v87, v248, v249\ns_add_u32 s82, %[so21], 0x2000\ns_nop 0\n" \
    "v_permlane16_swap_b32_e32 v84, v86\nv_permlane16_swap_b32_e32 v85, v87\nbuffer_store_dwordx4 v[84:87], v244, %[srdO2], s82 offen sc1\nv_accvgpr_read_b32 v250, a204\nv_accvgpr_read_b32 v251, a205\n" \
    "v_accvgpr_read_b32 v252, a206\nv_accvgpr_read_b32 v253, a207\nv_mul_f32_e32 v92, %[scl], v92\nv_mul_f32_e32 v93, %[scl], v93\nv_mul_f32_e32 v94, %[scl], v94\nv_mul_f32_e32 v95, %[scl], v95\n" \
    "v_mul_f32_e32 v250, %[scl], v250\nv_mul_f32_e32 v251, %[scl], v251\nv_mul_f32_e32 v252, %[scl], v252\nv_mul_f32_e32 v253, %[scl], v253\nv_cvt_pk_bf16_f32 v92, v92, v93\nv_cvt_pk_bf16_f32 v93, v94, v95\n" \
    "v_cvt_pk_bf16_f32 v94, v250, v251\nv_cvt_pk_bf16_f32 v95, v252, v253\ns_add_u32 s82, %[so21], 0x3000\ns_nop 0\nv_permlane16_swap_b32_e32 v92, v94\nv_permlane16_swap_b32_e32 v93, v95\n" \
    "buffer_store_dwordx4 v[92:95], v244, %[srdO2], s82 offen sc1\nv_accvgpr_read_b32 v246, a208\nv_accvgpr_read_b32 v247, a209\nv_accvgpr_read_b32 v248, a210\nv_accvgpr_read_b32 v249, a211\nv_mul_f32_e32 v100, %[scl], v100\n" \
    "v_mul_f32_e32 v101, %[scl], v101\nv_mul_f32_e32 v102, %[scl], v102\nv_mul_f32_e32 v103, %[scl], v103\nv_mul_f32_e32 v246, %[scl], v246\nv_mul_f32_e32 v247, %[scl], v247\nv_mul_f32_e32 v248, %[scl], v248\n" \
    "v_mul_f32_e32 v249, %[scl], v249\nv_cvt_pk_bf16_f32 v100, v100, v101\nv_cvt_pk_bf16_f32 v101, v102, v103\nv_cvt_pk_bf16_f32 v102, v246, v247\nv_cvt_pk_bf16_f32 v103, v248, v249\ns_add_u32 s82, %[so21], 0x4000\ns_nop 0\n" \
    "v_permlane16_swap_b32_e32 v100, v102\nv_permlane16_swap_b32_e32 v101, v103\nbuffer_store_dwordx4 v[100:103], v244, %[srdO2], s82 offen sc1\nv_accvgpr_read_b32 v250, a212\nv_accvgpr_read_b32 v251, a213\n" \
    "v_accvgpr_read_b32 v252, a214\nv_accvgpr_read_b32 v253, a215\nv_mul_f32_e32 v108, %[scl], v108\nv_mul_f32_e32 v109, %[scl], v109\nv_mul_f32_e32 v110, %[scl], v110\nv_mul_f32_e32 v111, %[scl], v111\n" \
    "v_mul_f32_e32 v250, %[scl], v250\nv_mul_f32_e32 v251, %[scl], v251\nv_mul_f32_e32 v252, %[scl], v252\nv_mul_f32_e32 v253, %[scl], v253\nv_cvt_pk_bf16_f32 v108, v108, v109\nv_cvt_pk_bf16_f32 v109, v110, v111\n" \
    "v_cvt_pk_bf16_f32 v110, v250, v251\nv_cvt_pk_bf16_f32 v111, v252, v253\ns_add_u32 s82, %[so21], 0x5000\ns_nop 0\nv_permlane16_swap_b32_e32 v108, v110\nv_permlane16_swap_b32_e32 v109, v111\n" \
    "buffer_store_dwordx4 v[108:111], v244, %[srdO2], s82 offen sc1\nv_accvgpr_read_b32 v246, a216\nv_accvgpr_read_b32 v247, a217\nv_accvgpr_read_b32 v248, a218\nv_accvgpr_read_b32 v249, a219\n" \
    "v_mul_f32_e32 v116, %[scl], v116\nv_mul_f32_e32 v117, %[scl], v117\nv_mul_f32_e32 v118, %[scl], v118\nv_mul_f32_e32 v119, %[scl], v119\nv_mul_f32_e32 v246, %[scl], v246\nv_mul_f32_e32 v247, %[scl], v247\n" \
    "v_mul_f32_e32 v248, %[scl], v248\nv_mul_f32_e32 v249, %[scl], v249\nv_cvt_pk_bf16_f32 v116, v116, v117\nv_cvt_pk_bf16_f32 v117, v118, v119\nv_cvt_pk_bf16_f32 v118, v246, v247\nv_cvt_pk_bf16_f32 v119, v248, v249\n" \
    "s_add_u32 s82, %[so21], 0x6000\ns_nop 0\nv_permlane16_swap_b32_e32 v116, v118\nv_permlane16_swap_b32_e32 v117, v119\nbuffer_store_dwordx4 v[116:119], v244, %[srdO2], s82 offen sc1\nv_accvgpr_read_b32 v250, a220\n" \
    "v_accvgpr_read_b32 v251, a221\nv_accvgpr_read_b32 v252, a222\nv_accvgpr_read_b32 v253, a223\nv_mul_f32_e32 v124, %[scl], v124\nv_mul_f32_e32 v125, %[scl], v125\nv_mul_f32_e32 v126, %[scl], v126\n" \
    "v_mul_f32_e32 v127, %[scl], v127\nv_mul_f32_e32 v250, %[scl], v250\nv_mul_f32_e32 v251, %[scl], v251\nv_mul_f32_e32 v252, %[scl], v252\nv_mul_f32_e32 v253, %[scl], v253\nv_cvt_pk_bf16_f32 v124, v124, v125\n" \
    "v_cvt_pk_bf16_f32 v125, v126, v127\nv_cvt_pk_bf16_f32 v126, v250, v251\nv_cvt_pk_bf16_f32 v127, v252, v253\ns_add_u32 s82, %[so21], 0x7000\ns_nop 0\nv_permlane16_swap_b32_e32 v124, v126\n" \
    "v_permlane16_swap_b32_e32 v125, v127\nbuffer_store_dwordx4 v[124:127], v244, %[srdO2], s82 offen sc1\nv_accvgpr_read_b32 v246, a224\nv_accvgpr_read_b32 v247, a225\nv_accvgpr_read_b32 v248, a226\n" \
    "v_accvgpr_read_b32 v249, a227\nv_mul_f32_e32 v132, %[scl], v132\nv_mul_f32_e32 v133, %[scl], v133\nv_mul_f32_e32 v134, %[scl], v134\nv_mul_f32_e32 v135, %[scl], v135\nv_mul_f32_e32 v246, %[scl], v246\n" \
    "v_mul_f32_e32 v247, %[scl], v247\nv_mul_f32_e32 v248, %[scl], v248\nv_mul_f32_e32 v249, %[scl], v249\nv_cvt_pk_bf16_f32 v132, v132, v133\nv_cvt_pk_bf16_f32 v133, v134, v135\nv_cvt_pk_bf16_f32 v134, v246, v247\n" \
    "v_cvt_pk_bf16_f32 v135, v248, v249\ns_add_u32 s82, %[so22], 0x0\ns_nop 0\nv_permlane16_swap_b32_e32 v132, v134\nv_permlane16_swap_b32_e32 v133, v135\nbuffer_store_dwordx4 v[132:135], v244, %[srdO2], s82 offen sc1\n" \
    "v_accvgpr_read_b32 v250, a228\nv_accvgpr_read_b32 v251, a229\nv_accvgpr_read_b32 v252, a230\nv_accvgpr_read_b32 v253, a231\nv_mul_f32_e32 v140, %[scl], v140\nv_mul_f32_e32 v141, %[scl], v141\n" \
    "v_mul_f32_e32 v142, %[scl], v142\nv_mul_f32_e32 v143, %[scl], v143\nv_mul_f32_e32 v250, %[scl], v250\nv_mul_f32_e32 v251, %[scl], v251\nv_mul_f32_e32 v252, %[scl], v252\nv_mul_f32_e32 v253, %[scl], v253\n" \
    "v_cvt_pk_bf16_f32 v140, v140, v141\nv_cvt_pk_bf16_f32 v141, v142, v143\nv_cvt_pk_bf16_f32 v142, v250, v251\nv_cvt_pk_bf16_f32 v143, v252, v253\ns_add_u32 s82, %[so22], 0x1000\ns_nop 0\n" \
    "v_permlane16_swap_b32_e32 v140, v142\nv_permlane16_swap_b32_e32 v141, v143\nbuffer_store_dwordx4 v[140:143], v244, %[srdO2], s82 offen sc1\nv_accvgpr_read_b32 v246, a232\nv_accvgpr_read_b32 v247, a233\n" \
    "v_accvgpr_read_b32 v248, a234\nv_accvgpr_read_b32 v249, a235\nv_mul_f32_e32 v148, %[scl], v148\nv_mul_f32_e32 v149, %[scl], v149\nv_mul_f32_e32 v150, %[scl], v150\nv_mul_f32_e32 v151, %[scl], v151\n" \
    "v_mul_f32_e32 v246, %[scl], v246\nv_mul_f32_e32 v247, %[scl], v247\nv_mul_f32_e32 v248, %[scl], v248\nv_mul_f32_e32 v249, %[scl], v249\nv_cvt_pk_bf16_f32 v148, v148, v149\nv_cvt_pk_bf16_f32 v149, v150, v151\n" \
    "v_cvt_pk_bf16_f32 v150, v246, v247\nv_cvt_pk_bf16_f32 v151, v248, v249\ns_add_u32 s82, %[so22], 0x2000\ns_nop 0\nv_permlane16_swap_b32_e32 v148, v150\nv_permlane16_swap_b32_e32 v149, v151\n" \
    "buffer_store_dwordx4 v[148:151], v244, %[srdO2], s82 offen sc1\nv_accvgpr_read_b32 v250, a236\nv_accvgpr_read_b32 v251, a237\nv_accvgpr_read_b32 v252, a238\nv_accvgpr_read_b32 v253, a239\n" \
    "v_mul_f32_e32 v156, %[scl], v156\nv_mul_f32_e32 v157, %[scl], v157\nv_mul_f32_e32 v158, %[scl], v158\nv_mul_f32_e32 v159, %[scl], v159\nv_mul_f32_e32 v250, %[scl], v250\nv_mul_f32_e32 v251, %[scl], v251\n" \
    "v_mul_f32_e32 v252, %[scl], v252\nv_mul_f32_e32 v253, %[scl], v253\nv_cvt_pk_bf16_f32 v156, v156, v157\nv_cvt_pk_bf16_f32 v157, v158, v159\nv_cvt_pk_bf16_f32 v158, v250, v251\nv_cvt_pk_bf16_f32 v159, v252, v253\n" \
    "s_add_u32 s82, %[so22], 0x3000\ns_nop 0\nv_permlane16_swap_b32_e32 v156, v158\nv_permlane16_swap_b32_e32 v157, v159\nbuffer_store_dwordx4 v[156:159], v244, %[srdO2], s82 offen sc1\nv_accvgpr_read_b32 v246, a240\n" \
    "v_accvgpr_read_b32 v247, a241\nv_accvgpr_read_b32 v248, a242\nv_accvgpr_read_b32 v249, a243\nv_mul_f32_e32 v164, %[scl], v164\nv_mul_f32_e32 v165, %[scl], v165\nv_mul_f32_e32 v166, %[scl], v166\n" \
    "v_mul_f32_e32 v167, %[scl], v167\nv_mul_f32_e32 v246, %[scl], v246\nv_mul_f32_e32 v247, %[scl], v247\nv_mul_f32_e32 v248, %[scl], v248\nv_mul_f32_e32 v249, %[scl], v249\nv_cvt_pk_bf16_f32 v164, v164, v165\n" \
    "v_cvt_pk_bf16_f32 v165, v166, v167\nv_cvt_pk_bf16_f32 v166, v246, v247\nv_cvt_pk_bf16_f32 v167, v248, v249\ns_add_u32 s82, %[so22], 0x4000\ns_nop 0\nv_permlane16_swap_b32_e32 v164, v166\n" \
    "v_permlane16_swap_b32_e32 v165, v167\nbuffer_store_dwordx4 v[164:167], v244, %[srdO2], s82 offen sc1\nv_accvgpr_read_b32 v250, a244\nv_accvgpr_read_b32 v251, a245\nv_accvgpr_read_b32 v252, a246\n" \
    "v_accvgpr_read_b32 v253, a247\nv_mul_f32_e32 v172, %[scl], v172\nv_mul_f32_e32 v173, %[scl], v173\nv_mul_f32_e32 v174, %[scl], v174\nv_mul_f32_e32 v175, %[scl], v175\nv_mul_f32_e32 v250, %[scl], v250\n" \
    "v_mul_f32_e32 v251, %[scl], v251\nv_mul_f32_e32 v252, %[scl], v252\nv_mul_f32_e32 v253, %[scl], v253\nv_cvt_pk_bf16_f32 v172, v172, v173\nv_cvt_pk_bf16_f32 v173, v174, v175\nv_cvt_pk_bf16_f32 v174, v250, v251\n" \
    "v_cvt_pk_bf16_f32 v175, v252, v253\ns_add_u32 s82, %[so22], 0x5000\ns_nop 0\nv_permlane16_swap_b32_e32 v172, v174\nv_permlane16_swap_b32_e32 v173, v175\nbuffer_store_dwordx4 v[172:175], v244, %[srdO2], s82 offen sc1\n" \
    "v_accvgpr_read_b32 v246, a248\nv_accvgpr_read_b32 v247, a249\nv_accvgpr_read_b32 v248, a250\nv_accvgpr_read_b32 v249, a251\nv_mul_f32_e32 v180, %[scl], v180\nv_mul_f32_e32 v181, %[scl], v181\n" \
    "v_mul_f32_e32 v182, %[scl], v182\nv_mul_f32_e32 v183, %[scl], v183\nv_mul_f32_e32 v246, %[scl], v246\nv_mul_f32_e32 v247, %[scl], v247\nv_mul_f32_e32 v248, %[scl], v248\nv_mul_f32_e32 v249, %[scl], v249\n" \
    "v_cvt_pk_bf16_f32 v180, v180, v181\nv_cvt_pk_bf16_f32 v181, v182, v183\nv_cvt_pk_bf16_f32 v182, v246, v247\nv_cvt_pk_bf16_f32 v183, v248, v249\ns_add_u32 s82, %[so22], 0x6000\ns_nop 0\n" \
    "v_permlane16_swap_b32_e32 v180, v182\nv_permlane16_swap_b32_e32 v181, v183\nbuffer_store_dwordx4 v[180:183], v244, %[srdO2], s82 offen sc1\nv_accvgpr_read_b32 v250, a252\nv_accvgpr_read_b32 v251, a253\n" \
    "v_accvgpr_read_b32 v252, a254\nv_accvgpr_read_b32 v253, a255\nv_mul_f32_e32 v188, %[scl], v188\nv_mul_f32_e32 v189, %[scl], v189\nv_mul_f32_e32 v190, %[scl], v190\nv_mul_f32_e32 v191, %[scl], v191\n" \
    "v_mul_f32_e32 v250, %[scl], v250\nv_mul_f32_e32 v251, %[scl], v251\nv_mul_f32_e32 v252, %[scl], v252\nv_mul_f32_e32 v253, %[scl], v253\nv_cvt_pk_bf16_f32 v188, v188, v189\nv_cvt_pk_bf16_f32 v189, v190, v191\n" \
    "v_cvt_pk_bf16_f32 v190, v250, v251\nv_cvt_pk_bf16_f32 v191, v252, v253\ns_add_u32 s82, %[so22], 0x7000\ns_nop 0\nv_permlane16_swap_b32_e32 v188, v190\nv_permlane16_swap_b32_e32 v189, v191\n" \
    "buffer_store_dwordx4 v[188:191], v244, %[srdO2], s82 offen sc1\ns_waitcnt vmcnt(0) lgkmcnt(0)\n"
#define G10_ALONE_U \
    "s_add_u32 m0, %[m0w2], 0x22000\ns_add_u32 s81, %[sw2], 0x0\nbuffer_load_dwordx4 v240, %[srdW2], s81 offen offset:0 lds\nbuffer_load_dwordx4 v241, %[srdW2], s81 offen offset:1024 lds\n" \
    "buffer_load_dwordx4 v242, %[srdW2], s81 offen offset:2048 lds\ns_add_u32 m0, %[m0w2], 0x23800\ns_add_u32 s81, %[sw2], 0xd800\nbuffer_load_dwordx4 v240, %[srdW2], s81 offen offset:0 lds\n" \
    "buffer_load_dwordx4 v241, %[srdW2], s81 offen offset:1024 lds\nbuffer_load_dwordx4 v242, %[srdW2], s81 offen offset:2048 lds\ns_add_u32 %[sw2], %[sw2], 0x36000\ns_mov_b32 m0, %[m0x20]\ns_add_u32 s80, %[sx2], 0x0\n" \
    "buffer_load_dwordx4 v236, %[srdX2], s80 offen offset:0 sc1 lds\nbuffer_load_dwordx4 v236, %[srdX2], s80 offen offset:1024 sc1 lds\nbuffer_load_dwordx4 v236, %[srdX2], s80 offen offset:2048 sc1 lds\n" \
    "buffer_load_dwordx4 v236, %[srdX2], s80 offen offset:3072 sc1 lds\ns_add_u32 m0, m0, 0x1000\ns_add_u32 s80, s80, 0x1000\nbuffer_load_dwordx4 v236, %[srdX2], s80 offen offset:0 sc1 lds\ns_mov_b32 m0, %[m0x21]\n" \
    "s_add_u32 s80, %[sx2], 0x2800\nbuffer_load_dwordx4 v236, %[srdX2], s80 offen offset:0 sc1 lds\nbuffer_load_dwordx4 v236, %[srdX2], s80 offen offset:1024 sc1 lds\n" \
    "buffer_load_dwordx4 v236, %[srdX2], s80 offen offset:2048 sc1 lds\nbuffer_load_dwordx4 v236, %[srdX2], s80 offen offset:3072 sc1 lds\ns_add_u32 m0, m0, 0x1000\ns_add_u32 s80, s80, 0x1000\n" \
    "buffer_load_dwordx4 v236, %[srdX2], s80 offen offset:0 sc1 lds\ns_add_u32 %[sx2], %[sx2], 0x14000\ns_add_u32 m0, %[m0w2], 0x10000\ns_add_u32 s81, %[sw2], 0x0\nbuffer_load_dwordx4 v240, %[srdW2], s81 offen offset:0 lds\n" \
    "buffer_load_dwordx4 v241, %[srdW2], s81 offen offset:1024 lds\nbuffer_load_dwordx4 v242, %[srdW2], s81 offen offset:2048 lds\ns_add_u32 m0, %[m0w2], 0x11800\ns_add_u32 s81, %[sw2], 0xd800\n" \
    "buffer_load_dwordx4 v240, %[srdW2], s81 offen offset:0 lds\nbuffer_load_dwordx4 v241, %[srdW2], s81 offen offset:1024 lds\nbuffer_load_dwordx4 v242, %[srdW2], s81 offen offset:2048 lds\n" \
    "s_add_u32 %[sw2], %[sw2], 0x36000\ns_mov_b32 m0, %[m0x22]\ns_add_u32 s80, %[sx2], 0x0\nbuffer_load_dwordx4 v236, %[srdX2], s80 offen offset:0 sc1 lds\nbuffer_load_dwordx4 v236, %[srdX2], s80 offen offset:1024 sc1 lds\n" \
    "buffer_load_dwordx4 v236, %[srdX2], s80 offen offset:2048 sc1 lds\nbuffer_load_dwordx4 v236, %[srdX2], s80 offen offset:3072 sc1 lds\ns_add_u32 m0, m0, 0x1000\ns_add_u32 s80, s80, 0x1000\n" \
    "buffer_load_dwordx4 v236, %[srdX2], s80 offen offset:0 sc1 lds\ns_mov_b32 m0, %[m0x23]\ns_add_u32 s80, %[sx2], 0x2800\nbuffer_load_dwordx4 v236, %[srdX2], s80 offen offset:0 sc1 lds\n" \
    "buffer_load_dwordx4 v236, %[srdX2], s80 offen offset:1024 sc1 lds\nbuffer_load_dwordx4 v236, %[srdX2], s80 offen offset:2048 sc1 lds\nbuffer_load_dwordx4 v236, %[srdX2], s80 offen offset:3072 sc1 lds\n" \
    "s_add_u32 m0, m0, 0x1000\ns_add_u32 s80, s80, 0x1000\nbuffer_load_dwordx4 v236, %[srdX2], s80 offen offset:0 sc1 lds\ns_add_u32 %[sx2], %[sx2], 0x14000\ns_waitcnt vmcnt(16)\ns_barrier\n" \
    "ds_read_b128 a[32:35], v224 offset:0\nds_read_b128 a[36:39], v224 offset:256\nds_read_b128 a[40:43], v224 offset:512\nds_read_b128 a[44:47], v224 offset:768\nds_read_b128 a[48:51], v224 offset:1024\n" \
    "ds_read_b128 a[52:55], v224 offset:1280\nds_read_b128 a[56:59], v224 offset:1536\nds_read_b128 a[60:63], v224 offset:1792\nds_read_b128 a[64:67], v224 offset:2048\nds_read_b128 a[68:71], v224 offset:2304\n" \
    "ds_read_b64_tr_b16 a[72:73], v228 offset:0\nds_read_b64_tr_b16 a[74:75], v228 offset:1536\nds_read_b64_tr_b16 a[76:77], v229 offset:0\nds_read_b64_tr_b16 a[78:79], v229 offset:1536\n" \
    "ds_read_b64_tr_b16 a[80:81], v230 offset:0\nds_read_b64_tr_b16 a[82:83], v230 offset:1536\nds_read_b64_tr_b16 a[84:85], v231 offset:0\nds_read_b64_tr_b16 a[86:87], v231 offset:1536\n" \
    "ds_read_b64_tr_b16 a[88:89], v232 offset:0\nds_read_b64_tr_b16 a[90:91], v232 offset:1536\nds_read_b64_tr_b16 a[92:93], v233 offset:0\nds_read_b64_tr_b16 a[94:95], v233 offset:1536\ns_waitcnt lgkmcnt(0)\n" \
    "v_mfma_f32_16x16x32_bf16 v[4:7], a[32:35], a[72:75], 0\nds_read_b128 a[96:99], v225 offset:0\nv_mfma_f32_16x16x32_bf16 a[160:163], a[36:39], a[72:75], 0\nds_read_b128 a[100:103], v225 offset:256\ns_mov_b32 s83, %[sw2]\n" \
    "v_mfma_f32_16x16x32_bf16 v[12:15], a[32:35], a[76:79], 0\ns_add_u32 %[sw2], %[sw2], 0x36000\nv_mfma_f32_16x16x32_bf16 a[164:167], a[36:39], a[76:79], 0\nds_read_b128 a[104:107], v225 offset:512\n" \
    "s_add_u32 m0, %[m0w2], 0x17400\ns_add_u32 s81, s83, 0x0\nbuffer_load_dwordx4 v240, %[srdW2], s81 offen offset:0 lds\nv_mfma_f32_16x16x32_bf16 v[20:23], a[32:35], a[80:83], 0\n" \
    "v_mfma_f32_16x16x32_bf16 a[168:171], a[36:39], a[80:83], 0\nds_read_b128 a[108:111], v225 offset:768\nv_mfma_f32_16x16x32_bf16 v[28:31], a[32:35], a[84:87], 0\nv_mfma_f32_16x16x32_bf16 a[172:175], a[36:39], a[84:87], 0\n" \
    "ds_read_b128 a[112:115], v225 offset:1024\nv_mfma_f32_16x16x32_bf16 v[36:39], a[32:35], a[88:91], 0\nv_mfma_f32_16x16x32_bf16 a[176:179], a[36:39], a[88:91], 0\nds_read_b128 a[116:119], v225 offset:1280\n" \
    "v_mfma_f32_16x16x32_bf16 v[44:47], a[32:35], a[92:95], 0\nbuffer_load_dwordx4 v241, %[srdW2], s81 offen offset:1024 lds\nv_mfma_f32_16x16x32_bf16 a[180:183], a[36:39], a[92:95], 0\n" \
    "ds_read_b128 a[120:123], v225 offset:1536\nv_mfma_f32_16x16x32_bf16 v[52:55], a[40:43], a[72:75], 0\nv_mfma_f32_16x16x32_bf16 a[184:187], a[44:47], a[72:75], 0\nds_read_b128 a[124:127], v225 offset:1792\n" \
    "v_mfma_f32_16x16x32_bf16 v[60:63], a[40:43], a[76:79], 0\nv_mfma_f32_16x16x32_bf16 a[188:191], a[44:47], a[76:79], 0\nds_read_b128 a[128:131], v225 offset:2048\nv_mfma_f32_16x16x32_bf16 v[68:71], a[40:43], a[80:83], 0\n" \
    "v_mfma_f32_16x16x32_bf16 a[192:195], a[44:47], a[80:83], 0\nds_read_b128 a[132:135], v225 offset:2304\nv_mfma_f32_16x16x32_bf16 v[76:79], a[40:43], a[84:87], 0\n" \
    "buffer_load_dwordx4 v242, %[srdW2], s81 offen offset:2048 lds\nv_mfma_f32_16x16x32_bf16 a[196:199], a[44:47], a[84:87], 0\nds_read_b64_tr_b16 a[136:137], v228 offset:12288\n" \
    "v_mfma_f32_16x16x32_bf16 v[84:87], a[40:43], a[88:91], 0\nv_mfma_f32_16x16x32_bf16 a[200:203], a[44:47], a[88:91], 0\nds_read_b64_tr_b16 a[138:139], v228 offset:13824\n" \
    "v_mfma_f32_16x16x32_bf16 v[92:95], a[40:43], a[92:95], 0\nds_read_b64_tr_b16 a[140:141], v229 offset:12288\nv_mfma_f32_16x16x32_bf16 a[204:207], a[44:47], a[92:95], 0\n" \
    "v_mfma_f32_16x16x32_bf16 v[100:103], a[48:51], a[72:75], 0\nds_read_b64_tr_b16 a[142:143], v229 offset:13824\nv_mfma_f32_16x16x32_bf16 a[208:211], a[52:55], a[72:75], 0\n" \
    "v_mfma_f32_16x16x32_bf16 v[108:111], a[48:51], a[76:79], 0\nds_read_b64_tr_b16 a[144:145], v230 offset:12288\ns_add_u32 m0, %[m0w2], 0x18c00\ns_add_u32 s81, s83, 0xd800\n" \
    "buffer_load_dwordx4 v240, %[srdW2], s81 offen offset:0 lds\nv_mfma_f32_16x16x32_bf16 a[212:215], a[52:55], a[76:79], 0\nv_mfma_f32_16x16x32_bf16 v[116:119], a[48:51], a[80:83], 0\n" \
    "ds_read_b64_tr_b16 a[146:147], v230 offset:13824\nv_mfma_f32_16x16x32_bf16 a[216:219], a[52:55], a[80:83], 0\nv_mfma_f32_16x16x32_bf16 v[124:127], a[48:51], a[84:87], 0\nds_read_b64_tr_b16 a[148:149], v231 offset:12288\n" \
    "v_mfma_f32_16x16x32_bf16 a[220:223], a[52:55], a[84:87], 0\nv_mfma_f32_16x16x32_bf16 v[132:135], a[48:51], a[88:91], 0\nds_read_b64_tr_b16 a[150:151], v231 offset:13824\n" \
    "v_mfma_f32_16x16x32_bf16 a[224:227], a[52:55], a[88:91], 0\nbuffer_load_dwordx4 v241, %[srdW2], s81 offen offset:1024 lds\nv_mfma_f32_16x16x32_bf16 v[140:143], a[48:51], a[92:95], 0\n" \
    "ds_read_b64_tr_b16 a[152:153], v232 offset:12288\nv_mfma_f32_16x16x32_bf16 a[228:231], a[52:55], a[92:95], 0\nv_mfma_f32_16x16x32_bf16 v[148:151], a[56:59], a[72:75], 0\nds_read_b64_tr_b16 a[154:155], v232 offset:13824\n" \
    "v_mfma_f32_16x16x32_bf16 a[232:235], a[60:63], a[72:75], 0\nv_mfma_f32_16x16x32_bf16 v[156:159], a[56:59], a[76:79], 0\nds_read_b64_tr_b16 a[156:157], v233 offset:12288\n" \
    "v_mfma_f32_16x16x32_bf16 a[236:239], a[60:63], a[76:79], 0\nv_mfma_f32_16x16x32_bf16 v[164:167], a[56:59], a[80:83], 0\nds_read_b64_tr_b16 a[158:159], v233 offset:13824\n" \
    "v_mfma_f32_16x16x32_bf16 a[240:243], a[60:63], a[80:83], 0\nbuffer_load_dwordx4 v242, %[srdW2], s81 offen offset:2048 lds\nv_mfma_f32_16x16x32_bf16 v[172:175], a[56:59], a[84:87], 0\n" \
    "v_mfma_f32_16x16x32_bf16 a[244:247], a[60:63], a[84:87], 0\nv_mfma_f32_16x16x32_bf16 v[180:183], a[56:59], a[88:91], 0\nv_mfma_f32_16x16x32_bf16 a[248:251], a[60:63], a[88:91], 0\n" \
    "v_mfma_f32_16x16x32_bf16 v[188:191], a[56:59], a[92:95], 0\nv_mfma_f32_16x16x32_bf16 a[252:255], a[60:63], a[92:95], 0\nv_mfma_f32_16x16x32_bf16 v[196:199], a[64:67], a[72:75], 0\n" \
    "v_mfma_f32_16x16x32_bf16 a[0:3], a[68:71], a[72:75], 0\nv_mfma_f32_16x16x32_bf16 v[204:207], a[64:67], a[76:79], 0\nv_mfma_f32_16x16x32_bf16 a[4:7], a[68:71], a[76:79], 0\n" \
    "v_mfma_f32_16x16x32_bf16 v[212:215], a[64:67], a[80:83], 0\nv_mfma_f32_16x16x32_bf16 a[8:11], a[68:71], a[80:83], 0\nv_mfma_f32_16x16x32_bf16 v[220:223], a[64:67], a[84:87], 0\n" \
    "v_mfma_f32_16x16x32_bf16 a[12:15], a[68:71], a[84:87], 0\nv_mfma_f32_16x16x32_bf16 a[24:27], a[64:67], a[88:91], 0\nv_mfma_f32_16x16x32_bf16 a[16:19], a[68:71], a[88:91], 0\n" \
    "v_mfma_f32_16x16x32_bf16 a[28:31], a[64:67], a[92:95], 0\nv_mfma_f32_16x16x32_bf16 a[20:23], a[68:71], a[92:95], 0\ns_waitcnt vmcnt(6) lgkmcnt(0)\ns_barrier\ns_mov_b32 s84, 0x22000\ns_mov_b32 s85, 0x10000\n" \
    "s_mov_b32 s86, 0x17400\ns_mov_b32 %[cnt], 8\nL_U0l%=:\nv_mfma_f32_16x16x32_bf16 v[4:7], a[96:99], a[136:139], v[4:7]\ns_cmp_gt_u32 %[cnt], 2\ns_cselect_b32 s83, %[sx2], 0x70000000\n" \
    "v_mfma_f32_16x16x32_bf16 a[160:163], a[100:103], a[136:139], a[160:163]\ns_sub_u32 s81, s85, s84\ns_mov_b32 s87, s84\ns_mov_b32 m0, %[m0x20]\ns_add_u32 s80, s83, 0x0\n" \
    "buffer_load_dwordx4 v236, %[srdX2], s80 offen offset:0 sc1 lds\nv_mfma_f32_16x16x32_bf16 v[12:15], a[96:99], a[140:143], v[12:15]\ns_mov_b32 s84, s85\ns_mov_b32 s85, s86\n" \
    "v_mfma_f32_16x16x32_bf16 a[164:167], a[100:103], a[140:143], a[164:167]\ns_add_u32 %[sx2], %[sx2], 0x14000\ns_mov_b32 s86, s87\nv_mfma_f32_16x16x32_bf16 v[20:23], a[96:99], a[144:147], v[20:23]\n" \
    "v_add_u32_e32 v228, s81, v228\nds_read_b128 a[32:35], v226 offset:0\nbuffer_load_dwordx4 v236, %[srdX2], s80 offen offset:1024 sc1 lds\nv_mfma_f32_16x16x32_bf16 a[168:171], a[100:103], a[144:147], a[168:171]\n" \
    "v_add_u32_e32 v229, s81, v229\nv_mfma_f32_16x16x32_bf16 v[28:31], a[96:99], a[148:151], v[28:31]\nv_add_u32_e32 v230, s81, v230\nds_read_b128 a[36:39], v226 offset:256\n" \
    "v_mfma_f32_16x16x32_bf16 a[172:175], a[100:103], a[148:151], a[172:175]\nv_add_u32_e32 v231, s81, v231\nbuffer_load_dwordx4 v236, %[srdX2], s80 offen offset:2048 sc1 lds\n" \
    "v_mfma_f32_16x16x32_bf16 v[36:39], a[96:99], a[152:155], v[36:39]\nv_add_u32_e32 v232, s81, v232\nds_read_b128 a[40:43], v226 offset:512\nv_mfma_f32_16x16x32_bf16 a[176:179], a[100:103], a[152:155], a[176:179]\n" \
    "v_add_u32_e32 v233, s81, v233\nv_mfma_f32_16x16x32_bf16 v[44:47], a[96:99], a[156:159], v[44:47]\nds_read_b128 a[44:47], v226 offset:768\ns_sub_u32 %[cnt], %[cnt], 1\n" \
    "buffer_load_dwordx4 v236, %[srdX2], s80 offen offset:3072 sc1 lds\nv_mfma_f32_16x16x32_bf16 a[180:183], a[100:103], a[156:159], a[180:183]\nv_mfma_f32_16x16x32_bf16 v[52:55], a[104:107], a[136:139], v[52:55]\n" \
    "ds_read_b128 a[48:51], v226 offset:1024\nv_mfma_f32_16x16x32_bf16 a[184:187], a[108:111], a[136:139], a[184:187]\nv_mfma_f32_16x16x32_bf16 v[60:63], a[104:107], a[140:143], v[60:63]\n" \
    "ds_read_b128 a[52:55], v226 offset:1280\ns_add_u32 m0, m0, 0x1000\ns_add_u32 s80, s80, 0x1000\nbuffer_load_dwordx4 v236, %[srdX2], s80 offen offset:0 sc1 lds\n" \
    "v_mfma_f32_16x16x32_bf16 a[188:191], a[108:111], a[140:143], a[188:191]\nv_mfma_f32_16x16x32_bf16 v[68:71], a[104:107], a[144:147], v[68:71]\nds_read_b128 a[56:59], v226 offset:1536\n" \
    "v_mfma_f32_16x16x32_bf16 a[192:195], a[108:111], a[144:147], a[192:195]\ns_mov_b32 m0, %[m0x21]\ns_add_u32 s80, s83, 0x2800\nbuffer_load_dwordx4 v236, %[srdX2], s80 offen offset:0 sc1 lds\n" \
    "v_mfma_f32_16x16x32_bf16 v[76:79], a[104:107], a[148:151], v[76:79]\nds_read_b128 a[60:63], v226 offset:1792\nv_mfma_f32_16x16x32_bf16 a[196:199], a[108:111], a[148:151], a[196:199]\n" \
    "v_mfma_f32_16x16x32_bf16 v[84:87], a[104:107], a[152:155], v[84:87]\nds_read_b128 a[64:67], v226 offset:2048\nbuffer_load_dwordx4 v236, %[srdX2], s80 offen offset:1024 sc1 lds\n" \
    "v_mfma_f32_16x16x32_bf16 a[200:203], a[108:111], a[152:155], a[200:203]\nv_mfma_f32_16x16x32_bf16 v[92:95], a[104:107], a[156:159], v[92:95]\nds_read_b128 a[68:71], v226 offset:2304\n" \
    "v_mfma_f32_16x16x32_bf16 a[204:207], a[108:111], a[156:159], a[204:207]\nv_mfma_f32_16x16x32_bf16 v[100:103], a[112:115], a[136:139], v[100:103]\nds_read_b64_tr_b16 a[72:73], v228 offset:0\n" \
    "buffer_load_dwordx4 v236, %[srdX2], s80 offen offset:2048 sc1 lds\nv_mfma_f32_16x16x32_bf16 a[208:211], a[116:119], a[136:139], a[208:211]\nds_read_b64_tr_b16 a[74:75], v228 offset:1536\n" \
    "v_mfma_f32_16x16x32_bf16 v[108:111], a[112:115], a[140:143], v[108:111]\nv_mfma_f32_16x16x32_bf16 a[212:215], a[116:119], a[140:143], a[212:215]\nds_read_b64_tr_b16 a[76:77], v229 offset:0\n" \
    "buffer_load_dwordx4 v236, %[srdX2], s80 offen offset:3072 sc1 lds\nv_mfma_f32_16x16x32_bf16 v[116:119], a[112:115], a[144:147], v[116:119]\nds_read_b64_tr_b16 a[78:79], v229 offset:1536\n" \
    "v_mfma_f32_16x16x32_bf16 a[216:219], a[116:119], a[144:147], a[216:219]\nv_mfma_f32_16x16x32_bf16 v[124:127], a[112:115], a[148:151], v[124:127]\nds_read_b64_tr_b16 a[80:81], v230 offset:0\ns_add_u32 m0, m0, 0x1000\n" \
    "s_add_u32 s80, s80, 0x1000\nbuffer_load_dwordx4 v236, %[srdX2], s80 offen offset:0 sc1 lds\nv_mfma_f32_16x16x32_bf16 a[220:223], a[116:119], a[148:151], a[220:223]\nds_read_b64_tr_b16 a[82:83], v230 offset:1536\n" \
    "v_mfma_f32_16x16x32_bf16 v[132:135], a[112:115], a[152:155], v[132:135]\nv_mfma_f32_16x16x32_bf16 a[224:227], a[116:119], a[152:155], a[224:227]\nds_read_b64_tr_b16 a[84:85], v231 offset:0\n" \
    "v_mfma_f32_16x16x32_bf16 v[140:143], a[112:115], a[156:159], v[140:143]\nds_read_b64_tr_b16 a[86:87], v231 offset:1536\nv_mfma_f32_16x16x32_bf16 a[228:231], a[116:119], a[156:159], a[228:231]\n" \
    "v_mfma_f32_16x16x32_bf16 v[148:151], a[120:123], a[136:139], v[148:151]\nds_read_b64_tr_b16 a[88:89], v232 offset:0\nv_mfma_f32_16x16x32_bf16 a[232:235], a[124:127], a[136:139], a[232:235]\n" \
    "ds_read_b64_tr_b16 a[90:91], v232 offset:1536\nv_mfma_f32_16x16x32_bf16 v[156:159], a[120:123], a[140:143], v[156:159]\nv_mfma_f32_16x16x32_bf16 a[236:239], a[124:127], a[140:143], a[236:239]\n" \
    "ds_read_b64_tr_b16 a[92:93], v233 offset:0\nv_mfma_f32_16x16x32_bf16 v[164:167], a[120:123], a[144:147], v[164:167]\nds_read_b64_tr_b16 a[94:95], v233 offset:1536\n" \
    "v_mfma_f32_16x16x32_bf16 a[240:243], a[124:127], a[144:147], a[240:243]\nv_mfma_f32_16x16x32_bf16 v[172:175], a[120:123], a[148:151], v[172:175]\nv_mfma_f32_16x16x32_bf16 a[244:247], a[124:127], a[148:151], a[244:247]\n" \
    "v_mfma_f32_16x16x32_bf16 v[180:183], a[120:123], a[152:155], v[180:183]\nv_mfma_f32_16x16x32_bf16 a[248:251], a[124:127], a[152:155], a[248:251]\nv_mfma_f32_16x16x32_bf16 v[188:191], a[120:123], a[156:159], v[188:191]\n" \
    "v_mfma_f32_16x16x32_bf16 a[252:255], a[124:127], a[156:159], a[252:255]\nv_mfma_f32_16x16x32_bf16 v[196:199], a[128:131], a[136:139], v[196:199]\nv_mfma_f32_16x16x32_bf16 a[0:3], a[132:135], a[136:139], a[0:3]\n" \
    "v_mfma_f32_16x16x32_bf16 v[204:207], a[128:131], a[140:143], v[204:207]\nv_mfma_f32_16x16x32_bf16 a[4:7], a[132:135], a[140:143], a[4:7]\nv_mfma_f32_16x16x32_bf16 v[212:215], a[128:131], a[144:147], v[212:215]\n" \
    "v_mfma_f32_16x16x32_bf16 a[8:11], a[132:135], a[144:147], a[8:11]\nv_mfma_f32_16x16x32_bf16 v[220:223], a[128:131], a[148:151], v[220:223]\nv_mfma_f32_16x16x32_bf16 a[12:15], a[132:135], a[148:151], a[12:15]\n" \
    "v_mfma_f32_16x16x32_bf16 a[24:27], a[128:131], a[152:155], a[24:27]\nv_mfma_f32_16x16x32_bf16 a[16:19], a[132:135], a[152:155], a[16:19]\nv_mfma_f32_16x16x32_bf16 a[28:31], a[128:131], a[156:159], a[28:31]\n" \
    "v_mfma_f32_16x16x32_bf16 a[20:23], a[132:135], a[156:159], a[20:23]\ns_waitcnt lgkmcnt(0)\nv_mfma_f32_16x16x32_bf16 v[4:7], a[32:35], a[72:75], v[4:7]\nds_read_b128 a[96:99], v227 offset:0\n" \
    "v_mfma_f32_16x16x32_bf16 a[160:163], a[36:39], a[72:75], a[160:163]\nds_read_b128 a[100:103], v227 offset:256\ns_cmp_gt_u32 %[cnt], 2\ns_cselect_b32 s83, %[sw2], 0x70000000\n" \
    "v_mfma_f32_16x16x32_bf16 v[12:15], a[32:35], a[76:79], v[12:15]\ns_add_u32 %[sw2], %[sw2], 0x36000\nv_mfma_f32_16x16x32_bf16 a[164:167], a[36:39], a[76:79], a[164:167]\nds_read_b128 a[104:107], v227 offset:512\n" \
    "s_add_u32 m0, %[m0w2], s86\ns_add_u32 s81, s83, 0x0\nbuffer_load_dwordx4 v240, %[srdW2], s81 offen offset:0 lds\nv_mfma_f32_16x16x32_bf16 v[20:23], a[32:35], a[80:83], v[20:23]\n" \
    "v_mfma_f32_16x16x32_bf16 a[168:171], a[36:39], a[80:83], a[168:171]\nds_read_b128 a[108:111], v227 offset:768\nv_mfma_f32_16x16x32_bf16 v[28:31], a[32:35], a[84:87], v[28:31]\n" \
    "v_mfma_f32_16x16x32_bf16 a[172:175], a[36:39], a[84:87], a[172:175]\nds_read_b128 a[112:115], v227 offset:1024\nv_mfma_f32_16x16x32_bf16 v[36:39], a[32:35], a[88:91], v[36:39]\n" \
    "v_mfma_f32_16x16x32_bf16 a[176:179], a[36:39], a[88:91], a[176:179]\nds_read_b128 a[116:119], v227 offset:1280\nv_mfma_f32_16x16x32_bf16 v[44:47], a[32:35], a[92:95], v[44:47]\n" \
    "buffer_load_dwordx4 v241, %[srdW2], s81 offen offset:1024 lds\nv_mfma_f32_16x16x32_bf16 a[180:183], a[36:39], a[92:95], a[180:183]\nds_read_b128 a[120:123], v227 offset:1536\n" \
    "v_mfma_f32_16x16x32_bf16 v[52:55], a[40:43], a[72:75], v[52:55]\nv_mfma_f32_16x16x32_bf16 a[184:187], a[44:47], a[72:75], a[184:187]\nds_read_b128 a[124:127], v227 offset:1792\n" \
    "v_mfma_f32_16x16x32_bf16 v[60:63], a[40:43], a[76:79], v[60:63]\nv_mfma_f32_16x16x32_bf16 a[188:191], a[44:47], a[76:79], a[188:191]\nds_read_b128 a[128:131], v227 offset:2048\n" \
    "v_mfma_f32_16x16x32_bf16 v[68:71], a[40:43], a[80:83], v[68:71]\nv_mfma_f32_16x16x32_bf16 a[192:195], a[44:47], a[80:83], a[192:195]\nds_read_b128 a[132:135], v227 offset:2304\n" \
    "v_mfma_f32_16x16x32_bf16 v[76:79], a[40:43], a[84:87], v[76:79]\nbuffer_load_dwordx4 v242, %[srdW2], s81 offen offset:2048 lds\nv_mfma_f32_16x16x32_bf16 a[196:199], a[44:47], a[84:87], a[196:199]\n" \
    "ds_read_b64_tr_b16 a[136:137], v228 offset:12288\nv_mfma_f32_16x16x32_bf16 v[84:87], a[40:43], a[88:91], v[84:87]\nv_mfma_f32_16x16x32_bf16 a[200:203], a[44:47], a[88:91], a[200:203]\n" \
    "ds_read_b64_tr_b16 a[138:139], v228 offset:13824\nv_mfma_f32_16x16x32_bf16 v[92:95], a[40:43], a[92:95], v[92:95]\nds_read_b64_tr_b16 a[140:141], v229 offset:12288\n" \
    "v_mfma_f32_16x16x32_bf16 a[204:207], a[44:47], a[92:95], a[204:207]\nv_mfma_f32_16x16x32_bf16 v[100:103], a[48:51], a[72:75], v[100:103]\nds_read_b64_tr_b16 a[142:143], v229 offset:13824\n" \
    "v_mfma_f32_16x16x32_bf16 a[208:211], a[52:55], a[72:75], a[208:211]\nv_mfma_f32_16x16x32_bf16 v[108:111], a[48:51], a[76:79], v[108:111]\nds_read_b64_tr_b16 a[144:145], v230 offset:12288\ns_add_u32 m0, m0, 0x1800\n" \
    "s_add_u32 s81, s83, 0xd800\nbuffer_load_dwordx4 v240, %[srdW2], s81 offen offset:0 lds\nv_mfma_f32_16x16x32_bf16 a[212:215], a[52:55], a[76:79], a[212:215]\n" \
    "v_mfma_f32_16x16x32_bf16 v[116:119], a[48:51], a[80:83], v[116:119]\nds_read_b64_tr_b16 a[146:147], v230 offset:13824\nv_mfma_f32_16x16x32_bf16 a[216:219], a[52:55], a[80:83], a[216:219]\n" \
    "v_mfma_f32_16x16x32_bf16 v[124:127], a[48:51], a[84:87], v[124:127]\nds_read_b64_tr_b16 a[148:149], v231 offset:12288\nv_mfma_f32_16x16x32_bf16 a[220:223], a[52:55], a[84:87], a[220:223]\n" \
    "v_mfma_f32_16x16x32_bf16 v[132:135], a[48:51], a[88:91], v[132:135]\nds_read_b64_tr_b16 a[150:151], v231 offset:13824\nv_mfma_f32_16x16x32_bf16 a[224:227], a[52:55], a[88:91], a[224:227]\n" \
    "buffer_load_dwordx4 v241, %[srdW2], s81 offen offset:1024 lds\nv_mfma_f32_16x16x32_bf16 v[140:143], a[48:51], a[92:95], v[140:143]\nds_read_b64_tr_b16 a[152:153], v232 offset:12288\n" \
    "v_mfma_f32_16x16x32_bf16 a[228:231], a[52:55], a[92:95], a[228:231]\nv_mfma_f32_16x16x32_bf16 v[148:151], a[56:59], a[72:75], v[148:151]\nds_read_b64_tr_b16 a[154:155], v232 offset:13824\n" \
    "v_mfma_f32_16x16x32_bf16 a[232:235], a[60:63], a[72:75], a[232:235]\nv_mfma_f32_16x16x32_bf16 v[156:159], a[56:59], a[76:79], v[156:159]\nds_read_b64_tr_b16 a[156:157], v233 offset:12288\n" \
    "v_mfma_f32_16x16x32_bf16 a[236:239], a[60:63], a[76:79], a[236:239]\nv_mfma_f32_16x16x32_bf16 v[164:167], a[56:59], a[80:83], v[164:167]\nds_read_b64_tr_b16 a[158:159], v233 offset:13824\n" \
    "v_mfma_f32_16x16x32_bf16 a[240:243], a[60:63], a[80:83], a[240:243]\nbuffer_load_dwordx4 v242, %[srdW2], s81 offen offset:2048 lds\nv_mfma_f32_16x16x32_bf16 v[172:175], a[56:59], a[84:87], v[172:175]\n" \
    "v_mfma_f32_16x16x32_bf16 a[244:247], a[60:63], a[84:87], a[244:247]\nv_mfma_f32_16x16x32_bf16 v[180:183], a[56:59], a[88:91], v[180:183]\nv_mfma_f32_16x16x32_bf16 a[248:251], a[60:63], a[88:91], a[248:251]\n" \
    "v_mfma_f32_16x16x32_bf16 v[188:191], a[56:59], a[92:95], v[188:191]\nv_mfma_f32_16x16x32_bf16 a[252:255], a[60:63], a[92:95], a[252:255]\nv_mfma_f32_16x16x32_bf16 v[196:199], a[64:67], a[72:75], v[196:199]\n" \
    "v_mfma_f32_16x16x32_bf16 a[0:3], a[68:71], a[72:75], a[0:3]\nv_mfma_f32_16x16x32_bf16 v[204:207], a[64:67], a[76:79], v[204:207]\nv_mfma_f32_16x16x32_bf16 a[4:7], a[68:71], a[76:79], a[4:7]\n" \
    "v_mfma_f32_16x16x32_bf16 v[212:215], a[64:67], a[80:83], v[212:215]\nv_mfma_f32_16x16x32_bf16 a[8:11], a[68:71], a[80:83], a[8:11]\nv_mfma_f32_16x16x32_bf16 v[220:223], a[64:67], a[84:87], v[220:223]\n" \
    "v_mfma_f32_16x16x32_bf16 a[12:15], a[68:71], a[84:87], a[12:15]\nv_mfma_f32_16x16x32_bf16 a[24:27], a[64:67], a[88:91], a[24:27]\nv_mfma_f32_16x16x32_bf16 a[16:19], a[68:71], a[88:91], a[16:19]\n" \
    "v_mfma_f32_16x16x32_bf16 a[28:31], a[64:67], a[92:95], a[28:31]\nv_mfma_f32_16x16x32_bf16 a[20:23], a[68:71], a[92:95], a[20:23]\ns_waitcnt vmcnt(6) lgkmcnt(0)\ns_barrier\ns_cmp_eq_u32 %[cnt], 1\ns_cbranch_scc1 L_U0x%=\n" \
    "v_mfma_f32_16x16x32_bf16 v[4:7], a[96:99], a[136:139], v[4:7]\ns_cmp_gt_u32 %[cnt], 2\ns_cselect_b32 s83, %[sx2], 0x70000000\nv_mfma_f32_16x16x32_bf16 a[160:163], a[100:103], a[136:139], a[160:163]\n" \
    "s_sub_u32 s81, s85, s84\ns_mov_b32 s87, s84\ns_mov_b32 m0, %[m0x22]\ns_add_u32 s80, s83, 0x0\nbuffer_load_dwordx4 v236, %[srdX2], s80 offen offset:0 sc1 lds\n" \
    "v_mfma_f32_16x16x32_bf16 v[12:15], a[96:99], a[140:143], v[12:15]\ns_mov_b32 s84, s85\ns_mov_b32 s85, s86\nv_mfma_f32_16x16x32_bf16 a[164:167], a[100:103], a[140:143], a[164:167]\ns_add_u32 %[sx2], %[sx2], 0x14000\n" \
    "s_mov_b32 s86, s87\nv_mfma_f32_16x16x32_bf16 v[20:23], a[96:99], a[144:147], v[20:23]\nv_add_u32_e32 v228, s81, v228\nds_read_b128 a[32:35], v224 offset:0\n" \
    "buffer_load_dwordx4 v236, %[srdX2], s80 offen offset:1024 sc1 lds\nv_mfma_f32_16x16x32_bf16 a[168:171], a[100:103], a[144:147], a[168:171]\nv_add_u32_e32 v229, s81, v229\n" \
    "v_mfma_f32_16x16x32_bf16 v[28:31], a[96:99], a[148:151], v[28:31]\nv_add_u32_e32 v230, s81, v230\nds_read_b128 a[36:39], v224 offset:256\nv_mfma_f32_16x16x32_bf16 a[172:175], a[100:103], a[148:151], a[172:175]\n" \
    "v_add_u32_e32 v231, s81, v231\nbuffer_load_dwordx4 v236, %[srdX2], s80 offen offset:2048 sc1 lds\nv_mfma_f32_16x16x32_bf16 v[36:39], a[96:99], a[152:155], v[36:39]\nv_add_u32_e32 v232, s81, v232\n" \
    "ds_read_b128 a[40:43], v224 offset:512\nv_mfma_f32_16x16x32_bf16 a[176:179], a[100:103], a[152:155], a[176:179]\nv_add_u32_e32 v233, s81, v233\nv_mfma_f32_16x16x32_bf16 v[44:47], a[96:99], a[156:159], v[44:47]\n" \
    "ds_read_b128 a[44:47], v224 offset:768\ns_sub_u32 %[cnt], %[cnt], 1\nbuffer_load_dwordx4 v236, %[srdX2], s80 offen offset:3072 sc1 lds\nv_mfma_f32_16x16x32_bf16 a[180:183], a[100:103], a[156:159], a[180:183]\n" \
    "v_mfma_f32_16x16x32_bf16 v[52:55], a[104:107], a[136:139], v[52:55]\nds_read_b128 a[48:51], v224 offset:1024\nv_mfma_f32_16x16x32_bf16 a[184:187], a[108:111], a[136:139], a[184:187]\n" \
    "v_mfma_f32_16x16x32_bf16 v[60:63], a[104:107], a[140:143], v[60:63]\nds_read_b128 a[52:55], v224 offset:1280\ns_add_u32 m0, m0, 0x1000\ns_add_u32 s80, s80, 0x1000\n" \
    "buffer_load_dwordx4 v236, %[srdX2], s80 offen offset:0 sc1 lds\nv_mfma_f32_16x16x32_bf16 a[188:191], a[108:111], a[140:143], a[188:191]\nv_mfma_f32_16x16x32_bf16 v[68:71], a[104:107], a[144:147], v[68:71]\n" \
    "ds_read_b128 a[56:59], v224 offset:1536\nv_mfma_f32_16x16x32_bf16 a[192:195], a[108:111], a[144:147], a[192:195]\ns_mov_b32 m0, %[m0x23]\ns_add_u32 s80, s83, 0x2800\n" \
    "buffer_load_dwordx4 v236, %[srdX2], s80 offen offset:0 sc1 lds\nv_mfma_f32_16x16x32_bf16 v[76:79], a[104:107], a[148:151], v[76:79]\nds_read_b128 a[60:63], v224 offset:1792\n" \
    "v_mfma_f32_16x16x32_bf16 a[196:199], a[108:111], a[148:151], a[196:199]\nv_mfma_f32_16x16x32_bf16 v[84:87], a[104:107], a[152:155], v[84:87]\nds_read_b128 a[64:67], v224 offset:2048\n" \
    "buffer_load_dwordx4 v236, %[srdX2], s80 offen offset:1024 sc1 lds\nv_mfma_f32_16x16x32_bf16 a[200:203], a[108:111], a[152:155], a[200:203]\nv_mfma_f32_16x16x32_bf16 v[92:95], a[104:107], a[156:159], v[92:95]\n" \
    "ds_read_b128 a[68:71], v224 offset:2304\nv_mfma_f32_16x16x32_bf16 a[204:207], a[108:111], a[156:159], a[204:207]\nv_mfma_f32_16x16x32_bf16 v[100:103], a[112:115], a[136:139], v[100:103]\n" \
    "ds_read_b64_tr_b16 a[72:73], v228 offset:0\nbuffer_load_dwordx4 v236, %[srdX2], s80 offen offset:2048 sc1 lds\nv_mfma_f32_16x16x32_bf16 a[208:211], a[116:119], a[136:139], a[208:211]\n" \
    "ds_read_b64_tr_b16 a[74:75], v228 offset:1536\nv_mfma_f32_16x16x32_bf16 v[108:111], a[112:115], a[140:143], v[108:111]\nv_mfma_f32_16x16x32_bf16 a[212:215], a[116:119], a[140:143], a[212:215]\n" \
    "ds_read_b64_tr_b16 a[76:77], v229 offset:0\nbuffer_load_dwordx4 v236, %[srdX2], s80 offen offset:3072 sc1 lds\nv_mfma_f32_16x16x32_bf16 v[116:119], a[112:115], a[144:147], v[116:119]\n" \
    "ds_read_b64_tr_b16 a[78:79], v229 offset:1536\nv_mfma_f32_16x16x32_bf16 a[216:219], a[116:119], a[144:147], a[216:219]\nv_mfma_f32_16x16x32_bf16 v[124:127], a[112:115], a[148:151], v[124:127]\n" \
    "ds_read_b64_tr_b16 a[80:81], v230 offset:0\ns_add_u32 m0, m0, 0x1000\ns_add_u32 s80, s80, 0x1000\nbuffer_load_dwordx4 v236, %[srdX2], s80 offen offset:0 sc1 lds\n" \
    "v_mfma_f32_16x16x32_bf16 a[220:223], a[116:119], a[148:151], a[220:223]\nds_read_b64_tr_b16 a[82:83], v230 offset:1536\nv_mfma_f32_16x16x32_bf16 v[132:135], a[112:115], a[152:155], v[132:135]\n" \
    "v_mfma_f32_16x16x32_bf16 a[224:227], a[116:119], a[152:155], a[224:227]\nds_read_b64_tr_b16 a[84:85], v231 offset:0\nv_mfma_f32_16x16x32_bf16 v[140:143], a[112:115], a[156:159], v[140:143]\n" \
    "ds_read_b64_tr_b16 a[86:87], v231 offset:1536\nv_mfma_f32_16x16x32_bf16 a[228:231], a[116:119], a[156:159], a[228:231]\nv_mfma_f32_16x16x32_bf16 v[148:151], a[120:123], a[136:139], v[148:151]\n" \
    "ds_read_b64_tr_b16 a[88:89], v232 offset:0\nv_mfma_f32_16x16x32_bf16 a[232:235], a[124:127], a[136:139], a[232:235]\nds_read_b64_tr_b16 a[90:91], v232 offset:1536\n" \
    "v_mfma_f32_16x16x32_bf16 v[156:159], a[120:123], a[140:143], v[156:159]\nv_mfma_f32_16x16x32_bf16 a[236:239], a[124:127], a[140:143], a[236:239]\nds_read_b64_tr_b16 a[92:93], v233 offset:0\n" \
    "v_mfma_f32_16x16x32_bf16 v[164:167], a[120:123], a[144:147], v[164:167]\nds_read_b64_tr_b16 a[94:95], v233 offset:1536\nv_mfma_f32_16x16x32_bf16 a[240:243], a[124:127], a[144:147], a[240:243]\n" \
    "v_mfma_f32_16x16x32_bf16 v[172:175], a[120:123], a[148:151], v[172:175]\nv_mfma_f32_16x16x32_bf16 a[244:247], a[124:127], a[148:151], a[244:247]\nv_mfma_f32_16x16x32_bf16 v[180:183], a[120:123], a[152:155], v[180:183]\n" \
    "v_mfma_f32_16x16x32_bf16 a[248:251], a[124:127], a[152:155], a[248:251]\nv_mfma_f32_16x16x32_bf16 v[188:191], a[120:123], a[156:159], v[188:191]\nv_mfma_f32_16x16x32_bf16 a[252:255], a[124:127], a[156:159], a[252:255]\n" \
    "v_mfma_f32_16x16x32_bf16 v[196:199], a[128:131], a[136:139], v[196:199]\nv_mfma_f32_16x16x32_bf16 a[0:3], a[132:135], a[136:139], a[0:3]\nv_mfma_f32_16x16x32_bf16 v[204:207], a[128:131], a[140:143], v[204:207]\n" \
    "v_mfma_f32_16x16x32_bf16 a[4:7], a[132:135], a[140:143], a[4:7]\nv_mfma_f32_16x16x32_bf16 v[212:215], a[128:131], a[144:147], v[212:215]\nv_mfma_f32_16x16x32_bf16 a[8:11], a[132:135], a[144:147], a[8:11]\n" \
    "v_mfma_f32_16x16x32_bf16 v[220:223], a[128:131], a[148:151], v[220:223]\nv_mfma_f32_16x16x32_bf16 a[12:15], a[132:135], a[148:151], a[12:15]\nv_mfma_f32_16x16x32_bf16 a[24:27], a[128:131], a[152:155], a[24:27]\n" \
    "v_mfma_f32_16x16x32_bf16 a[16:19], a[132:135], a[152:155], a[16:19]\nv_mfma_f32_16x16x32_bf16 a[28:31], a[128:131], a[156:159], a[28:31]\nv_mfma_f32_16x16x32_bf16 a[20:23], a[132:135], a[156:159], a[20:23]\n" \
    "s_waitcnt lgkmcnt(0)\nv_mfma_f32_16x16x32_bf16 v[4:7], a[32:35], a[72:75], v[4:7]\nds_read_b128 a[96:99], v225 offset:0\nv_mfma_f32_16x16x32_bf16 a[160:163], a[36:39], a[72:75], a[160:163]\n" \
    "ds_read_b128 a[100:103], v225 offset:256\ns_cmp_gt_u32 %[cnt], 2\ns_cselect_b32 s83, %[sw2], 0x70000000\nv_mfma_f32_16x16x32_bf16 v[12:15], a[32:35], a[76:79], v[12:15]\ns_add_u32 %[sw2], %[sw2], 0x36000\n" \
    "v_mfma_f32_16x16x32_bf16 a[164:167], a[36:39], a[76:79], a[164:167]\nds_read_b128 a[104:107], v225 offset:512\ns_add_u32 m0, %[m0w2], s86\ns_add_u32 s81, s83, 0x0\n" \
    "buffer_load_dwordx4 v240, %[srdW2], s81 offen offset:0 lds\nv_mfma_f32_16x16x32_bf16 v[20:23], a[32:35], a[80:83], v[20:23]\nv_mfma_f32_16x16x32_bf16 a[168:171], a[36:39], a[80:83], a[168:171]\n" \
    "ds_read_b128 a[108:111], v225 offset:768\nv_mfma_f32_16x16x32_bf16 v[28:31], a[32:35], a[84:87], v[28:31]\nv_mfma_f32_16x16x32_bf16 a[172:175], a[36:39], a[84:87], a[172:175]\nds_read_b128 a[112:115], v225 offset:1024\n" \
    "v_mfma_f32_16x16x32_bf16 v[36:39], a[32:35], a[88:91], v[36:39]\nv_mfma_f32_16x16x32_bf16 a[176:179], a[36:39], a[88:91], a[176:179]\nds_read_b128 a[116:119], v225 offset:1280\n" \
    "v_mfma_f32_16x16x32_bf16 v[44:47], a[32:35], a[92:95], v[44:47]\nbuffer_load_dwordx4 v241, %[srdW2], s81 offen offset:1024 lds\nv_mfma_f32_16x16x32_bf16 a[180:183], a[36:39], a[92:95], a[180:183]\n" \
    "ds_read_b128 a[120:123], v225 offset:1536\nv_mfma_f32_16x16x32_bf16 v[52:55], a[40:43], a[72:75], v[52:55]\nv_mfma_f32_16x16x32_bf16 a[184:187], a[44:47], a[72:75], a[184:187]\nds_read_b128 a[124:127], v225 offset:1792\n" \
    "v_mfma_f32_16x16x32_bf16 v[60:63], a[40:43], a[76:79], v[60:63]\nv_mfma_f32_16x16x32_bf16 a[188:191], a[44:47], a[76:79], a[188:191]\nds_read_b128 a[128:131], v225 offset:2048\n" \
    "v_mfma_f32_16x16x32_bf16 v[68:71], a[40:43], a[80:83], v[68:71]\nv_mfma_f32_16x16x32_bf16 a[192:195], a[44:47], a[80:83], a[192:195]\nds_read_b128 a[132:135], v225 offset:2304\n" \
    "v_mfma_f32_16x16x32_bf16 v[76:79], a[40:43], a[84:87], v[76:79]\nbuffer_load_dwordx4 v242, %[srdW2], s81 offen offset:2048 lds\nv_mfma_f32_16x16x32_bf16 a[196:199], a[44:47], a[84:87], a[196:199]\n" \
    "ds_read_b64_tr_b16 a[136:137], v228 offset:12288\nv_mfma_f32_16x16x32_bf16 v[84:87], a[40:43], a[88:91], v[84:87]\nv_mfma_f32_16x16x32_bf16 a[200:203], a[44:47], a[88:91], a[200:203]\n" \
    "ds_read_b64_tr_b16 a[138:139], v228 offset:13824\nv_mfma_f32_16x16x32_bf16 v[92:95], a[40:43], a[92:95], v[92:95]\nds_read_b64_tr_b16 a[140:141], v229 offset:12288\n" \
    "v_mfma_f32_16x16x32_bf16 a[204:207], a[44:47], a[92:95], a[204:207]\nv_mfma_f32_16x16x32_bf16 v[100:103], a[48:51], a[72:75], v[100:103]\nds_read_b64_tr_b16 a[142:143], v229 offset:13824\n" \
    "v_mfma_f32_16x16x32_bf16 a[208:211], a[52:55], a[72:75], a[208:211]\nv_mfma_f32_16x16x32_bf16 v[108:111], a[48:51], a[76:79], v[108:111]\nds_read_b64_tr_b16 a[144:145], v230 offset:12288\ns_add_u32 m0, m0, 0x1800\n" \
    "s_add_u32 s81, s83, 0xd800\nbuffer_load_dwordx4 v240, %[srdW2], s81 offen offset:0 lds\nv_mfma_f32_16x16x32_bf16 a[212:215], a[52:55], a[76:79], a[212:215]\n" \
    "v_mfma_f32_16x16x32_bf16 v[116:119], a[48:51], a[80:83], v[116:119]\nds_read_b64_tr_b16 a[146:147], v230 offset:13824\nv_mfma_f32_16x16x32_bf16 a[216:219], a[52:55], a[80:83], a[216:219]\n" \
    "v_mfma_f32_16x16x32_bf16 v[124:127], a[48:51], a[84:87], v[124:127]\nds_read_b64_tr_b16 a[148:149], v231 offset:12288\nv_mfma_f32_16x16x32_bf16 a[220:223], a[52:55], a[84:87], a[220:223]\n" \
    "v_mfma_f32_16x16x32_bf16 v[132:135], a[48:51], a[88:91], v[132:135]\nds_read_b64_tr_b16 a[150:151], v231 offset:13824\nv_mfma_f32_16x16x32_bf16 a[224:227], a[52:55], a[88:91], a[224:227]\n" \
    "buffer_load_dwordx4 v241, %[srdW2], s81 offen offset:1024 lds\nv_mfma_f32_16x16x32_bf16 v[140:143], a[48:51], a[92:95], v[140:143]\nds_read_b64_tr_b16 a[152:153], v232 offset:12288\n" \
    "v_mfma_f32_16x16x32_bf16 a[228:231], a[52:55], a[92:95], a[228:231]\nv_mfma_f32_16x16x32_bf16 v[148:151], a[56:59], a[72:75], v[148:151]\nds_read_b64_tr_b16 a[154:155], v232 offset:13824\n" \
    "v_mfma_f32_16x16x32_bf16 a[232:235], a[60:63], a[72:75], a[232:235]\nv_mfma_f32_16x16x32_bf16 v[156:159], a[56:59], a[76:79], v[156:159]\nds_read_b64_tr_b16 a[156:157], v233 offset:12288\n" \
    "v_mfma_f32_16x16x32_bf16 a[236:239], a[60:63], a[76:79], a[236:239]\nv_mfma_f32_16x16x32_bf16 v[164:167], a[56:59], a[80:83], v[164:167]\nds_read_b64_tr_b16 a[158:159], v233 offset:13824\n" \
    "v_mfma_f32_16x16x32_bf16 a[240:243], a[60:63], a[80:83], a[240:243]\nbuffer_load_dwordx4 v242, %[srdW2], s81 offen offset:2048 lds\nv_mfma_f32_16x16x32_bf16 v[172:175], a[56:59], a[84:87], v[172:175]\n" \
    "v_mfma_f32_16x16x32_bf16 a[244:247], a[60:63], a[84:87], a[244:247]\nv_mfma_f32_16x16x32_bf16 v[180:183], a[56:59], a[88:91], v[180:183]\nv_mfma_f32_16x16x32_bf16 a[248:251], a[60:63], a[88:91], a[248:251]\n" \
    "v_mfma_f32_16x16x32_bf16 v[188:191], a[56:59], a[92:95], v[188:191]\nv_mfma_f32_16x16x32_bf16 a[252:255], a[60:63], a[92:95], a[252:255]\nv_mfma_f32_16x16x32_bf16 v[196:199], a[64:67], a[72:75], v[196:199]\n" \
    "v_mfma_f32_16x16x32_bf16 a[0:3], a[68:71], a[72:75], a[0:3]\nv_mfma_f32_16x16x32_bf16 v[204:207], a[64:67], a[76:79], v[204:207]\nv_mfma_f32_16x16x32_bf16 a[4:7], a[68:71], a[76:79], a[4:7]\n" \
    "v_mfma_f32_16x16x32_bf16 v[212:215], a[64:67], a[80:83], v[212:215]\nv_mfma_f32_16x16x32_bf16 a[8:11], a[68:71], a[80:83], a[8:11]\nv_mfma_f32_16x16x32_bf16 v[220:223], a[64:67], a[84:87], v[220:223]\n" \
    "v_mfma_f32_16x16x32_bf16 a[12:15], a[68:71], a[84:87], a[12:15]\nv_mfma_f32_16x16x32_bf16 a[24:27], a[64:67], a[88:91], a[24:27]\nv_mfma_f32_16x16x32_bf16 a[16:19], a[68:71], a[88:91], a[16:19]\n" \
    "v_mfma_f32_16x16x32_bf16 a[28:31], a[64:67], a[92:95], a[28:31]\nv_mfma_f32_16x16x32_bf16 a[20:23], a[68:71], a[92:95], a[20:23]\ns_waitcnt vmcnt(6) lgkmcnt(0)\ns_barrier\ns_branch L_U0l%=\nL_U0x%=:\n" \
    "v_mfma_f32_16x16x32_bf16 v[4:7], a[96:99], a[136:139], v[4:7]\nv_mfma_f32_16x16x32_bf16 a[160:163], a[100:103], a[136:139], a[160:163]\nv_mfma_f32_16x16x32_bf16 v[12:15], a[96:99], a[140:143], v[12:15]\n" \
    "v_mfma_f32_16x16x32_bf16 a[164:167], a[100:103], a[140:143], a[164:167]\nv_mfma_f32_16x16x32_bf16 v[20:23], a[96:99], a[144:147], v[20:23]\nv_mfma_f32_16x16x32_bf16 a[168:171], a[100:103], a[144:147], a[168:171]\n" \
    "v_mfma_f32_16x16x32_bf16 v[28:31], a[96:99], a[148:151], v[28:31]\nv_mfma_f32_16x16x32_bf16 a[172:175], a[100:103], a[148:151], a[172:175]\nv_accvgpr_read_b32 v246, a160\nv_accvgpr_read_b32 v247, a161\n" \
    "v_mfma_f32_16x16x32_bf16 v[36:39], a[96:99], a[152:155], v[36:39]\nv_accvgpr_read_b32 v248, a162\nv_accvgpr_read_b32 v249, a163\nv_mfma_f32_16x16x32_bf16 a[176:179], a[100:103], a[152:155], a[176:179]\n" \
    "v_cvt_pk_bf16_f32 v4, v4, v5\nv_cvt_pk_bf16_f32 v5, v6, v7\nv_mfma_f32_16x16x32_bf16 v[44:47], a[96:99], a[156:159], v[44:47]\nv_cvt_pk_bf16_f32 v6, v246, v247\nv_cvt_pk_bf16_f32 v7, v248, v249\n" \
    "v_mfma_f32_16x16x32_bf16 a[180:183], a[100:103], a[156:159], a[180:183]\ns_add_u32 s82, %[so20], 0x0\ns_nop 0\nv_permlane16_swap_b32_e32 v4, v6\nv_mfma_f32_16x16x32_bf16 v[52:55], a[104:107], a[136:139], v[52:55]\n" \
    "v_permlane16_swap_b32_e32 v5, v7\nbuffer_store_dwordx4 v[4:7], v244, %[srdO2], s82 offen sc1\nv_mfma_f32_16x16x32_bf16 a[184:187], a[108:111], a[136:139], a[184:187]\nv_accvgpr_read_b32 v250, a164\n" \
    "v_accvgpr_read_b32 v251, a165\nv_mfma_f32_16x16x32_bf16 v[60:63], a[104:107], a[140:143], v[60:63]\nv_accvgpr_read_b32 v252, a166\nv_accvgpr_read_b32 v253, a167\n" \
    "v_mfma_f32_16x16x32_bf16 a[188:191], a[108:111], a[140:143], a[188:191]\nv_cvt_pk_bf16_f32 v12, v12, v13\nv_cvt_pk_bf16_f32 v13, v14, v15\nv_mfma_f32_16x16x32_bf16 v[68:71], a[104:107], a[144:147], v[68:71]\n" \
    "v_cvt_pk_bf16_f32 v14, v250, v251\nv_cvt_pk_bf16_f32 v15, v252, v253\nv_mfma_f32_16x16x32_bf16 a[192:195], a[108:111], a[144:147], a[192:195]\ns_add_u32 s82, %[so20], 0x5000\ns_nop 0\nv_permlane16_swap_b32_e32 v12, v14\n" \
    "v_mfma_f32_16x16x32_bf16 v[76:79], a[104:107], a[148:151], v[76:79]\nv_permlane16_swap_b32_e32 v13, v15\nbuffer_store_dwordx4 v[12:15], v244, %[srdO2], s82 offen sc1\n" \
    "v_mfma_f32_16x16x32_bf16 a[196:199], a[108:111], a[148:151], a[196:199]\nv_accvgpr_read_b32 v246, a168\nv_accvgpr_read_b32 v247, a169\nv_mfma_f32_16x16x32_bf16 v[84:87], a[104:107], a[152:155], v[84:87]\n" \
    "v_accvgpr_read_b32 v248, a170\nv_accvgpr_read_b32 v249, a171\nv_mfma_f32_16x16x32_bf16 a[200:203], a[108:111], a[152:155], a[200:203]\nv_cvt_pk_bf16_f32 v20, v20, v21\nv_cvt_pk_bf16_f32 v21, v22, v23\n" \
    "v_mfma_f32_16x16x32_bf16 v[92:95], a[104:107], a[156:159], v[92:95]\nv_cvt_pk_bf16_f32 v22, v246, v247\nv_cvt_pk_bf16_f32 v23, v248, v249\nv_mfma_f32_16x16x32_bf16 a[204:207], a[108:111], a[156:159], a[204:207]\n" \
    "s_add_u32 s82, %[so20], 0xa000\ns_nop 0\nv_permlane16_swap_b32_e32 v20, v22\nv_mfma_f32_16x16x32_bf16 v[100:103], a[112:115], a[136:139], v[100:103]\nv_permlane16_swap_b32_e32 v21, v23\n" \
    "buffer_store_dwordx4 v[20:23], v244, %[srdO2], s82 offen sc1\nv_mfma_f32_16x16x32_bf16 a[208:211], a[116:119], a[136:139], a[208:211]\nv_accvgpr_read_b32 v250, a172\nv_accvgpr_read_b32 v251, a173\n" \
    "v_mfma_f32_16x16x32_bf16 v[108:111], a[112:115], a[140:143], v[108:111]\nv_accvgpr_read_b32 v252, a174\nv_accvgpr_read_b32 v253, a175\nv_mfma_f32_16x16x32_bf16 a[212:215], a[116:119], a[140:143], a[212:215]\n" \
    "v_cvt_pk_bf16_f32 v28, v28, v29\nv_cvt_pk_bf16_f32 v29, v30, v31\nv_mfma_f32_16x16x32_bf16 v[116:119], a[112:115], a[144:147], v[116:119]\nv_cvt_pk_bf16_f32 v30, v250, v251\nv_cvt_pk_bf16_f32 v31, v252, v253\n" \
    "v_mfma_f32_16x16x32_bf16 a[216:219], a[116:119], a[144:147], a[216:219]\ns_add_u32 s82, %[so20], 0xf000\ns_nop 0\nv_permlane16_swap_b32_e32 v28, v30\n" \
    "v_mfma_f32_16x16x32_bf16 v[124:127], a[112:115], a[148:151], v[124:127]\nv_permlane16_swap_b32_e32 v29, v31\nbuffer_store_dwordx4 v[28:31], v244, %[srdO2], s82 offen sc1\n" \
    "v_mfma_f32_16x16x32_bf16 a[220:223], a[116:119], a[148:151], a[220:223]\nv_accvgpr_read_b32 v246, a176\nv_accvgpr_read_b32 v247, a177\nv_mfma_f32_16x16x32_bf16 v[132:135], a[112:115], a[152:155], v[132:135]\n" \
    "v_accvgpr_read_b32 v248, a178\nv_accvgpr_read_b32 v249, a179\nv_mfma_f32_16x16x32_bf16 a[224:227], a[116:119], a[152:155], a[224:227]\nv_cvt_pk_bf16_f32 v36, v36, v37\nv_cvt_pk_bf16_f32 v37, v38, v39\n" \
    "v_mfma_f32_16x16x32_bf16 v[140:143], a[112:115], a[156:159], v[140:143]\nv_cvt_pk_bf16_f32 v38, v246, v247\nv_cvt_pk_bf16_f32 v39, v248, v249\nv_mfma_f32_16x16x32_bf16 a[228:231], a[116:119], a[156:159], a[228:231]\n" \
    "s_add_u32 s82, %[so20], 0x14000\ns_nop 0\nv_permlane16_swap_b32_e32 v36, v38\nv_mfma_f32_16x16x32_bf16 v[148:151], a[120:123], a[136:139], v[148:151]\nv_permlane16_swap_b32_e32 v37, v39\n" \
    "buffer_store_dwordx4 v[36:39], v244, %[srdO2], s82 offen sc1\nv_mfma_f32_16x16x32_bf16 a[232:235], a[124:127], a[136:139], a[232:235]\nv_accvgpr_read_b32 v250, a180\nv_accvgpr_read_b32 v251, a181\n" \
    "v_mfma_f32_16x16x32_bf16 v[156:159], a[120:123], a[140:143], v[156:159]\nv_accvgpr_read_b32 v252, a182\nv_accvgpr_read_b32 v253, a183\nv_mfma_f32_16x16x32_bf16 a[236:239], a[124:127], a[140:143], a[236:239]\n" \
    "v_cvt_pk_bf16_f32 v44, v44, v45\nv_cvt_pk_bf16_f32 v45, v46, v47\nv_mfma_f32_16x16x32_bf16 v[164:167], a[120:123], a[144:147], v[164:167]\nv_cvt_pk_bf16_f32 v46, v250, v251\nv_cvt_pk_bf16_f32 v47, v252, v253\n" \
    "v_mfma_f32_16x16x32_bf16 a[240:243], a[124:127], a[144:147], a[240:243]\ns_add_u32 s82, %[so20], 0x19000\ns_nop 0\nv_permlane16_swap_b32_e32 v44, v46\n" \
    "v_mfma_f32_16x16x32_bf16 v[172:175], a[120:123], a[148:151], v[172:175]\nv_permlane16_swap_b32_e32 v45, v47\nbuffer_store_dwordx4 v[44:47], v244, %[srdO2], s82 offen sc1\n" \
    "v_mfma_f32_16x16x32_bf16 a[244:247], a[124:127], a[148:151], a[244:247]\nv_accvgpr_read_b32 v246, a184\nv_accvgpr_read_b32 v247, a185\nv_mfma_f32_16x16x32_bf16 v[180:183], a[120:123], a[152:155], v[180:183]\n" \
    "v_accvgpr_read_b32 v248, a186\nv_accvgpr_read_b32 v249, a187\nv_mfma_f32_16x16x32_bf16 a[248:251], a[124:127], a[152:155], a[248:251]\nv_cvt_pk_bf16_f32 v52, v52, v53\nv_cvt_pk_bf16_f32 v53, v54, v55\n" \
    "v_mfma_f32_16x16x32_bf16 v[188:191], a[120:123], a[156:159], v[188:191]\nv_cvt_pk_bf16_f32 v54, v246, v247\nv_cvt_pk_bf16_f32 v55, v248, v249\nv_mfma_f32_16x16x32_bf16 a[252:255], a[124:127], a[156:159], a[252:255]\n" \
    "s_add_u32 s82, %[so20], 0x400\ns_nop 0\nv_permlane16_swap_b32_e32 v52, v54\nv_mfma_f32_16x16x32_bf16 v[196:199], a[128:131], a[136:139], v[196:199]\nv_permlane16_swap_b32_e32 v53, v55\n" \
    "buffer_store_dwordx4 v[52:55], v244, %[srdO2], s82 offen sc1\nv_mfma_f32_16x16x32_bf16 a[0:3], a[132:135], a[136:139], a[0:3]\nv_accvgpr_read_b32 v250, a188\nv_accvgpr_read_b32 v251, a189\n" \
    "v_mfma_f32_16x16x32_bf16 v[204:207], a[128:131], a[140:143], v[204:207]\nv_accvgpr_read_b32 v252, a190\nv_accvgpr_read_b32 v253, a191\nv_mfma_f32_16x16x32_bf16 a[4:7], a[132:135], a[140:143], a[4:7]\n" \
    "v_cvt_pk_bf16_f32 v60, v60, v61\nv_cvt_pk_bf16_f32 v61, v62, v63\nv_mfma_f32_16x16x32_bf16 v[212:215], a[128:131], a[144:147], v[212:215]\nv_cvt_pk_bf16_f32 v62, v250, v251\nv_cvt_pk_bf16_f32 v63, v252, v253\n" \
    "v_mfma_f32_16x16x32_bf16 a[8:11], a[132:135], a[144:147], a[8:11]\ns_add_u32 s82, %[so20], 0x5400\ns_nop 0\nv_permlane16_swap_b32_e32 v60, v62\nv_mfma_f32_16x16x32_bf16 v[220:223], a[128:131], a[148:151], v[220:223]\n" \
    "v_permlane16_swap_b32_e32 v61, v63\nbuffer_store_dwordx4 v[60:63], v244, %[srdO2], s82 offen sc1\nv_mfma_f32_16x16x32_bf16 a[12:15], a[132:135], a[148:151], a[12:15]\nv_accvgpr_read_b32 v246, a192\n" \
    "v_accvgpr_read_b32 v247, a193\nv_mfma_f32_16x16x32_bf16 a[24:27], a[128:131], a[152:155], a[24:27]\nv_accvgpr_read_b32 v248, a194\nv_accvgpr_read_b32 v249, a195\n" \
    "v_mfma_f32_16x16x32_bf16 a[16:19], a[132:135], a[152:155], a[16:19]\nv_cvt_pk_bf16_f32 v68, v68, v69\nv_cvt_pk_bf16_f32 v69, v70, v71\nv_mfma_f32_16x16x32_bf16 a[28:31], a[128:131], a[156:159], a[28:31]\n" \
    "v_cvt_pk_bf16_f32 v70, v246, v247\nv_cvt_pk_bf16_f32 v71, v248, v249\nv_mfma_f32_16x16x32_bf16 a[20:23], a[132:135], a[156:159], a[20:23]\ns_add_u32 s82, %[so20], 0xa400\ns_nop 0\nv_permlane16_swap_b32_e32 v68, v70\n" \
    "s_nop 7\ns_nop 3\nv_permlane16_swap_b32_e32 v69, v71\nbuffer_store_dwordx4 v[68:71], v244, %[srdO2], s82 offen sc1\nv_accvgpr_read_b32 v250, a196\nv_accvgpr_read_b32 v251, a197\nv_accvgpr_read_b32 v252, a198\n" \
    "v_accvgpr_read_b32 v253, a199\nv_cvt_pk_bf16_f32 v76, v76, v77\nv_cvt_pk_bf16_f32 v77, v78, v79\nv_cvt_pk_bf16_f32 v78, v250, v251\nv_cvt_pk_bf16_f32 v79, v252, v253\ns_add_u32 s82, %[so20], 0xf400\ns_nop 0\n" \
    "v_permlane16_swap_b32_e32 v76, v78\nv_permlane16_swap_b32_e32 v77, v79\nbuffer_store_dwordx4 v[76:79], v244, %[srdO2], s82 offen sc1\nv_accvgpr_read_b32 v246, a200\nv_accvgpr_read_b32 v247, a201\n" \
    "v_accvgpr_read_b32 v248, a202\nv_accvgpr_read_b32 v249, a203\nv_cvt_pk_bf16_f32 v84, v84, v85\nv_cvt_pk_bf16_f32 v85, v86, v87\nv_cvt_pk_bf16_f32 v86, v246, v247\nv_cvt_pk_bf16_f32 v87, v248, v249\n" \
    "s_add_u32 s82, %[so20], 0x14400\ns_nop 0\nv_permlane16_swap_b32_e32 v84, v86\nv_permlane16_swap_b32_e32 v85, v87\nbuffer_store_dwordx4 v[84:87], v244, %[srdO2], s82 offen sc1\nv_accvgpr_read_b32 v250, a204\n" \
    "v_accvgpr_read_b32 v251, a205\nv_accvgpr_read_b32 v252, a206\nv_accvgpr_read_b32 v253, a207\nv_cvt_pk_bf16_f32 v92, v92, v93\nv_cvt_pk_bf16_f32 v93, v94, v95\nv_cvt_pk_bf16_f32 v94, v250, v251\n" \
    "v_cvt_pk_bf16_f32 v95, v252, v253\ns_add_u32 s82, %[so20], 0x19400\ns_nop 0\nv_permlane16_swap_b32_e32 v92, v94\nv_permlane16_swap_b32_e32 v93, v95\nbuffer_store_dwordx4 v[92:95], v244, %[srdO2], s82 offen sc1\n" \
    "v_accvgpr_read_b32 v246, a208\nv_accvgpr_read_b32 v247, a209\nv_accvgpr_read_b32 v248, a210\nv_accvgpr_read_b32 v249, a211\nv_cvt_pk_bf16_f32 v100, v100, v101\nv_cvt_pk_bf16_f32 v101, v102, v103\n" \
    "v_cvt_pk_bf16_f32 v102, v246, v247\nv_cvt_pk_bf16_f32 v103, v248, v249\ns_add_u32 s82, %[so20], 0x800\ns_nop 0\nv_permlane16_swap_b32_e32 v100, v102\nv_permlane16_swap_b32_e32 v101, v103\n" \
    "buffer_store_dwordx4 v[100:103], v244, %[srdO2], s82 offen sc1\nv_accvgpr_read_b32 v250, a212\nv_accvgpr_read_b32 v251, a213\nv_accvgpr_read_b32 v252, a214\nv_accvgpr_read_b32 v253, a215\n" \
    "v_cvt_pk_bf16_f32 v108, v108, v109\nv_cvt_pk_bf16_f32 v109, v110, v111\nv_cvt_pk_bf16_f32 v110, v250, v251\nv_cvt_pk_bf16_f32 v111, v252, v253\ns_add_u32 s82, %[so20], 0x5800\ns_nop 0\n" \
    "v_permlane16_swap_b32_e32 v108, v110\nv_permlane16_swap_b32_e32 v109, v111\nbuffer_store_dwordx4 v[108:111], v244, %[srdO2], s82 offen sc1\nv_accvgpr_read_b32 v246, a216\nv_accvgpr_read_b32 v247, a217\n" \
    "v_accvgpr_read_b32 v248, a218\nv_accvgpr_read_b32 v249, a219\nv_cvt_pk_bf16_f32 v116, v116, v117\nv_cvt_pk_bf16_f32 v117, v118, v119\nv_cvt_pk_bf16_f32 v118, v246, v247\nv_cvt_pk_bf16_f32 v119, v248, v249\n" \
    "s_add_u32 s82, %[so20], 0xa800\ns_nop 0\nv_permlane16_swap_b32_e32 v116, v118\nv_permlane16_swap_b32_e32 v117, v119\nbuffer_store_dwordx4 v[116:119], v244, %[srdO2], s82 offen sc1\nv_accvgpr_read_b32 v250, a220\n" \
    "v_accvgpr_read_b32 v251, a221\nv_accvgpr_read_b32 v252, a222\nv_accvgpr_read_b32 v253, a223\nv_cvt_pk_bf16_f32 v124, v124, v125\nv_cvt_pk_bf16_f32 v125, v126, v127\nv_cvt_pk_bf16_f32 v126, v250, v251\n" \
    "v_cvt_pk_bf16_f32 v127, v252, v253\ns_add_u32 s82, %[so20], 0xf800\ns_nop 0\nv_permlane16_swap_b32_e32 v124, v126\nv_permlane16_swap_b32_e32 v125, v127\nbuffer_store_dwordx4 v[124:127], v244, %[srdO2], s82 offen sc1\n" \
    "v_accvgpr_read_b32 v246, a224\nv_accvgpr_read_b32 v247, a225\nv_accvgpr_read_b32 v248, a226\nv_accvgpr_read_b32 v249, a227\nv_cvt_pk_bf16_f32 v132, v132, v133\nv_cvt_pk_bf16_f32 v133, v134, v135\n" \
    "v_cvt_pk_bf16_f32 v134, v246, v247\nv_cvt_pk_bf16_f32 v135, v248, v249\ns_add_u32 s82, %[so20], 0x14800\ns_nop 0\nv_permlane16_swap_b32_e32 v132, v134\nv_permlane16_swap_b32_e32 v133, v135\n" \
    "buffer_store_dwordx4 v[132:135], v244, %[srdO2], s82 offen sc1\nv_accvgpr_read_b32 v250, a228\nv_accvgpr_read_b32 v251, a229\nv_accvgpr_read_b32 v252, a230\nv_accvgpr_read_b32 v253, a231\n" \
    "v_cvt_pk_bf16_f32 v140, v140, v141\nv_cvt_pk_bf16_f32 v141, v142, v143\nv_cvt_pk_bf16_f32 v142, v250, v251\nv_cvt_pk_bf16_f32 v143, v252, v253\ns_add_u32 s82, %[so20], 0x19800\ns_nop 0\n" \
    "v_permlane16_swap_b32_e32 v140, v142\nv_permlane16_swap_b32_e32 v141, v143\nbuffer_store_dwordx4 v[140:143], v244, %[srdO2], s82 offen sc1\nv_accvgpr_read_b32 v246, a232\nv_accvgpr_read_b32 v247, a233\n" \
    "v_accvgpr_read_b32 v248, a234\nv_accvgpr_read_b32 v249, a235\nv_cvt_pk_bf16_f32 v148, v148, v149\nv_cvt_pk_bf16_f32 v149, v150, v151\nv_cvt_pk_bf16_f32 v150, v246, v247\nv_cvt_pk_bf16_f32 v151, v248, v249\n" \
    "s_add_u32 s82, %[so20], 0xc00\ns_nop 0\nv_permlane16_swap_b32_e32 v148, v150\nv_permlane16_swap_b32_e32 v149, v151\nbuffer_store_dwordx4 v[148:151], v244, %[srdO2], s82 offen sc1\nv_accvgpr_read_b32 v250, a236\n" \
    "v_accvgpr_read_b32 v251, a237\nv_accvgpr_read_b32 v252, a238\nv_accvgpr_read_b32 v253, a239\nv_cvt_pk_bf16_f32 v156, v156, v157\nv_cvt_pk_bf16_f32 v157, v158, v159\nv_cvt_pk_bf16_f32 v158, v250, v251\n" \
    "v_cvt_pk_bf16_f32 v159, v252, v253\ns_add_u32 s82, %[so20], 0x5c00\ns_nop 0\nv_permlane16_swap_b32_e32 v156, v158\nv_permlane16_swap_b32_e32 v157, v159\nbuffer_store_dwordx4 v[156:159], v244, %[srdO2], s82 offen sc1\n" \
    "v_accvgpr_read_b32 v246, a240\nv_accvgpr_read_b32 v247, a241\nv_accvgpr_read_b32 v248, a242\nv_accvgpr_read_b32 v249, a243\nv_cvt_pk_bf16_f32 v164, v164, v165\nv_cvt_pk_bf16_f32 v165, v166, v167\n" \
    "v_cvt_pk_bf16_f32 v166, v246, v247\nv_cvt_pk_bf16_f32 v167, v248, v249\ns_add_u32 s82, %[so20], 0xac00\ns_nop 0\nv_permlane16_swap_b32_e32 v164, v166\nv_permlane16_swap_b32_e32 v165, v167\n" \
    "buffer_store_dwordx4 v[164:167], v244, %[srdO2], s82 offen sc1\nv_accvgpr_read_b32 v250, a244\nv_accvgpr_read_b32 v251, a245\nv_accvgpr_read_b32 v252, a246\nv_accvgpr_read_b32 v253, a247\n" \
    "v_cvt_pk_bf16_f32 v172, v172, v173\nv_cvt_pk_bf16_f32 v173, v174, v175\nv_cvt_pk_bf16_f32 v174, v250, v251\nv_cvt_pk_bf16_f32 v175, v252, v253\ns_add_u32 s82, %[so20], 0xfc00\ns_nop 0\n" \
    "v_permlane16_swap_b32_e32 v172, v174\nv_permlane16_swap_b32_e32 v173, v175\nbuffer_store_dwordx4 v[172:175], v244, %[srdO2], s82 offen sc1\nv_accvgpr_read_b32 v246, a248\nv_accvgpr_read_b32 v247, a249\n" \
    "v_accvgpr_read_b32 v248, a250\nv_accvgpr_read_b32 v249, a251\nv_cvt_pk_bf16_f32 v180, v180, v181\nv_cvt_pk_bf16_f32 v181, v182, v183\nv_cvt_pk_bf16_f32 v182, v246, v247\nv_cvt_pk_bf16_f32 v183, v248, v249\n" \
    "s_add_u32 s82, %[so20], 0x14c00\ns_nop 0\nv_permlane16_swap_b32_e32 v180, v182\nv_permlane16_swap_b32_e32 v181, v183\nbuffer_store_dwordx4 v[180:183], v244, %[srdO2], s82 offen sc1\nv_accvgpr_read_b32 v250, a252\n" \
    "v_accvgpr_read_b32 v251, a253\nv_accvgpr_read_b32 v252, a254\nv_accvgpr_read_b32 v253, a255\nv_cvt_pk_bf16_f32 v188, v188, v189\nv_cvt_pk_bf16_f32 v189, v190, v191\nv_cvt_pk_bf16_f32 v190, v250, v251\n" \
    "v_cvt_pk_bf16_f32 v191, v252, v253\ns_add_u32 s82, %[so20], 0x19c00\ns_nop 0\nv_permlane16_swap_b32_e32 v188, v190\nv_permlane16_swap_b32_e32 v189, v191\nbuffer_store_dwordx4 v[188:191], v244, %[srdO2], s82 offen sc1\n" \
    "v_accvgpr_read_b32 v246, a0\nv_accvgpr_read_b32 v247, a1\nv_accvgpr_read_b32 v248, a2\nv_accvgpr_read_b32 v249, a3\nv_cvt_pk_bf16_f32 v196, v196, v197\nv_cvt_pk_bf16_f32 v197, v198, v199\n" \
    "v_cvt_pk_bf16_f32 v198, v246, v247\nv_cvt_pk_bf16_f32 v199, v248, v249\ns_add_u32 s82, %[so20], 0x1000\ns_nop 0\nv_permlane16_swap_b32_e32 v196, v198\nv_permlane16_swap_b32_e32 v197, v199\n" \
    "buffer_store_dwordx4 v[196:199], v244, %[srdO2], s82 offen sc1\nv_accvgpr_read_b32 v250, a4\nv_accvgpr_read_b32 v251, a5\nv_accvgpr_read_b32 v252, a6\nv_accvgpr_read_b32 v253, a7\nv_cvt_pk_bf16_f32 v204, v204, v205\n" \
    "v_cvt_pk_bf16_f32 v205, v206, v207\nv_cvt_pk_bf16_f32 v206, v250, v251\nv_cvt_pk_bf16_f32 v207, v252, v253\ns_add_u32 s82, %[so20], 0x6000\ns_nop 0\nv_permlane16_swap_b32_e32 v204, v206\n" \
    "v_permlane16_swap_b32_e32 v205, v207\nbuffer_store_dwordx4 v[204:207], v244, %[srdO2], s82 offen sc1\nv_accvgpr_read_b32 v246, a8\nv_accvgpr_read_b32 v247, a9\nv_accvgpr_read_b32 v248, a10\nv_accvgpr_read_b32 v249, a11\n" \
    "v_cvt_pk_bf16_f32 v212, v212, v213\nv_cvt_pk_bf16_f32 v213, v214, v215\nv_cvt_pk_bf16_f32 v214, v246, v247\nv_cvt_pk_bf16_f32 v215, v248, v249\ns_add_u32 s82, %[so20], 0xb000\ns_nop 0\n" \
    "v_permlane16_swap_b32_e32 v212, v214\nv_permlane16_swap_b32_e32 v213, v215\nbuffer_store_dwordx4 v[212:215], v244, %[srdO2], s82 offen sc1\nv_accvgpr_read_b32 v250, a12\nv_accvgpr_read_b32 v251, a13\n" \
    "v_accvgpr_read_b32 v252, a14\nv_accvgpr_read_b32 v253, a15\nv_cvt_pk_bf16_f32 v220, v220, v221\nv_cvt_pk_bf16_f32 v221, v222, v223\nv_cvt_pk_bf16_f32 v222, v250, v251\nv_cvt_pk_bf16_f32 v223, v252, v253\n" \
    "s_add_u32 s82, %[so20], 0x10000\ns_nop 0\nv_permlane16_swap_b32_e32 v220, v222\nv_permlane16_swap_b32_e32 v221, v223\nbuffer_store_dwordx4 v[220:223], v244, %[srdO2], s82 offen sc1\nv_accvgpr_read_b32 v246, a24\n" \
    "v_accvgpr_read_b32 v247, a25\nv_accvgpr_read_b32 v248, a26\nv_accvgpr_read_b32 v249, a27\nv_accvgpr_read_b32 v250, a16\nv_accvgpr_read_b32 v251, a17\nv_accvgpr_read_b32 v252, a18\nv_accvgpr_read_b32 v253, a19\n" \
    "v_cvt_pk_bf16_f32 v246, v246, v247\nv_cvt_pk_bf16_f32 v247, v248, v249\nv_cvt_pk_bf16_f32 v248, v250, v251\nv_cvt_pk_bf16_f32 v249, v252, v253\ns_add_u32 s82, %[so20], 0x15000\ns_nop 0\n" \
    "v_permlane16_swap_b32_e32 v246, v248\nv_permlane16_swap_b32_e32 v247, v249\nbuffer_store_dwordx4 v[246:249], v244, %[srdO2], s82 offen sc1\ns_nop 1\nv_accvgpr_read_b32 v246, a28\nv_accvgpr_read_b32 v247, a29\n" \
    "v_accvgpr_read_b32 v248, a30\nv_accvgpr_read_b32 v249, a31\nv_accvgpr_read_b32 v250, a20\nv_accvgpr_read_b32 v251, a21\nv_accvgpr_read_b32 v252, a22\nv_accvgpr_read_b32 v253, a23\nv_cvt_pk_bf16_f32 v246, v246, v247\n" \
    "v_cvt_pk_bf16_f32 v247, v248, v249\nv_cvt_pk_bf16_f32 v248, v250, v251\nv_cvt_pk_bf16_f32 v249, v252, v253\ns_add_u32 s82, %[so20], 0x1a000\ns_nop 0\nv_permlane16_swap_b32_e32 v246, v248\n" \
    "v_permlane16_swap_b32_e32 v247, v249\nbuffer_store_dwordx4 v[246:249], v244, %[srdO2], s82 offen sc1\ns_nop 1\ns_waitcnt vmcnt(0) lgkmcnt(0)\n"
namespace g9 {
struct Lane { unsigned xb[4], wb[8], vx[4], vw[4], vst, m0x[4], m0w; };
template <int MBW, int NBW, bool XBLK, int W0> __host__ __device__ constexpr __forceinline__ void setup(Lane& L, unsigned ldsb, int wid, int lane, unsigned lda, unsigned ldb) {
    constexpr int RB = NBW * 64, RPW = MBW * 8;
    lane &= 63;
    const int wm = wid & 1, wn = wid >> 1, r = lane & 15, kq = lane >> 4, q = r >> 2;
    if (XBLK) {
        const unsigned o = ldsb + (unsigned)((wm * MBW * 16 + r) * 16);
        L.xb[0] = o + (unsigned)(kq * 5120); L.xb[2] = L.xb[0] + 30720u;
        L.xb[1] = o + (unsigned)(kq < 2 ? 20480 + kq * 5120 : kq == 2 ? 90112 : 131072); L.xb[3] = o + (unsigned)(kq < 2 ? 51200 + kq * 5120 : 109568 + kq * 5120);
        L.m0x[0] = ldsb + (unsigned)(wid < 3 ? wid * 10240 : 90112); L.m0x[1] = ldsb + (unsigned)(wid < 3 ? wid * 10240 + 5120 : 131072);
        L.m0x[2] = ldsb + (unsigned)(wid < 3 ? 30720 + wid * 10240 : 119808); L.m0x[3] = L.m0x[2] + 5120u;
    } else {
        L.xb[0] = ldsb + (unsigned)((wm * MBW * 16 + r) * 128 + 16 * (kq ^ (r >> 1))); L.xb[1] = L.xb[0] ^ 64u; L.xb[2] = L.xb[3] = 0;
        L.m0x[0] = ldsb + (unsigned)(wid * RPW * 128); L.m0x[1] = L.m0x[2] = L.m0x[3] = 0;
    }
    const int g = RB == 512 ? (q | ((kq & 1) << 2)) : ((q >> 1) | ((kq & 1) << 1));
#pragma unroll
    for (int nb = 0; nb < 8; ++nb) L.wb[nb] = ldsb + (unsigned)(W0 + (8 * kq + q) * RB + (((wn * NBW + nb) ^ g) * 32) + (lane & 3) * 8);
    const int b0 = (wid & 1) + 4 * (wid >> 1);
#pragma unroll
    for (int p = 0; p < 4; ++p) {
        const int row = RPW * wid + 8 * p + (lane >> 3), c = (lane & 7) ^ ((row >> 1) & 7);
        L.vx[p] = XBLK ? (unsigned)(4096 + wid * 20480 + lane * 16) : (unsigned)(4096 - 1024 * p) + (unsigned)row * lda + (unsigned)c * 16u;
        const int b = p * 1024 + lane * 16, rl = b / RB, pos = (b % RB) >> 4, gw = RB == 512 ? ((rl & 3) | ((b0 & 1) << 2)) : (((rl >> 1) & 1) | ((b0 & 1) << 1));
        L.vw[p] = (unsigned)(4096 - 1024 * p) + (unsigned)(8 * b0 + rl) * ldb + (unsigned)((pos ^ (gw << 1)) * 16);
    }
    L.vst = (unsigned)(r * 64 + ((kq & 1) * 2 + (kq >> 1)) * 16);
    L.m0w = ldsb + (unsigned)(8 * b0 * RB);
}
__device__ __forceinline__ __amdgpu_buffer_rsrc_t srd(const void* p, size_t off, size_t bytes) {
    return __builtin_amdgcn_make_buffer_rsrc((void*)((const char*)p + off - 4096), (short)0, (int)(bytes - off + 4096), 0x00020000);
}
struct Tab { unsigned v[2][4][5][64][4]; };
constexpr Tab make_tab() {
    Tab t = {};
    for (int var = 0; var < 2; ++var) for (int wid = 0; wid < 4; ++wid) for (int lane = 0; lane < 64; ++lane) {
        Lane B = {};
        if (var == 0) setup<8, 6, false, 131072>(B, 0u, wid, lane, M_ * 2, HD_ * 2); else setup<10, 6, true, 139264>(B, 0u, wid, lane, 0u, F_ * 2);
        const unsigned mbox = var == 0 ? 155648u : 136192u;
        const unsigned a[20] = {(unsigned)((var == 0 ? lane & 31 : lane) * 8), mbox, mbox + (unsigned)wid * 4u, B.xb[0], B.xb[1], B.xb[2], B.xb[3], B.wb[0], B.wb[1], B.wb[2], B.wb[3], B.wb[4], B.wb[5],
                                B.vx[0], B.vx[1], B.vx[2], B.vx[3], B.vw[0], B.vw[1], B.vw[2]};
        for (int k = 0; k < 20; ++k) t.v[var][wid][k >> 2][lane][k & 3] = a[k];
    }
    return t;
}
static __constant__ const Tab tab = make_tab();
template <class H> __device__ __forceinline__ void ln_part1(int blk, unsigned (&w)[2][5], __amdgpu_buffer_rsrc_t srdN, int wid, int lane, H&& hook) {
    ln2(w, hook);
#pragma unroll
    for (int e = 0; e < 2; ++e) {
        const unsigned ro = (unsigned)((blk * 8 + wid * 2 + e) * 1280);
        __builtin_amdgcn_raw_buffer_store_b128((u32x4){w[e][0], w[e][1], w[e][2], w[e][3]}, srdN, (unsigned)(lane * 16), ro, 16);
        __builtin_amdgcn_raw_buffer_store_b32(w[e][4], srdN, (unsigned)(1024 + lane * 4), ro, 16);
    }
}
template <class H> __device__ __forceinline__ void ln_part2(int blk, unsigned (&w)[2][5], __amdgpu_buffer_rsrc_t srdH, LAS unsigned* tile, int wid, int lane, H&& hook) {
    ln2(w, hook);
    {
        u32x4 a, b; u32x2 c;
#pragma unroll
        for (int i = 0; i < 2; ++i) {
            a[2 * i] = __builtin_amdgcn_perm(w[1][i], w[0][i], 0x05040100u); a[2 * i + 1] = __builtin_amdgcn_perm(w[1][i], w[0][i], 0x07060302u);
            b[2 * i] = __builtin_amdgcn_perm(w[1][2 + i], w[0][2 + i], 0x05040100u); b[2 * i + 1] = __builtin_amdgcn_perm(w[1][2 + i], w[0][2 + i], 0x07060302u);
        }
        c[0] = __builtin_amdgcn_perm(w[1][4], w[0][4], 0x05040100u); c[1] = __builtin_amdgcn_perm(w[1][4], w[0][4], 0x07060302u);
        LAS unsigned* tp = tile + wid * 648;
        *(LAS u32x4*)(tp + lane * 8) = a; *(LAS u32x4*)(tp + lane * 8 + 4) = b; *(LAS u32x2*)(tp + 512 + lane * 2) = c;
    }
    WAIT_L(0); BAR;
    u32x4 o[3];
#pragma unroll
    for (int i = 0; i < 3; ++i) {
        const int d = i < 2 ? wid * 128 + i * 64 + lane : 512 + (wid & 1) * 64 + lane;
#pragma unroll
        for (int e = 0; e < 4; ++e) o[i][e] = tile[e * 648 + d];
    }
#pragma unroll
    for (int i = 0; i < 3; ++i) {
        const int d = i < 2 ? wid * 128 + i * 64 + lane : 512 + (wid & 1) * 64 + lane;
        __builtin_amdgcn_raw_buffer_store_b128(o[i], srdH, (unsigned)(d * 16), (i == 2 && wid > 1) ? 0x70000000u : (unsigned)(blk * 10240), 16);
    }
    WAIT_L(0);
}
}
#define G9_SCL "s80", "s81", "s82", "s83", "s84", "s85", "s86", "s87", "s88", "s90", "s91", "s92", "s93"
#define G9_VIO(L) "+{v224}"(L.xb[0]), "+{v225}"(L.xb[1]), "+{v226}"(L.xb[2]), "+{v227}"(L.xb[3]), "+{v228}"(L.wb[0]), "+{v229}"(L.wb[1]), "+{v230}"(L.wb[2]), "+{v231}"(L.wb[3]), \
    "+{v232}"(L.wb[4]), "+{v233}"(L.wb[5]), "+{v234}"(L.wb[6]), "+{v235}"(L.wb[7]), "+{v236}"(L.vx[0]), "+{v237}"(L.vx[1]), "+{v238}"(L.vx[2]), "+{v239}"(L.vx[3]), \
    "+{v240}"(L.vw[0]), "+{v241}"(L.vw[1]), "+{v242}"(L.vw[2]), "+{v243}"(L.vw[3]), "+{v244}"(L.vst)
#define G9_INB [srdX2] "s"(srdX2), [srdW2] "s"(srdW2), [srdO2] "s"(srdO2), [m0x20] "s"(mx2[0]), [m0x21] "s"(mx2[1]), [m0x22] "s"(mx2[2]), [m0x23] "s"(mx2[3]), [m0w2] "s"(mw2), \
    [so20] "s"(so2[0]), [so21] "s"(so2[1]), [so22] "s"(so2[2]), [scl] "s"(scl)
#define G9_ST

#define FILLPOS 2
#define FILL_NA 1
#define FILL2_AUX 16
namespace wsl {
constexpr size_t XN = 8192, HB = XN + (size_t)S_ * M_ * 2, QO = HB + (size_t)S_ * M_ * 2, KO = QO + (size_t)H_ * S_ * D_ * 2, VO = KO + (size_t)H_ * S_ * D_ * 2,
                 UP = VO + (size_t)H_ * S_ * D_ * 2, GT = UP + (size_t)4 * M_ * F_ * 2, CT = GT + (size_t)F_ * M_ * 2, PA = CT + (size_t)HD_ * S_ * 2,
                 PX = PA + (size_t)4 * S_ * M_ * 2, FL = PX + (size_t)128 * 2 * 69632, CN = FL + 4096;
}
__global__ __launch_bounds__(256, 1) void k_proj(const u16* __restrict__ x, const u16* __restrict__ ckv, const u16* __restrict__ wuk, const u16* __restrict__ wuv,
                                                 const u16* __restrict__ wq, const u16* __restrict__ w1, u16* __restrict__ xn, u16* __restrict__ hb, u16* __restrict__ ko, u16* __restrict__ vo,
                                                 u16* __restrict__ qo, u16* __restrict__ upart, unsigned* __restrict__ flags) {
    extern __shared__ __attribute__((aligned(16))) unsigned char shm_[];
    LAS unsigned char* lds = (LAS unsigned char*)shm_;
    const unsigned long long tlo = __builtin_amdgcn_s_memrealtime(), thi = tlo + (1ull << 24);
    const int wid = __builtin_amdgcn_readfirstlane(threadIdx.x >> 6);
    const int c_ = blockIdx.x, xcd = c_ & 7, j = c_ >> 3, pm = (xcd & 1) * 4 + (j & 3), pn = (xcd >> 1) * 8 + (j >> 2), m0 = pm * 256, n0 = (pn & 15) * 256;
    const unsigned ldsb = __builtin_amdgcn_readfirstlane((unsigned)(uintptr_t)lds);
    const int lane = lane_id();
    u32x4 xa[2]; unsigned xw[2][5];
    { const char* xp = (const char*)x + (size_t)(c_ * 8 + wid * 2) * 1280;
      asm volatile("global_load_dwordx4 %0, %4, off\n\tglobal_load_dword %1, %5, off offset:1024\n\tglobal_load_dwordx4 %2, %4, off offset:1280\n\tglobal_load_dword %3, %5, off offset:2304"
                   : "=&v"(xa[0]), "=&v"(xw[0][4]), "=&v"(xa[1]), "=&v"(xw[1][4]) : "v"(xp + lane * 16), "v"(xp + lane * 4) : "memory"); }
    g9::Lane L, L1, L2;
    g9::setup<8, 8, false, 65536>(L, ldsb, wid, lane, DC_ * 2, HD_ * 2);
    const __amdgpu_buffer_rsrc_t srdX = g9::srd(ckv, (size_t)m0 * (DC_ * 2), (size_t)S_ * DC_ * 2);
    const __amdgpu_buffer_rsrc_t srdW = g9::srd((pn >> 4) ? wuv : wuk, (size_t)n0 * 2, (size_t)DC_ * HD_ * 2);
#define PD(K, ...) asm volatile("s_add_u32 m0, %[mb], %[mo]\n\ts_mov_b32 s81, %[so]\n\tbuffer_load_dwordx4 %[vo], %[srd], s81 offen offset:%[of] lds" : __VA_ARGS__ \
        : [mb] "s"(((K) & 8) ? L.m0x[0] : L.m0w), [mo] "i"(((K) & 8) ? ((K) >> 4) * 0x8000 + (((K) >> 2) & 1) * 0x1000 : 0x10000 + ((K) >> 4) * 0x8000 + (((K) >> 2) & 1) * 0x2000), \
          [so] "i"(((K) & 8) ? ((K) >> 4) * 0x80 + (((K) >> 2) & 1) * 0x8000 : ((K) >> 4) * 0x80000 + (((K) >> 2) & 1) * 0x20000), [vo] "v"(((K) & 8) ? L.vx[(K) & 3] : L.vw[(K) & 3]), \
          [srd] "s"(((K) & 8) ? srdX : srdW), [of] "i"(((K) & 3) * 1024) : "memory", "scc", "m0", "s81")
#define PS(...) asm volatile("" : __VA_ARGS__)
    int ll = lane, jj = j, xc = xcd, ww = wid;
    PD(0, "+v"(ll));
    g9::setup<8, 8, false, 65536>(L1, ldsb, wid, ll, DC_ * 2, HD_ * 2);
    PD(1, "+v"(L1.vx[0]), "+v"(L1.vx[1]));
    PD(2, "+v"(L1.vx[2]), "+v"(L1.vx[3]), "+v"(ll));
#pragma unroll
    for (int p = 0; p < 4; ++p) L.vx[p] = L1.vx[p];
    g9::setup<8, 8, false, 65536>(L2, ldsb, wid, ll, DC_ * 2, HD_ * 2);
    PD(3, "+v"(L2.xb[0]), "+v"(L2.xb[1]), "+v"(L2.vst));
    PD(4, "+v"(L2.wb[0]), "+v"(L2.wb[1]), "+v"(L2.wb[2]), "+v"(L2.wb[3]));
    PS("+s"(jj), "+s"(xc), "+s"(ww)); PD(5, "+v"(L2.wb[4]), "+v"(L2.wb[5]), "+v"(L2.wb[6]), "+v"(L2.wb[7]));
#pragma unroll
    for (int p = 0; p < 8; ++p) L.wb[p] = L2.wb[p];
    L.xb[0] = L2.xb[0]; L.xb[1] = L2.xb[1]; L.vst = L2.vst;
    unsigned sx = 0x100, sw = 0x100000;
    const bool isq = jj < 22;
    int qm0 = (2 * (xc & 3) + (jj & 1)) * 256; const int nt = 11 * (xc >> 2) + (jj >> 1); int qn0 = nt == 21 ? 3904 : nt * 192;
    int ks = xc >> 1, um0 = (xc & 1) * 320, un0 = (jj == 31 ? 0 : jj - 22) * 192;
    PS("+s"(qm0), "+s"(qn0), "+s"(ks), "+s"(um0), "+s"(un0)); PD(6);
    unsigned ox2 = isq ? (unsigned)(qm0 * (M_ * 2)) : (unsigned)(ks * 655360 + um0 * 16), ow2 = isq ? (unsigned)(qn0 * 2) : (unsigned)(ks * 512 * (F_ * 2) + un0 * 2);
    const char* px2 = (const char*)(isq ? xn : hb) + ox2 - 4096;
    unsigned nx2 = (unsigned)(S_ * M_ * 2 + 4096) - ox2;
    PS("+s"(px2), "+s"(nx2), "+s"(ow2)); PD(7);
    const char* pw2 = (const char*)(isq ? wq : w1) + ow2 - 4096;
    unsigned nw2 = (unsigned)((isq ? M_ * HD_ * 2 : S_ * F_ * 2) + 4096) - ow2;
    const char* po2 = (const char*)(isq ? qo : upart);
    unsigned no2 = isq ? HD_ * S_ * 2 : 4 * F_ * M_ * 2;
    PS("+s"(pw2), "+s"(nw2), "+s"(po2), "+s"(no2), "+s"(qn0)); PD(8);
    const float scl = 0.08838834764831845f * 1.4426950408889634f;
    unsigned so2[3], mx2[4], mw2, sx2 = 0, sw2 = 0, cnt = 0, sel = isq ? 1u : jj < 31 ? 2u : 0u;
#pragma unroll
    for (int p = 0; p < 3; ++p) { const int nb = (qn0 >> 5) + (ww >> 1) * 3 + p; so2[p] = (unsigned)((nb >> 2) * 524288 + ((qm0 >> 4) + (ww & 1) * 8) * 4096 + (nb & 3) * 1024); }
    PS("+s"(so2[0]), "+s"(so2[1])); PD(9);
    PS("+s"(so2[2]), "+s"(un0)); PD(10);
    if (!isq) so2[0] = (unsigned)(ks * (F_ * M_ * 2) + (((un0 >> 4) + (ww >> 1) * 6) * 20 + (um0 >> 5) + (ww & 1) * 5) * 1024);
    if (ldsb != 0) sel = 0;
    PS("+s"(so2[0]), "+s"(sel), "+s"(ww)); PD(11);
    mx2[0] = ldsb + (unsigned)(isq ? ww * 8192 : ww < 3 ? ww * 10240 : 90112); mx2[1] = ldsb + (unsigned)(ww < 3 ? ww * 10240 + 5120 : 131072);
    mx2[2] = ldsb + (unsigned)(ww < 3 ? 30720 + ww * 10240 : 119808); mx2[3] = mx2[2] + 5120u;
    mw2 = ldsb + (unsigned)(((ww & 1) + 4 * (ww >> 1)) * 3072);
    PS("+s"(mx2[0]), "+s"(mx2[1]), "+s"(mx2[2]), "+s"(mx2[3]), "+s"(mw2), "+s"(ww), "+s"(jj)); PD(12);
    const __amdgpu_buffer_rsrc_t srdN = __builtin_amdgcn_make_buffer_rsrc((void*)xn, (short)0, S_ * M_ * 2, 0x00020000);
    const __amdgpu_buffer_rsrc_t srdH = __builtin_amdgcn_make_buffer_rsrc((void*)hb, (short)0, S_ * M_ * 2, 0x00020000);
    const __amdgpu_buffer_rsrc_t srdX2 = __builtin_amdgcn_make_buffer_rsrc((void*)px2, (short)0, (int)nx2, 0x00020000);
    const __amdgpu_buffer_rsrc_t srdW2 = __builtin_amdgcn_make_buffer_rsrc((void*)pw2, (short)0, (int)nw2, 0x00020000);
    const __amdgpu_buffer_rsrc_t srdO2 = __builtin_amdgcn_make_buffer_rsrc((void*)po2, (short)0, (int)no2, 0x00020000);
    {
        unsigned so0 = (unsigned)(((n0 >> 7) + (ww >> 1)) * 524288 + ((m0 >> 4) + (ww & 1) * 8) * 4096), fso = ww == 0 ? (unsigned)(c_ * 8) : 0x70000000u;
        unsigned flo = (unsigned)(isq ? qm0 : ks * 512), to = (unsigned)((isq ? 0 : 20480) + ww * 5120);
        const bool p1 = jj < 18;
        PS("+s"(so0), "+s"(fso), "+s"(flo), "+s"(to), "+s"(jj)); PD(13);
        unsigned sp = p1 ? (unsigned)(ks * 512 * (F_ * 2) + ((jj * 4 + ww) * 2 + (xc & 1)) * 1024) : (unsigned)(((jj - 18) * 4 + ww) * (HD_ * 2) + xc * 1024);
        unsigned spst = p1 ? 147456u : 56u * (HD_ * 2), np = p1 ? S_ * F_ * 2 : M_ * HD_ * 2;
        const char* pp = (const char*)(p1 ? w1 : wq);
        PS("+s"(sp), "+s"(spst), "+s"(np), "+s"(pp)); PD(14);
        const char* po = (const char*)((pn >> 4) ? vo : ko);
        PS("+s"(po)); PD(15);
        const unsigned kpx = (unsigned)((pn * 8 + ww * 2) * 1024 + 4096), kpw = (unsigned)((128 + 2 * (pm * 4 + ww)) * (HD_ * 2) + 4096);
#define KP(I) do { if constexpr ((I) < 2) asm volatile("buffer_load_dwordx4 a[168:171], %0, %1, %2 offen offset:%3" :: "v"(lane * 16), "s"(srdX), "s"(kpx), "i"((I) * 1024) : "memory", ACL_HI); \
        else asm volatile("s_add_u32 s81, %2, %3\n\tbuffer_load_dwordx4 a[168:171], %0, %1, s81 offen" :: "v"((lane >> 5) * (HD_ * 2) + (lane & 31) * 16), "s"(srdW), "s"(kpw), "i"(((I) - 2) * 64 * HD_ * 2) : "memory", "scc", "s81", ACL_HI); } while (0)
#define KPR(LO, HI) do { if constexpr (LO <= 0 && 0 < HI) KP(0); if constexpr (LO <= 1 && 1 < HI) KP(1); if constexpr (LO <= 2 && 2 < HI) KP(2); if constexpr (LO <= 3 && 3 < HI) KP(3); \
        if constexpr (LO <= 4 && 4 < HI) KP(4); if constexpr (LO <= 5 && 5 < HI) KP(5); if constexpr (LO <= 6 && 6 < HI) KP(6); if constexpr (LO <= 7 && 7 < HI) KP(7); } while (0)
        KPR(0, KPE);
        const __amdgpu_buffer_rsrc_t srdO = __builtin_amdgcn_make_buffer_rsrc((void*)po, (short)0, HD_ * S_ * 2, 0x00020000);
        const __amdgpu_buffer_rsrc_t srdF = __builtin_amdgcn_make_buffer_rsrc((void*)(flags + 256), (short)0, 2048, 0x00020000);
        const __amdgpu_buffer_rsrc_t srdP = __builtin_amdgcn_make_buffer_rsrc((void*)pp, (short)0, (int)np, 0x00020000);
        const unsigned so[4] = {so0, so0 + 1024u, so0 + 2048u, so0 + 3072u};
        asm volatile("s_waitcnt vmcnt(%4)" : "+v"(xa[0]), "+v"(xw[0][4]), "+v"(xa[1]), "+v"(xw[1][4]) : "i"(16 + KPE) : "memory");
#pragma unroll
        for (int i = 0; i < 4; ++i) { xw[0][i] = xa[0][i]; xw[1][i] = xa[1][i]; }
        const void* tabp = &g9::tab;
        g9::ln_part1(c_, xw, srdN, wid, lane, [L = L, srdX = srdX, srdW = srdW, tv = lane * 16 + to + 2048, tabp](auto k, auto& a, auto& b) {
            constexpr int K = decltype(k)::value;
            PD(16 + K, "+v"(a), "+v"(b));
            if constexpr (K < 5) asm volatile("global_load_dwordx4 a[%2:%3], %0, %1 offset:%4" :: "v"(tv), "s"(tabp), "i"(236 + 4 * K), "i"(239 + 4 * K), "i"(1024 * K - 2048) : "memory", ACL_HI); });
        if (c_ == 0 && wid < 2) flags[wid * 64 + lane] = 0u;
        g9::ln_part2(c_, xw, srdH, (LAS unsigned*)(lds + 131072), wid, lane, [L = L, srdX = srdX, srdW = srdW](auto k, auto& a, auto& b) { PD(24 + decltype(k)::value, "+v"(a), "+v"(b)); });
        KPR(KPE, NKP);
        asm volatile(G10_MAIN : G9_ST [sx] "+s"(sx), [sw] "+s"(sw), [sx2] "+s"(sx2), [sw2] "+s"(sw2), [cnt] "+s"(cnt), [sel] "+s"(sel), [fso] "+s"(fso), [sp] "+s"(sp), G9_VIO(L)
            : [srdX] "s"(srdX), [srdW] "s"(srdW), [srdO] "s"(srdO), [srdF] "s"(srdF), [m0x] "s"(L.m0x[0]), [m0w] "s"(L.m0w), [so0] "s"(so[0]), [so1] "s"(so[1]), [so2] "s"(so[2]), [so3] "s"(so[3]),
              [tlo] "s"(tlo), [thi] "s"(thi), [flo] "s"(flo), [srdP] "s"(srdP), [spst] "s"(spst), G9_INB : "memory", "scc", "m0", G9_SCL, VCL9, ACL_LO, ACL_HI);
    }
    if (jj == 31 && lane_id() < 8) flags[1024 + ((xc * 4 + wid) * 8 + lane_id()) * 64] = 0u;
    if (jj == 31) {
        const __amdgpu_buffer_rsrc_t srdE = __builtin_amdgcn_make_buffer_rsrc((void*)((char*)flags - (wsl::FL - wsl::PA)), (short)0, 512 * 15360, 0x00020000);
        const unsigned vo_ = (unsigned)(lane_id() * 16), sb_ = (unsigned)((xc * 4 + wid) * ((60 - 8 * FILL_NA) * 4096));
#pragma unroll 1
        for (int i = 0; i < 60 - 8 * FILL_NA; ++i) {
#pragma unroll
            for (int p = 0; p < 4; ++p) __builtin_amdgcn_raw_buffer_store_b128((u32x4){~0u, ~0u, ~0u, ~0u}, srdE, vo_ + p * 1024, sb_ + (unsigned)i * 4096u, FILL2_AUX);
        }
    }
    if (sel == 0 && jj < 31) {
        const int lane = lane_id(), b0 = isq ? qm0 >> 3 : ks * 64, nb = isq ? 32 : 64;
#pragma unroll 1
        for (int b = b0; b < b0 + nb; ++b) {
            unsigned w[2][5];
            const char* xp = (const char*)x + (size_t)(b * 8 + wid * 2) * 1280;
#pragma unroll
            for (int e = 0; e < 2; ++e) { const u32x4 t = *(const u32x4*)(xp + e * 1280 + lane * 16); w[e][0] = t[0]; w[e][1] = t[1]; w[e][2] = t[2]; w[e][3] = t[3]; w[e][4] = *(const unsigned*)(xp + e * 1280 + 1024 + lane * 4); }
            auto nohook = [](auto, auto&, auto&) {};
            g9::ln_part1(b, w, srdN, wid, lane, nohook);
            g9::ln_part2(b, w, srdH, (LAS unsigned*)lds, wid, lane, nohook);
            BAR;
        }
        WAIT_V(0); BAR;
        g9::Lane B;
        if (isq) {
            g9::setup<8, 6, false, 131072>(B, ldsb, wid, lane_id(), M_ * 2, HD_ * 2);
            asm volatile(G10_ALONE_Q : [sx2] "+s"(sx2), [sw2] "+s"(sw2), [cnt] "+s"(cnt), G9_VIO(B) : G9_INB : "memory", "scc", "m0", G9_SCL, VCL9, ACL_LO, ACL_HI);
        } else {
            g9::setup<10, 6, true, 139264>(B, ldsb, wid, lane_id(), 0, F_ * 2);
            asm volatile(G10_ALONE_U : [sx2] "+s"(sx2), [sw2] "+s"(sw2), [cnt] "+s"(cnt), G9_VIO(B) : G9_INB : "memory", "scc", "m0", G9_SCL, VCL9, ACL_LO, ACL_HI);
        }
    }
}

#define CTX_AUX 16
#define CLRW 1
#define FILL_AUX 16
#define VCL "v0","v1","v2","v3","v4","v5","v6","v7","v8","v9","v10","v11","v12","v13","v14","v15","v16","v17","v18","v19","v20","v21","v22","v23","v24","v25","v26","v27","v28","v29","v30","v31","v32","v33","v34","v35","v36","v37","v38","v39","v40","v41","v42","v43","v44","v45","v46","v47","v48","v49","v50","v51","v52","v53","v54","v55","v56","v57","v58","v59","v60","v61","v62","v63","v64","v65","v66","v67","v68","v69","v70","v71","v72","v73","v74","v75","v76","v77","v78","v79","v80","v81","v82","v83","v84","v85","v86","v87","v88","v89","v90","v91","v92","v93","v94","v95","v96","v97","v98","v99","v100","v101","v102","v103","v104","v105","v106","v107","v108","v109","v110","v111","v112","v113","v114","v115","v116","v117","v118","v119","v120","v121","v122","v123","v124","v125","v126","v127","v128","v129","v130","v131","v132","v133","v134","v135","v136","v137","v138","v139","v140","v141","v142","v143","v144","v145","v146","v147","v148","v149","v150","v151","v152","v153","v154","v155","v156","v157","v158","v159","v160","v161","v162","v163","v164","v165","v166","v167","v168","v169","v170","v171","v172","v173","v174","v175","v176","v177","v178","v179","v180","v181","v182","v183","v184","v185","v186","v187","v188","v189","v190","v191","v192","v193","v194","v195","v196","v197","v198","v199","v200","v201","v202","v203","v204","v205","v206","v207","v208","v209","v210","v211","v212","v213","v214","v215","v216","v217","v218","v219","v220","v221","v222","v223","v224","v225","v226","v227","v228","v229","v230","v231","v232","v233","v234","v235","v236","v237","v238","v239","v243","v245"
#define DI __device__ __forceinline__
template <int... Is> struct iseq {};
template <int V> struct icst { static constexpr int value = V; };
template <int N, int... Is> struct mkseq : mkseq<N - 1, N - 1, Is...> {};
template <int... Is> struct mkseq<0, Is...> { typedef iseq<Is...> type; };
namespace at {
constexpr int KOFF = 0, VOFF = 32768, XOFF = 65536, BOFF = XOFF + 4096, UOFF = XOFF + 8192, LDS_BYTES = UOFF + 4 * 20480 + 1024;
typedef float f32x2 __attribute__((ext_vector_type(2)));
typedef __bf16 bf2_t __attribute__((ext_vector_type(2)));
DI constexpr int crow(int r, int hi) { return (r & 3) + 8 * (r >> 2) + 4 * hi; }
constexpr int AO_(int qb, int c) { return 16 * (4 * qb + c); }
constexpr int AQ_(int qb, int d0) { return 128 + 32 * qb + 4 * d0; }
constexpr int AK_(int kb, int d0) { return 192 + 32 * kb + 4 * d0; }
DI unsigned cvtpk(float lo, float hi) { f32x2 v = {lo, hi}; bf2_t b = __builtin_convertvector(v, bf2_t); return __builtin_bit_cast(unsigned, b); }
DI float swap_add(float x) { auto r = __builtin_amdgcn_permlane32_swap(__float_as_uint(x), __float_as_uint(x), false, false); return __uint_as_float(r[0]) + __uint_as_float(r[1]); }
template <int D0> DI void k_read(int kb0) {
    constexpr int a0 = AK_(0, D0), a1 = AK_(1, D0);
    asm volatile("ds_read_b128 a[%c1:%c2], %0\n\tds_read_b128 a[%c3:%c4], %0 offset:8192" :: "v"(kb0 ^ (32 * D0)), "i"(a0), "i"(a0 + 3), "i"(a1), "i"(a1 + 3) : "memory", ACL_HI);
}
template <int... Is> DI void k_reads(int kb0, iseq<Is...>) { (k_read<Is>(kb0), ...); }
#define AT_DMA4(m0v_, v0_, v1_, v2_, v3_, srd_, so_) asm volatile("s_mov_b32 m0, %0\n\ts_nop 0\n\tbuffer_load_dwordx4 %1, %5, %6 offen lds\n\tbuffer_load_dwordx4 %2, %5, %6 offen offset:1024 lds\n\t" \
        "buffer_load_dwordx4 %3, %5, %6 offen offset:2048 lds\n\tbuffer_load_dwordx4 %4, %5, %6 offen offset:3072 lds" :: "s"(m0v_), "v"(v0_), "v"(v1_), "v"(v2_), "v"(v3_), "s"(srd_), "s"(so_) : "memory", "m0")
template <int N> DI float acc_read() { float r; asm volatile("v_accvgpr_read_b32 %0, a%c1" : "=v"(r) : "i"(N)); return r; }
template <int B, int... Is> DI void acc_read16(float (&o)[16], iseq<Is...>) { ((o[Is] = acc_read<B + Is>()), ...); }
template <int I> DI void q_load1(const char* p0, const char* p1) {
    constexpr int qb = I >> 3, d0 = I & 7, aq = AQ_(qb, d0);
    asm volatile("global_load_dwordx4 a[%c1:%c2], %0, off offset:%c3" :: "v"(qb ? p1 : p0), "i"(aq), "i"(aq + 3), "i"((d0 >> 1) * 1024 + (d0 & 1) * 32) : "memory", ACL_HI);
}
template <int... Is> DI void q_load(const char* p0, const char* p1, iseq<Is...>) { (q_load1<Is>(p0, p1), ...); }
}

__global__ __launch_bounds__(256, 1) void k_attn(char* __restrict__ ws, const u16* __restrict__ b1, const u16* __restrict__ wo, const u16* __restrict__ w2) {
    const u16* const Q = (const u16*)(ws + wsl::QO); const u16* const K = (const u16*)(ws + wsl::KO); const u16* const V = (const u16*)(ws + wsl::VO);
    u16* const ctxT = (u16*)(ws + wsl::CT); const u16* const upart = (const u16*)(ws + wsl::UP); u16* const gT = (u16*)(ws + wsl::GT);
    u16* const px = (u16*)(ws + wsl::PX); unsigned* const flags = (unsigned*)(ws + wsl::FL); u16* const part = (u16*)(ws + wsl::PA);
    using namespace at;
    extern __shared__ __attribute__((aligned(16))) unsigned char shm_[];
    LAS unsigned char* lds = (LAS unsigned char*)shm_;
    const int tid = threadIdx.x;
    int wid = __builtin_amdgcn_readfirstlane(tid >> 6);
    int c_ = blockIdx.x, xcd = c_ & 7, jj = c_ >> 3, head = xcd * 4 + (jj >> 3), role = (jj >> 2) & 1, rr = jj & 3;
    int sp = 14 - 4 * rr, q0 = 7 - rr;
    const int o0 = role ? 0 : sp;
    const char* Qh = (const char*)(Q + (size_t)head * S_ * D_);
    const __amdgpu_buffer_rsrc_t srdK = __builtin_amdgcn_make_buffer_rsrc((void*)((const char*)(K + (size_t)head * S_ * D_) - 4096), (short)0, S_ * D_ * 2 + 4096, 0x00020000);
    const __amdgpu_buffer_rsrc_t srdV = __builtin_amdgcn_make_buffer_rsrc((void*)(V + (size_t)head * S_ * D_), (short)0, S_ * D_ * 2, 0x00020000);
    const unsigned ldsb = __builtin_amdgcn_readfirstlane((unsigned)(uintptr_t)lds), m0k = ldsb + KOFF + wid * 4096;
    LAS unsigned* xw = (LAS unsigned*)(lds + XOFF + 2048);
    unsigned sk0 = (unsigned)o0 * 16384u, sk1 = sk0 + 16384u;
    {
        const int lane = tid & 63;
        unsigned vk[4];
#pragma unroll
        for (int p = 0; p < 4; ++p) vk[p] = (unsigned)(4096 - 768 * p + wid * 4096 + ((((lane >> 2) & 3) ^ p) << 10) + (lane >> 4) * 64 + (((lane & 3) ^ (lane >> 4)) << 4));
        const unsigned vv = (unsigned)(wid * 4096 + lane * 16);
        { const char* qp0 = Qh + (size_t)((q0 * 16 + wid * 2 + ((lane >> 4) & 1)) * 4096 + (lane & 15) * 64 + (lane >> 5) * 16); const char* qp1 = qp0 + (size_t)((14 - 4 * wid) * 4096);
          q_load(qp0, qp1, mkseq<16>::type{}); }
        asm volatile("s_nop 4");
        AT_DMA4(m0k, vk[0], vk[1], vk[2], vk[3], srdK, sk0);
        AT_DMA4(m0k + 16384, vk[0], vk[1], vk[2], vk[3], srdK, sk1);
        AT_DMA4(m0k + VOFF, vv, vv, vv, vv, srdV, sk0);
    }
    asm volatile("" : "+s"(wid), "+s"(xcd), "+s"(jj), "+s"(head), "+s"(role), "+s"(rr), "+s"(sp), "+s"(q0));
    {
        const __amdgpu_buffer_rsrc_t srdE = __builtin_amdgcn_make_buffer_rsrc((void*)part, (short)0, 512 * 15360, 0x00020000);
#pragma unroll
        for (int i = 0; i < FILL_NA; ++i) __builtin_amdgcn_raw_buffer_store_b128((u32x4){~0u, ~0u, ~0u, ~0u}, srdE, (tid & 63) * 16 + i * 1024, 32 * (60 - 8 * FILL_NA) * 4096 + (c_ * 4 + wid) * (FILL_NA * 1024), FILL_AUX);
        asm volatile("" ::: "memory");
    }
    LAS float* al = (LAS float*)(lds + XOFF) + wid * 128;
    constexpr unsigned long long ONES = 0x041041041041ull, BASE0 = 0x3f40e30a206102ull + 0ull;
    const unsigned long long dl = role ? 0ull - (unsigned long long)sp * ((ONES >> (3 * sp - 6)) << (3 * sp - 6)) : (unsigned long long)sp * ONES;
    unsigned long long tab0 = BASE0 + dl, tab1 = BASE0 + ONES + dl;
    const int pair = head * 4 + rr;
    unsigned* flg = flags + pair;
    const __amdgpu_buffer_rsrc_t srdX = __builtin_amdgcn_make_buffer_rsrc((void*)(px + (size_t)pair * 2 * 34816), (short)0, 2 * 69632, 0x00020000);
    const char* qn = Qh + (size_t)((rr * 16 + wid * 2) * 4096);
    const __amdgpu_buffer_rsrc_t srdU = __builtin_amdgcn_make_buffer_rsrc((void*)((const char*)upart - 16384), (short)0, 4 * M_ * F_ * 2 + 16384, 0x00020000);
    const int gw = (((jj >> 3) * 4 + rr) * 8 + xcd) * 4 + wid;
    unsigned uo[5];
#pragma unroll
    for (int n = 0; n < 5; ++n) { const int item = gw + n * 512; uo[n] = item < 2160 ? (unsigned)(item * 1024 + 12288 - wid * 4096) : 0x7f000000u; }
    const __amdgpu_buffer_rsrc_t srdP = __builtin_amdgcn_make_buffer_rsrc((void*)((const char*)(role ? wo : w2) - 16384), (short)0, (role ? HD_ * M_ * 2 : F_ * S_ * 2) + 16384, 0x00020000);
    unsigned spf = (unsigned)(gw * 1024 + 16384 - wid * 4096);
    const char* b1p = (const char*)b1;
    asm volatile("" : "+s"(b1p), "+s"(tab0), "+s"(tab1), "+s"(flg), "+s"(qn), "+s"(uo[0]), "+s"(uo[1]), "+s"(uo[2]), "+s"(uo[3]), "+s"(uo[4]));
    int sig = 0;
#pragma unroll 1
    for (int u = 0; u < 2; ++u) {
        if (u == 1 && role == 0) break;
        const int qblk = u ? rr : q0; const bool split = u == 0;
        unsigned cnt = u ? 2 + 2 * rr : (role ? 7 - 2 * rr : 9), mthr = (u || role == 0) ? 2u : 0u;
        const unsigned ctl = __builtin_amdgcn_readfirstlane((unsigned)((sig && wid == 0 ? 1 : 0) | (split && wid == 0 ? 2 : 0) | (split && role ? 4 : 0) | (role ? 0 : 8)));
        float m0_, m1_, l0_, l1_; unsigned seen, old_; unsigned t0_, pend_, dq0_, dq1_; unsigned long long mk0_, mk1_, mk2_;
        if (u == 0) {
            const int lane = lane_id(), r32 = lane & 31, hi = lane >> 5;
            WAIT_V(9);
            BAR;
            k_reads((int)ldsb + KOFF + 256 * r32 + 16 * (hi ^ (r32 & 15)), mkseq<8>::type{});
            if (role == 0 && wid * 64 + lane < 216) asm volatile("s_mov_b32 m0, %0\n\ts_nop 0\n\tglobal_load_lds_dwordx4 %1, off" :: "s"(ldsb + BOFF + wid * 1024), "v"(b1p + (wid * 64 + lane) * 16) : "memory", "m0");
            WAIT_L(0);
        }
        asm volatile(
        ".p2align 2\ns_mov_b32 %[pend], 0\nv_mfma_f32_32x32x16_bf16 v[0:15], a[192:195], a[128:131], 0\nv_mbcnt_lo_u32_b32 v160, -1, 0\nv_mbcnt_hi_u32_b32 v160, -1, v160\nv_and_b32_e32 v161, 31, v160\nv_lshrrev_b32_e32 v162, 5, v160\n"
        "v_and_b32_e32 v163, 15, v160\nv_xor_b32_e32 v164, v162, v163\nv_mfma_f32_32x32x16_bf16 v[16:31], a[192:195], a[160:163], 0\nv_lshlrev_b32_e32 v164, 4, v164\nv_lshl_add_u32 v164, v161, 8, v164\nv_add_u32_e32 v224, %[ldsb], v164\n"
        "v_xor_b32_e32 v225, 0x20, v224\nv_xor_b32_e32 v226, 0x40, v224\nv_xor_b32_e32 v227, 0x60, v224\nv_mfma_f32_32x32x16_bf16 v[32:47], a[224:227], a[128:131], 0\nv_xor_b32_e32 v228, 0x80, v224\nv_xor_b32_e32 v229, 0xa0, v224\n"
        "v_xor_b32_e32 v230, 0xc0, v224\nv_xor_b32_e32 v231, 0xe0, v224\nv_lshrrev_b32_e32 v164, 2, v163\nv_lshl_add_u32 v164, v162, 2, v164\nv_mfma_f32_32x32x16_bf16 v[48:63], a[224:227], a[160:163], 0\nv_lshlrev_b32_e32 v164, 6, v164\n"
        "v_bfe_u32 v165, v160, 4, 1\nv_lshl_add_u32 v164, v165, 5, v164\nv_and_b32_e32 v166, 3, v160\nv_lshl_add_u32 v164, v166, 3, v164\nv_add_u32_e32 v164, %[ldsb], v164\n"
        "v_mfma_f32_32x32x16_bf16 v[0:15], a[196:199], a[132:135], v[0:15]\nv_add_u32_e32 v232, 0x8000, v164\ns_lshl_b32 %[t0], %[wid], 12\nv_lshlrev_b32_e32 v237, 4, v160\nv_add_u32_e32 v237, %[t0], v237\nv_lshrrev_b32_e32 v167, 4, v160\n"
        "v_xor_b32_e32 v168, v166, v167\nv_mfma_f32_32x32x16_bf16 v[16:31], a[196:199], a[164:167], v[16:31]\nv_lshlrev_b32_e32 v168, 4, v168\nv_lshl_add_u32 v168, v167, 6, v168\nv_add_u32_e32 v168, %[t0], v168\n"
        "v_bfe_u32 v169, v160, 2, 2\nv_xor_b32_e32 v170, 0, v169\nv_lshl_add_u32 v170, v170, 10, v168\nv_mfma_f32_32x32x16_bf16 v[32:47], a[228:231], a[132:135], v[32:47]\nv_add_u32_e32 v233, 0x1000, v170\nv_xor_b32_e32 v170, 1, v169\n"
        "v_lshl_add_u32 v170, v170, 10, v168\nv_add_u32_e32 v234, 0xd00, v170\nv_xor_b32_e32 v170, 2, v169\nv_lshl_add_u32 v170, v170, 10, v168\nv_mfma_f32_32x32x16_bf16 v[48:63], a[228:231], a[164:167], v[48:63]\n"
        "v_add_u32_e32 v235, 0xa00, v170\nv_xor_b32_e32 v170, 3, v169\nv_lshl_add_u32 v170, v170, 10, v168\nv_add_u32_e32 v236, 0x700, v170\nv_lshlrev_b32_e32 v164, 2, v162\nv_sub_u32_e32 v238, v161, v164\n"
        "v_mfma_f32_32x32x16_bf16 v[0:15], a[200:203], a[136:139], v[0:15]\ns_mov_b32 %[dq0], %[wid]\ns_sub_u32 %[dq1], 7, %[wid]\nv_mov_b32_e32 v242, 0\nv_mov_b32_e32 v243, 0\nv_mov_b32_e32 v244, 0\nv_mov_b32_e32 v245, 0\n"
        "v_mfma_f32_32x32x16_bf16 v[16:31], a[200:203], a[168:171], v[16:31]\nv_mov_b32_e32 v246, 0\nv_mov_b32_e32 v247, 0\nv_accvgpr_write_b32 a0, 0\nv_accvgpr_write_b32 a1, 0\nv_accvgpr_write_b32 a2, 0\nv_accvgpr_write_b32 a3, 0\n"
        "v_mfma_f32_32x32x16_bf16 v[32:47], a[232:235], a[136:139], v[32:47]\nv_accvgpr_write_b32 a4, 0\nv_accvgpr_write_b32 a5, 0\nv_accvgpr_write_b32 a6, 0\nv_accvgpr_write_b32 a7, 0\nv_accvgpr_write_b32 a8, 0\n"
        "v_accvgpr_write_b32 a9, 0\nv_mfma_f32_32x32x16_bf16 v[48:63], a[232:235], a[168:171], v[48:63]\nv_accvgpr_write_b32 a10, 0\nv_accvgpr_write_b32 a11, 0\nv_accvgpr_write_b32 a12, 0\nv_accvgpr_write_b32 a13, 0\n"
        "v_accvgpr_write_b32 a14, 0\nv_accvgpr_write_b32 a15, 0\nv_mfma_f32_32x32x16_bf16 v[0:15], a[204:207], a[140:143], v[0:15]\nv_accvgpr_write_b32 a16, 0\nv_accvgpr_write_b32 a17, 0\nv_accvgpr_write_b32 a18, 0\n"
        "v_accvgpr_write_b32 a19, 0\nv_accvgpr_write_b32 a20, 0\nv_accvgpr_write_b32 a21, 0\nv_mfma_f32_32x32x16_bf16 v[16:31], a[204:207], a[172:175], v[16:31]\nv_accvgpr_write_b32 a22, 0\nv_accvgpr_write_b32 a23, 0\n"
        "v_accvgpr_write_b32 a24, 0\nv_accvgpr_write_b32 a25, 0\nv_accvgpr_write_b32 a26, 0\nv_accvgpr_write_b32 a27, 0\nv_mfma_f32_32x32x16_bf16 v[32:47], a[236:239], a[140:143], v[32:47]\nv_accvgpr_write_b32 a28, 0\n"
        "v_accvgpr_write_b32 a29, 0\nv_accvgpr_write_b32 a30, 0\nv_accvgpr_write_b32 a31, 0\nv_accvgpr_write_b32 a32, 0\nv_accvgpr_write_b32 a33, 0\nv_mfma_f32_32x32x16_bf16 v[48:63], a[236:239], a[172:175], v[48:63]\n"
        "v_accvgpr_write_b32 a34, 0\nv_accvgpr_write_b32 a35, 0\nv_accvgpr_write_b32 a36, 0\nv_accvgpr_write_b32 a37, 0\nv_accvgpr_write_b32 a38, 0\nv_accvgpr_write_b32 a39, 0\n"
        "v_mfma_f32_32x32x16_bf16 v[0:15], a[208:211], a[144:147], v[0:15]\nv_accvgpr_write_b32 a40, 0\nv_accvgpr_write_b32 a41, 0\nv_accvgpr_write_b32 a42, 0\nv_accvgpr_write_b32 a43, 0\nv_accvgpr_write_b32 a44, 0\n"
        "v_accvgpr_write_b32 a45, 0\nv_mfma_f32_32x32x16_bf16 v[16:31], a[208:211], a[176:179], v[16:31]\nv_accvgpr_write_b32 a46, 0\nv_accvgpr_write_b32 a47, 0\nv_accvgpr_write_b32 a48, 0\nv_accvgpr_write_b32 a49, 0\n"
        "v_accvgpr_write_b32 a50, 0\nv_accvgpr_write_b32 a51, 0\nv_mfma_f32_32x32x16_bf16 v[32:47], a[240:243], a[144:147], v[32:47]\nv_accvgpr_write_b32 a52, 0\nv_accvgpr_write_b32 a53, 0\nv_accvgpr_write_b32 a54, 0\n"
        "v_accvgpr_write_b32 a55, 0\nv_accvgpr_write_b32 a56, 0\nv_accvgpr_write_b32 a57, 0\nv_mfma_f32_32x32x16_bf16 v[48:63], a[240:243], a[176:179], v[48:63]\nv_accvgpr_write_b32 a58, 0\nv_accvgpr_write_b32 a59, 0\n"
        "v_accvgpr_write_b32 a60, 0\nv_accvgpr_write_b32 a61, 0\nv_accvgpr_write_b32 a62, 0\nv_accvgpr_write_b32 a63, 0\nv_mfma_f32_32x32x16_bf16 v[0:15], a[212:215], a[148:151], v[0:15]\nv_accvgpr_write_b32 a64, 0\n"
        "v_accvgpr_write_b32 a65, 0\nv_accvgpr_write_b32 a66, 0\nv_accvgpr_write_b32 a67, 0\nv_accvgpr_write_b32 a68, 0\nv_accvgpr_write_b32 a69, 0\nv_mfma_f32_32x32x16_bf16 v[16:31], a[212:215], a[180:183], v[16:31]\n"
        "v_accvgpr_write_b32 a70, 0\nv_accvgpr_write_b32 a71, 0\nv_accvgpr_write_b32 a72, 0\nv_accvgpr_write_b32 a73, 0\nv_accvgpr_write_b32 a74, 0\nv_accvgpr_write_b32 a75, 0\n"
        "v_mfma_f32_32x32x16_bf16 v[32:47], a[244:247], a[148:151], v[32:47]\nv_accvgpr_write_b32 a76, 0\nv_accvgpr_write_b32 a77, 0\nv_accvgpr_write_b32 a78, 0\nv_accvgpr_write_b32 a79, 0\nv_accvgpr_write_b32 a80, 0\n"
        "v_accvgpr_write_b32 a81, 0\nv_mfma_f32_32x32x16_bf16 v[48:63], a[244:247], a[180:183], v[48:63]\nv_accvgpr_write_b32 a82, 0\nv_accvgpr_write_b32 a83, 0\nv_accvgpr_write_b32 a84, 0\nv_accvgpr_write_b32 a85, 0\n"
        "v_accvgpr_write_b32 a86, 0\nv_accvgpr_write_b32 a87, 0\nv_mfma_f32_32x32x16_bf16 v[0:15], a[216:219], a[152:155], v[0:15]\nv_accvgpr_write_b32 a88, 0\nv_accvgpr_write_b32 a89, 0\nv_accvgpr_write_b32 a90, 0\n"
        "v_accvgpr_write_b32 a91, 0\nv_accvgpr_write_b32 a92, 0\nv_accvgpr_write_b32 a93, 0\nv_mfma_f32_32x32x16_bf16 v[16:31], a[216:219], a[184:187], v[16:31]\nv_accvgpr_write_b32 a94, 0\nv_accvgpr_write_b32 a95, 0\n"
        "v_accvgpr_write_b32 a96, 0\nv_accvgpr_write_b32 a97, 0\nv_accvgpr_write_b32 a98, 0\nv_accvgpr_write_b32 a99, 0\nv_mfma_f32_32x32x16_bf16 v[32:47], a[248:251], a[152:155], v[32:47]\nv_accvgpr_write_b32 a100, 0\n"
        "v_accvgpr_write_b32 a101, 0\nv_accvgpr_write_b32 a102, 0\nv_accvgpr_write_b32 a103, 0\nv_accvgpr_write_b32 a104, 0\nv_accvgpr_write_b32 a105, 0\nv_mfma_f32_32x32x16_bf16 v[48:63], a[248:251], a[184:187], v[48:63]\n"
        "v_accvgpr_write_b32 a106, 0\nv_accvgpr_write_b32 a107, 0\nv_accvgpr_write_b32 a108, 0\nv_accvgpr_write_b32 a109, 0\nv_accvgpr_write_b32 a110, 0\nv_accvgpr_write_b32 a111, 0\n"
        "v_mfma_f32_32x32x16_bf16 v[0:15], a[220:223], a[156:159], v[0:15]\nv_accvgpr_write_b32 a112, 0\nv_accvgpr_write_b32 a113, 0\nv_accvgpr_write_b32 a114, 0\nv_accvgpr_write_b32 a115, 0\nv_accvgpr_write_b32 a116, 0\n"
        "v_accvgpr_write_b32 a117, 0\nv_mfma_f32_32x32x16_bf16 v[16:31], a[220:223], a[188:191], v[16:31]\nv_accvgpr_write_b32 a118, 0\nv_accvgpr_write_b32 a119, 0\nv_accvgpr_write_b32 a120, 0\nv_accvgpr_write_b32 a121, 0\n"
        "v_accvgpr_write_b32 a122, 0\nv_accvgpr_write_b32 a123, 0\nv_mfma_f32_32x32x16_bf16 v[32:47], a[252:255], a[156:159], v[32:47]\nv_accvgpr_write_b32 a124, 0\nv_accvgpr_write_b32 a125, 0\nv_accvgpr_write_b32 a126, 0\n"
        "v_accvgpr_write_b32 a127, 0\nv_mfma_f32_32x32x16_bf16 v[48:63], a[252:255], a[188:191], v[48:63]\ns_waitcnt vmcnt(0) lgkmcnt(0)\ns_barrier\ns_bitcmp1_b32 %[ctl], 0\ns_cbranch_scc0 L_nosig%=\nv_mov_b32_e32 v160, 0\n"
        "v_mov_b32_e32 v161, 1\ns_mov_b64 exec, 1\ns_nop 1\nglobal_atomic_add v247, v160, v161, %[flg] sc0\ns_mov_b64 exec, -1\nL_nosig%=:\ns_and_b32 %[t0], s40, 63\ns_lshl_b32 %[sk0], %[t0], 14\ns_lshr_b64 s[40:41], s[40:41], 6\n"
        "s_add_u32 m0, %[m0k], 0x0\ns_nop 0\nbuffer_load_dwordx4 v233, %[srdK], %[sk0] offen offset:0 lds\nbuffer_load_dwordx4 v234, %[srdK], %[sk0] offen offset:1024 lds\nbuffer_load_dwordx4 v235, %[srdK], %[sk0] offen offset:2048 lds\n"
        "buffer_load_dwordx4 v236, %[srdK], %[sk0] offen offset:3072 lds\ns_add_u32 m0, %[m0k], 0xc000\ns_nop 0\nbuffer_load_dwordx4 v237, %[srdV], %[sk1] offen offset:0 lds\n"
        "buffer_load_dwordx4 v237, %[srdV], %[sk1] offen offset:1024 lds\nbuffer_load_dwordx4 v237, %[srdV], %[sk1] offen offset:2048 lds\nbuffer_load_dwordx4 v237, %[srdV], %[sk1] offen offset:3072 lds\n"
        "ds_read_b128 a[192:195], v224 offset:16384\nds_read_b128 a[224:227], v224 offset:24576\nds_read_b128 a[196:199], v225 offset:16384\nds_read_b128 a[228:231], v225 offset:24576\nds_read_b128 a[200:203], v226 offset:16384\n"
        "ds_read_b128 a[232:235], v226 offset:24576\nds_read_b128 a[204:207], v227 offset:16384\nds_read_b128 a[236:239], v227 offset:24576\nds_read_b128 a[208:211], v228 offset:16384\nds_read_b128 a[240:243], v228 offset:24576\n"
        "ds_read_b128 a[212:215], v229 offset:16384\nds_read_b128 a[244:247], v229 offset:24576\nds_read_b128 a[216:219], v230 offset:16384\nds_read_b128 a[248:251], v230 offset:24576\nds_read_b128 a[220:223], v231 offset:16384\n"
        "ds_read_b128 a[252:255], v231 offset:24576\ns_cmp_le_u32 %[cnt], %[mthr]\ns_cbranch_scc1 L_maskf%=\nL_maskfb%=:\nv_max_f32_e32 v68, v0, v1\nv_max_f32_e32 v69, v2, v3\nv_max3_f32 v68, v68, v4, v5\nv_max3_f32 v69, v69, v6, v7\n"
        "v_max3_f32 v68, v68, v8, v9\nv_max3_f32 v69, v69, v10, v11\nv_max3_f32 v68, v68, v12, v13\nv_max3_f32 v69, v69, v14, v15\nv_max_f32_e32 v70, v16, v17\nv_max_f32_e32 v71, v18, v19\nv_max3_f32 v70, v70, v20, v21\n"
        "v_max3_f32 v71, v71, v22, v23\nv_max3_f32 v70, v70, v24, v25\nv_max3_f32 v71, v71, v26, v27\nv_max3_f32 v70, v70, v28, v29\nv_max3_f32 v71, v71, v30, v31\nv_max3_f32 v68, v68, v32, v33\nv_max3_f32 v69, v69, v34, v35\n"
        "v_max3_f32 v68, v68, v36, v37\nv_max3_f32 v69, v69, v38, v39\nv_max3_f32 v68, v68, v40, v41\nv_max3_f32 v69, v69, v42, v43\nv_max3_f32 v68, v68, v44, v45\nv_max3_f32 v69, v69, v46, v47\nv_max3_f32 v70, v70, v48, v49\n"
        "v_max3_f32 v71, v71, v50, v51\nv_max3_f32 v70, v70, v52, v53\nv_max3_f32 v71, v71, v54, v55\nv_max3_f32 v70, v70, v56, v57\nv_max3_f32 v71, v71, v58, v59\nv_max3_f32 v70, v70, v60, v61\nv_max3_f32 v71, v71, v62, v63\n"
        "v_max_f32_e32 v68, v68, v69\nv_mov_b32_e32 v69, v68\ns_nop 1\nv_permlane32_swap_b32_e32 v68, v69\ns_nop 0\nv_max_f32_e32 v68, v68, v69\nv_add_f32_e32 v68, 0x41800000, v68\nv_mov_b32_e32 v240, v68\nv_sub_f32_e32 v0, v0, v68\n"
        "v_sub_f32_e32 v1, v1, v68\nv_sub_f32_e32 v2, v2, v68\nv_sub_f32_e32 v3, v3, v68\nv_sub_f32_e32 v4, v4, v68\nv_sub_f32_e32 v5, v5, v68\nv_sub_f32_e32 v6, v6, v68\nv_sub_f32_e32 v7, v7, v68\nv_sub_f32_e32 v8, v8, v68\n"
        "v_sub_f32_e32 v9, v9, v68\nv_sub_f32_e32 v10, v10, v68\nv_sub_f32_e32 v11, v11, v68\nv_sub_f32_e32 v12, v12, v68\nv_sub_f32_e32 v13, v13, v68\nv_sub_f32_e32 v14, v14, v68\nv_sub_f32_e32 v15, v15, v68\nv_sub_f32_e32 v32, v32, v68\n"
        "v_sub_f32_e32 v33, v33, v68\nv_sub_f32_e32 v34, v34, v68\nv_sub_f32_e32 v35, v35, v68\nv_sub_f32_e32 v36, v36, v68\nv_sub_f32_e32 v37, v37, v68\nv_sub_f32_e32 v38, v38, v68\nv_sub_f32_e32 v39, v39, v68\n"
        "v_sub_f32_e32 v40, v40, v68\nv_sub_f32_e32 v41, v41, v68\nv_sub_f32_e32 v42, v42, v68\nv_sub_f32_e32 v43, v43, v68\nv_sub_f32_e32 v44, v44, v68\nv_sub_f32_e32 v45, v45, v68\nv_sub_f32_e32 v46, v46, v68\n"
        "v_sub_f32_e32 v47, v47, v68\nv_sub_f32_e32 v192, 0, v240\nv_mov_b32_e32 v193, v192\nv_mov_b32_e32 v194, v192\nv_mov_b32_e32 v195, v192\nv_mov_b32_e32 v196, v192\nv_mov_b32_e32 v197, v192\nv_mov_b32_e32 v198, v192\n"
        "v_mov_b32_e32 v199, v192\nv_mov_b32_e32 v200, v192\nv_mov_b32_e32 v201, v192\nv_mov_b32_e32 v202, v192\nv_mov_b32_e32 v203, v192\nv_mov_b32_e32 v204, v192\nv_mov_b32_e32 v205, v192\nv_mov_b32_e32 v206, v192\n"
        "v_mov_b32_e32 v207, v192\nv_max_f32_e32 v70, v70, v71\nv_mov_b32_e32 v71, v70\ns_nop 1\nv_permlane32_swap_b32_e32 v70, v71\ns_nop 0\nv_max_f32_e32 v70, v70, v71\nv_add_f32_e32 v70, 0x41800000, v70\nv_mov_b32_e32 v241, v70\n"
        "v_sub_f32_e32 v16, v16, v70\nv_sub_f32_e32 v17, v17, v70\nv_sub_f32_e32 v18, v18, v70\nv_sub_f32_e32 v19, v19, v70\nv_sub_f32_e32 v20, v20, v70\nv_sub_f32_e32 v21, v21, v70\nv_sub_f32_e32 v22, v22, v70\n"
        "v_sub_f32_e32 v23, v23, v70\nv_sub_f32_e32 v24, v24, v70\nv_sub_f32_e32 v25, v25, v70\nv_sub_f32_e32 v26, v26, v70\nv_sub_f32_e32 v27, v27, v70\nv_sub_f32_e32 v28, v28, v70\nv_sub_f32_e32 v29, v29, v70\n"
        "v_sub_f32_e32 v30, v30, v70\nv_sub_f32_e32 v31, v31, v70\nv_sub_f32_e32 v48, v48, v70\nv_sub_f32_e32 v49, v49, v70\nv_sub_f32_e32 v50, v50, v70\nv_sub_f32_e32 v51, v51, v70\nv_sub_f32_e32 v52, v52, v70\n"
        "v_sub_f32_e32 v53, v53, v70\nv_sub_f32_e32 v54, v54, v70\nv_sub_f32_e32 v55, v55, v70\nv_sub_f32_e32 v56, v56, v70\nv_sub_f32_e32 v57, v57, v70\nv_sub_f32_e32 v58, v58, v70\nv_sub_f32_e32 v59, v59, v70\n"
        "v_sub_f32_e32 v60, v60, v70\nv_sub_f32_e32 v61, v61, v70\nv_sub_f32_e32 v62, v62, v70\nv_sub_f32_e32 v63, v63, v70\nv_sub_f32_e32 v208, 0, v241\nv_mov_b32_e32 v209, v208\nv_mov_b32_e32 v210, v208\nv_mov_b32_e32 v211, v208\n"
        "v_mov_b32_e32 v212, v208\nv_mov_b32_e32 v213, v208\nv_mov_b32_e32 v214, v208\nv_mov_b32_e32 v215, v208\nv_mov_b32_e32 v216, v208\nv_mov_b32_e32 v217, v208\nv_mov_b32_e32 v218, v208\nv_mov_b32_e32 v219, v208\n"
        "v_mov_b32_e32 v220, v208\nv_mov_b32_e32 v221, v208\nv_mov_b32_e32 v222, v208\nv_mov_b32_e32 v223, v208\nv_exp_f32_e32 v0, v0\nv_exp_f32_e32 v1, v1\nv_add_f32_e32 v242, v242, v0\nv_exp_f32_e32 v2, v2\nv_add_f32_e32 v243, v243, v1\n"
        "v_exp_f32_e32 v3, v3\nv_add_f32_e32 v242, v242, v2\nv_exp_f32_e32 v4, v4\nv_add_f32_e32 v243, v243, v3\nv_exp_f32_e32 v5, v5\nv_add_f32_e32 v242, v242, v4\nv_exp_f32_e32 v6, v6\nv_add_f32_e32 v243, v243, v5\nv_exp_f32_e32 v7, v7\n"
        "v_add_f32_e32 v242, v242, v6\nv_exp_f32_e32 v8, v8\nv_add_f32_e32 v243, v243, v7\nv_exp_f32_e32 v9, v9\nv_add_f32_e32 v242, v242, v8\nv_exp_f32_e32 v10, v10\nv_add_f32_e32 v243, v243, v9\nv_exp_f32_e32 v11, v11\n"
        "v_add_f32_e32 v242, v242, v10\nv_exp_f32_e32 v12, v12\nv_add_f32_e32 v243, v243, v11\nv_exp_f32_e32 v13, v13\nv_add_f32_e32 v242, v242, v12\nv_exp_f32_e32 v14, v14\nv_add_f32_e32 v243, v243, v13\nv_exp_f32_e32 v15, v15\n"
        "v_add_f32_e32 v242, v242, v14\nv_exp_f32_e32 v16, v16\nv_add_f32_e32 v243, v243, v15\nv_exp_f32_e32 v17, v17\nv_add_f32_e32 v244, v244, v16\nv_exp_f32_e32 v18, v18\nv_add_f32_e32 v245, v245, v17\nv_exp_f32_e32 v19, v19\n"
        "v_add_f32_e32 v244, v244, v18\nv_exp_f32_e32 v20, v20\nv_add_f32_e32 v245, v245, v19\nv_exp_f32_e32 v21, v21\nv_add_f32_e32 v244, v244, v20\nv_exp_f32_e32 v22, v22\nv_add_f32_e32 v245, v245, v21\nv_exp_f32_e32 v23, v23\n"
        "v_add_f32_e32 v244, v244, v22\nv_exp_f32_e32 v24, v24\nv_add_f32_e32 v245, v245, v23\nv_exp_f32_e32 v25, v25\nv_add_f32_e32 v244, v244, v24\nv_exp_f32_e32 v26, v26\nv_add_f32_e32 v245, v245, v25\nv_exp_f32_e32 v27, v27\n"
        "v_add_f32_e32 v244, v244, v26\nv_exp_f32_e32 v28, v28\nv_add_f32_e32 v245, v245, v27\nv_exp_f32_e32 v29, v29\nv_add_f32_e32 v244, v244, v28\nv_exp_f32_e32 v30, v30\nv_add_f32_e32 v245, v245, v29\nv_exp_f32_e32 v31, v31\n"
        "v_add_f32_e32 v244, v244, v30\ns_nop 0\nv_add_f32_e32 v245, v245, v31\ns_waitcnt lgkmcnt(0)\n.p2align 2\nL_loop%=:\nv_mfma_f32_32x32x16_bf16 v[64:79], a[192:195], a[128:131], v[192:207]\n"
        "ds_read_b64_tr_b16 v[128:129], v232 offset:0\nv_exp_f32_e32 v32, v32\nv_exp_f32_e32 v33, v33\nv_cvt_pk_bf16_f32 v0, v0, v1\nv_mfma_f32_32x32x16_bf16 v[80:95], a[192:195], a[160:163], v[208:223]\ns_add_u32 m0, %[ldsb], 0x26000\n"
        "ds_read_b64_tr_b16 v[130:131], v232 offset:512\nv_exp_f32_e32 v34, v34\nv_add_f32_e32 v242, v242, v32\nv_add_f32_e32 v243, v243, v33\nv_cvt_pk_bf16_f32 v32, v32, v33\n"
        "v_mfma_f32_32x32x16_bf16 v[96:111], a[224:227], a[128:131], v[192:207]\nds_read_b64_tr_b16 v[132:133], v232 offset:1024\nv_exp_f32_e32 v35, v35\nv_add_f32_e32 v242, v242, v34\nv_cvt_pk_bf16_f32 v1, v2, v3\n"
        "buffer_load_dwordx4 v237, %[srdP], %[sp] offen lds\ns_add_u32 %[sp], %[sp], 0x80000\nv_mfma_f32_32x32x16_bf16 v[112:127], a[224:227], a[160:163], v[208:223]\nds_read_b64_tr_b16 v[134:135], v232 offset:1536\n"
        "v_exp_f32_e32 v36, v36\nv_add_f32_e32 v243, v243, v35\nv_cvt_pk_bf16_f32 v33, v34, v35\nv_mfma_f32_32x32x16_bf16 v[64:79], a[196:199], a[132:135], v[64:79]\nds_read_b64_tr_b16 v[136:137], v232 offset:2048\n"
        "ds_read_b64_tr_b16 v[138:139], v232 offset:2560\nv_exp_f32_e32 v37, v37\nv_add_f32_e32 v242, v242, v36\nv_cvt_pk_bf16_f32 v2, v4, v5\nv_mfma_f32_32x32x16_bf16 v[80:95], a[196:199], a[164:167], v[80:95]\n"
        "ds_read_b64_tr_b16 v[140:141], v232 offset:3072\nv_exp_f32_e32 v38, v38\nv_add_f32_e32 v243, v243, v37\nv_cvt_pk_bf16_f32 v34, v36, v37\nv_mfma_f32_32x32x16_bf16 v[96:111], a[228:231], a[132:135], v[96:111]\n"
        "ds_read_b64_tr_b16 v[142:143], v232 offset:3584\nv_exp_f32_e32 v39, v39\nv_add_f32_e32 v242, v242, v38\nv_cvt_pk_bf16_f32 v3, v6, v7\nv_mfma_f32_32x32x16_bf16 v[112:127], a[228:231], a[164:167], v[112:127]\n"
        "ds_read_b64_tr_b16 v[144:145], v232 offset:4096\nv_exp_f32_e32 v40, v40\nv_add_f32_e32 v243, v243, v39\nv_cvt_pk_bf16_f32 v35, v38, v39\nv_mfma_f32_32x32x16_bf16 v[64:79], a[200:203], a[136:139], v[64:79]\n"
        "ds_read_b64_tr_b16 v[146:147], v232 offset:4608\nv_exp_f32_e32 v41, v41\nv_add_f32_e32 v242, v242, v40\nv_cvt_pk_bf16_f32 v8, v8, v9\nv_mfma_f32_32x32x16_bf16 v[80:95], a[200:203], a[168:171], v[80:95]\n"
        "ds_read_b64_tr_b16 v[148:149], v232 offset:5120\nv_exp_f32_e32 v42, v42\nv_add_f32_e32 v243, v243, v41\nv_cvt_pk_bf16_f32 v40, v40, v41\nv_mfma_f32_32x32x16_bf16 v[96:111], a[232:235], a[136:139], v[96:111]\n"
        "ds_read_b64_tr_b16 v[150:151], v232 offset:5632\nds_read_b64_tr_b16 v[152:153], v232 offset:6144\nv_exp_f32_e32 v43, v43\nv_add_f32_e32 v242, v242, v42\nv_cvt_pk_bf16_f32 v9, v10, v11\n"
        "v_mfma_f32_32x32x16_bf16 v[112:127], a[232:235], a[168:171], v[112:127]\nds_read_b64_tr_b16 v[154:155], v232 offset:6656\nv_exp_f32_e32 v44, v44\nv_add_f32_e32 v243, v243, v43\nv_cvt_pk_bf16_f32 v41, v42, v43\n"
        "v_mfma_f32_32x32x16_bf16 v[64:79], a[204:207], a[140:143], v[64:79]\nds_read_b64_tr_b16 v[156:157], v232 offset:7168\nv_exp_f32_e32 v45, v45\nv_add_f32_e32 v242, v242, v44\nv_cvt_pk_bf16_f32 v10, v12, v13\n"
        "v_mfma_f32_32x32x16_bf16 v[80:95], a[204:207], a[172:175], v[80:95]\nds_read_b64_tr_b16 v[158:159], v232 offset:7680\nv_exp_f32_e32 v46, v46\nv_add_f32_e32 v243, v243, v45\nv_cvt_pk_bf16_f32 v42, v44, v45\n"
        "v_mfma_f32_32x32x16_bf16 v[96:111], a[236:239], a[140:143], v[96:111]\nds_read_b64_tr_b16 v[160:161], v232 offset:8192\nv_exp_f32_e32 v47, v47\nv_add_f32_e32 v242, v242, v46\nv_cvt_pk_bf16_f32 v11, v14, v15\n"
        "v_mfma_f32_32x32x16_bf16 v[112:127], a[236:239], a[172:175], v[112:127]\nds_read_b64_tr_b16 v[162:163], v232 offset:8704\nv_exp_f32_e32 v48, v48\nv_add_f32_e32 v243, v243, v47\nv_cvt_pk_bf16_f32 v43, v46, v47\n"
        "v_mfma_f32_32x32x16_bf16 v[64:79], a[208:211], a[144:147], v[64:79]\nds_read_b64_tr_b16 v[164:165], v232 offset:9216\nds_read_b64_tr_b16 v[166:167], v232 offset:9728\nv_exp_f32_e32 v49, v49\nv_add_f32_e32 v244, v244, v48\n"
        "v_cvt_pk_bf16_f32 v16, v16, v17\nv_mfma_f32_32x32x16_bf16 v[80:95], a[208:211], a[176:179], v[80:95]\nds_read_b64_tr_b16 v[168:169], v232 offset:10240\nv_exp_f32_e32 v50, v50\nv_add_f32_e32 v245, v245, v49\n"
        "v_cvt_pk_bf16_f32 v48, v48, v49\nv_mfma_f32_32x32x16_bf16 v[96:111], a[240:243], a[144:147], v[96:111]\nds_read_b64_tr_b16 v[170:171], v232 offset:10752\nv_exp_f32_e32 v51, v51\nv_add_f32_e32 v244, v244, v50\n"
        "v_cvt_pk_bf16_f32 v17, v18, v19\nv_mfma_f32_32x32x16_bf16 v[112:127], a[240:243], a[176:179], v[112:127]\nds_read_b64_tr_b16 v[172:173], v232 offset:11264\nv_exp_f32_e32 v52, v52\nv_add_f32_e32 v245, v245, v51\n"
        "v_cvt_pk_bf16_f32 v49, v50, v51\nv_mfma_f32_32x32x16_bf16 v[64:79], a[212:215], a[148:151], v[64:79]\nds_read_b64_tr_b16 v[174:175], v232 offset:11776\nv_exp_f32_e32 v53, v53\nv_add_f32_e32 v244, v244, v52\n"
        "v_cvt_pk_bf16_f32 v18, v20, v21\nv_mfma_f32_32x32x16_bf16 v[80:95], a[212:215], a[180:183], v[80:95]\nds_read_b64_tr_b16 v[176:177], v232 offset:12288\nv_exp_f32_e32 v54, v54\nv_add_f32_e32 v245, v245, v53\n"
        "v_cvt_pk_bf16_f32 v50, v52, v53\nv_mfma_f32_32x32x16_bf16 v[96:111], a[244:247], a[148:151], v[96:111]\nds_read_b64_tr_b16 v[178:179], v232 offset:12800\nds_read_b64_tr_b16 v[180:181], v232 offset:13312\nv_exp_f32_e32 v55, v55\n"
        "v_add_f32_e32 v244, v244, v54\nv_cvt_pk_bf16_f32 v19, v22, v23\nv_mfma_f32_32x32x16_bf16 v[112:127], a[244:247], a[180:183], v[112:127]\nds_read_b64_tr_b16 v[182:183], v232 offset:13824\nv_exp_f32_e32 v56, v56\n"
        "v_add_f32_e32 v245, v245, v55\nv_cvt_pk_bf16_f32 v51, v54, v55\nv_mfma_f32_32x32x16_bf16 v[64:79], a[216:219], a[152:155], v[64:79]\nds_read_b64_tr_b16 v[184:185], v232 offset:14336\nv_exp_f32_e32 v57, v57\n"
        "v_add_f32_e32 v244, v244, v56\nv_cvt_pk_bf16_f32 v24, v24, v25\nv_mfma_f32_32x32x16_bf16 v[80:95], a[216:219], a[184:187], v[80:95]\nds_read_b64_tr_b16 v[186:187], v232 offset:14848\nv_exp_f32_e32 v58, v58\n"
        "v_add_f32_e32 v245, v245, v57\nv_cvt_pk_bf16_f32 v56, v56, v57\nv_mfma_f32_32x32x16_bf16 v[96:111], a[248:251], a[152:155], v[96:111]\nds_read_b64_tr_b16 v[188:189], v232 offset:15360\nv_exp_f32_e32 v59, v59\n"
        "v_add_f32_e32 v244, v244, v58\nv_cvt_pk_bf16_f32 v25, v26, v27\nv_mfma_f32_32x32x16_bf16 v[112:127], a[248:251], a[184:187], v[112:127]\nds_read_b64_tr_b16 v[190:191], v232 offset:15872\nv_exp_f32_e32 v60, v60\n"
        "v_add_f32_e32 v245, v245, v59\nv_cvt_pk_bf16_f32 v57, v58, v59\nv_mfma_f32_32x32x16_bf16 v[64:79], a[220:223], a[156:159], v[64:79]\nv_exp_f32_e32 v61, v61\nv_add_f32_e32 v244, v244, v60\nv_cvt_pk_bf16_f32 v26, v28, v29\n"
        "v_mfma_f32_32x32x16_bf16 v[80:95], a[220:223], a[188:191], v[80:95]\nv_exp_f32_e32 v62, v62\nv_add_f32_e32 v245, v245, v61\nv_cvt_pk_bf16_f32 v58, v60, v61\nv_mfma_f32_32x32x16_bf16 v[96:111], a[252:255], a[156:159], v[96:111]\n"
        "v_exp_f32_e32 v63, v63\nv_add_f32_e32 v244, v244, v62\nv_cvt_pk_bf16_f32 v27, v30, v31\nv_mfma_f32_32x32x16_bf16 v[112:127], a[252:255], a[188:191], v[112:127]\nv_add_f32_e32 v245, v245, v63\nv_cvt_pk_bf16_f32 v59, v62, v63\n"
        "s_waitcnt vmcnt(1) lgkmcnt(0)\ns_barrier\nv_mfma_f32_32x32x16_bf16 a[0:15], v[0:3], v[128:131], a[0:15]\ns_cmp_le_u32 %[cnt], %[mthr]\ns_cbranch_scc1 L_mask1%=\nL_maskb1%=:\nv_max_f32_e32 v4, v64, v65\nv_max_f32_e32 v5, v66, v67\n"
        "v_max3_f32 v4, v4, v68, v69\nv_mfma_f32_32x32x16_bf16 a[64:79], v[16:19], v[128:131], a[64:79]\nds_read_b128 a[192:195], v224 offset:0\nv_max3_f32 v5, v5, v70, v71\nv_max3_f32 v4, v4, v72, v73\nv_max3_f32 v5, v5, v74, v75\n"
        "v_max3_f32 v4, v4, v76, v77\nv_max3_f32 v5, v5, v78, v79\nv_mfma_f32_32x32x16_bf16 a[16:31], v[0:3], v[132:135], a[16:31]\nds_read_b128 a[224:227], v224 offset:8192\nv_max_f32_e32 v6, v80, v81\nv_max_f32_e32 v7, v82, v83\n"
        "v_max3_f32 v6, v6, v84, v85\nv_max3_f32 v7, v7, v86, v87\nv_max3_f32 v6, v6, v88, v89\nv_mfma_f32_32x32x16_bf16 a[80:95], v[16:19], v[132:135], a[80:95]\nds_read_b128 a[196:199], v225 offset:0\nv_max3_f32 v7, v7, v90, v91\n"
        "v_max3_f32 v6, v6, v92, v93\nv_max3_f32 v7, v7, v94, v95\nv_max3_f32 v4, v4, v96, v97\nv_max3_f32 v5, v5, v98, v99\nv_mfma_f32_32x32x16_bf16 a[32:47], v[0:3], v[136:139], a[32:47]\nds_read_b128 a[228:231], v225 offset:8192\n"
        "v_max3_f32 v4, v4, v100, v101\nv_max3_f32 v5, v5, v102, v103\nv_max3_f32 v4, v4, v104, v105\nv_max3_f32 v5, v5, v106, v107\nv_max3_f32 v4, v4, v108, v109\nv_mfma_f32_32x32x16_bf16 a[96:111], v[16:19], v[136:139], a[96:111]\n"
        "ds_read_b128 a[200:203], v226 offset:0\nv_max3_f32 v5, v5, v110, v111\nv_max3_f32 v6, v6, v112, v113\nv_max3_f32 v7, v7, v114, v115\nv_max3_f32 v6, v6, v116, v117\nv_max3_f32 v7, v7, v118, v119\n"
        "v_mfma_f32_32x32x16_bf16 a[48:63], v[0:3], v[140:143], a[48:63]\nds_read_b128 a[232:235], v226 offset:8192\nv_max3_f32 v6, v6, v120, v121\nv_max3_f32 v7, v7, v122, v123\nv_max3_f32 v6, v6, v124, v125\n"
        "v_max3_f32 v7, v7, v126, v127\nv_mfma_f32_32x32x16_bf16 a[112:127], v[16:19], v[140:143], a[112:127]\nds_read_b128 a[204:207], v227 offset:0\nv_max3_f32 v14, v4, v5, v6\nv_max_f32_e32 v14, v14, v7\n"
        "v_cmp_lt_f32_e32 vcc, 0x41800000, v14\ns_cbranch_vccnz L_rareA1%=\nL_rareAb1%=:\nv_mfma_f32_32x32x16_bf16 a[0:15], v[8:11], v[144:147], a[0:15]\ns_add_u32 m0, %[m0k], 0x4000\nds_read_b128 a[236:239], v227 offset:8192\n"
        "s_and_b32 %[t0], s42, 63\ns_lshl_b32 %[sk1], %[t0], 14\ns_lshr_b64 s[42:43], s[42:43], 6\nv_exp_f32_e32 v64, v64\nv_exp_f32_e32 v65, v65\nv_mfma_f32_32x32x16_bf16 a[64:79], v[24:27], v[144:147], a[64:79]\n"
        "buffer_load_dwordx4 v233, %[srdK], %[sk1] offen offset:0 lds\nv_exp_f32_e32 v66, v66\nv_add_f32_e32 v242, v242, v64\nv_add_f32_e32 v243, v243, v65\nv_mfma_f32_32x32x16_bf16 a[16:31], v[8:11], v[148:151], a[16:31]\n"
        "ds_read_b128 a[208:211], v228 offset:0\nv_exp_f32_e32 v67, v67\nv_exp_f32_e32 v68, v68\nv_add_f32_e32 v242, v242, v66\nv_mfma_f32_32x32x16_bf16 a[80:95], v[24:27], v[148:151], a[80:95]\nds_read_b128 a[240:243], v228 offset:8192\n"
        "v_exp_f32_e32 v69, v69\nv_exp_f32_e32 v70, v70\nv_add_f32_e32 v243, v243, v67\nv_add_f32_e32 v242, v242, v68\nv_mfma_f32_32x32x16_bf16 a[32:47], v[8:11], v[152:155], a[32:47]\n"
        "buffer_load_dwordx4 v234, %[srdK], %[sk1] offen offset:1024 lds\nv_exp_f32_e32 v71, v71\nv_add_f32_e32 v243, v243, v69\nv_add_f32_e32 v242, v242, v70\nv_mfma_f32_32x32x16_bf16 a[96:111], v[24:27], v[152:155], a[96:111]\n"
        "ds_read_b128 a[212:215], v229 offset:0\nv_exp_f32_e32 v72, v72\nv_add_f32_e32 v243, v243, v71\nv_mfma_f32_32x32x16_bf16 a[48:63], v[8:11], v[156:159], a[48:63]\nds_read_b128 a[244:247], v229 offset:8192\nv_exp_f32_e32 v73, v73\n"
        "v_add_f32_e32 v242, v242, v72\nv_mfma_f32_32x32x16_bf16 a[112:127], v[24:27], v[156:159], a[112:127]\nbuffer_load_dwordx4 v235, %[srdK], %[sk1] offen offset:2048 lds\nv_exp_f32_e32 v74, v74\nv_add_f32_e32 v243, v243, v73\n"
        "v_mfma_f32_32x32x16_bf16 a[0:15], v[32:35], v[160:163], a[0:15]\nds_read_b128 a[216:219], v230 offset:0\nv_exp_f32_e32 v75, v75\nv_add_f32_e32 v242, v242, v74\nv_mfma_f32_32x32x16_bf16 a[64:79], v[48:51], v[160:163], a[64:79]\n"
        "ds_read_b128 a[248:251], v230 offset:8192\nv_exp_f32_e32 v76, v76\nv_add_f32_e32 v243, v243, v75\nv_mfma_f32_32x32x16_bf16 a[16:31], v[32:35], v[164:167], a[16:31]\nbuffer_load_dwordx4 v236, %[srdK], %[sk1] offen offset:3072 lds\n"
        "v_exp_f32_e32 v77, v77\nv_add_f32_e32 v242, v242, v76\nv_mfma_f32_32x32x16_bf16 a[80:95], v[48:51], v[164:167], a[80:95]\nds_read_b128 a[220:223], v231 offset:0\nv_exp_f32_e32 v78, v78\nv_add_f32_e32 v243, v243, v77\n"
        "v_mfma_f32_32x32x16_bf16 a[32:47], v[32:35], v[168:171], a[32:47]\ns_add_u32 m0, %[m0k], 0x8000\nds_read_b128 a[252:255], v231 offset:8192\nv_exp_f32_e32 v79, v79\nv_add_f32_e32 v242, v242, v78\n"
        "v_mfma_f32_32x32x16_bf16 a[96:111], v[48:51], v[168:171], a[96:111]\nbuffer_load_dwordx4 v237, %[srdV], %[sk0] offen offset:0 lds\nv_exp_f32_e32 v80, v80\nv_add_f32_e32 v243, v243, v79\n"
        "v_mfma_f32_32x32x16_bf16 a[48:63], v[32:35], v[172:175], a[48:63]\nv_exp_f32_e32 v81, v81\nv_exp_f32_e32 v82, v82\nv_add_f32_e32 v244, v244, v80\nv_mfma_f32_32x32x16_bf16 a[112:127], v[48:51], v[172:175], a[112:127]\n"
        "v_exp_f32_e32 v83, v83\nv_exp_f32_e32 v84, v84\nv_add_f32_e32 v245, v245, v81\nv_add_f32_e32 v244, v244, v82\nv_mfma_f32_32x32x16_bf16 a[0:15], v[40:43], v[176:179], a[0:15]\n"
        "buffer_load_dwordx4 v237, %[srdV], %[sk0] offen offset:1024 lds\nv_exp_f32_e32 v85, v85\nv_add_f32_e32 v245, v245, v83\nv_add_f32_e32 v244, v244, v84\nv_mfma_f32_32x32x16_bf16 a[64:79], v[56:59], v[176:179], a[64:79]\n"
        "v_exp_f32_e32 v86, v86\nv_exp_f32_e32 v87, v87\nv_add_f32_e32 v245, v245, v85\nv_mfma_f32_32x32x16_bf16 a[16:31], v[40:43], v[180:183], a[16:31]\nv_exp_f32_e32 v88, v88\nv_exp_f32_e32 v89, v89\nv_add_f32_e32 v244, v244, v86\n"
        "v_add_f32_e32 v245, v245, v87\nv_mfma_f32_32x32x16_bf16 a[80:95], v[56:59], v[180:183], a[80:95]\nbuffer_load_dwordx4 v237, %[srdV], %[sk0] offen offset:2048 lds\nv_exp_f32_e32 v90, v90\nv_add_f32_e32 v244, v244, v88\n"
        "v_add_f32_e32 v245, v245, v89\nv_mfma_f32_32x32x16_bf16 a[32:47], v[40:43], v[184:187], a[32:47]\nv_exp_f32_e32 v91, v91\nv_exp_f32_e32 v92, v92\nv_add_f32_e32 v244, v244, v90\n"
        "v_mfma_f32_32x32x16_bf16 a[96:111], v[56:59], v[184:187], a[96:111]\nv_exp_f32_e32 v93, v93\nv_exp_f32_e32 v94, v94\nv_add_f32_e32 v245, v245, v91\nv_add_f32_e32 v244, v244, v92\ns_sub_u32 %[cnt], %[cnt], 1\n"
        "v_mfma_f32_32x32x16_bf16 a[48:63], v[40:43], v[188:191], a[48:63]\nbuffer_load_dwordx4 v237, %[srdV], %[sk0] offen offset:3072 lds\nv_exp_f32_e32 v95, v95\nv_add_f32_e32 v245, v245, v93\nv_add_f32_e32 v244, v244, v94\n"
        "v_mfma_f32_32x32x16_bf16 a[112:127], v[56:59], v[188:191], a[112:127]\nv_add_f32_e32 v245, v245, v95\ns_cmp_lg_u32 %[pend], 0\ns_cbranch_scc1 L_rareB1%=\nL_rareBb1%=:\ns_waitcnt lgkmcnt(0)\ns_lshr_b32 %[t0], %[mthr], 1\n"
        "s_cmp_eq_u32 %[cnt], %[t0]\ns_cbranch_scc1 L_exit%=\nv_mfma_f32_32x32x16_bf16 v[0:15], a[192:195], a[128:131], v[192:207]\nds_read_b64_tr_b16 v[128:129], v232 offset:16384\nv_exp_f32_e32 v96, v96\nv_exp_f32_e32 v97, v97\n"
        "v_cvt_pk_bf16_f32 v64, v64, v65\nv_mfma_f32_32x32x16_bf16 v[16:31], a[192:195], a[160:163], v[208:223]\ns_add_u32 m0, %[ldsb], 0x26000\nds_read_b64_tr_b16 v[130:131], v232 offset:16896\nv_exp_f32_e32 v98, v98\n"
        "v_add_f32_e32 v242, v242, v96\nv_add_f32_e32 v243, v243, v97\nv_cvt_pk_bf16_f32 v96, v96, v97\nv_mfma_f32_32x32x16_bf16 v[32:47], a[224:227], a[128:131], v[192:207]\nds_read_b64_tr_b16 v[132:133], v232 offset:17408\n"
        "v_exp_f32_e32 v99, v99\nv_add_f32_e32 v242, v242, v98\nv_cvt_pk_bf16_f32 v65, v66, v67\nbuffer_load_dwordx4 v237, %[srdP], %[sp] offen lds\ns_add_u32 %[sp], %[sp], 0x80000\n"
        "v_mfma_f32_32x32x16_bf16 v[48:63], a[224:227], a[160:163], v[208:223]\nds_read_b64_tr_b16 v[134:135], v232 offset:17920\nv_exp_f32_e32 v100, v100\nv_add_f32_e32 v243, v243, v99\nv_cvt_pk_bf16_f32 v97, v98, v99\n"
        "v_mfma_f32_32x32x16_bf16 v[0:15], a[196:199], a[132:135], v[0:15]\nds_read_b64_tr_b16 v[136:137], v232 offset:18432\nds_read_b64_tr_b16 v[138:139], v232 offset:18944\nv_exp_f32_e32 v101, v101\nv_add_f32_e32 v242, v242, v100\n"
        "v_cvt_pk_bf16_f32 v66, v68, v69\nv_mfma_f32_32x32x16_bf16 v[16:31], a[196:199], a[164:167], v[16:31]\nds_read_b64_tr_b16 v[140:141], v232 offset:19456\nv_exp_f32_e32 v102, v102\nv_add_f32_e32 v243, v243, v101\n"
        "v_cvt_pk_bf16_f32 v98, v100, v101\nv_mfma_f32_32x32x16_bf16 v[32:47], a[228:231], a[132:135], v[32:47]\nds_read_b64_tr_b16 v[142:143], v232 offset:19968\nv_exp_f32_e32 v103, v103\nv_add_f32_e32 v242, v242, v102\n"
        "v_cvt_pk_bf16_f32 v67, v70, v71\nv_mfma_f32_32x32x16_bf16 v[48:63], a[228:231], a[164:167], v[48:63]\nds_read_b64_tr_b16 v[144:145], v232 offset:20480\nv_exp_f32_e32 v104, v104\nv_add_f32_e32 v243, v243, v103\n"
        "v_cvt_pk_bf16_f32 v99, v102, v103\nv_mfma_f32_32x32x16_bf16 v[0:15], a[200:203], a[136:139], v[0:15]\nds_read_b64_tr_b16 v[146:147], v232 offset:20992\nv_exp_f32_e32 v105, v105\nv_add_f32_e32 v242, v242, v104\n"
        "v_cvt_pk_bf16_f32 v72, v72, v73\nv_mfma_f32_32x32x16_bf16 v[16:31], a[200:203], a[168:171], v[16:31]\nds_read_b64_tr_b16 v[148:149], v232 offset:21504\nv_exp_f32_e32 v106, v106\nv_add_f32_e32 v243, v243, v105\n"
        "v_cvt_pk_bf16_f32 v104, v104, v105\nv_mfma_f32_32x32x16_bf16 v[32:47], a[232:235], a[136:139], v[32:47]\nds_read_b64_tr_b16 v[150:151], v232 offset:22016\nds_read_b64_tr_b16 v[152:153], v232 offset:22528\n"
        "v_exp_f32_e32 v107, v107\nv_add_f32_e32 v242, v242, v106\nv_cvt_pk_bf16_f32 v73, v74, v75\nv_mfma_f32_32x32x16_bf16 v[48:63], a[232:235], a[168:171], v[48:63]\nds_read_b64_tr_b16 v[154:155], v232 offset:23040\n"
        "v_exp_f32_e32 v108, v108\nv_add_f32_e32 v243, v243, v107\nv_cvt_pk_bf16_f32 v105, v106, v107\nv_mfma_f32_32x32x16_bf16 v[0:15], a[204:207], a[140:143], v[0:15]\nds_read_b64_tr_b16 v[156:157], v232 offset:23552\n"
        "v_exp_f32_e32 v109, v109\nv_add_f32_e32 v242, v242, v108\nv_cvt_pk_bf16_f32 v74, v76, v77\nv_mfma_f32_32x32x16_bf16 v[16:31], a[204:207], a[172:175], v[16:31]\nds_read_b64_tr_b16 v[158:159], v232 offset:24064\n"
        "v_exp_f32_e32 v110, v110\nv_add_f32_e32 v243, v243, v109\nv_cvt_pk_bf16_f32 v106, v108, v109\nv_mfma_f32_32x32x16_bf16 v[32:47], a[236:239], a[140:143], v[32:47]\nds_read_b64_tr_b16 v[160:161], v232 offset:24576\n"
        "v_exp_f32_e32 v111, v111\nv_add_f32_e32 v242, v242, v110\nv_cvt_pk_bf16_f32 v75, v78, v79\nv_mfma_f32_32x32x16_bf16 v[48:63], a[236:239], a[172:175], v[48:63]\nds_read_b64_tr_b16 v[162:163], v232 offset:25088\n"
        "v_exp_f32_e32 v112, v112\nv_add_f32_e32 v243, v243, v111\nv_cvt_pk_bf16_f32 v107, v110, v111\nv_mfma_f32_32x32x16_bf16 v[0:15], a[208:211], a[144:147], v[0:15]\nds_read_b64_tr_b16 v[164:165], v232 offset:25600\n"
        "ds_read_b64_tr_b16 v[166:167], v232 offset:26112\nv_exp_f32_e32 v113, v113\nv_add_f32_e32 v244, v244, v112\nv_cvt_pk_bf16_f32 v80, v80, v81\nv_mfma_f32_32x32x16_bf16 v[16:31], a[208:211], a[176:179], v[16:31]\n"
        "ds_read_b64_tr_b16 v[168:169], v232 offset:26624\nv_exp_f32_e32 v114, v114\nv_add_f32_e32 v245, v245, v113\nv_cvt_pk_bf16_f32 v112, v112, v113\nv_mfma_f32_32x32x16_bf16 v[32:47], a[240:243], a[144:147], v[32:47]\n"
        "ds_read_b64_tr_b16 v[170:171], v232 offset:27136\nv_exp_f32_e32 v115, v115\nv_add_f32_e32 v244, v244, v114\nv_cvt_pk_bf16_f32 v81, v82, v83\nv_mfma_f32_32x32x16_bf16 v[48:63], a[240:243], a[176:179], v[48:63]\n"
        "ds_read_b64_tr_b16 v[172:173], v232 offset:27648\nv_exp_f32_e32 v116, v116\nv_add_f32_e32 v245, v245, v115\nv_cvt_pk_bf16_f32 v113, v114, v115\nv_mfma_f32_32x32x16_bf16 v[0:15], a[212:215], a[148:151], v[0:15]\n"
        "ds_read_b64_tr_b16 v[174:175], v232 offset:28160\nv_exp_f32_e32 v117, v117\nv_add_f32_e32 v244, v244, v116\nv_cvt_pk_bf16_f32 v82, v84, v85\nv_mfma_f32_32x32x16_bf16 v[16:31], a[212:215], a[180:183], v[16:31]\n"
        "ds_read_b64_tr_b16 v[176:177], v232 offset:28672\nv_exp_f32_e32 v118, v118\nv_add_f32_e32 v245, v245, v117\nv_cvt_pk_bf16_f32 v114, v116, v117\nv_mfma_f32_32x32x16_bf16 v[32:47], a[244:247], a[148:151], v[32:47]\n"
        "ds_read_b64_tr_b16 v[178:179], v232 offset:29184\nds_read_b64_tr_b16 v[180:181], v232 offset:29696\nv_exp_f32_e32 v119, v119\nv_add_f32_e32 v244, v244, v118\nv_cvt_pk_bf16_f32 v83, v86, v87\n"
        "v_mfma_f32_32x32x16_bf16 v[48:63], a[244:247], a[180:183], v[48:63]\nds_read_b64_tr_b16 v[182:183], v232 offset:30208\nv_exp_f32_e32 v120, v120\nv_add_f32_e32 v245, v245, v119\nv_cvt_pk_bf16_f32 v115, v118, v119\n"
        "v_mfma_f32_32x32x16_bf16 v[0:15], a[216:219], a[152:155], v[0:15]\nds_read_b64_tr_b16 v[184:185], v232 offset:30720\nv_exp_f32_e32 v121, v121\nv_add_f32_e32 v244, v244, v120\nv_cvt_pk_bf16_f32 v88, v88, v89\n"
        "v_mfma_f32_32x32x16_bf16 v[16:31], a[216:219], a[184:187], v[16:31]\nds_read_b64_tr_b16 v[186:187], v232 offset:31232\nv_exp_f32_e32 v122, v122\nv_add_f32_e32 v245, v245, v121\nv_cvt_pk_bf16_f32 v120, v120, v121\n"
        "v_mfma_f32_32x32x16_bf16 v[32:47], a[248:251], a[152:155], v[32:47]\nds_read_b64_tr_b16 v[188:189], v232 offset:31744\nv_exp_f32_e32 v123, v123\nv_add_f32_e32 v244, v244, v122\nv_cvt_pk_bf16_f32 v89, v90, v91\n"
        "v_mfma_f32_32x32x16_bf16 v[48:63], a[248:251], a[184:187], v[48:63]\nds_read_b64_tr_b16 v[190:191], v232 offset:32256\nv_exp_f32_e32 v124, v124\nv_add_f32_e32 v245, v245, v123\nv_cvt_pk_bf16_f32 v121, v122, v123\n"
        "v_mfma_f32_32x32x16_bf16 v[0:15], a[220:223], a[156:159], v[0:15]\nv_exp_f32_e32 v125, v125\nv_add_f32_e32 v244, v244, v124\nv_cvt_pk_bf16_f32 v90, v92, v93\nv_mfma_f32_32x32x16_bf16 v[16:31], a[220:223], a[188:191], v[16:31]\n"
        "v_exp_f32_e32 v126, v126\nv_add_f32_e32 v245, v245, v125\nv_cvt_pk_bf16_f32 v122, v124, v125\nv_mfma_f32_32x32x16_bf16 v[32:47], a[252:255], a[156:159], v[32:47]\nv_exp_f32_e32 v127, v127\nv_add_f32_e32 v244, v244, v126\n"
        "v_cvt_pk_bf16_f32 v91, v94, v95\nv_mfma_f32_32x32x16_bf16 v[48:63], a[252:255], a[188:191], v[48:63]\nv_add_f32_e32 v245, v245, v127\nv_cvt_pk_bf16_f32 v123, v126, v127\ns_waitcnt vmcnt(1) lgkmcnt(0)\ns_barrier\n"
        "v_mfma_f32_32x32x16_bf16 a[0:15], v[64:67], v[128:131], a[0:15]\ns_cmp_le_u32 %[cnt], %[mthr]\ns_cbranch_scc1 L_mask0%=\nL_maskb0%=:\nv_max_f32_e32 v68, v0, v1\nv_max_f32_e32 v69, v2, v3\nv_max3_f32 v68, v68, v4, v5\n"
        "v_mfma_f32_32x32x16_bf16 a[64:79], v[80:83], v[128:131], a[64:79]\nds_read_b128 a[192:195], v224 offset:16384\nv_max3_f32 v69, v69, v6, v7\nv_max3_f32 v68, v68, v8, v9\nv_max3_f32 v69, v69, v10, v11\n"
        "v_max3_f32 v68, v68, v12, v13\nv_max3_f32 v69, v69, v14, v15\nv_mfma_f32_32x32x16_bf16 a[16:31], v[64:67], v[132:135], a[16:31]\nds_read_b128 a[224:227], v224 offset:24576\nv_max_f32_e32 v70, v16, v17\n"
        "v_max_f32_e32 v71, v18, v19\nv_max3_f32 v70, v70, v20, v21\nv_max3_f32 v71, v71, v22, v23\nv_max3_f32 v70, v70, v24, v25\nv_mfma_f32_32x32x16_bf16 a[80:95], v[80:83], v[132:135], a[80:95]\n"
        "ds_read_b128 a[196:199], v225 offset:16384\nv_max3_f32 v71, v71, v26, v27\nv_max3_f32 v70, v70, v28, v29\nv_max3_f32 v71, v71, v30, v31\nv_max3_f32 v68, v68, v32, v33\nv_max3_f32 v69, v69, v34, v35\n"
        "v_mfma_f32_32x32x16_bf16 a[32:47], v[64:67], v[136:139], a[32:47]\nds_read_b128 a[228:231], v225 offset:24576\nv_max3_f32 v68, v68, v36, v37\nv_max3_f32 v69, v69, v38, v39\nv_max3_f32 v68, v68, v40, v41\n"
        "v_max3_f32 v69, v69, v42, v43\nv_max3_f32 v68, v68, v44, v45\nv_mfma_f32_32x32x16_bf16 a[96:111], v[80:83], v[136:139], a[96:111]\nds_read_b128 a[200:203], v226 offset:16384\nv_max3_f32 v69, v69, v46, v47\n"
        "v_max3_f32 v70, v70, v48, v49\nv_max3_f32 v71, v71, v50, v51\nv_max3_f32 v70, v70, v52, v53\nv_max3_f32 v71, v71, v54, v55\nv_mfma_f32_32x32x16_bf16 a[48:63], v[64:67], v[140:143], a[48:63]\n"
        "ds_read_b128 a[232:235], v226 offset:24576\nv_max3_f32 v70, v70, v56, v57\nv_max3_f32 v71, v71, v58, v59\nv_max3_f32 v70, v70, v60, v61\nv_max3_f32 v71, v71, v62, v63\n"
        "v_mfma_f32_32x32x16_bf16 a[112:127], v[80:83], v[140:143], a[112:127]\nds_read_b128 a[204:207], v227 offset:16384\nv_max3_f32 v78, v68, v69, v70\nv_max_f32_e32 v78, v78, v71\nv_cmp_lt_f32_e32 vcc, 0x41800000, v78\n"
        "s_cbranch_vccnz L_rareA0%=\nL_rareAb0%=:\nv_mfma_f32_32x32x16_bf16 a[0:15], v[72:75], v[144:147], a[0:15]\ns_add_u32 m0, %[m0k], 0x0\nds_read_b128 a[236:239], v227 offset:24576\ns_and_b32 %[t0], s40, 63\n"
        "s_lshl_b32 %[sk0], %[t0], 14\ns_lshr_b64 s[40:41], s[40:41], 6\nv_exp_f32_e32 v0, v0\nv_exp_f32_e32 v1, v1\nv_mfma_f32_32x32x16_bf16 a[64:79], v[88:91], v[144:147], a[64:79]\n"
        "buffer_load_dwordx4 v233, %[srdK], %[sk0] offen offset:0 lds\nv_exp_f32_e32 v2, v2\nv_add_f32_e32 v242, v242, v0\nv_add_f32_e32 v243, v243, v1\nv_mfma_f32_32x32x16_bf16 a[16:31], v[72:75], v[148:151], a[16:31]\n"
        "ds_read_b128 a[208:211], v228 offset:16384\nv_exp_f32_e32 v3, v3\nv_exp_f32_e32 v4, v4\nv_add_f32_e32 v242, v242, v2\nv_mfma_f32_32x32x16_bf16 a[80:95], v[88:91], v[148:151], a[80:95]\nds_read_b128 a[240:243], v228 offset:24576\n"
        "v_exp_f32_e32 v5, v5\nv_exp_f32_e32 v6, v6\nv_add_f32_e32 v243, v243, v3\nv_add_f32_e32 v242, v242, v4\nv_mfma_f32_32x32x16_bf16 a[32:47], v[72:75], v[152:155], a[32:47]\n"
        "buffer_load_dwordx4 v234, %[srdK], %[sk0] offen offset:1024 lds\nv_exp_f32_e32 v7, v7\nv_add_f32_e32 v243, v243, v5\nv_add_f32_e32 v242, v242, v6\nv_mfma_f32_32x32x16_bf16 a[96:111], v[88:91], v[152:155], a[96:111]\n"
        "ds_read_b128 a[212:215], v229 offset:16384\nv_exp_f32_e32 v8, v8\nv_add_f32_e32 v243, v243, v7\nv_mfma_f32_32x32x16_bf16 a[48:63], v[72:75], v[156:159], a[48:63]\nds_read_b128 a[244:247], v229 offset:24576\nv_exp_f32_e32 v9, v9\n"
        "v_add_f32_e32 v242, v242, v8\nv_mfma_f32_32x32x16_bf16 a[112:127], v[88:91], v[156:159], a[112:127]\nbuffer_load_dwordx4 v235, %[srdK], %[sk0] offen offset:2048 lds\nv_exp_f32_e32 v10, v10\nv_add_f32_e32 v243, v243, v9\n"
        "v_mfma_f32_32x32x16_bf16 a[0:15], v[96:99], v[160:163], a[0:15]\nds_read_b128 a[216:219], v230 offset:16384\nv_exp_f32_e32 v11, v11\nv_add_f32_e32 v242, v242, v10\n"
        "v_mfma_f32_32x32x16_bf16 a[64:79], v[112:115], v[160:163], a[64:79]\nds_read_b128 a[248:251], v230 offset:24576\nv_exp_f32_e32 v12, v12\nv_add_f32_e32 v243, v243, v11\n"
        "v_mfma_f32_32x32x16_bf16 a[16:31], v[96:99], v[164:167], a[16:31]\nbuffer_load_dwordx4 v236, %[srdK], %[sk0] offen offset:3072 lds\nv_exp_f32_e32 v13, v13\nv_add_f32_e32 v242, v242, v12\n"
        "v_mfma_f32_32x32x16_bf16 a[80:95], v[112:115], v[164:167], a[80:95]\nds_read_b128 a[220:223], v231 offset:16384\nv_exp_f32_e32 v14, v14\nv_add_f32_e32 v243, v243, v13\n"
        "v_mfma_f32_32x32x16_bf16 a[32:47], v[96:99], v[168:171], a[32:47]\ns_add_u32 m0, %[m0k], 0xc000\nds_read_b128 a[252:255], v231 offset:24576\nv_exp_f32_e32 v15, v15\nv_add_f32_e32 v242, v242, v14\n"
        "v_mfma_f32_32x32x16_bf16 a[96:111], v[112:115], v[168:171], a[96:111]\nbuffer_load_dwordx4 v237, %[srdV], %[sk1] offen offset:0 lds\nv_exp_f32_e32 v16, v16\nv_add_f32_e32 v243, v243, v15\n"
        "v_mfma_f32_32x32x16_bf16 a[48:63], v[96:99], v[172:175], a[48:63]\nv_exp_f32_e32 v17, v17\nv_exp_f32_e32 v18, v18\nv_add_f32_e32 v244, v244, v16\nv_mfma_f32_32x32x16_bf16 a[112:127], v[112:115], v[172:175], a[112:127]\n"
        "v_exp_f32_e32 v19, v19\nv_exp_f32_e32 v20, v20\nv_add_f32_e32 v245, v245, v17\nv_add_f32_e32 v244, v244, v18\nv_mfma_f32_32x32x16_bf16 a[0:15], v[104:107], v[176:179], a[0:15]\n"
        "buffer_load_dwordx4 v237, %[srdV], %[sk1] offen offset:1024 lds\nv_exp_f32_e32 v21, v21\nv_add_f32_e32 v245, v245, v19\nv_add_f32_e32 v244, v244, v20\nv_mfma_f32_32x32x16_bf16 a[64:79], v[120:123], v[176:179], a[64:79]\n"
        "v_exp_f32_e32 v22, v22\nv_exp_f32_e32 v23, v23\nv_add_f32_e32 v245, v245, v21\nv_mfma_f32_32x32x16_bf16 a[16:31], v[104:107], v[180:183], a[16:31]\nv_exp_f32_e32 v24, v24\nv_exp_f32_e32 v25, v25\nv_add_f32_e32 v244, v244, v22\n"
        "v_add_f32_e32 v245, v245, v23\nv_mfma_f32_32x32x16_bf16 a[80:95], v[120:123], v[180:183], a[80:95]\nbuffer_load_dwordx4 v237, %[srdV], %[sk1] offen offset:2048 lds\nv_exp_f32_e32 v26, v26\nv_add_f32_e32 v244, v244, v24\n"
        "v_add_f32_e32 v245, v245, v25\nv_mfma_f32_32x32x16_bf16 a[32:47], v[104:107], v[184:187], a[32:47]\nv_exp_f32_e32 v27, v27\nv_exp_f32_e32 v28, v28\nv_add_f32_e32 v244, v244, v26\n"
        "v_mfma_f32_32x32x16_bf16 a[96:111], v[120:123], v[184:187], a[96:111]\nv_exp_f32_e32 v29, v29\nv_exp_f32_e32 v30, v30\nv_add_f32_e32 v245, v245, v27\nv_add_f32_e32 v244, v244, v28\n"
        "v_mfma_f32_32x32x16_bf16 a[48:63], v[104:107], v[188:191], a[48:63]\nbuffer_load_dwordx4 v237, %[srdV], %[sk1] offen offset:3072 lds\nv_exp_f32_e32 v31, v31\nv_add_f32_e32 v245, v245, v29\nv_add_f32_e32 v244, v244, v30\n"
        "v_mfma_f32_32x32x16_bf16 a[112:127], v[120:123], v[188:191], a[112:127]\nv_add_f32_e32 v245, v245, v31\ns_cmp_lg_u32 %[pend], 0\ns_cbranch_scc1 L_rareB0%=\nL_rareBb0%=:\ns_waitcnt lgkmcnt(0)\ns_branch L_loop%=\nL_maskf%=:\n"
        "s_nop 7\ns_nop 3\nv_mov_b32_e32 v84, 0xff800000\ns_cmp_gt_i32 %[dq0], 0\ns_cbranch_scc1 L_maskf_n00%=\ns_cmp_eq_i32 %[dq0], 0\ns_cbranch_scc1 L_maskf_t00%=\nv_mov_b32_e32 v0, v84\nv_mov_b32_e32 v1, v84\nv_mov_b32_e32 v2, v84\n"
        "v_mov_b32_e32 v3, v84\nv_mov_b32_e32 v4, v84\nv_mov_b32_e32 v5, v84\nv_mov_b32_e32 v6, v84\nv_mov_b32_e32 v7, v84\nv_mov_b32_e32 v8, v84\nv_mov_b32_e32 v9, v84\nv_mov_b32_e32 v10, v84\nv_mov_b32_e32 v11, v84\n"
        "v_mov_b32_e32 v12, v84\nv_mov_b32_e32 v13, v84\nv_mov_b32_e32 v14, v84\nv_mov_b32_e32 v15, v84\ns_branch L_maskf_n00%=\nL_maskf_t00%=:\nv_cmp_gt_i32_e64 %[mk0], 0, v238\nv_cmp_gt_i32_e64 %[mk1], 1, v238\n"
        "v_cmp_gt_i32_e64 %[mk2], 2, v238\nv_cndmask_b32_e64 v0, v0, v84, %[mk0]\nv_cmp_gt_i32_e64 %[mk0], 3, v238\nv_cndmask_b32_e64 v1, v1, v84, %[mk1]\nv_cmp_gt_i32_e64 %[mk1], 8, v238\nv_cndmask_b32_e64 v2, v2, v84, %[mk2]\n"
        "v_cmp_gt_i32_e64 %[mk2], 9, v238\nv_cndmask_b32_e64 v3, v3, v84, %[mk0]\nv_cmp_gt_i32_e64 %[mk0], 10, v238\nv_cndmask_b32_e64 v4, v4, v84, %[mk1]\nv_cmp_gt_i32_e64 %[mk1], 11, v238\nv_cndmask_b32_e64 v5, v5, v84, %[mk2]\n"
        "v_cmp_gt_i32_e64 %[mk2], 16, v238\nv_cndmask_b32_e64 v6, v6, v84, %[mk0]\nv_cmp_gt_i32_e64 %[mk0], 17, v238\nv_cndmask_b32_e64 v7, v7, v84, %[mk1]\nv_cmp_gt_i32_e64 %[mk1], 18, v238\nv_cndmask_b32_e64 v8, v8, v84, %[mk2]\n"
        "v_cmp_gt_i32_e64 %[mk2], 19, v238\nv_cndmask_b32_e64 v9, v9, v84, %[mk0]\nv_cmp_gt_i32_e64 %[mk0], 24, v238\nv_cndmask_b32_e64 v10, v10, v84, %[mk1]\nv_cmp_gt_i32_e64 %[mk1], 25, v238\nv_cndmask_b32_e64 v11, v11, v84, %[mk2]\n"
        "v_cmp_gt_i32_e64 %[mk2], 26, v238\nv_cndmask_b32_e64 v12, v12, v84, %[mk0]\nv_cmp_gt_i32_e64 %[mk0], 27, v238\nv_cndmask_b32_e64 v13, v13, v84, %[mk1]\ns_nop 1\nv_cndmask_b32_e64 v14, v14, v84, %[mk2]\n"
        "v_cndmask_b32_e64 v15, v15, v84, %[mk0]\nL_maskf_n00%=:\ns_cmp_gt_i32 %[dq0], 1\ns_cbranch_scc1 L_maskf_n01%=\ns_cmp_eq_i32 %[dq0], 1\ns_cbranch_scc1 L_maskf_t01%=\nv_mov_b32_e32 v32, v84\nv_mov_b32_e32 v33, v84\n"
        "v_mov_b32_e32 v34, v84\nv_mov_b32_e32 v35, v84\nv_mov_b32_e32 v36, v84\nv_mov_b32_e32 v37, v84\nv_mov_b32_e32 v38, v84\nv_mov_b32_e32 v39, v84\nv_mov_b32_e32 v40, v84\nv_mov_b32_e32 v41, v84\nv_mov_b32_e32 v42, v84\n"
        "v_mov_b32_e32 v43, v84\nv_mov_b32_e32 v44, v84\nv_mov_b32_e32 v45, v84\nv_mov_b32_e32 v46, v84\nv_mov_b32_e32 v47, v84\ns_branch L_maskf_n01%=\nL_maskf_t01%=:\nv_cmp_gt_i32_e64 %[mk0], 0, v238\nv_cmp_gt_i32_e64 %[mk1], 1, v238\n"
        "v_cmp_gt_i32_e64 %[mk2], 2, v238\nv_cndmask_b32_e64 v32, v32, v84, %[mk0]\nv_cmp_gt_i32_e64 %[mk0], 3, v238\nv_cndmask_b32_e64 v33, v33, v84, %[mk1]\nv_cmp_gt_i32_e64 %[mk1], 8, v238\nv_cndmask_b32_e64 v34, v34, v84, %[mk2]\n"
        "v_cmp_gt_i32_e64 %[mk2], 9, v238\nv_cndmask_b32_e64 v35, v35, v84, %[mk0]\nv_cmp_gt_i32_e64 %[mk0], 10, v238\nv_cndmask_b32_e64 v36, v36, v84, %[mk1]\nv_cmp_gt_i32_e64 %[mk1], 11, v238\nv_cndmask_b32_e64 v37, v37, v84, %[mk2]\n"
        "v_cmp_gt_i32_e64 %[mk2], 16, v238\nv_cndmask_b32_e64 v38, v38, v84, %[mk0]\nv_cmp_gt_i32_e64 %[mk0], 17, v238\nv_cndmask_b32_e64 v39, v39, v84, %[mk1]\nv_cmp_gt_i32_e64 %[mk1], 18, v238\nv_cndmask_b32_e64 v40, v40, v84, %[mk2]\n"
        "v_cmp_gt_i32_e64 %[mk2], 19, v238\nv_cndmask_b32_e64 v41, v41, v84, %[mk0]\nv_cmp_gt_i32_e64 %[mk0], 24, v238\nv_cndmask_b32_e64 v42, v42, v84, %[mk1]\nv_cmp_gt_i32_e64 %[mk1], 25, v238\nv_cndmask_b32_e64 v43, v43, v84, %[mk2]\n"
        "v_cmp_gt_i32_e64 %[mk2], 26, v238\nv_cndmask_b32_e64 v44, v44, v84, %[mk0]\nv_cmp_gt_i32_e64 %[mk0], 27, v238\nv_cndmask_b32_e64 v45, v45, v84, %[mk1]\ns_nop 1\nv_cndmask_b32_e64 v46, v46, v84, %[mk2]\n"
        "v_cndmask_b32_e64 v47, v47, v84, %[mk0]\nL_maskf_n01%=:\ns_cmp_gt_i32 %[dq1], 0\ns_cbranch_scc1 L_maskf_n10%=\ns_cmp_eq_i32 %[dq1], 0\ns_cbranch_scc1 L_maskf_t10%=\nv_mov_b32_e32 v16, v84\nv_mov_b32_e32 v17, v84\n"
        "v_mov_b32_e32 v18, v84\nv_mov_b32_e32 v19, v84\nv_mov_b32_e32 v20, v84\nv_mov_b32_e32 v21, v84\nv_mov_b32_e32 v22, v84\nv_mov_b32_e32 v23, v84\nv_mov_b32_e32 v24, v84\nv_mov_b32_e32 v25, v84\nv_mov_b32_e32 v26, v84\n"
        "v_mov_b32_e32 v27, v84\nv_mov_b32_e32 v28, v84\nv_mov_b32_e32 v29, v84\nv_mov_b32_e32 v30, v84\nv_mov_b32_e32 v31, v84\ns_branch L_maskf_n10%=\nL_maskf_t10%=:\nv_cmp_gt_i32_e64 %[mk0], 0, v238\nv_cmp_gt_i32_e64 %[mk1], 1, v238\n"
        "v_cmp_gt_i32_e64 %[mk2], 2, v238\nv_cndmask_b32_e64 v16, v16, v84, %[mk0]\nv_cmp_gt_i32_e64 %[mk0], 3, v238\nv_cndmask_b32_e64 v17, v17, v84, %[mk1]\nv_cmp_gt_i32_e64 %[mk1], 8, v238\nv_cndmask_b32_e64 v18, v18, v84, %[mk2]\n"
        "v_cmp_gt_i32_e64 %[mk2], 9, v238\nv_cndmask_b32_e64 v19, v19, v84, %[mk0]\nv_cmp_gt_i32_e64 %[mk0], 10, v238\nv_cndmask_b32_e64 v20, v20, v84, %[mk1]\nv_cmp_gt_i32_e64 %[mk1], 11, v238\nv_cndmask_b32_e64 v21, v21, v84, %[mk2]\n"
        "v_cmp_gt_i32_e64 %[mk2], 16, v238\nv_cndmask_b32_e64 v22, v22, v84, %[mk0]\nv_cmp_gt_i32_e64 %[mk0], 17, v238\nv_cndmask_b32_e64 v23, v23, v84, %[mk1]\nv_cmp_gt_i32_e64 %[mk1], 18, v238\nv_cndmask_b32_e64 v24, v24, v84, %[mk2]\n"
        "v_cmp_gt_i32_e64 %[mk2], 19, v238\nv_cndmask_b32_e64 v25, v25, v84, %[mk0]\nv_cmp_gt_i32_e64 %[mk0], 24, v238\nv_cndmask_b32_e64 v26, v26, v84, %[mk1]\nv_cmp_gt_i32_e64 %[mk1], 25, v238\nv_cndmask_b32_e64 v27, v27, v84, %[mk2]\n"
        "v_cmp_gt_i32_e64 %[mk2], 26, v238\nv_cndmask_b32_e64 v28, v28, v84, %[mk0]\nv_cmp_gt_i32_e64 %[mk0], 27, v238\nv_cndmask_b32_e64 v29, v29, v84, %[mk1]\ns_nop 1\nv_cndmask_b32_e64 v30, v30, v84, %[mk2]\n"
        "v_cndmask_b32_e64 v31, v31, v84, %[mk0]\nL_maskf_n10%=:\ns_cmp_gt_i32 %[dq1], 1\ns_cbranch_scc1 L_maskf_n11%=\ns_cmp_eq_i32 %[dq1], 1\ns_cbranch_scc1 L_maskf_t11%=\nv_mov_b32_e32 v48, v84\nv_mov_b32_e32 v49, v84\n"
        "v_mov_b32_e32 v50, v84\nv_mov_b32_e32 v51, v84\nv_mov_b32_e32 v52, v84\nv_mov_b32_e32 v53, v84\nv_mov_b32_e32 v54, v84\nv_mov_b32_e32 v55, v84\nv_mov_b32_e32 v56, v84\nv_mov_b32_e32 v57, v84\nv_mov_b32_e32 v58, v84\n"
        "v_mov_b32_e32 v59, v84\nv_mov_b32_e32 v60, v84\nv_mov_b32_e32 v61, v84\nv_mov_b32_e32 v62, v84\nv_mov_b32_e32 v63, v84\ns_branch L_maskf_n11%=\nL_maskf_t11%=:\nv_cmp_gt_i32_e64 %[mk0], 0, v238\nv_cmp_gt_i32_e64 %[mk1], 1, v238\n"
        "v_cmp_gt_i32_e64 %[mk2], 2, v238\nv_cndmask_b32_e64 v48, v48, v84, %[mk0]\nv_cmp_gt_i32_e64 %[mk0], 3, v238\nv_cndmask_b32_e64 v49, v49, v84, %[mk1]\nv_cmp_gt_i32_e64 %[mk1], 8, v238\nv_cndmask_b32_e64 v50, v50, v84, %[mk2]\n"
        "v_cmp_gt_i32_e64 %[mk2], 9, v238\nv_cndmask_b32_e64 v51, v51, v84, %[mk0]\nv_cmp_gt_i32_e64 %[mk0], 10, v238\nv_cndmask_b32_e64 v52, v52, v84, %[mk1]\nv_cmp_gt_i32_e64 %[mk1], 11, v238\nv_cndmask_b32_e64 v53, v53, v84, %[mk2]\n"
        "v_cmp_gt_i32_e64 %[mk2], 16, v238\nv_cndmask_b32_e64 v54, v54, v84, %[mk0]\nv_cmp_gt_i32_e64 %[mk0], 17, v238\nv_cndmask_b32_e64 v55, v55, v84, %[mk1]\nv_cmp_gt_i32_e64 %[mk1], 18, v238\nv_cndmask_b32_e64 v56, v56, v84, %[mk2]\n"
        "v_cmp_gt_i32_e64 %[mk2], 19, v238\nv_cndmask_b32_e64 v57, v57, v84, %[mk0]\nv_cmp_gt_i32_e64 %[mk0], 24, v238\nv_cndmask_b32_e64 v58, v58, v84, %[mk1]\nv_cmp_gt_i32_e64 %[mk1], 25, v238\nv_cndmask_b32_e64 v59, v59, v84, %[mk2]\n"
        "v_cmp_gt_i32_e64 %[mk2], 26, v238\nv_cndmask_b32_e64 v60, v60, v84, %[mk0]\nv_cmp_gt_i32_e64 %[mk0], 27, v238\nv_cndmask_b32_e64 v61, v61, v84, %[mk1]\ns_nop 1\nv_cndmask_b32_e64 v62, v62, v84, %[mk2]\n"
        "v_cndmask_b32_e64 v63, v63, v84, %[mk0]\nL_maskf_n11%=:\ns_sub_i32 %[dq0], %[dq0], 2\ns_sub_i32 %[dq1], %[dq1], 2\ns_branch L_maskfb%=\nL_mask0%=:\ns_nop 7\ns_nop 3\nv_mov_b32_e32 v84, 0xff800000\ns_cmp_gt_i32 %[dq0], 0\n"
        "s_cbranch_scc1 L_mask0_n00%=\ns_cmp_eq_i32 %[dq0], 0\ns_cbranch_scc1 L_mask0_t00%=\nv_mov_b32_e32 v0, v84\nv_mov_b32_e32 v1, v84\nv_mov_b32_e32 v2, v84\nv_mov_b32_e32 v3, v84\nv_mov_b32_e32 v4, v84\nv_mov_b32_e32 v5, v84\n"
        "v_mov_b32_e32 v6, v84\nv_mov_b32_e32 v7, v84\nv_mov_b32_e32 v8, v84\nv_mov_b32_e32 v9, v84\nv_mov_b32_e32 v10, v84\nv_mov_b32_e32 v11, v84\nv_mov_b32_e32 v12, v84\nv_mov_b32_e32 v13, v84\nv_mov_b32_e32 v14, v84\n"
        "v_mov_b32_e32 v15, v84\ns_branch L_mask0_n00%=\nL_mask0_t00%=:\nv_cmp_gt_i32_e64 %[mk0], 0, v238\nv_cmp_gt_i32_e64 %[mk1], 1, v238\nv_cmp_gt_i32_e64 %[mk2], 2, v238\nv_cndmask_b32_e64 v0, v0, v84, %[mk0]\n"
        "v_cmp_gt_i32_e64 %[mk0], 3, v238\nv_cndmask_b32_e64 v1, v1, v84, %[mk1]\nv_cmp_gt_i32_e64 %[mk1], 8, v238\nv_cndmask_b32_e64 v2, v2, v84, %[mk2]\nv_cmp_gt_i32_e64 %[mk2], 9, v238\nv_cndmask_b32_e64 v3, v3, v84, %[mk0]\n"
        "v_cmp_gt_i32_e64 %[mk0], 10, v238\nv_cndmask_b32_e64 v4, v4, v84, %[mk1]\nv_cmp_gt_i32_e64 %[mk1], 11, v238\nv_cndmask_b32_e64 v5, v5, v84, %[mk2]\nv_cmp_gt_i32_e64 %[mk2], 16, v238\nv_cndmask_b32_e64 v6, v6, v84, %[mk0]\n"
        "v_cmp_gt_i32_e64 %[mk0], 17, v238\nv_cndmask_b32_e64 v7, v7, v84, %[mk1]\nv_cmp_gt_i32_e64 %[mk1], 18, v238\nv_cndmask_b32_e64 v8, v8, v84, %[mk2]\nv_cmp_gt_i32_e64 %[mk2], 19, v238\nv_cndmask_b32_e64 v9, v9, v84, %[mk0]\n"
        "v_cmp_gt_i32_e64 %[mk0], 24, v238\nv_cndmask_b32_e64 v10, v10, v84, %[mk1]\nv_cmp_gt_i32_e64 %[mk1], 25, v238\nv_cndmask_b32_e64 v11, v11, v84, %[mk2]\nv_cmp_gt_i32_e64 %[mk2], 26, v238\nv_cndmask_b32_e64 v12, v12, v84, %[mk0]\n"
        "v_cmp_gt_i32_e64 %[mk0], 27, v238\nv_cndmask_b32_e64 v13, v13, v84, %[mk1]\ns_nop 1\nv_cndmask_b32_e64 v14, v14, v84, %[mk2]\nv_cndmask_b32_e64 v15, v15, v84, %[mk0]\nL_mask0_n00%=:\ns_cmp_gt_i32 %[dq0], 1\n"
        "s_cbranch_scc1 L_mask0_n01%=\ns_cmp_eq_i32 %[dq0], 1\ns_cbranch_scc1 L_mask0_t01%=\nv_mov_b32_e32 v32, v84\nv_mov_b32_e32 v33, v84\nv_mov_b32_e32 v34, v84\nv_mov_b32_e32 v35, v84\nv_mov_b32_e32 v36, v84\nv_mov_b32_e32 v37, v84\n"
        "v_mov_b32_e32 v38, v84\nv_mov_b32_e32 v39, v84\nv_mov_b32_e32 v40, v84\nv_mov_b32_e32 v41, v84\nv_mov_b32_e32 v42, v84\nv_mov_b32_e32 v43, v84\nv_mov_b32_e32 v44, v84\nv_mov_b32_e32 v45, v84\nv_mov_b32_e32 v46, v84\n"
        "v_mov_b32_e32 v47, v84\ns_branch L_mask0_n01%=\nL_mask0_t01%=:\nv_cmp_gt_i32_e64 %[mk0], 0, v238\nv_cmp_gt_i32_e64 %[mk1], 1, v238\nv_cmp_gt_i32_e64 %[mk2], 2, v238\nv_cndmask_b32_e64 v32, v32, v84, %[mk0]\n"
        "v_cmp_gt_i32_e64 %[mk0], 3, v238\nv_cndmask_b32_e64 v33, v33, v84, %[mk1]\nv_cmp_gt_i32_e64 %[mk1], 8, v238\nv_cndmask_b32_e64 v34, v34, v84, %[mk2]\nv_cmp_gt_i32_e64 %[mk2], 9, v238\nv_cndmask_b32_e64 v35, v35, v84, %[mk0]\n"
        "v_cmp_gt_i32_e64 %[mk0], 10, v238\nv_cndmask_b32_e64 v36, v36, v84, %[mk1]\nv_cmp_gt_i32_e64 %[mk1], 11, v238\nv_cndmask_b32_e64 v37, v37, v84, %[mk2]\nv_cmp_gt_i32_e64 %[mk2], 16, v238\nv_cndmask_b32_e64 v38, v38, v84, %[mk0]\n"
        "v_cmp_gt_i32_e64 %[mk0], 17, v238\nv_cndmask_b32_e64 v39, v39, v84, %[mk1]\nv_cmp_gt_i32_e64 %[mk1], 18, v238\nv_cndmask_b32_e64 v40, v40, v84, %[mk2]\nv_cmp_gt_i32_e64 %[mk2], 19, v238\nv_cndmask_b32_e64 v41, v41, v84, %[mk0]\n"
        "v_cmp_gt_i32_e64 %[mk0], 24, v238\nv_cndmask_b32_e64 v42, v42, v84, %[mk1]\nv_cmp_gt_i32_e64 %[mk1], 25, v238\nv_cndmask_b32_e64 v43, v43, v84, %[mk2]\nv_cmp_gt_i32_e64 %[mk2], 26, v238\nv_cndmask_b32_e64 v44, v44, v84, %[mk0]\n"
        "v_cmp_gt_i32_e64 %[mk0], 27, v238\nv_cndmask_b32_e64 v45, v45, v84, %[mk1]\ns_nop 1\nv_cndmask_b32_e64 v46, v46, v84, %[mk2]\nv_cndmask_b32_e64 v47, v47, v84, %[mk0]\nL_mask0_n01%=:\ns_cmp_gt_i32 %[dq1], 0\n"
        "s_cbranch_scc1 L_mask0_n10%=\ns_cmp_eq_i32 %[dq1], 0\ns_cbranch_scc1 L_mask0_t10%=\nv_mov_b32_e32 v16, v84\nv_mov_b32_e32 v17, v84\nv_mov_b32_e32 v18, v84\nv_mov_b32_e32 v19, v84\nv_mov_b32_e32 v20, v84\nv_mov_b32_e32 v21, v84\n"
        "v_mov_b32_e32 v22, v84\nv_mov_b32_e32 v23, v84\nv_mov_b32_e32 v24, v84\nv_mov_b32_e32 v25, v84\nv_mov_b32_e32 v26, v84\nv_mov_b32_e32 v27, v84\nv_mov_b32_e32 v28, v84\nv_mov_b32_e32 v29, v84\nv_mov_b32_e32 v30, v84\n"
        "v_mov_b32_e32 v31, v84\ns_branch L_mask0_n10%=\nL_mask0_t10%=:\nv_cmp_gt_i32_e64 %[mk0], 0, v238\nv_cmp_gt_i32_e64 %[mk1], 1, v238\nv_cmp_gt_i32_e64 %[mk2], 2, v238\nv_cndmask_b32_e64 v16, v16, v84, %[mk0]\n"
        "v_cmp_gt_i32_e64 %[mk0], 3, v238\nv_cndmask_b32_e64 v17, v17, v84, %[mk1]\nv_cmp_gt_i32_e64 %[mk1], 8, v238\nv_cndmask_b32_e64 v18, v18, v84, %[mk2]\nv_cmp_gt_i32_e64 %[mk2], 9, v238\nv_cndmask_b32_e64 v19, v19, v84, %[mk0]\n"
        "v_cmp_gt_i32_e64 %[mk0], 10, v238\nv_cndmask_b32_e64 v20, v20, v84, %[mk1]\nv_cmp_gt_i32_e64 %[mk1], 11, v238\nv_cndmask_b32_e64 v21, v21, v84, %[mk2]\nv_cmp_gt_i32_e64 %[mk2], 16, v238\nv_cndmask_b32_e64 v22, v22, v84, %[mk0]\n"
        "v_cmp_gt_i32_e64 %[mk0], 17, v238\nv_cndmask_b32_e64 v23, v23, v84, %[mk1]\nv_cmp_gt_i32_e64 %[mk1], 18, v238\nv_cndmask_b32_e64 v24, v24, v84, %[mk2]\nv_cmp_gt_i32_e64 %[mk2], 19, v238\nv_cndmask_b32_e64 v25, v25, v84, %[mk0]\n"
        "v_cmp_gt_i32_e64 %[mk0], 24, v238\nv_cndmask_b32_e64 v26, v26, v84, %[mk1]\nv_cmp_gt_i32_e64 %[mk1], 25, v238\nv_cndmask_b32_e64 v27, v27, v84, %[mk2]\nv_cmp_gt_i32_e64 %[mk2], 26, v238\nv_cndmask_b32_e64 v28, v28, v84, %[mk0]\n"
        "v_cmp_gt_i32_e64 %[mk0], 27, v238\nv_cndmask_b32_e64 v29, v29, v84, %[mk1]\ns_nop 1\nv_cndmask_b32_e64 v30, v30, v84, %[mk2]\nv_cndmask_b32_e64 v31, v31, v84, %[mk0]\nL_mask0_n10%=:\ns_cmp_gt_i32 %[dq1], 1\n"
        "s_cbranch_scc1 L_mask0_n11%=\ns_cmp_eq_i32 %[dq1], 1\ns_cbranch_scc1 L_mask0_t11%=\nv_mov_b32_e32 v48, v84\nv_mov_b32_e32 v49, v84\nv_mov_b32_e32 v50, v84\nv_mov_b32_e32 v51, v84\nv_mov_b32_e32 v52, v84\nv_mov_b32_e32 v53, v84\n"
        "v_mov_b32_e32 v54, v84\nv_mov_b32_e32 v55, v84\nv_mov_b32_e32 v56, v84\nv_mov_b32_e32 v57, v84\nv_mov_b32_e32 v58, v84\nv_mov_b32_e32 v59, v84\nv_mov_b32_e32 v60, v84\nv_mov_b32_e32 v61, v84\nv_mov_b32_e32 v62, v84\n"
        "v_mov_b32_e32 v63, v84\ns_branch L_mask0_n11%=\nL_mask0_t11%=:\nv_cmp_gt_i32_e64 %[mk0], 0, v238\nv_cmp_gt_i32_e64 %[mk1], 1, v238\nv_cmp_gt_i32_e64 %[mk2], 2, v238\nv_cndmask_b32_e64 v48, v48, v84, %[mk0]\n"
        "v_cmp_gt_i32_e64 %[mk0], 3, v238\nv_cndmask_b32_e64 v49, v49, v84, %[mk1]\nv_cmp_gt_i32_e64 %[mk1], 8, v238\nv_cndmask_b32_e64 v50, v50, v84, %[mk2]\nv_cmp_gt_i32_e64 %[mk2], 9, v238\nv_cndmask_b32_e64 v51, v51, v84, %[mk0]\n"
        "v_cmp_gt_i32_e64 %[mk0], 10, v238\nv_cndmask_b32_e64 v52, v52, v84, %[mk1]\nv_cmp_gt_i32_e64 %[mk1], 11, v238\nv_cndmask_b32_e64 v53, v53, v84, %[mk2]\nv_cmp_gt_i32_e64 %[mk2], 16, v238\nv_cndmask_b32_e64 v54, v54, v84, %[mk0]\n"
        "v_cmp_gt_i32_e64 %[mk0], 17, v238\nv_cndmask_b32_e64 v55, v55, v84, %[mk1]\nv_cmp_gt_i32_e64 %[mk1], 18, v238\nv_cndmask_b32_e64 v56, v56, v84, %[mk2]\nv_cmp_gt_i32_e64 %[mk2], 19, v238\nv_cndmask_b32_e64 v57, v57, v84, %[mk0]\n"
        "v_cmp_gt_i32_e64 %[mk0], 24, v238\nv_cndmask_b32_e64 v58, v58, v84, %[mk1]\nv_cmp_gt_i32_e64 %[mk1], 25, v238\nv_cndmask_b32_e64 v59, v59, v84, %[mk2]\nv_cmp_gt_i32_e64 %[mk2], 26, v238\nv_cndmask_b32_e64 v60, v60, v84, %[mk0]\n"
        "v_cmp_gt_i32_e64 %[mk0], 27, v238\nv_cndmask_b32_e64 v61, v61, v84, %[mk1]\ns_nop 1\nv_cndmask_b32_e64 v62, v62, v84, %[mk2]\nv_cndmask_b32_e64 v63, v63, v84, %[mk0]\nL_mask0_n11%=:\ns_sub_i32 %[dq0], %[dq0], 2\n"
        "s_sub_i32 %[dq1], %[dq1], 2\ns_branch L_maskb0%=\nL_mask1%=:\ns_nop 7\ns_nop 3\nv_mov_b32_e32 v20, 0xff800000\ns_cmp_gt_i32 %[dq0], 0\ns_cbranch_scc1 L_mask1_n00%=\ns_cmp_eq_i32 %[dq0], 0\ns_cbranch_scc1 L_mask1_t00%=\n"
        "v_mov_b32_e32 v64, v20\nv_mov_b32_e32 v65, v20\nv_mov_b32_e32 v66, v20\nv_mov_b32_e32 v67, v20\nv_mov_b32_e32 v68, v20\nv_mov_b32_e32 v69, v20\nv_mov_b32_e32 v70, v20\nv_mov_b32_e32 v71, v20\nv_mov_b32_e32 v72, v20\n"
        "v_mov_b32_e32 v73, v20\nv_mov_b32_e32 v74, v20\nv_mov_b32_e32 v75, v20\nv_mov_b32_e32 v76, v20\nv_mov_b32_e32 v77, v20\nv_mov_b32_e32 v78, v20\nv_mov_b32_e32 v79, v20\ns_branch L_mask1_n00%=\nL_mask1_t00%=:\n"
        "v_cmp_gt_i32_e64 %[mk0], 0, v238\nv_cmp_gt_i32_e64 %[mk1], 1, v238\nv_cmp_gt_i32_e64 %[mk2], 2, v238\nv_cndmask_b32_e64 v64, v64, v20, %[mk0]\nv_cmp_gt_i32_e64 %[mk0], 3, v238\nv_cndmask_b32_e64 v65, v65, v20, %[mk1]\n"
        "v_cmp_gt_i32_e64 %[mk1], 8, v238\nv_cndmask_b32_e64 v66, v66, v20, %[mk2]\nv_cmp_gt_i32_e64 %[mk2], 9, v238\nv_cndmask_b32_e64 v67, v67, v20, %[mk0]\nv_cmp_gt_i32_e64 %[mk0], 10, v238\nv_cndmask_b32_e64 v68, v68, v20, %[mk1]\n"
        "v_cmp_gt_i32_e64 %[mk1], 11, v238\nv_cndmask_b32_e64 v69, v69, v20, %[mk2]\nv_cmp_gt_i32_e64 %[mk2], 16, v238\nv_cndmask_b32_e64 v70, v70, v20, %[mk0]\nv_cmp_gt_i32_e64 %[mk0], 17, v238\nv_cndmask_b32_e64 v71, v71, v20, %[mk1]\n"
        "v_cmp_gt_i32_e64 %[mk1], 18, v238\nv_cndmask_b32_e64 v72, v72, v20, %[mk2]\nv_cmp_gt_i32_e64 %[mk2], 19, v238\nv_cndmask_b32_e64 v73, v73, v20, %[mk0]\nv_cmp_gt_i32_e64 %[mk0], 24, v238\nv_cndmask_b32_e64 v74, v74, v20, %[mk1]\n"
        "v_cmp_gt_i32_e64 %[mk1], 25, v238\nv_cndmask_b32_e64 v75, v75, v20, %[mk2]\nv_cmp_gt_i32_e64 %[mk2], 26, v238\nv_cndmask_b32_e64 v76, v76, v20, %[mk0]\nv_cmp_gt_i32_e64 %[mk0], 27, v238\nv_cndmask_b32_e64 v77, v77, v20, %[mk1]\n"
        "s_nop 1\nv_cndmask_b32_e64 v78, v78, v20, %[mk2]\nv_cndmask_b32_e64 v79, v79, v20, %[mk0]\nL_mask1_n00%=:\ns_cmp_gt_i32 %[dq0], 1\ns_cbranch_scc1 L_mask1_n01%=\ns_cmp_eq_i32 %[dq0], 1\ns_cbranch_scc1 L_mask1_t01%=\n"
        "v_mov_b32_e32 v96, v20\nv_mov_b32_e32 v97, v20\nv_mov_b32_e32 v98, v20\nv_mov_b32_e32 v99, v20\nv_mov_b32_e32 v100, v20\nv_mov_b32_e32 v101, v20\nv_mov_b32_e32 v102, v20\nv_mov_b32_e32 v103, v20\nv_mov_b32_e32 v104, v20\n"
        "v_mov_b32_e32 v105, v20\nv_mov_b32_e32 v106, v20\nv_mov_b32_e32 v107, v20\nv_mov_b32_e32 v108, v20\nv_mov_b32_e32 v109, v20\nv_mov_b32_e32 v110, v20\nv_mov_b32_e32 v111, v20\ns_branch L_mask1_n01%=\nL_mask1_t01%=:\n"
        "v_cmp_gt_i32_e64 %[mk0], 0, v238\nv_cmp_gt_i32_e64 %[mk1], 1, v238\nv_cmp_gt_i32_e64 %[mk2], 2, v238\nv_cndmask_b32_e64 v96, v96, v20, %[mk0]\nv_cmp_gt_i32_e64 %[mk0], 3, v238\nv_cndmask_b32_e64 v97, v97, v20, %[mk1]\n"
        "v_cmp_gt_i32_e64 %[mk1], 8, v238\nv_cndmask_b32_e64 v98, v98, v20, %[mk2]\nv_cmp_gt_i32_e64 %[mk2], 9, v238\nv_cndmask_b32_e64 v99, v99, v20, %[mk0]\nv_cmp_gt_i32_e64 %[mk0], 10, v238\nv_cndmask_b32_e64 v100, v100, v20, %[mk1]\n"
        "v_cmp_gt_i32_e64 %[mk1], 11, v238\nv_cndmask_b32_e64 v101, v101, v20, %[mk2]\nv_cmp_gt_i32_e64 %[mk2], 16, v238\nv_cndmask_b32_e64 v102, v102, v20, %[mk0]\nv_cmp_gt_i32_e64 %[mk0], 17, v238\n"
        "v_cndmask_b32_e64 v103, v103, v20, %[mk1]\nv_cmp_gt_i32_e64 %[mk1], 18, v238\nv_cndmask_b32_e64 v104, v104, v20, %[mk2]\nv_cmp_gt_i32_e64 %[mk2], 19, v238\nv_cndmask_b32_e64 v105, v105, v20, %[mk0]\n"
        "v_cmp_gt_i32_e64 %[mk0], 24, v238\nv_cndmask_b32_e64 v106, v106, v20, %[mk1]\nv_cmp_gt_i32_e64 %[mk1], 25, v238\nv_cndmask_b32_e64 v107, v107, v20, %[mk2]\nv_cmp_gt_i32_e64 %[mk2], 26, v238\n"
        "v_cndmask_b32_e64 v108, v108, v20, %[mk0]\nv_cmp_gt_i32_e64 %[mk0], 27, v238\nv_cndmask_b32_e64 v109, v109, v20, %[mk1]\ns_nop 1\nv_cndmask_b32_e64 v110, v110, v20, %[mk2]\nv_cndmask_b32_e64 v111, v111, v20, %[mk0]\n"
        "L_mask1_n01%=:\ns_cmp_gt_i32 %[dq1], 0\ns_cbranch_scc1 L_mask1_n10%=\ns_cmp_eq_i32 %[dq1], 0\ns_cbranch_scc1 L_mask1_t10%=\nv_mov_b32_e32 v80, v20\nv_mov_b32_e32 v81, v20\nv_mov_b32_e32 v82, v20\nv_mov_b32_e32 v83, v20\n"
        "v_mov_b32_e32 v84, v20\nv_mov_b32_e32 v85, v20\nv_mov_b32_e32 v86, v20\nv_mov_b32_e32 v87, v20\nv_mov_b32_e32 v88, v20\nv_mov_b32_e32 v89, v20\nv_mov_b32_e32 v90, v20\nv_mov_b32_e32 v91, v20\nv_mov_b32_e32 v92, v20\n"
        "v_mov_b32_e32 v93, v20\nv_mov_b32_e32 v94, v20\nv_mov_b32_e32 v95, v20\ns_branch L_mask1_n10%=\nL_mask1_t10%=:\nv_cmp_gt_i32_e64 %[mk0], 0, v238\nv_cmp_gt_i32_e64 %[mk1], 1, v238\nv_cmp_gt_i32_e64 %[mk2], 2, v238\n"
        "v_cndmask_b32_e64 v80, v80, v20, %[mk0]\nv_cmp_gt_i32_e64 %[mk0], 3, v238\nv_cndmask_b32_e64 v81, v81, v20, %[mk1]\nv_cmp_gt_i32_e64 %[mk1], 8, v238\nv_cndmask_b32_e64 v82, v82, v20, %[mk2]\nv_cmp_gt_i32_e64 %[mk2], 9, v238\n"
        "v_cndmask_b32_e64 v83, v83, v20, %[mk0]\nv_cmp_gt_i32_e64 %[mk0], 10, v238\nv_cndmask_b32_e64 v84, v84, v20, %[mk1]\nv_cmp_gt_i32_e64 %[mk1], 11, v238\nv_cndmask_b32_e64 v85, v85, v20, %[mk2]\nv_cmp_gt_i32_e64 %[mk2], 16, v238\n"
        "v_cndmask_b32_e64 v86, v86, v20, %[mk0]\nv_cmp_gt_i32_e64 %[mk0], 17, v238\nv_cndmask_b32_e64 v87, v87, v20, %[mk1]\nv_cmp_gt_i32_e64 %[mk1], 18, v238\nv_cndmask_b32_e64 v88, v88, v20, %[mk2]\nv_cmp_gt_i32_e64 %[mk2], 19, v238\n"
        "v_cndmask_b32_e64 v89, v89, v20, %[mk0]\nv_cmp_gt_i32_e64 %[mk0], 24, v238\nv_cndmask_b32_e64 v90, v90, v20, %[mk1]\nv_cmp_gt_i32_e64 %[mk1], 25, v238\nv_cndmask_b32_e64 v91, v91, v20, %[mk2]\nv_cmp_gt_i32_e64 %[mk2], 26, v238\n"
        "v_cndmask_b32_e64 v92, v92, v20, %[mk0]\nv_cmp_gt_i32_e64 %[mk0], 27, v238\nv_cndmask_b32_e64 v93, v93, v20, %[mk1]\ns_nop 1\nv_cndmask_b32_e64 v94, v94, v20, %[mk2]\nv_cndmask_b32_e64 v95, v95, v20, %[mk0]\nL_mask1_n10%=:\n"
        "s_cmp_gt_i32 %[dq1], 1\ns_cbranch_scc1 L_mask1_n11%=\ns_cmp_eq_i32 %[dq1], 1\ns_cbranch_scc1 L_mask1_t11%=\nv_mov_b32_e32 v112, v20\nv_mov_b32_e32 v113, v20\nv_mov_b32_e32 v114, v20\nv_mov_b32_e32 v115, v20\n"
        "v_mov_b32_e32 v116, v20\nv_mov_b32_e32 v117, v20\nv_mov_b32_e32 v118, v20\nv_mov_b32_e32 v119, v20\nv_mov_b32_e32 v120, v20\nv_mov_b32_e32 v121, v20\nv_mov_b32_e32 v122, v20\nv_mov_b32_e32 v123, v20\nv_mov_b32_e32 v124, v20\n"
        "v_mov_b32_e32 v125, v20\nv_mov_b32_e32 v126, v20\nv_mov_b32_e32 v127, v20\ns_branch L_mask1_n11%=\nL_mask1_t11%=:\nv_cmp_gt_i32_e64 %[mk0], 0, v238\nv_cmp_gt_i32_e64 %[mk1], 1, v238\nv_cmp_gt_i32_e64 %[mk2], 2, v238\n"
        "v_cndmask_b32_e64 v112, v112, v20, %[mk0]\nv_cmp_gt_i32_e64 %[mk0], 3, v238\nv_cndmask_b32_e64 v113, v113, v20, %[mk1]\nv_cmp_gt_i32_e64 %[mk1], 8, v238\nv_cndmask_b32_e64 v114, v114, v20, %[mk2]\n"
        "v_cmp_gt_i32_e64 %[mk2], 9, v238\nv_cndmask_b32_e64 v115, v115, v20, %[mk0]\nv_cmp_gt_i32_e64 %[mk0], 10, v238\nv_cndmask_b32_e64 v116, v116, v20, %[mk1]\nv_cmp_gt_i32_e64 %[mk1], 11, v238\n"
        "v_cndmask_b32_e64 v117, v117, v20, %[mk2]\nv_cmp_gt_i32_e64 %[mk2], 16, v238\nv_cndmask_b32_e64 v118, v118, v20, %[mk0]\nv_cmp_gt_i32_e64 %[mk0], 17, v238\nv_cndmask_b32_e64 v119, v119, v20, %[mk1]\n"
        "v_cmp_gt_i32_e64 %[mk1], 18, v238\nv_cndmask_b32_e64 v120, v120, v20, %[mk2]\nv_cmp_gt_i32_e64 %[mk2], 19, v238\nv_cndmask_b32_e64 v121, v121, v20, %[mk0]\nv_cmp_gt_i32_e64 %[mk0], 24, v238\n"
        "v_cndmask_b32_e64 v122, v122, v20, %[mk1]\nv_cmp_gt_i32_e64 %[mk1], 25, v238\nv_cndmask_b32_e64 v123, v123, v20, %[mk2]\nv_cmp_gt_i32_e64 %[mk2], 26, v238\nv_cndmask_b32_e64 v124, v124, v20, %[mk0]\n"
        "v_cmp_gt_i32_e64 %[mk0], 27, v238\nv_cndmask_b32_e64 v125, v125, v20, %[mk1]\ns_nop 1\nv_cndmask_b32_e64 v126, v126, v20, %[mk2]\nv_cndmask_b32_e64 v127, v127, v20, %[mk0]\nL_mask1_n11%=:\ns_sub_i32 %[dq0], %[dq0], 2\n"
        "s_sub_i32 %[dq1], %[dq1], 2\ns_branch L_maskb1%=\nL_rareA0%=:\nv_max_f32_e32 v68, v68, v69\nv_mov_b32_e32 v69, v68\ns_nop 1\nv_permlane32_swap_b32_e32 v68, v69\ns_nop 0\nv_max_f32_e32 v68, v68, v69\n"
        "v_add_f32_e32 v68, 0x41800000, v68\nv_max_f32_e32 v68, 0, v68\nv_add_f32_e32 v240, v240, v68\nv_exp_f32_e64 v76, -v68\ns_nop 0\nv_mul_f32_e32 v242, v242, v76\nv_mul_f32_e32 v243, v243, v76\nv_sub_f32_e32 v0, v0, v68\n"
        "v_sub_f32_e32 v1, v1, v68\nv_sub_f32_e32 v2, v2, v68\nv_sub_f32_e32 v3, v3, v68\nv_sub_f32_e32 v4, v4, v68\nv_sub_f32_e32 v5, v5, v68\nv_sub_f32_e32 v6, v6, v68\nv_sub_f32_e32 v7, v7, v68\nv_sub_f32_e32 v8, v8, v68\n"
        "v_sub_f32_e32 v9, v9, v68\nv_sub_f32_e32 v10, v10, v68\nv_sub_f32_e32 v11, v11, v68\nv_sub_f32_e32 v12, v12, v68\nv_sub_f32_e32 v13, v13, v68\nv_sub_f32_e32 v14, v14, v68\nv_sub_f32_e32 v15, v15, v68\nv_sub_f32_e32 v32, v32, v68\n"
        "v_sub_f32_e32 v33, v33, v68\nv_sub_f32_e32 v34, v34, v68\nv_sub_f32_e32 v35, v35, v68\nv_sub_f32_e32 v36, v36, v68\nv_sub_f32_e32 v37, v37, v68\nv_sub_f32_e32 v38, v38, v68\nv_sub_f32_e32 v39, v39, v68\n"
        "v_sub_f32_e32 v40, v40, v68\nv_sub_f32_e32 v41, v41, v68\nv_sub_f32_e32 v42, v42, v68\nv_sub_f32_e32 v43, v43, v68\nv_sub_f32_e32 v44, v44, v68\nv_sub_f32_e32 v45, v45, v68\nv_sub_f32_e32 v46, v46, v68\n"
        "v_sub_f32_e32 v47, v47, v68\nv_sub_f32_e32 v192, 0, v240\nv_mov_b32_e32 v193, v192\nv_mov_b32_e32 v194, v192\nv_mov_b32_e32 v195, v192\nv_mov_b32_e32 v196, v192\nv_mov_b32_e32 v197, v192\nv_mov_b32_e32 v198, v192\n"
        "v_mov_b32_e32 v199, v192\nv_mov_b32_e32 v200, v192\nv_mov_b32_e32 v201, v192\nv_mov_b32_e32 v202, v192\nv_mov_b32_e32 v203, v192\nv_mov_b32_e32 v204, v192\nv_mov_b32_e32 v205, v192\nv_mov_b32_e32 v206, v192\n"
        "v_mov_b32_e32 v207, v192\nv_max_f32_e32 v70, v70, v71\nv_mov_b32_e32 v71, v70\ns_nop 1\nv_permlane32_swap_b32_e32 v70, v71\ns_nop 0\nv_max_f32_e32 v70, v70, v71\nv_add_f32_e32 v70, 0x41800000, v70\nv_max_f32_e32 v70, 0, v70\n"
        "v_add_f32_e32 v241, v241, v70\nv_exp_f32_e64 v77, -v70\ns_nop 0\nv_mul_f32_e32 v244, v244, v77\nv_mul_f32_e32 v245, v245, v77\nv_sub_f32_e32 v16, v16, v70\nv_sub_f32_e32 v17, v17, v70\nv_sub_f32_e32 v18, v18, v70\n"
        "v_sub_f32_e32 v19, v19, v70\nv_sub_f32_e32 v20, v20, v70\nv_sub_f32_e32 v21, v21, v70\nv_sub_f32_e32 v22, v22, v70\nv_sub_f32_e32 v23, v23, v70\nv_sub_f32_e32 v24, v24, v70\nv_sub_f32_e32 v25, v25, v70\n"
        "v_sub_f32_e32 v26, v26, v70\nv_sub_f32_e32 v27, v27, v70\nv_sub_f32_e32 v28, v28, v70\nv_sub_f32_e32 v29, v29, v70\nv_sub_f32_e32 v30, v30, v70\nv_sub_f32_e32 v31, v31, v70\nv_sub_f32_e32 v48, v48, v70\n"
        "v_sub_f32_e32 v49, v49, v70\nv_sub_f32_e32 v50, v50, v70\nv_sub_f32_e32 v51, v51, v70\nv_sub_f32_e32 v52, v52, v70\nv_sub_f32_e32 v53, v53, v70\nv_sub_f32_e32 v54, v54, v70\nv_sub_f32_e32 v55, v55, v70\n"
        "v_sub_f32_e32 v56, v56, v70\nv_sub_f32_e32 v57, v57, v70\nv_sub_f32_e32 v58, v58, v70\nv_sub_f32_e32 v59, v59, v70\nv_sub_f32_e32 v60, v60, v70\nv_sub_f32_e32 v61, v61, v70\nv_sub_f32_e32 v62, v62, v70\n"
        "v_sub_f32_e32 v63, v63, v70\nv_sub_f32_e32 v208, 0, v241\nv_mov_b32_e32 v209, v208\nv_mov_b32_e32 v210, v208\nv_mov_b32_e32 v211, v208\nv_mov_b32_e32 v212, v208\nv_mov_b32_e32 v213, v208\nv_mov_b32_e32 v214, v208\n"
        "v_mov_b32_e32 v215, v208\nv_mov_b32_e32 v216, v208\nv_mov_b32_e32 v217, v208\nv_mov_b32_e32 v218, v208\nv_mov_b32_e32 v219, v208\nv_mov_b32_e32 v220, v208\nv_mov_b32_e32 v221, v208\nv_mov_b32_e32 v222, v208\n"
        "v_mov_b32_e32 v223, v208\ns_mov_b32 %[pend], 1\ns_branch L_rareAb0%=\nL_rareA1%=:\nv_max_f32_e32 v4, v4, v5\nv_mov_b32_e32 v5, v4\ns_nop 1\nv_permlane32_swap_b32_e32 v4, v5\ns_nop 0\nv_max_f32_e32 v4, v4, v5\n"
        "v_add_f32_e32 v4, 0x41800000, v4\nv_max_f32_e32 v4, 0, v4\nv_add_f32_e32 v240, v240, v4\nv_exp_f32_e64 v12, -v4\ns_nop 0\nv_mul_f32_e32 v242, v242, v12\nv_mul_f32_e32 v243, v243, v12\nv_sub_f32_e32 v64, v64, v4\n"
        "v_sub_f32_e32 v65, v65, v4\nv_sub_f32_e32 v66, v66, v4\nv_sub_f32_e32 v67, v67, v4\nv_sub_f32_e32 v68, v68, v4\nv_sub_f32_e32 v69, v69, v4\nv_sub_f32_e32 v70, v70, v4\nv_sub_f32_e32 v71, v71, v4\nv_sub_f32_e32 v72, v72, v4\n"
        "v_sub_f32_e32 v73, v73, v4\nv_sub_f32_e32 v74, v74, v4\nv_sub_f32_e32 v75, v75, v4\nv_sub_f32_e32 v76, v76, v4\nv_sub_f32_e32 v77, v77, v4\nv_sub_f32_e32 v78, v78, v4\nv_sub_f32_e32 v79, v79, v4\nv_sub_f32_e32 v96, v96, v4\n"
        "v_sub_f32_e32 v97, v97, v4\nv_sub_f32_e32 v98, v98, v4\nv_sub_f32_e32 v99, v99, v4\nv_sub_f32_e32 v100, v100, v4\nv_sub_f32_e32 v101, v101, v4\nv_sub_f32_e32 v102, v102, v4\nv_sub_f32_e32 v103, v103, v4\n"
        "v_sub_f32_e32 v104, v104, v4\nv_sub_f32_e32 v105, v105, v4\nv_sub_f32_e32 v106, v106, v4\nv_sub_f32_e32 v107, v107, v4\nv_sub_f32_e32 v108, v108, v4\nv_sub_f32_e32 v109, v109, v4\nv_sub_f32_e32 v110, v110, v4\n"
        "v_sub_f32_e32 v111, v111, v4\nv_sub_f32_e32 v192, 0, v240\nv_mov_b32_e32 v193, v192\nv_mov_b32_e32 v194, v192\nv_mov_b32_e32 v195, v192\nv_mov_b32_e32 v196, v192\nv_mov_b32_e32 v197, v192\nv_mov_b32_e32 v198, v192\n"
        "v_mov_b32_e32 v199, v192\nv_mov_b32_e32 v200, v192\nv_mov_b32_e32 v201, v192\nv_mov_b32_e32 v202, v192\nv_mov_b32_e32 v203, v192\nv_mov_b32_e32 v204, v192\nv_mov_b32_e32 v205, v192\nv_mov_b32_e32 v206, v192\n"
        "v_mov_b32_e32 v207, v192\nv_max_f32_e32 v6, v6, v7\nv_mov_b32_e32 v7, v6\ns_nop 1\nv_permlane32_swap_b32_e32 v6, v7\ns_nop 0\nv_max_f32_e32 v6, v6, v7\nv_add_f32_e32 v6, 0x41800000, v6\nv_max_f32_e32 v6, 0, v6\n"
        "v_add_f32_e32 v241, v241, v6\nv_exp_f32_e64 v13, -v6\ns_nop 0\nv_mul_f32_e32 v244, v244, v13\nv_mul_f32_e32 v245, v245, v13\nv_sub_f32_e32 v80, v80, v6\nv_sub_f32_e32 v81, v81, v6\nv_sub_f32_e32 v82, v82, v6\n"
        "v_sub_f32_e32 v83, v83, v6\nv_sub_f32_e32 v84, v84, v6\nv_sub_f32_e32 v85, v85, v6\nv_sub_f32_e32 v86, v86, v6\nv_sub_f32_e32 v87, v87, v6\nv_sub_f32_e32 v88, v88, v6\nv_sub_f32_e32 v89, v89, v6\nv_sub_f32_e32 v90, v90, v6\n"
        "v_sub_f32_e32 v91, v91, v6\nv_sub_f32_e32 v92, v92, v6\nv_sub_f32_e32 v93, v93, v6\nv_sub_f32_e32 v94, v94, v6\nv_sub_f32_e32 v95, v95, v6\nv_sub_f32_e32 v112, v112, v6\nv_sub_f32_e32 v113, v113, v6\nv_sub_f32_e32 v114, v114, v6\n"
        "v_sub_f32_e32 v115, v115, v6\nv_sub_f32_e32 v116, v116, v6\nv_sub_f32_e32 v117, v117, v6\nv_sub_f32_e32 v118, v118, v6\nv_sub_f32_e32 v119, v119, v6\nv_sub_f32_e32 v120, v120, v6\nv_sub_f32_e32 v121, v121, v6\n"
        "v_sub_f32_e32 v122, v122, v6\nv_sub_f32_e32 v123, v123, v6\nv_sub_f32_e32 v124, v124, v6\nv_sub_f32_e32 v125, v125, v6\nv_sub_f32_e32 v126, v126, v6\nv_sub_f32_e32 v127, v127, v6\nv_sub_f32_e32 v208, 0, v241\n"
        "v_mov_b32_e32 v209, v208\nv_mov_b32_e32 v210, v208\nv_mov_b32_e32 v211, v208\nv_mov_b32_e32 v212, v208\nv_mov_b32_e32 v213, v208\nv_mov_b32_e32 v214, v208\nv_mov_b32_e32 v215, v208\nv_mov_b32_e32 v216, v208\n"
        "v_mov_b32_e32 v217, v208\nv_mov_b32_e32 v218, v208\nv_mov_b32_e32 v219, v208\nv_mov_b32_e32 v220, v208\nv_mov_b32_e32 v221, v208\nv_mov_b32_e32 v222, v208\nv_mov_b32_e32 v223, v208\ns_mov_b32 %[pend], 1\ns_branch L_rareAb1%=\n"
        "L_rareB0%=:\nv_mbcnt_lo_u32_b32 v160, -1, 0\nv_mbcnt_hi_u32_b32 v160, -1, v160\ns_lshl_b32 %[t0], %[wid], 9\ns_add_u32 %[t0], %[t0], %[ldsb]\ns_add_u32 %[t0], %[t0], 0x10000\nv_and_b32_e32 v161, 31, v160\n"
        "v_lshl_add_u32 v161, v161, 2, %[t0]\nv_lshrrev_b32_e32 v162, 5, v160\nv_lshl_add_u32 v162, v162, 4, %[t0]\nds_write_b32 v161, v76\nds_write_b32 v161, v77 offset:128\ns_waitcnt lgkmcnt(0)\nds_read_b128 v[128:131], v162 offset:0\n"
        "ds_read_b128 v[132:135], v162 offset:32\nds_read_b128 v[136:139], v162 offset:64\nds_read_b128 v[140:143], v162 offset:96\nds_read_b128 v[144:147], v162 offset:128\nds_read_b128 v[148:151], v162 offset:160\n"
        "ds_read_b128 v[152:155], v162 offset:192\nds_read_b128 v[156:159], v162 offset:224\ns_waitcnt lgkmcnt(0)\ns_nop 7\nv_accvgpr_read_b32 v164, a0\nv_mul_f32_e32 v164, v164, v128\nv_accvgpr_write_b32 a0, v164\n"
        "v_accvgpr_read_b32 v165, a1\nv_mul_f32_e32 v165, v165, v129\nv_accvgpr_write_b32 a1, v165\nv_accvgpr_read_b32 v166, a2\nv_mul_f32_e32 v166, v166, v130\nv_accvgpr_write_b32 a2, v166\nv_accvgpr_read_b32 v167, a3\n"
        "v_mul_f32_e32 v167, v167, v131\nv_accvgpr_write_b32 a3, v167\nv_accvgpr_read_b32 v164, a4\nv_mul_f32_e32 v164, v164, v132\nv_accvgpr_write_b32 a4, v164\nv_accvgpr_read_b32 v165, a5\nv_mul_f32_e32 v165, v165, v133\n"
        "v_accvgpr_write_b32 a5, v165\nv_accvgpr_read_b32 v166, a6\nv_mul_f32_e32 v166, v166, v134\nv_accvgpr_write_b32 a6, v166\nv_accvgpr_read_b32 v167, a7\nv_mul_f32_e32 v167, v167, v135\nv_accvgpr_write_b32 a7, v167\n"
        "v_accvgpr_read_b32 v164, a8\nv_mul_f32_e32 v164, v164, v136\nv_accvgpr_write_b32 a8, v164\nv_accvgpr_read_b32 v165, a9\nv_mul_f32_e32 v165, v165, v137\nv_accvgpr_write_b32 a9, v165\nv_accvgpr_read_b32 v166, a10\n"
        "v_mul_f32_e32 v166, v166, v138\nv_accvgpr_write_b32 a10, v166\nv_accvgpr_read_b32 v167, a11\nv_mul_f32_e32 v167, v167, v139\nv_accvgpr_write_b32 a11, v167\nv_accvgpr_read_b32 v164, a12\nv_mul_f32_e32 v164, v164, v140\n"
        "v_accvgpr_write_b32 a12, v164\nv_accvgpr_read_b32 v165, a13\nv_mul_f32_e32 v165, v165, v141\nv_accvgpr_write_b32 a13, v165\nv_accvgpr_read_b32 v166, a14\nv_mul_f32_e32 v166, v166, v142\nv_accvgpr_write_b32 a14, v166\n"
        "v_accvgpr_read_b32 v167, a15\nv_mul_f32_e32 v167, v167, v143\nv_accvgpr_write_b32 a15, v167\nv_accvgpr_read_b32 v164, a16\nv_mul_f32_e32 v164, v164, v128\nv_accvgpr_write_b32 a16, v164\nv_accvgpr_read_b32 v165, a17\n"
        "v_mul_f32_e32 v165, v165, v129\nv_accvgpr_write_b32 a17, v165\nv_accvgpr_read_b32 v166, a18\nv_mul_f32_e32 v166, v166, v130\nv_accvgpr_write_b32 a18, v166\nv_accvgpr_read_b32 v167, a19\nv_mul_f32_e32 v167, v167, v131\n"
        "v_accvgpr_write_b32 a19, v167\nv_accvgpr_read_b32 v164, a20\nv_mul_f32_e32 v164, v164, v132\nv_accvgpr_write_b32 a20, v164\nv_accvgpr_read_b32 v165, a21\nv_mul_f32_e32 v165, v165, v133\nv_accvgpr_write_b32 a21, v165\n"
        "v_accvgpr_read_b32 v166, a22\nv_mul_f32_e32 v166, v166, v134\nv_accvgpr_write_b32 a22, v166\nv_accvgpr_read_b32 v167, a23\nv_mul_f32_e32 v167, v167, v135\nv_accvgpr_write_b32 a23, v167\nv_accvgpr_read_b32 v164, a24\n"
        "v_mul_f32_e32 v164, v164, v136\nv_accvgpr_write_b32 a24, v164\nv_accvgpr_read_b32 v165, a25\nv_mul_f32_e32 v165, v165, v137\nv_accvgpr_write_b32 a25, v165\nv_accvgpr_read_b32 v166, a26\nv_mul_f32_e32 v166, v166, v138\n"
        "v_accvgpr_write_b32 a26, v166\nv_accvgpr_read_b32 v167, a27\nv_mul_f32_e32 v167, v167, v139\nv_accvgpr_write_b32 a27, v167\nv_accvgpr_read_b32 v164, a28\nv_mul_f32_e32 v164, v164, v140\nv_accvgpr_write_b32 a28, v164\n"
        "v_accvgpr_read_b32 v165, a29\nv_mul_f32_e32 v165, v165, v141\nv_accvgpr_write_b32 a29, v165\nv_accvgpr_read_b32 v166, a30\nv_mul_f32_e32 v166, v166, v142\nv_accvgpr_write_b32 a30, v166\nv_accvgpr_read_b32 v167, a31\n"
        "v_mul_f32_e32 v167, v167, v143\nv_accvgpr_write_b32 a31, v167\nv_accvgpr_read_b32 v164, a32\nv_mul_f32_e32 v164, v164, v128\nv_accvgpr_write_b32 a32, v164\nv_accvgpr_read_b32 v165, a33\nv_mul_f32_e32 v165, v165, v129\n"
        "v_accvgpr_write_b32 a33, v165\nv_accvgpr_read_b32 v166, a34\nv_mul_f32_e32 v166, v166, v130\nv_accvgpr_write_b32 a34, v166\nv_accvgpr_read_b32 v167, a35\nv_mul_f32_e32 v167, v167, v131\nv_accvgpr_write_b32 a35, v167\n"
        "v_accvgpr_read_b32 v164, a36\nv_mul_f32_e32 v164, v164, v132\nv_accvgpr_write_b32 a36, v164\nv_accvgpr_read_b32 v165, a37\nv_mul_f32_e32 v165, v165, v133\nv_accvgpr_write_b32 a37, v165\nv_accvgpr_read_b32 v166, a38\n"
        "v_mul_f32_e32 v166, v166, v134\nv_accvgpr_write_b32 a38, v166\nv_accvgpr_read_b32 v167, a39\nv_mul_f32_e32 v167, v167, v135\nv_accvgpr_write_b32 a39, v167\nv_accvgpr_read_b32 v164, a40\nv_mul_f32_e32 v164, v164, v136\n"
        "v_accvgpr_write_b32 a40, v164\nv_accvgpr_read_b32 v165, a41\nv_mul_f32_e32 v165, v165, v137\nv_accvgpr_write_b32 a41, v165\nv_accvgpr_read_b32 v166, a42\nv_mul_f32_e32 v166, v166, v138\nv_accvgpr_write_b32 a42, v166\n"
        "v_accvgpr_read_b32 v167, a43\nv_mul_f32_e32 v167, v167, v139\nv_accvgpr_write_b32 a43, v167\nv_accvgpr_read_b32 v164, a44\nv_mul_f32_e32 v164, v164, v140\nv_accvgpr_write_b32 a44, v164\nv_accvgpr_read_b32 v165, a45\n"
        "v_mul_f32_e32 v165, v165, v141\nv_accvgpr_write_b32 a45, v165\nv_accvgpr_read_b32 v166, a46\nv_mul_f32_e32 v166, v166, v142\nv_accvgpr_write_b32 a46, v166\nv_accvgpr_read_b32 v167, a47\nv_mul_f32_e32 v167, v167, v143\n"
        "v_accvgpr_write_b32 a47, v167\nv_accvgpr_read_b32 v164, a48\nv_mul_f32_e32 v164, v164, v128\nv_accvgpr_write_b32 a48, v164\nv_accvgpr_read_b32 v165, a49\nv_mul_f32_e32 v165, v165, v129\nv_accvgpr_write_b32 a49, v165\n"
        "v_accvgpr_read_b32 v166, a50\nv_mul_f32_e32 v166, v166, v130\nv_accvgpr_write_b32 a50, v166\nv_accvgpr_read_b32 v167, a51\nv_mul_f32_e32 v167, v167, v131\nv_accvgpr_write_b32 a51, v167\nv_accvgpr_read_b32 v164, a52\n"
        "v_mul_f32_e32 v164, v164, v132\nv_accvgpr_write_b32 a52, v164\nv_accvgpr_read_b32 v165, a53\nv_mul_f32_e32 v165, v165, v133\nv_accvgpr_write_b32 a53, v165\nv_accvgpr_read_b32 v166, a54\nv_mul_f32_e32 v166, v166, v134\n"
        "v_accvgpr_write_b32 a54, v166\nv_accvgpr_read_b32 v167, a55\nv_mul_f32_e32 v167, v167, v135\nv_accvgpr_write_b32 a55, v167\nv_accvgpr_read_b32 v164, a56\nv_mul_f32_e32 v164, v164, v136\nv_accvgpr_write_b32 a56, v164\n"
        "v_accvgpr_read_b32 v165, a57\nv_mul_f32_e32 v165, v165, v137\nv_accvgpr_write_b32 a57, v165\nv_accvgpr_read_b32 v166, a58\nv_mul_f32_e32 v166, v166, v138\nv_accvgpr_write_b32 a58, v166\nv_accvgpr_read_b32 v167, a59\n"
        "v_mul_f32_e32 v167, v167, v139\nv_accvgpr_write_b32 a59, v167\nv_accvgpr_read_b32 v164, a60\nv_mul_f32_e32 v164, v164, v140\nv_accvgpr_write_b32 a60, v164\nv_accvgpr_read_b32 v165, a61\nv_mul_f32_e32 v165, v165, v141\n"
        "v_accvgpr_write_b32 a61, v165\nv_accvgpr_read_b32 v166, a62\nv_mul_f32_e32 v166, v166, v142\nv_accvgpr_write_b32 a62, v166\nv_accvgpr_read_b32 v167, a63\nv_mul_f32_e32 v167, v167, v143\nv_accvgpr_write_b32 a63, v167\n"
        "v_accvgpr_read_b32 v164, a64\nv_mul_f32_e32 v164, v164, v144\nv_accvgpr_write_b32 a64, v164\nv_accvgpr_read_b32 v165, a65\nv_mul_f32_e32 v165, v165, v145\nv_accvgpr_write_b32 a65, v165\nv_accvgpr_read_b32 v166, a66\n"
        "v_mul_f32_e32 v166, v166, v146\nv_accvgpr_write_b32 a66, v166\nv_accvgpr_read_b32 v167, a67\nv_mul_f32_e32 v167, v167, v147\nv_accvgpr_write_b32 a67, v167\nv_accvgpr_read_b32 v164, a68\nv_mul_f32_e32 v164, v164, v148\n"
        "v_accvgpr_write_b32 a68, v164\nv_accvgpr_read_b32 v165, a69\nv_mul_f32_e32 v165, v165, v149\nv_accvgpr_write_b32 a69, v165\nv_accvgpr_read_b32 v166, a70\nv_mul_f32_e32 v166, v166, v150\nv_accvgpr_write_b32 a70, v166\n"
        "v_accvgpr_read_b32 v167, a71\nv_mul_f32_e32 v167, v167, v151\nv_accvgpr_write_b32 a71, v167\nv_accvgpr_read_b32 v164, a72\nv_mul_f32_e32 v164, v164, v152\nv_accvgpr_write_b32 a72, v164\nv_accvgpr_read_b32 v165, a73\n"
        "v_mul_f32_e32 v165, v165, v153\nv_accvgpr_write_b32 a73, v165\nv_accvgpr_read_b32 v166, a74\nv_mul_f32_e32 v166, v166, v154\nv_accvgpr_write_b32 a74, v166\nv_accvgpr_read_b32 v167, a75\nv_mul_f32_e32 v167, v167, v155\n"
        "v_accvgpr_write_b32 a75, v167\nv_accvgpr_read_b32 v164, a76\nv_mul_f32_e32 v164, v164, v156\nv_accvgpr_write_b32 a76, v164\nv_accvgpr_read_b32 v165, a77\nv_mul_f32_e32 v165, v165, v157\nv_accvgpr_write_b32 a77, v165\n"
        "v_accvgpr_read_b32 v166, a78\nv_mul_f32_e32 v166, v166, v158\nv_accvgpr_write_b32 a78, v166\nv_accvgpr_read_b32 v167, a79\nv_mul_f32_e32 v167, v167, v159\nv_accvgpr_write_b32 a79, v167\nv_accvgpr_read_b32 v164, a80\n"
        "v_mul_f32_e32 v164, v164, v144\nv_accvgpr_write_b32 a80, v164\nv_accvgpr_read_b32 v165, a81\nv_mul_f32_e32 v165, v165, v145\nv_accvgpr_write_b32 a81, v165\nv_accvgpr_read_b32 v166, a82\nv_mul_f32_e32 v166, v166, v146\n"
        "v_accvgpr_write_b32 a82, v166\nv_accvgpr_read_b32 v167, a83\nv_mul_f32_e32 v167, v167, v147\nv_accvgpr_write_b32 a83, v167\nv_accvgpr_read_b32 v164, a84\nv_mul_f32_e32 v164, v164, v148\nv_accvgpr_write_b32 a84, v164\n"
        "v_accvgpr_read_b32 v165, a85\nv_mul_f32_e32 v165, v165, v149\nv_accvgpr_write_b32 a85, v165\nv_accvgpr_read_b32 v166, a86\nv_mul_f32_e32 v166, v166, v150\nv_accvgpr_write_b32 a86, v166\nv_accvgpr_read_b32 v167, a87\n"
        "v_mul_f32_e32 v167, v167, v151\nv_accvgpr_write_b32 a87, v167\nv_accvgpr_read_b32 v164, a88\nv_mul_f32_e32 v164, v164, v152\nv_accvgpr_write_b32 a88, v164\nv_accvgpr_read_b32 v165, a89\nv_mul_f32_e32 v165, v165, v153\n"
        "v_accvgpr_write_b32 a89, v165\nv_accvgpr_read_b32 v166, a90\nv_mul_f32_e32 v166, v166, v154\nv_accvgpr_write_b32 a90, v166\nv_accvgpr_read_b32 v167, a91\nv_mul_f32_e32 v167, v167, v155\nv_accvgpr_write_b32 a91, v167\n"
        "v_accvgpr_read_b32 v164, a92\nv_mul_f32_e32 v164, v164, v156\nv_accvgpr_write_b32 a92, v164\nv_accvgpr_read_b32 v165, a93\nv_mul_f32_e32 v165, v165, v157\nv_accvgpr_write_b32 a93, v165\nv_accvgpr_read_b32 v166, a94\n"
        "v_mul_f32_e32 v166, v166, v158\nv_accvgpr_write_b32 a94, v166\nv_accvgpr_read_b32 v167, a95\nv_mul_f32_e32 v167, v167, v159\nv_accvgpr_write_b32 a95, v167\nv_accvgpr_read_b32 v164, a96\nv_mul_f32_e32 v164, v164, v144\n"
        "v_accvgpr_write_b32 a96, v164\nv_accvgpr_read_b32 v165, a97\nv_mul_f32_e32 v165, v165, v145\nv_accvgpr_write_b32 a97, v165\nv_accvgpr_read_b32 v166, a98\nv_mul_f32_e32 v166, v166, v146\nv_accvgpr_write_b32 a98, v166\n"
        "v_accvgpr_read_b32 v167, a99\nv_mul_f32_e32 v167, v167, v147\nv_accvgpr_write_b32 a99, v167\nv_accvgpr_read_b32 v164, a100\nv_mul_f32_e32 v164, v164, v148\nv_accvgpr_write_b32 a100, v164\nv_accvgpr_read_b32 v165, a101\n"
        "v_mul_f32_e32 v165, v165, v149\nv_accvgpr_write_b32 a101, v165\nv_accvgpr_read_b32 v166, a102\nv_mul_f32_e32 v166, v166, v150\nv_accvgpr_write_b32 a102, v166\nv_accvgpr_read_b32 v167, a103\nv_mul_f32_e32 v167, v167, v151\n"
        "v_accvgpr_write_b32 a103, v167\nv_accvgpr_read_b32 v164, a104\nv_mul_f32_e32 v164, v164, v152\nv_accvgpr_write_b32 a104, v164\nv_accvgpr_read_b32 v165, a105\nv_mul_f32_e32 v165, v165, v153\nv_accvgpr_write_b32 a105, v165\n"
        "v_accvgpr_read_b32 v166, a106\nv_mul_f32_e32 v166, v166, v154\nv_accvgpr_write_b32 a106, v166\nv_accvgpr_read_b32 v167, a107\nv_mul_f32_e32 v167, v167, v155\nv_accvgpr_write_b32 a107, v167\nv_accvgpr_read_b32 v164, a108\n"
        "v_mul_f32_e32 v164, v164, v156\nv_accvgpr_write_b32 a108, v164\nv_accvgpr_read_b32 v165, a109\nv_mul_f32_e32 v165, v165, v157\nv_accvgpr_write_b32 a109, v165\nv_accvgpr_read_b32 v166, a110\nv_mul_f32_e32 v166, v166, v158\n"
        "v_accvgpr_write_b32 a110, v166\nv_accvgpr_read_b32 v167, a111\nv_mul_f32_e32 v167, v167, v159\nv_accvgpr_write_b32 a111, v167\nv_accvgpr_read_b32 v164, a112\nv_mul_f32_e32 v164, v164, v144\nv_accvgpr_write_b32 a112, v164\n"
        "v_accvgpr_read_b32 v165, a113\nv_mul_f32_e32 v165, v165, v145\nv_accvgpr_write_b32 a113, v165\nv_accvgpr_read_b32 v166, a114\nv_mul_f32_e32 v166, v166, v146\nv_accvgpr_write_b32 a114, v166\nv_accvgpr_read_b32 v167, a115\n"
        "v_mul_f32_e32 v167, v167, v147\nv_accvgpr_write_b32 a115, v167\nv_accvgpr_read_b32 v164, a116\nv_mul_f32_e32 v164, v164, v148\nv_accvgpr_write_b32 a116, v164\nv_accvgpr_read_b32 v165, a117\nv_mul_f32_e32 v165, v165, v149\n"
        "v_accvgpr_write_b32 a117, v165\nv_accvgpr_read_b32 v166, a118\nv_mul_f32_e32 v166, v166, v150\nv_accvgpr_write_b32 a118, v166\nv_accvgpr_read_b32 v167, a119\nv_mul_f32_e32 v167, v167, v151\nv_accvgpr_write_b32 a119, v167\n"
        "v_accvgpr_read_b32 v164, a120\nv_mul_f32_e32 v164, v164, v152\nv_accvgpr_write_b32 a120, v164\nv_accvgpr_read_b32 v165, a121\nv_mul_f32_e32 v165, v165, v153\nv_accvgpr_write_b32 a121, v165\nv_accvgpr_read_b32 v166, a122\n"
        "v_mul_f32_e32 v166, v166, v154\nv_accvgpr_write_b32 a122, v166\nv_accvgpr_read_b32 v167, a123\nv_mul_f32_e32 v167, v167, v155\nv_accvgpr_write_b32 a123, v167\nv_accvgpr_read_b32 v164, a124\nv_mul_f32_e32 v164, v164, v156\n"
        "v_accvgpr_write_b32 a124, v164\nv_accvgpr_read_b32 v165, a125\nv_mul_f32_e32 v165, v165, v157\nv_accvgpr_write_b32 a125, v165\nv_accvgpr_read_b32 v166, a126\nv_mul_f32_e32 v166, v166, v158\nv_accvgpr_write_b32 a126, v166\n"
        "v_accvgpr_read_b32 v167, a127\nv_mul_f32_e32 v167, v167, v159\nv_accvgpr_write_b32 a127, v167\ns_mov_b32 %[pend], 0\ns_nop 1\ns_branch L_rareBb0%=\nL_rareB1%=:\nv_mbcnt_lo_u32_b32 v160, -1, 0\nv_mbcnt_hi_u32_b32 v160, -1, v160\n"
        "s_lshl_b32 %[t0], %[wid], 9\ns_add_u32 %[t0], %[t0], %[ldsb]\ns_add_u32 %[t0], %[t0], 0x10000\nv_and_b32_e32 v161, 31, v160\nv_lshl_add_u32 v161, v161, 2, %[t0]\nv_lshrrev_b32_e32 v162, 5, v160\n"
        "v_lshl_add_u32 v162, v162, 4, %[t0]\nds_write_b32 v161, v12\nds_write_b32 v161, v13 offset:128\ns_waitcnt lgkmcnt(0)\nds_read_b128 v[128:131], v162 offset:0\nds_read_b128 v[132:135], v162 offset:32\n"
        "ds_read_b128 v[136:139], v162 offset:64\nds_read_b128 v[140:143], v162 offset:96\nds_read_b128 v[144:147], v162 offset:128\nds_read_b128 v[148:151], v162 offset:160\nds_read_b128 v[152:155], v162 offset:192\n"
        "ds_read_b128 v[156:159], v162 offset:224\ns_waitcnt lgkmcnt(0)\ns_nop 7\nv_accvgpr_read_b32 v164, a0\nv_mul_f32_e32 v164, v164, v128\nv_accvgpr_write_b32 a0, v164\nv_accvgpr_read_b32 v165, a1\nv_mul_f32_e32 v165, v165, v129\n"
        "v_accvgpr_write_b32 a1, v165\nv_accvgpr_read_b32 v166, a2\nv_mul_f32_e32 v166, v166, v130\nv_accvgpr_write_b32 a2, v166\nv_accvgpr_read_b32 v167, a3\nv_mul_f32_e32 v167, v167, v131\nv_accvgpr_write_b32 a3, v167\n"
        "v_accvgpr_read_b32 v164, a4\nv_mul_f32_e32 v164, v164, v132\nv_accvgpr_write_b32 a4, v164\nv_accvgpr_read_b32 v165, a5\nv_mul_f32_e32 v165, v165, v133\nv_accvgpr_write_b32 a5, v165\nv_accvgpr_read_b32 v166, a6\n"
        "v_mul_f32_e32 v166, v166, v134\nv_accvgpr_write_b32 a6, v166\nv_accvgpr_read_b32 v167, a7\nv_mul_f32_e32 v167, v167, v135\nv_accvgpr_write_b32 a7, v167\nv_accvgpr_read_b32 v164, a8\nv_mul_f32_e32 v164, v164, v136\n"
        "v_accvgpr_write_b32 a8, v164\nv_accvgpr_read_b32 v165, a9\nv_mul_f32_e32 v165, v165, v137\nv_accvgpr_write_b32 a9, v165\nv_accvgpr_read_b32 v166, a10\nv_mul_f32_e32 v166, v166, v138\nv_accvgpr_write_b32 a10, v166\n"
        "v_accvgpr_read_b32 v167, a11\nv_mul_f32_e32 v167, v167, v139\nv_accvgpr_write_b32 a11, v167\nv_accvgpr_read_b32 v164, a12\nv_mul_f32_e32 v164, v164, v140\nv_accvgpr_write_b32 a12, v164\nv_accvgpr_read_b32 v165, a13\n"
        "v_mul_f32_e32 v165, v165, v141\nv_accvgpr_write_b32 a13, v165\nv_accvgpr_read_b32 v166, a14\nv_mul_f32_e32 v166, v166, v142\nv_accvgpr_write_b32 a14, v166\nv_accvgpr_read_b32 v167, a15\nv_mul_f32_e32 v167, v167, v143\n"
        "v_accvgpr_write_b32 a15, v167\nv_accvgpr_read_b32 v164, a16\nv_mul_f32_e32 v164, v164, v128\nv_accvgpr_write_b32 a16, v164\nv_accvgpr_read_b32 v165, a17\nv_mul_f32_e32 v165, v165, v129\nv_accvgpr_write_b32 a17, v165\n"
        "v_accvgpr_read_b32 v166, a18\nv_mul_f32_e32 v166, v166, v130\nv_accvgpr_write_b32 a18, v166\nv_accvgpr_read_b32 v167, a19\nv_mul_f32_e32 v167, v167, v131\nv_accvgpr_write_b32 a19, v167\nv_accvgpr_read_b32 v164, a20\n"
        "v_mul_f32_e32 v164, v164, v132\nv_accvgpr_write_b32 a20, v164\nv_accvgpr_read_b32 v165, a21\nv_mul_f32_e32 v165, v165, v133\nv_accvgpr_write_b32 a21, v165\nv_accvgpr_read_b32 v166, a22\nv_mul_f32_e32 v166, v166, v134\n"
        "v_accvgpr_write_b32 a22, v166\nv_accvgpr_read_b32 v167, a23\nv_mul_f32_e32 v167, v167, v135\nv_accvgpr_write_b32 a23, v167\nv_accvgpr_read_b32 v164, a24\nv_mul_f32_e32 v164, v164, v136\nv_accvgpr_write_b32 a24, v164\n"
        "v_accvgpr_read_b32 v165, a25\nv_mul_f32_e32 v165, v165, v137\nv_accvgpr_write_b32 a25, v165\nv_accvgpr_read_b32 v166, a26\nv_mul_f32_e32 v166, v166, v138\nv_accvgpr_write_b32 a26, v166\nv_accvgpr_read_b32 v167, a27\n"
        "v_mul_f32_e32 v167, v167, v139\nv_accvgpr_write_b32 a27, v167\nv_accvgpr_read_b32 v164, a28\nv_mul_f32_e32 v164, v164, v140\nv_accvgpr_write_b32 a28, v164\nv_accvgpr_read_b32 v165, a29\nv_mul_f32_e32 v165, v165, v141\n"
        "v_accvgpr_write_b32 a29, v165\nv_accvgpr_read_b32 v166, a30\nv_mul_f32_e32 v166, v166, v142\nv_accvgpr_write_b32 a30, v166\nv_accvgpr_read_b32 v167, a31\nv_mul_f32_e32 v167, v167, v143\nv_accvgpr_write_b32 a31, v167\n"
        "v_accvgpr_read_b32 v164, a32\nv_mul_f32_e32 v164, v164, v128\nv_accvgpr_write_b32 a32, v164\nv_accvgpr_read_b32 v165, a33\nv_mul_f32_e32 v165, v165, v129\nv_accvgpr_write_b32 a33, v165\nv_accvgpr_read_b32 v166, a34\n"
        "v_mul_f32_e32 v166, v166, v130\nv_accvgpr_write_b32 a34, v166\nv_accvgpr_read_b32 v167, a35\nv_mul_f32_e32 v167, v167, v131\nv_accvgpr_write_b32 a35, v167\nv_accvgpr_read_b32 v164, a36\nv_mul_f32_e32 v164, v164, v132\n"
        "v_accvgpr_write_b32 a36, v164\nv_accvgpr_read_b32 v165, a37\nv_mul_f32_e32 v165, v165, v133\nv_accvgpr_write_b32 a37, v165\nv_accvgpr_read_b32 v166, a38\nv_mul_f32_e32 v166, v166, v134\nv_accvgpr_write_b32 a38, v166\n"
        "v_accvgpr_read_b32 v167, a39\nv_mul_f32_e32 v167, v167, v135\nv_accvgpr_write_b32 a39, v167\nv_accvgpr_read_b32 v164, a40\nv_mul_f32_e32 v164, v164, v136\nv_accvgpr_write_b32 a40, v164\nv_accvgpr_read_b32 v165, a41\n"
        "v_mul_f32_e32 v165, v165, v137\nv_accvgpr_write_b32 a41, v165\nv_accvgpr_read_b32 v166, a42\nv_mul_f32_e32 v166, v166, v138\nv_accvgpr_write_b32 a42, v166\nv_accvgpr_read_b32 v167, a43\nv_mul_f32_e32 v167, v167, v139\n"
        "v_accvgpr_write_b32 a43, v167\nv_accvgpr_read_b32 v164, a44\nv_mul_f32_e32 v164, v164, v140\nv_accvgpr_write_b32 a44, v164\nv_accvgpr_read_b32 v165, a45\nv_mul_f32_e32 v165, v165, v141\nv_accvgpr_write_b32 a45, v165\n"
        "v_accvgpr_read_b32 v166, a46\nv_mul_f32_e32 v166, v166, v142\nv_accvgpr_write_b32 a46, v166\nv_accvgpr_read_b32 v167, a47\nv_mul_f32_e32 v167, v167, v143\nv_accvgpr_write_b32 a47, v167\nv_accvgpr_read_b32 v164, a48\n"
        "v_mul_f32_e32 v164, v164, v128\nv_accvgpr_write_b32 a48, v164\nv_accvgpr_read_b32 v165, a49\nv_mul_f32_e32 v165, v165, v129\nv_accvgpr_write_b32 a49, v165\nv_accvgpr_read_b32 v166, a50\nv_mul_f32_e32 v166, v166, v130\n"
        "v_accvgpr_write_b32 a50, v166\nv_accvgpr_read_b32 v167, a51\nv_mul_f32_e32 v167, v167, v131\nv_accvgpr_write_b32 a51, v167\nv_accvgpr_read_b32 v164, a52\nv_mul_f32_e32 v164, v164, v132\nv_accvgpr_write_b32 a52, v164\n"
        "v_accvgpr_read_b32 v165, a53\nv_mul_f32_e32 v165, v165, v133\nv_accvgpr_write_b32 a53, v165\nv_accvgpr_read_b32 v166, a54\nv_mul_f32_e32 v166, v166, v134\nv_accvgpr_write_b32 a54, v166\nv_accvgpr_read_b32 v167, a55\n"
        "v_mul_f32_e32 v167, v167, v135\nv_accvgpr_write_b32 a55, v167\nv_accvgpr_read_b32 v164, a56\nv_mul_f32_e32 v164, v164, v136\nv_accvgpr_write_b32 a56, v164\nv_accvgpr_read_b32 v165, a57\nv_mul_f32_e32 v165, v165, v137\n"
        "v_accvgpr_write_b32 a57, v165\nv_accvgpr_read_b32 v166, a58\nv_mul_f32_e32 v166, v166, v138\nv_accvgpr_write_b32 a58, v166\nv_accvgpr_read_b32 v167, a59\nv_mul_f32_e32 v167, v167, v139\nv_accvgpr_write_b32 a59, v167\n"
        "v_accvgpr_read_b32 v164, a60\nv_mul_f32_e32 v164, v164, v140\nv_accvgpr_write_b32 a60, v164\nv_accvgpr_read_b32 v165, a61\nv_mul_f32_e32 v165, v165, v141\nv_accvgpr_write_b32 a61, v165\nv_accvgpr_read_b32 v166, a62\n"
        "v_mul_f32_e32 v166, v166, v142\nv_accvgpr_write_b32 a62, v166\nv_accvgpr_read_b32 v167, a63\nv_mul_f32_e32 v167, v167, v143\nv_accvgpr_write_b32 a63, v167\nv_accvgpr_read_b32 v164, a64\nv_mul_f32_e32 v164, v164, v144\n"
        "v_accvgpr_write_b32 a64, v164\nv_accvgpr_read_b32 v165, a65\nv_mul_f32_e32 v165, v165, v145\nv_accvgpr_write_b32 a65, v165\nv_accvgpr_read_b32 v166, a66\nv_mul_f32_e32 v166, v166, v146\nv_accvgpr_write_b32 a66, v166\n"
        "v_accvgpr_read_b32 v167, a67\nv_mul_f32_e32 v167, v167, v147\nv_accvgpr_write_b32 a67, v167\nv_accvgpr_read_b32 v164, a68\nv_mul_f32_e32 v164, v164, v148\nv_accvgpr_write_b32 a68, v164\nv_accvgpr_read_b32 v165, a69\n"
        "v_mul_f32_e32 v165, v165, v149\nv_accvgpr_write_b32 a69, v165\nv_accvgpr_read_b32 v166, a70\nv_mul_f32_e32 v166, v166, v150\nv_accvgpr_write_b32 a70, v166\nv_accvgpr_read_b32 v167, a71\nv_mul_f32_e32 v167, v167, v151\n"
        "v_accvgpr_write_b32 a71, v167\nv_accvgpr_read_b32 v164, a72\nv_mul_f32_e32 v164, v164, v152\nv_accvgpr_write_b32 a72, v164\nv_accvgpr_read_b32 v165, a73\nv_mul_f32_e32 v165, v165, v153\nv_accvgpr_write_b32 a73, v165\n"
        "v_accvgpr_read_b32 v166, a74\nv_mul_f32_e32 v166, v166, v154\nv_accvgpr_write_b32 a74, v166\nv_accvgpr_read_b32 v167, a75\nv_mul_f32_e32 v167, v167, v155\nv_accvgpr_write_b32 a75, v167\nv_accvgpr_read_b32 v164, a76\n"
        "v_mul_f32_e32 v164, v164, v156\nv_accvgpr_write_b32 a76, v164\nv_accvgpr_read_b32 v165, a77\nv_mul_f32_e32 v165, v165, v157\nv_accvgpr_write_b32 a77, v165\nv_accvgpr_read_b32 v166, a78\nv_mul_f32_e32 v166, v166, v158\n"
        "v_accvgpr_write_b32 a78, v166\nv_accvgpr_read_b32 v167, a79\nv_mul_f32_e32 v167, v167, v159\nv_accvgpr_write_b32 a79, v167\nv_accvgpr_read_b32 v164, a80\nv_mul_f32_e32 v164, v164, v144\nv_accvgpr_write_b32 a80, v164\n"
        "v_accvgpr_read_b32 v165, a81\nv_mul_f32_e32 v165, v165, v145\nv_accvgpr_write_b32 a81, v165\nv_accvgpr_read_b32 v166, a82\nv_mul_f32_e32 v166, v166, v146\nv_accvgpr_write_b32 a82, v166\nv_accvgpr_read_b32 v167, a83\n"
        "v_mul_f32_e32 v167, v167, v147\nv_accvgpr_write_b32 a83, v167\nv_accvgpr_read_b32 v164, a84\nv_mul_f32_e32 v164, v164, v148\nv_accvgpr_write_b32 a84, v164\nv_accvgpr_read_b32 v165, a85\nv_mul_f32_e32 v165, v165, v149\n"
        "v_accvgpr_write_b32 a85, v165\nv_accvgpr_read_b32 v166, a86\nv_mul_f32_e32 v166, v166, v150\nv_accvgpr_write_b32 a86, v166\nv_accvgpr_read_b32 v167, a87\nv_mul_f32_e32 v167, v167, v151\nv_accvgpr_write_b32 a87, v167\n"
        "v_accvgpr_read_b32 v164, a88\nv_mul_f32_e32 v164, v164, v152\nv_accvgpr_write_b32 a88, v164\nv_accvgpr_read_b32 v165, a89\nv_mul_f32_e32 v165, v165, v153\nv_accvgpr_write_b32 a89, v165\nv_accvgpr_read_b32 v166, a90\n"
        "v_mul_f32_e32 v166, v166, v154\nv_accvgpr_write_b32 a90, v166\nv_accvgpr_read_b32 v167, a91\nv_mul_f32_e32 v167, v167, v155\nv_accvgpr_write_b32 a91, v167\nv_accvgpr_read_b32 v164, a92\nv_mul_f32_e32 v164, v164, v156\n"
        "v_accvgpr_write_b32 a92, v164\nv_accvgpr_read_b32 v165, a93\nv_mul_f32_e32 v165, v165, v157\nv_accvgpr_write_b32 a93, v165\nv_accvgpr_read_b32 v166, a94\nv_mul_f32_e32 v166, v166, v158\nv_accvgpr_write_b32 a94, v166\n"
        "v_accvgpr_read_b32 v167, a95\nv_mul_f32_e32 v167, v167, v159\nv_accvgpr_write_b32 a95, v167\nv_accvgpr_read_b32 v164, a96\nv_mul_f32_e32 v164, v164, v144\nv_accvgpr_write_b32 a96, v164\nv_accvgpr_read_b32 v165, a97\n"
        "v_mul_f32_e32 v165, v165, v145\nv_accvgpr_write_b32 a97, v165\nv_accvgpr_read_b32 v166, a98\nv_mul_f32_e32 v166, v166, v146\nv_accvgpr_write_b32 a98, v166\nv_accvgpr_read_b32 v167, a99\nv_mul_f32_e32 v167, v167, v147\n"
        "v_accvgpr_write_b32 a99, v167\nv_accvgpr_read_b32 v164, a100\nv_mul_f32_e32 v164, v164, v148\nv_accvgpr_write_b32 a100, v164\nv_accvgpr_read_b32 v165, a101\nv_mul_f32_e32 v165, v165, v149\nv_accvgpr_write_b32 a101, v165\n"
        "v_accvgpr_read_b32 v166, a102\nv_mul_f32_e32 v166, v166, v150\nv_accvgpr_write_b32 a102, v166\nv_accvgpr_read_b32 v167, a103\nv_mul_f32_e32 v167, v167, v151\nv_accvgpr_write_b32 a103, v167\nv_accvgpr_read_b32 v164, a104\n"
        "v_mul_f32_e32 v164, v164, v152\nv_accvgpr_write_b32 a104, v164\nv_accvgpr_read_b32 v165, a105\nv_mul_f32_e32 v165, v165, v153\nv_accvgpr_write_b32 a105, v165\nv_accvgpr_read_b32 v166, a106\nv_mul_f32_e32 v166, v166, v154\n"
        "v_accvgpr_write_b32 a106, v166\nv_accvgpr_read_b32 v167, a107\nv_mul_f32_e32 v167, v167, v155\nv_accvgpr_write_b32 a107, v167\nv_accvgpr_read_b32 v164, a108\nv_mul_f32_e32 v164, v164, v156\nv_accvgpr_write_b32 a108, v164\n"
        "v_accvgpr_read_b32 v165, a109\nv_mul_f32_e32 v165, v165, v157\nv_accvgpr_write_b32 a109, v165\nv_accvgpr_read_b32 v166, a110\nv_mul_f32_e32 v166, v166, v158\nv_accvgpr_write_b32 a110, v166\nv_accvgpr_read_b32 v167, a111\n"
        "v_mul_f32_e32 v167, v167, v159\nv_accvgpr_write_b32 a111, v167\nv_accvgpr_read_b32 v164, a112\nv_mul_f32_e32 v164, v164, v144\nv_accvgpr_write_b32 a112, v164\nv_accvgpr_read_b32 v165, a113\nv_mul_f32_e32 v165, v165, v145\n"
        "v_accvgpr_write_b32 a113, v165\nv_accvgpr_read_b32 v166, a114\nv_mul_f32_e32 v166, v166, v146\nv_accvgpr_write_b32 a114, v166\nv_accvgpr_read_b32 v167, a115\nv_mul_f32_e32 v167, v167, v147\nv_accvgpr_write_b32 a115, v167\n"
        "v_accvgpr_read_b32 v164, a116\nv_mul_f32_e32 v164, v164, v148\nv_accvgpr_write_b32 a116, v164\nv_accvgpr_read_b32 v165, a117\nv_mul_f32_e32 v165, v165, v149\nv_accvgpr_write_b32 a117, v165\nv_accvgpr_read_b32 v166, a118\n"
        "v_mul_f32_e32 v166, v166, v150\nv_accvgpr_write_b32 a118, v166\nv_accvgpr_read_b32 v167, a119\nv_mul_f32_e32 v167, v167, v151\nv_accvgpr_write_b32 a119, v167\nv_accvgpr_read_b32 v164, a120\nv_mul_f32_e32 v164, v164, v152\n"
        "v_accvgpr_write_b32 a120, v164\nv_accvgpr_read_b32 v165, a121\nv_mul_f32_e32 v165, v165, v153\nv_accvgpr_write_b32 a121, v165\nv_accvgpr_read_b32 v166, a122\nv_mul_f32_e32 v166, v166, v154\nv_accvgpr_write_b32 a122, v166\n"
        "v_accvgpr_read_b32 v167, a123\nv_mul_f32_e32 v167, v167, v155\nv_accvgpr_write_b32 a123, v167\nv_accvgpr_read_b32 v164, a124\nv_mul_f32_e32 v164, v164, v156\nv_accvgpr_write_b32 a124, v164\nv_accvgpr_read_b32 v165, a125\n"
        "v_mul_f32_e32 v165, v165, v157\nv_accvgpr_write_b32 a125, v165\nv_accvgpr_read_b32 v166, a126\nv_mul_f32_e32 v166, v166, v158\nv_accvgpr_write_b32 a126, v166\nv_accvgpr_read_b32 v167, a127\nv_mul_f32_e32 v167, v167, v159\n"
        "v_accvgpr_write_b32 a127, v167\ns_mov_b32 %[pend], 0\ns_nop 1\ns_branch L_rareBb1%=\nL_maskh0%=:\ns_nop 7\ns_nop 3\nv_mov_b32_e32 v84, 0xff800000\ns_cmp_gt_i32 %[dq1], 0\ns_cbranch_scc1 L_maskh0_n10%=\ns_cmp_eq_i32 %[dq1], 0\n"
        "s_cbranch_scc1 L_maskh0_t10%=\nv_mov_b32_e32 v16, v84\nv_mov_b32_e32 v17, v84\nv_mov_b32_e32 v18, v84\nv_mov_b32_e32 v19, v84\nv_mov_b32_e32 v20, v84\nv_mov_b32_e32 v21, v84\nv_mov_b32_e32 v22, v84\nv_mov_b32_e32 v23, v84\n"
        "v_mov_b32_e32 v24, v84\nv_mov_b32_e32 v25, v84\nv_mov_b32_e32 v26, v84\nv_mov_b32_e32 v27, v84\nv_mov_b32_e32 v28, v84\nv_mov_b32_e32 v29, v84\nv_mov_b32_e32 v30, v84\nv_mov_b32_e32 v31, v84\ns_branch L_maskh0_n10%=\n"
        "L_maskh0_t10%=:\nv_cmp_gt_i32_e64 %[mk0], 0, v238\nv_cmp_gt_i32_e64 %[mk1], 1, v238\nv_cmp_gt_i32_e64 %[mk2], 2, v238\nv_cndmask_b32_e64 v16, v16, v84, %[mk0]\nv_cmp_gt_i32_e64 %[mk0], 3, v238\n"
        "v_cndmask_b32_e64 v17, v17, v84, %[mk1]\nv_cmp_gt_i32_e64 %[mk1], 8, v238\nv_cndmask_b32_e64 v18, v18, v84, %[mk2]\nv_cmp_gt_i32_e64 %[mk2], 9, v238\nv_cndmask_b32_e64 v19, v19, v84, %[mk0]\nv_cmp_gt_i32_e64 %[mk0], 10, v238\n"
        "v_cndmask_b32_e64 v20, v20, v84, %[mk1]\nv_cmp_gt_i32_e64 %[mk1], 11, v238\nv_cndmask_b32_e64 v21, v21, v84, %[mk2]\nv_cmp_gt_i32_e64 %[mk2], 16, v238\nv_cndmask_b32_e64 v22, v22, v84, %[mk0]\nv_cmp_gt_i32_e64 %[mk0], 17, v238\n"
        "v_cndmask_b32_e64 v23, v23, v84, %[mk1]\nv_cmp_gt_i32_e64 %[mk1], 18, v238\nv_cndmask_b32_e64 v24, v24, v84, %[mk2]\nv_cmp_gt_i32_e64 %[mk2], 19, v238\nv_cndmask_b32_e64 v25, v25, v84, %[mk0]\nv_cmp_gt_i32_e64 %[mk0], 24, v238\n"
        "v_cndmask_b32_e64 v26, v26, v84, %[mk1]\nv_cmp_gt_i32_e64 %[mk1], 25, v238\nv_cndmask_b32_e64 v27, v27, v84, %[mk2]\nv_cmp_gt_i32_e64 %[mk2], 26, v238\nv_cndmask_b32_e64 v28, v28, v84, %[mk0]\nv_cmp_gt_i32_e64 %[mk0], 27, v238\n"
        "v_cndmask_b32_e64 v29, v29, v84, %[mk1]\ns_nop 1\nv_cndmask_b32_e64 v30, v30, v84, %[mk2]\nv_cndmask_b32_e64 v31, v31, v84, %[mk0]\nL_maskh0_n10%=:\ns_cmp_gt_i32 %[dq1], 1\ns_cbranch_scc1 L_maskh0_n11%=\ns_cmp_eq_i32 %[dq1], 1\n"
        "s_cbranch_scc1 L_maskh0_t11%=\nv_mov_b32_e32 v48, v84\nv_mov_b32_e32 v49, v84\nv_mov_b32_e32 v50, v84\nv_mov_b32_e32 v51, v84\nv_mov_b32_e32 v52, v84\nv_mov_b32_e32 v53, v84\nv_mov_b32_e32 v54, v84\nv_mov_b32_e32 v55, v84\n"
        "v_mov_b32_e32 v56, v84\nv_mov_b32_e32 v57, v84\nv_mov_b32_e32 v58, v84\nv_mov_b32_e32 v59, v84\nv_mov_b32_e32 v60, v84\nv_mov_b32_e32 v61, v84\nv_mov_b32_e32 v62, v84\nv_mov_b32_e32 v63, v84\ns_branch L_maskh0_n11%=\n"
        "L_maskh0_t11%=:\nv_cmp_gt_i32_e64 %[mk0], 0, v238\nv_cmp_gt_i32_e64 %[mk1], 1, v238\nv_cmp_gt_i32_e64 %[mk2], 2, v238\nv_cndmask_b32_e64 v48, v48, v84, %[mk0]\nv_cmp_gt_i32_e64 %[mk0], 3, v238\n"
        "v_cndmask_b32_e64 v49, v49, v84, %[mk1]\nv_cmp_gt_i32_e64 %[mk1], 8, v238\nv_cndmask_b32_e64 v50, v50, v84, %[mk2]\nv_cmp_gt_i32_e64 %[mk2], 9, v238\nv_cndmask_b32_e64 v51, v51, v84, %[mk0]\nv_cmp_gt_i32_e64 %[mk0], 10, v238\n"
        "v_cndmask_b32_e64 v52, v52, v84, %[mk1]\nv_cmp_gt_i32_e64 %[mk1], 11, v238\nv_cndmask_b32_e64 v53, v53, v84, %[mk2]\nv_cmp_gt_i32_e64 %[mk2], 16, v238\nv_cndmask_b32_e64 v54, v54, v84, %[mk0]\nv_cmp_gt_i32_e64 %[mk0], 17, v238\n"
        "v_cndmask_b32_e64 v55, v55, v84, %[mk1]\nv_cmp_gt_i32_e64 %[mk1], 18, v238\nv_cndmask_b32_e64 v56, v56, v84, %[mk2]\nv_cmp_gt_i32_e64 %[mk2], 19, v238\nv_cndmask_b32_e64 v57, v57, v84, %[mk0]\nv_cmp_gt_i32_e64 %[mk0], 24, v238\n"
        "v_cndmask_b32_e64 v58, v58, v84, %[mk1]\nv_cmp_gt_i32_e64 %[mk1], 25, v238\nv_cndmask_b32_e64 v59, v59, v84, %[mk2]\nv_cmp_gt_i32_e64 %[mk2], 26, v238\nv_cndmask_b32_e64 v60, v60, v84, %[mk0]\nv_cmp_gt_i32_e64 %[mk0], 27, v238\n"
        "v_cndmask_b32_e64 v61, v61, v84, %[mk1]\ns_nop 1\nv_cndmask_b32_e64 v62, v62, v84, %[mk2]\nv_cndmask_b32_e64 v63, v63, v84, %[mk0]\nL_maskh0_n11%=:\ns_sub_i32 %[dq0], %[dq0], 2\ns_sub_i32 %[dq1], %[dq1], 2\ns_branch L_maskhb0%=\n"
        "L_maskh1%=:\ns_nop 7\ns_nop 3\nv_mov_b32_e32 v20, 0xff800000\ns_cmp_gt_i32 %[dq1], 0\ns_cbranch_scc1 L_maskh1_n10%=\ns_cmp_eq_i32 %[dq1], 0\ns_cbranch_scc1 L_maskh1_t10%=\nv_mov_b32_e32 v80, v20\nv_mov_b32_e32 v81, v20\n"
        "v_mov_b32_e32 v82, v20\nv_mov_b32_e32 v83, v20\nv_mov_b32_e32 v84, v20\nv_mov_b32_e32 v85, v20\nv_mov_b32_e32 v86, v20\nv_mov_b32_e32 v87, v20\nv_mov_b32_e32 v88, v20\nv_mov_b32_e32 v89, v20\nv_mov_b32_e32 v90, v20\n"
        "v_mov_b32_e32 v91, v20\nv_mov_b32_e32 v92, v20\nv_mov_b32_e32 v93, v20\nv_mov_b32_e32 v94, v20\nv_mov_b32_e32 v95, v20\ns_branch L_maskh1_n10%=\nL_maskh1_t10%=:\nv_cmp_gt_i32_e64 %[mk0], 0, v238\nv_cmp_gt_i32_e64 %[mk1], 1, v238\n"
        "v_cmp_gt_i32_e64 %[mk2], 2, v238\nv_cndmask_b32_e64 v80, v80, v20, %[mk0]\nv_cmp_gt_i32_e64 %[mk0], 3, v238\nv_cndmask_b32_e64 v81, v81, v20, %[mk1]\nv_cmp_gt_i32_e64 %[mk1], 8, v238\nv_cndmask_b32_e64 v82, v82, v20, %[mk2]\n"
        "v_cmp_gt_i32_e64 %[mk2], 9, v238\nv_cndmask_b32_e64 v83, v83, v20, %[mk0]\nv_cmp_gt_i32_e64 %[mk0], 10, v238\nv_cndmask_b32_e64 v84, v84, v20, %[mk1]\nv_cmp_gt_i32_e64 %[mk1], 11, v238\nv_cndmask_b32_e64 v85, v85, v20, %[mk2]\n"
        "v_cmp_gt_i32_e64 %[mk2], 16, v238\nv_cndmask_b32_e64 v86, v86, v20, %[mk0]\nv_cmp_gt_i32_e64 %[mk0], 17, v238\nv_cndmask_b32_e64 v87, v87, v20, %[mk1]\nv_cmp_gt_i32_e64 %[mk1], 18, v238\nv_cndmask_b32_e64 v88, v88, v20, %[mk2]\n"
        "v_cmp_gt_i32_e64 %[mk2], 19, v238\nv_cndmask_b32_e64 v89, v89, v20, %[mk0]\nv_cmp_gt_i32_e64 %[mk0], 24, v238\nv_cndmask_b32_e64 v90, v90, v20, %[mk1]\nv_cmp_gt_i32_e64 %[mk1], 25, v238\nv_cndmask_b32_e64 v91, v91, v20, %[mk2]\n"
        "v_cmp_gt_i32_e64 %[mk2], 26, v238\nv_cndmask_b32_e64 v92, v92, v20, %[mk0]\nv_cmp_gt_i32_e64 %[mk0], 27, v238\nv_cndmask_b32_e64 v93, v93, v20, %[mk1]\ns_nop 1\nv_cndmask_b32_e64 v94, v94, v20, %[mk2]\n"
        "v_cndmask_b32_e64 v95, v95, v20, %[mk0]\nL_maskh1_n10%=:\ns_cmp_gt_i32 %[dq1], 1\ns_cbranch_scc1 L_maskh1_n11%=\ns_cmp_eq_i32 %[dq1], 1\ns_cbranch_scc1 L_maskh1_t11%=\nv_mov_b32_e32 v112, v20\nv_mov_b32_e32 v113, v20\n"
        "v_mov_b32_e32 v114, v20\nv_mov_b32_e32 v115, v20\nv_mov_b32_e32 v116, v20\nv_mov_b32_e32 v117, v20\nv_mov_b32_e32 v118, v20\nv_mov_b32_e32 v119, v20\nv_mov_b32_e32 v120, v20\nv_mov_b32_e32 v121, v20\nv_mov_b32_e32 v122, v20\n"
        "v_mov_b32_e32 v123, v20\nv_mov_b32_e32 v124, v20\nv_mov_b32_e32 v125, v20\nv_mov_b32_e32 v126, v20\nv_mov_b32_e32 v127, v20\ns_branch L_maskh1_n11%=\nL_maskh1_t11%=:\nv_cmp_gt_i32_e64 %[mk0], 0, v238\n"
        "v_cmp_gt_i32_e64 %[mk1], 1, v238\nv_cmp_gt_i32_e64 %[mk2], 2, v238\nv_cndmask_b32_e64 v112, v112, v20, %[mk0]\nv_cmp_gt_i32_e64 %[mk0], 3, v238\nv_cndmask_b32_e64 v113, v113, v20, %[mk1]\nv_cmp_gt_i32_e64 %[mk1], 8, v238\n"
        "v_cndmask_b32_e64 v114, v114, v20, %[mk2]\nv_cmp_gt_i32_e64 %[mk2], 9, v238\nv_cndmask_b32_e64 v115, v115, v20, %[mk0]\nv_cmp_gt_i32_e64 %[mk0], 10, v238\nv_cndmask_b32_e64 v116, v116, v20, %[mk1]\n"
        "v_cmp_gt_i32_e64 %[mk1], 11, v238\nv_cndmask_b32_e64 v117, v117, v20, %[mk2]\nv_cmp_gt_i32_e64 %[mk2], 16, v238\nv_cndmask_b32_e64 v118, v118, v20, %[mk0]\nv_cmp_gt_i32_e64 %[mk0], 17, v238\n"
        "v_cndmask_b32_e64 v119, v119, v20, %[mk1]\nv_cmp_gt_i32_e64 %[mk1], 18, v238\nv_cndmask_b32_e64 v120, v120, v20, %[mk2]\nv_cmp_gt_i32_e64 %[mk2], 19, v238\nv_cndmask_b32_e64 v121, v121, v20, %[mk0]\n"
        "v_cmp_gt_i32_e64 %[mk0], 24, v238\nv_cndmask_b32_e64 v122, v122, v20, %[mk1]\nv_cmp_gt_i32_e64 %[mk1], 25, v238\nv_cndmask_b32_e64 v123, v123, v20, %[mk2]\nv_cmp_gt_i32_e64 %[mk2], 26, v238\n"
        "v_cndmask_b32_e64 v124, v124, v20, %[mk0]\nv_cmp_gt_i32_e64 %[mk0], 27, v238\nv_cndmask_b32_e64 v125, v125, v20, %[mk1]\ns_nop 1\nv_cndmask_b32_e64 v126, v126, v20, %[mk2]\nv_cndmask_b32_e64 v127, v127, v20, %[mk0]\n"
        "L_maskh1_n11%=:\ns_sub_i32 %[dq0], %[dq0], 2\ns_sub_i32 %[dq1], %[dq1], 2\ns_branch L_maskhb1%=\nL_rareAh0%=:\nv_mov_b32_e32 v76, 1.0\nv_max_f32_e32 v70, v70, v71\nv_mov_b32_e32 v71, v70\ns_nop 1\n"
        "v_permlane32_swap_b32_e32 v70, v71\ns_nop 0\nv_max_f32_e32 v70, v70, v71\nv_add_f32_e32 v70, 0x41800000, v70\nv_max_f32_e32 v70, 0, v70\nv_add_f32_e32 v241, v241, v70\nv_exp_f32_e64 v77, -v70\ns_nop 0\n"
        "v_mul_f32_e32 v244, v244, v77\nv_mul_f32_e32 v245, v245, v77\nv_sub_f32_e32 v16, v16, v70\nv_sub_f32_e32 v17, v17, v70\nv_sub_f32_e32 v18, v18, v70\nv_sub_f32_e32 v19, v19, v70\nv_sub_f32_e32 v20, v20, v70\n"
        "v_sub_f32_e32 v21, v21, v70\nv_sub_f32_e32 v22, v22, v70\nv_sub_f32_e32 v23, v23, v70\nv_sub_f32_e32 v24, v24, v70\nv_sub_f32_e32 v25, v25, v70\nv_sub_f32_e32 v26, v26, v70\nv_sub_f32_e32 v27, v27, v70\n"
        "v_sub_f32_e32 v28, v28, v70\nv_sub_f32_e32 v29, v29, v70\nv_sub_f32_e32 v30, v30, v70\nv_sub_f32_e32 v31, v31, v70\nv_sub_f32_e32 v48, v48, v70\nv_sub_f32_e32 v49, v49, v70\nv_sub_f32_e32 v50, v50, v70\n"
        "v_sub_f32_e32 v51, v51, v70\nv_sub_f32_e32 v52, v52, v70\nv_sub_f32_e32 v53, v53, v70\nv_sub_f32_e32 v54, v54, v70\nv_sub_f32_e32 v55, v55, v70\nv_sub_f32_e32 v56, v56, v70\nv_sub_f32_e32 v57, v57, v70\n"
        "v_sub_f32_e32 v58, v58, v70\nv_sub_f32_e32 v59, v59, v70\nv_sub_f32_e32 v60, v60, v70\nv_sub_f32_e32 v61, v61, v70\nv_sub_f32_e32 v62, v62, v70\nv_sub_f32_e32 v63, v63, v70\nv_sub_f32_e32 v208, 0, v241\nv_mov_b32_e32 v209, v208\n"
        "v_mov_b32_e32 v210, v208\nv_mov_b32_e32 v211, v208\nv_mov_b32_e32 v212, v208\nv_mov_b32_e32 v213, v208\nv_mov_b32_e32 v214, v208\nv_mov_b32_e32 v215, v208\nv_mov_b32_e32 v216, v208\nv_mov_b32_e32 v217, v208\n"
        "v_mov_b32_e32 v218, v208\nv_mov_b32_e32 v219, v208\nv_mov_b32_e32 v220, v208\nv_mov_b32_e32 v221, v208\nv_mov_b32_e32 v222, v208\nv_mov_b32_e32 v223, v208\ns_mov_b32 %[pend], 1\ns_branch L_rareAhb0%=\nL_rareAh1%=:\n"
        "v_mov_b32_e32 v12, 1.0\nv_max_f32_e32 v6, v6, v7\nv_mov_b32_e32 v7, v6\ns_nop 1\nv_permlane32_swap_b32_e32 v6, v7\ns_nop 0\nv_max_f32_e32 v6, v6, v7\nv_add_f32_e32 v6, 0x41800000, v6\nv_max_f32_e32 v6, 0, v6\n"
        "v_add_f32_e32 v241, v241, v6\nv_exp_f32_e64 v13, -v6\ns_nop 0\nv_mul_f32_e32 v244, v244, v13\nv_mul_f32_e32 v245, v245, v13\nv_sub_f32_e32 v80, v80, v6\nv_sub_f32_e32 v81, v81, v6\nv_sub_f32_e32 v82, v82, v6\n"
        "v_sub_f32_e32 v83, v83, v6\nv_sub_f32_e32 v84, v84, v6\nv_sub_f32_e32 v85, v85, v6\nv_sub_f32_e32 v86, v86, v6\nv_sub_f32_e32 v87, v87, v6\nv_sub_f32_e32 v88, v88, v6\nv_sub_f32_e32 v89, v89, v6\nv_sub_f32_e32 v90, v90, v6\n"
        "v_sub_f32_e32 v91, v91, v6\nv_sub_f32_e32 v92, v92, v6\nv_sub_f32_e32 v93, v93, v6\nv_sub_f32_e32 v94, v94, v6\nv_sub_f32_e32 v95, v95, v6\nv_sub_f32_e32 v112, v112, v6\nv_sub_f32_e32 v113, v113, v6\nv_sub_f32_e32 v114, v114, v6\n"
        "v_sub_f32_e32 v115, v115, v6\nv_sub_f32_e32 v116, v116, v6\nv_sub_f32_e32 v117, v117, v6\nv_sub_f32_e32 v118, v118, v6\nv_sub_f32_e32 v119, v119, v6\nv_sub_f32_e32 v120, v120, v6\nv_sub_f32_e32 v121, v121, v6\n"
        "v_sub_f32_e32 v122, v122, v6\nv_sub_f32_e32 v123, v123, v6\nv_sub_f32_e32 v124, v124, v6\nv_sub_f32_e32 v125, v125, v6\nv_sub_f32_e32 v126, v126, v6\nv_sub_f32_e32 v127, v127, v6\nv_sub_f32_e32 v208, 0, v241\n"
        "v_mov_b32_e32 v209, v208\nv_mov_b32_e32 v210, v208\nv_mov_b32_e32 v211, v208\nv_mov_b32_e32 v212, v208\nv_mov_b32_e32 v213, v208\nv_mov_b32_e32 v214, v208\nv_mov_b32_e32 v215, v208\nv_mov_b32_e32 v216, v208\n"
        "v_mov_b32_e32 v217, v208\nv_mov_b32_e32 v218, v208\nv_mov_b32_e32 v219, v208\nv_mov_b32_e32 v220, v208\nv_mov_b32_e32 v221, v208\nv_mov_b32_e32 v222, v208\nv_mov_b32_e32 v223, v208\ns_mov_b32 %[pend], 1\ns_branch L_rareAhb1%=\n"
        "L_rareBh0%=:\nv_mbcnt_lo_u32_b32 v160, -1, 0\nv_mbcnt_hi_u32_b32 v160, -1, v160\ns_lshl_b32 %[t0], %[wid], 9\ns_add_u32 %[t0], %[t0], %[ldsb]\ns_add_u32 %[t0], %[t0], 0x10000\nv_and_b32_e32 v161, 31, v160\n"
        "v_lshl_add_u32 v161, v161, 2, %[t0]\nv_lshrrev_b32_e32 v162, 5, v160\nv_lshl_add_u32 v162, v162, 4, %[t0]\nds_write_b32 v161, v76\nds_write_b32 v161, v77 offset:128\ns_waitcnt lgkmcnt(0)\nds_read_b128 v[128:131], v162 offset:0\n"
        "ds_read_b128 v[132:135], v162 offset:32\nds_read_b128 v[136:139], v162 offset:64\nds_read_b128 v[140:143], v162 offset:96\nds_read_b128 v[144:147], v162 offset:128\nds_read_b128 v[148:151], v162 offset:160\n"
        "ds_read_b128 v[152:155], v162 offset:192\nds_read_b128 v[156:159], v162 offset:224\ns_waitcnt lgkmcnt(0)\ns_nop 7\nv_accvgpr_read_b32 v164, a0\nv_mul_f32_e32 v164, v164, v128\nv_accvgpr_write_b32 a0, v164\n"
        "v_accvgpr_read_b32 v165, a1\nv_mul_f32_e32 v165, v165, v129\nv_accvgpr_write_b32 a1, v165\nv_accvgpr_read_b32 v166, a2\nv_mul_f32_e32 v166, v166, v130\nv_accvgpr_write_b32 a2, v166\nv_accvgpr_read_b32 v167, a3\n"
        "v_mul_f32_e32 v167, v167, v131\nv_accvgpr_write_b32 a3, v167\nv_accvgpr_read_b32 v164, a4\nv_mul_f32_e32 v164, v164, v132\nv_accvgpr_write_b32 a4, v164\nv_accvgpr_read_b32 v165, a5\nv_mul_f32_e32 v165, v165, v133\n"
        "v_accvgpr_write_b32 a5, v165\nv_accvgpr_read_b32 v166, a6\nv_mul_f32_e32 v166, v166, v134\nv_accvgpr_write_b32 a6, v166\nv_accvgpr_read_b32 v167, a7\nv_mul_f32_e32 v167, v167, v135\nv_accvgpr_write_b32 a7, v167\n"
        "v_accvgpr_read_b32 v164, a8\nv_mul_f32_e32 v164, v164, v136\nv_accvgpr_write_b32 a8, v164\nv_accvgpr_read_b32 v165, a9\nv_mul_f32_e32 v165, v165, v137\nv_accvgpr_write_b32 a9, v165\nv_accvgpr_read_b32 v166, a10\n"
        "v_mul_f32_e32 v166, v166, v138\nv_accvgpr_write_b32 a10, v166\nv_accvgpr_read_b32 v167, a11\nv_mul_f32_e32 v167, v167, v139\nv_accvgpr_write_b32 a11, v167\nv_accvgpr_read_b32 v164, a12\nv_mul_f32_e32 v164, v164, v140\n"
        "v_accvgpr_write_b32 a12, v164\nv_accvgpr_read_b32 v165, a13\nv_mul_f32_e32 v165, v165, v141\nv_accvgpr_write_b32 a13, v165\nv_accvgpr_read_b32 v166, a14\nv_mul_f32_e32 v166, v166, v142\nv_accvgpr_write_b32 a14, v166\n"
        "v_accvgpr_read_b32 v167, a15\nv_mul_f32_e32 v167, v167, v143\nv_accvgpr_write_b32 a15, v167\nv_accvgpr_read_b32 v164, a16\nv_mul_f32_e32 v164, v164, v128\nv_accvgpr_write_b32 a16, v164\nv_accvgpr_read_b32 v165, a17\n"
        "v_mul_f32_e32 v165, v165, v129\nv_accvgpr_write_b32 a17, v165\nv_accvgpr_read_b32 v166, a18\nv_mul_f32_e32 v166, v166, v130\nv_accvgpr_write_b32 a18, v166\nv_accvgpr_read_b32 v167, a19\nv_mul_f32_e32 v167, v167, v131\n"
        "v_accvgpr_write_b32 a19, v167\nv_accvgpr_read_b32 v164, a20\nv_mul_f32_e32 v164, v164, v132\nv_accvgpr_write_b32 a20, v164\nv_accvgpr_read_b32 v165, a21\nv_mul_f32_e32 v165, v165, v133\nv_accvgpr_write_b32 a21, v165\n"
        "v_accvgpr_read_b32 v166, a22\nv_mul_f32_e32 v166, v166, v134\nv_accvgpr_write_b32 a22, v166\nv_accvgpr_read_b32 v167, a23\nv_mul_f32_e32 v167, v167, v135\nv_accvgpr_write_b32 a23, v167\nv_accvgpr_read_b32 v164, a24\n"
        "v_mul_f32_e32 v164, v164, v136\nv_accvgpr_write_b32 a24, v164\nv_accvgpr_read_b32 v165, a25\nv_mul_f32_e32 v165, v165, v137\nv_accvgpr_write_b32 a25, v165\nv_accvgpr_read_b32 v166, a26\nv_mul_f32_e32 v166, v166, v138\n"
        "v_accvgpr_write_b32 a26, v166\nv_accvgpr_read_b32 v167, a27\nv_mul_f32_e32 v167, v167, v139\nv_accvgpr_write_b32 a27, v167\nv_accvgpr_read_b32 v164, a28\nv_mul_f32_e32 v164, v164, v140\nv_accvgpr_write_b32 a28, v164\n"
        "v_accvgpr_read_b32 v165, a29\nv_mul_f32_e32 v165, v165, v141\nv_accvgpr_write_b32 a29, v165\nv_accvgpr_read_b32 v166, a30\nv_mul_f32_e32 v166, v166, v142\nv_accvgpr_write_b32 a30, v166\nv_accvgpr_read_b32 v167, a31\n"
        "v_mul_f32_e32 v167, v167, v143\nv_accvgpr_write_b32 a31, v167\nv_accvgpr_read_b32 v164, a32\nv_mul_f32_e32 v164, v164, v128\nv_accvgpr_write_b32 a32, v164\nv_accvgpr_read_b32 v165, a33\nv_mul_f32_e32 v165, v165, v129\n"
        "v_accvgpr_write_b32 a33, v165\nv_accvgpr_read_b32 v166, a34\nv_mul_f32_e32 v166, v166, v130\nv_accvgpr_write_b32 a34, v166\nv_accvgpr_read_b32 v167, a35\nv_mul_f32_e32 v167, v167, v131\nv_accvgpr_write_b32 a35, v167\n"
        "v_accvgpr_read_b32 v164, a36\nv_mul_f32_e32 v164, v164, v132\nv_accvgpr_write_b32 a36, v164\nv_accvgpr_read_b32 v165, a37\nv_mul_f32_e32 v165, v165, v133\nv_accvgpr_write_b32 a37, v165\nv_accvgpr_read_b32 v166, a38\n"
        "v_mul_f32_e32 v166, v166, v134\nv_accvgpr_write_b32 a38, v166\nv_accvgpr_read_b32 v167, a39\nv_mul_f32_e32 v167, v167, v135\nv_accvgpr_write_b32 a39, v167\nv_accvgpr_read_b32 v164, a40\nv_mul_f32_e32 v164, v164, v136\n"
        "v_accvgpr_write_b32 a40, v164\nv_accvgpr_read_b32 v165, a41\nv_mul_f32_e32 v165, v165, v137\nv_accvgpr_write_b32 a41, v165\nv_accvgpr_read_b32 v166, a42\nv_mul_f32_e32 v166, v166, v138\nv_accvgpr_write_b32 a42, v166\n"
        "v_accvgpr_read_b32 v167, a43\nv_mul_f32_e32 v167, v167, v139\nv_accvgpr_write_b32 a43, v167\nv_accvgpr_read_b32 v164, a44\nv_mul_f32_e32 v164, v164, v140\nv_accvgpr_write_b32 a44, v164\nv_accvgpr_read_b32 v165, a45\n"
        "v_mul_f32_e32 v165, v165, v141\nv_accvgpr_write_b32 a45, v165\nv_accvgpr_read_b32 v166, a46\nv_mul_f32_e32 v166, v166, v142\nv_accvgpr_write_b32 a46, v166\nv_accvgpr_read_b32 v167, a47\nv_mul_f32_e32 v167, v167, v143\n"
        "v_accvgpr_write_b32 a47, v167\nv_accvgpr_read_b32 v164, a48\nv_mul_f32_e32 v164, v164, v128\nv_accvgpr_write_b32 a48, v164\nv_accvgpr_read_b32 v165, a49\nv_mul_f32_e32 v165, v165, v129\nv_accvgpr_write_b32 a49, v165\n"
        "v_accvgpr_read_b32 v166, a50\nv_mul_f32_e32 v166, v166, v130\nv_accvgpr_write_b32 a50, v166\nv_accvgpr_read_b32 v167, a51\nv_mul_f32_e32 v167, v167, v131\nv_accvgpr_write_b32 a51, v167\nv_accvgpr_read_b32 v164, a52\n"
        "v_mul_f32_e32 v164, v164, v132\nv_accvgpr_write_b32 a52, v164\nv_accvgpr_read_b32 v165, a53\nv_mul_f32_e32 v165, v165, v133\nv_accvgpr_write_b32 a53, v165\nv_accvgpr_read_b32 v166, a54\nv_mul_f32_e32 v166, v166, v134\n"
        "v_accvgpr_write_b32 a54, v166\nv_accvgpr_read_b32 v167, a55\nv_mul_f32_e32 v167, v167, v135\nv_accvgpr_write_b32 a55, v167\nv_accvgpr_read_b32 v164, a56\nv_mul_f32_e32 v164, v164, v136\nv_accvgpr_write_b32 a56, v164\n"
        "v_accvgpr_read_b32 v165, a57\nv_mul_f32_e32 v165, v165, v137\nv_accvgpr_write_b32 a57, v165\nv_accvgpr_read_b32 v166, a58\nv_mul_f32_e32 v166, v166, v138\nv_accvgpr_write_b32 a58, v166\nv_accvgpr_read_b32 v167, a59\n"
        "v_mul_f32_e32 v167, v167, v139\nv_accvgpr_write_b32 a59, v167\nv_accvgpr_read_b32 v164, a60\nv_mul_f32_e32 v164, v164, v140\nv_accvgpr_write_b32 a60, v164\nv_accvgpr_read_b32 v165, a61\nv_mul_f32_e32 v165, v165, v141\n"
        "v_accvgpr_write_b32 a61, v165\nv_accvgpr_read_b32 v166, a62\nv_mul_f32_e32 v166, v166, v142\nv_accvgpr_write_b32 a62, v166\nv_accvgpr_read_b32 v167, a63\nv_mul_f32_e32 v167, v167, v143\nv_accvgpr_write_b32 a63, v167\n"
        "v_accvgpr_read_b32 v164, a64\nv_mul_f32_e32 v164, v164, v144\nv_accvgpr_write_b32 a64, v164\nv_accvgpr_read_b32 v165, a65\nv_mul_f32_e32 v165, v165, v145\nv_accvgpr_write_b32 a65, v165\nv_accvgpr_read_b32 v166, a66\n"
        "v_mul_f32_e32 v166, v166, v146\nv_accvgpr_write_b32 a66, v166\nv_accvgpr_read_b32 v167, a67\nv_mul_f32_e32 v167, v167, v147\nv_accvgpr_write_b32 a67, v167\nv_accvgpr_read_b32 v164, a68\nv_mul_f32_e32 v164, v164, v148\n"
        "v_accvgpr_write_b32 a68, v164\nv_accvgpr_read_b32 v165, a69\nv_mul_f32_e32 v165, v165, v149\nv_accvgpr_write_b32 a69, v165\nv_accvgpr_read_b32 v166, a70\nv_mul_f32_e32 v166, v166, v150\nv_accvgpr_write_b32 a70, v166\n"
        "v_accvgpr_read_b32 v167, a71\nv_mul_f32_e32 v167, v167, v151\nv_accvgpr_write_b32 a71, v167\nv_accvgpr_read_b32 v164, a72\nv_mul_f32_e32 v164, v164, v152\nv_accvgpr_write_b32 a72, v164\nv_accvgpr_read_b32 v165, a73\n"
        "v_mul_f32_e32 v165, v165, v153\nv_accvgpr_write_b32 a73, v165\nv_accvgpr_read_b32 v166, a74\nv_mul_f32_e32 v166, v166, v154\nv_accvgpr_write_b32 a74, v166\nv_accvgpr_read_b32 v167, a75\nv_mul_f32_e32 v167, v167, v155\n"
        "v_accvgpr_write_b32 a75, v167\nv_accvgpr_read_b32 v164, a76\nv_mul_f32_e32 v164, v164, v156\nv_accvgpr_write_b32 a76, v164\nv_accvgpr_read_b32 v165, a77\nv_mul_f32_e32 v165, v165, v157\nv_accvgpr_write_b32 a77, v165\n"
        "v_accvgpr_read_b32 v166, a78\nv_mul_f32_e32 v166, v166, v158\nv_accvgpr_write_b32 a78, v166\nv_accvgpr_read_b32 v167, a79\nv_mul_f32_e32 v167, v167, v159\nv_accvgpr_write_b32 a79, v167\nv_accvgpr_read_b32 v164, a80\n"
        "v_mul_f32_e32 v164, v164, v144\nv_accvgpr_write_b32 a80, v164\nv_accvgpr_read_b32 v165, a81\nv_mul_f32_e32 v165, v165, v145\nv_accvgpr_write_b32 a81, v165\nv_accvgpr_read_b32 v166, a82\nv_mul_f32_e32 v166, v166, v146\n"
        "v_accvgpr_write_b32 a82, v166\nv_accvgpr_read_b32 v167, a83\nv_mul_f32_e32 v167, v167, v147\nv_accvgpr_write_b32 a83, v167\nv_accvgpr_read_b32 v164, a84\nv_mul_f32_e32 v164, v164, v148\nv_accvgpr_write_b32 a84, v164\n"
        "v_accvgpr_read_b32 v165, a85\nv_mul_f32_e32 v165, v165, v149\nv_accvgpr_write_b32 a85, v165\nv_accvgpr_read_b32 v166, a86\nv_mul_f32_e32 v166, v166, v150\nv_accvgpr_write_b32 a86, v166\nv_accvgpr_read_b32 v167, a87\n"
        "v_mul_f32_e32 v167, v167, v151\nv_accvgpr_write_b32 a87, v167\nv_accvgpr_read_b32 v164, a88\nv_mul_f32_e32 v164, v164, v152\nv_accvgpr_write_b32 a88, v164\nv_accvgpr_read_b32 v165, a89\nv_mul_f32_e32 v165, v165, v153\n"
        "v_accvgpr_write_b32 a89, v165\nv_accvgpr_read_b32 v166, a90\nv_mul_f32_e32 v166, v166, v154\nv_accvgpr_write_b32 a90, v166\nv_accvgpr_read_b32 v167, a91\nv_mul_f32_e32 v167, v167, v155\nv_accvgpr_write_b32 a91, v167\n"
        "v_accvgpr_read_b32 v164, a92\nv_mul_f32_e32 v164, v164, v156\nv_accvgpr_write_b32 a92, v164\nv_accvgpr_read_b32 v165, a93\nv_mul_f32_e32 v165, v165, v157\nv_accvgpr_write_b32 a93, v165\nv_accvgpr_read_b32 v166, a94\n"
        "v_mul_f32_e32 v166, v166, v158\nv_accvgpr_write_b32 a94, v166\nv_accvgpr_read_b32 v167, a95\nv_mul_f32_e32 v167, v167, v159\nv_accvgpr_write_b32 a95, v167\nv_accvgpr_read_b32 v164, a96\nv_mul_f32_e32 v164, v164, v144\n"
        "v_accvgpr_write_b32 a96, v164\nv_accvgpr_read_b32 v165, a97\nv_mul_f32_e32 v165, v165, v145\nv_accvgpr_write_b32 a97, v165\nv_accvgpr_read_b32 v166, a98\nv_mul_f32_e32 v166, v166, v146\nv_accvgpr_write_b32 a98, v166\n"
        "v_accvgpr_read_b32 v167, a99\nv_mul_f32_e32 v167, v167, v147\nv_accvgpr_write_b32 a99, v167\nv_accvgpr_read_b32 v164, a100\nv_mul_f32_e32 v164, v164, v148\nv_accvgpr_write_b32 a100, v164\nv_accvgpr_read_b32 v165, a101\n"
        "v_mul_f32_e32 v165, v165, v149\nv_accvgpr_write_b32 a101, v165\nv_accvgpr_read_b32 v166, a102\nv_mul_f32_e32 v166, v166, v150\nv_accvgpr_write_b32 a102, v166\nv_accvgpr_read_b32 v167, a103\nv_mul_f32_e32 v167, v167, v151\n"
        "v_accvgpr_write_b32 a103, v167\nv_accvgpr_read_b32 v164, a104\nv_mul_f32_e32 v164, v164, v152\nv_accvgpr_write_b32 a104, v164\nv_accvgpr_read_b32 v165, a105\nv_mul_f32_e32 v165, v165, v153\nv_accvgpr_write_b32 a105, v165\n"
        "v_accvgpr_read_b32 v166, a106\nv_mul_f32_e32 v166, v166, v154\nv_accvgpr_write_b32 a106, v166\nv_accvgpr_read_b32 v167, a107\nv_mul_f32_e32 v167, v167, v155\nv_accvgpr_write_b32 a107, v167\nv_accvgpr_read_b32 v164, a108\n"
        "v_mul_f32_e32 v164, v164, v156\nv_accvgpr_write_b32 a108, v164\nv_accvgpr_read_b32 v165, a109\nv_mul_f32_e32 v165, v165, v157\nv_accvgpr_write_b32 a109, v165\nv_accvgpr_read_b32 v166, a110\nv_mul_f32_e32 v166, v166, v158\n"
        "v_accvgpr_write_b32 a110, v166\nv_accvgpr_read_b32 v167, a111\nv_mul_f32_e32 v167, v167, v159\nv_accvgpr_write_b32 a111, v167\nv_accvgpr_read_b32 v164, a112\nv_mul_f32_e32 v164, v164, v144\nv_accvgpr_write_b32 a112, v164\n"
        "v_accvgpr_read_b32 v165, a113\nv_mul_f32_e32 v165, v165, v145\nv_accvgpr_write_b32 a113, v165\nv_accvgpr_read_b32 v166, a114\nv_mul_f32_e32 v166, v166, v146\nv_accvgpr_write_b32 a114, v166\nv_accvgpr_read_b32 v167, a115\n"
        "v_mul_f32_e32 v167, v167, v147\nv_accvgpr_write_b32 a115, v167\nv_accvgpr_read_b32 v164, a116\nv_mul_f32_e32 v164, v164, v148\nv_accvgpr_write_b32 a116, v164\nv_accvgpr_read_b32 v165, a117\nv_mul_f32_e32 v165, v165, v149\n"
        "v_accvgpr_write_b32 a117, v165\nv_accvgpr_read_b32 v166, a118\nv_mul_f32_e32 v166, v166, v150\nv_accvgpr_write_b32 a118, v166\nv_accvgpr_read_b32 v167, a119\nv_mul_f32_e32 v167, v167, v151\nv_accvgpr_write_b32 a119, v167\n"
        "v_accvgpr_read_b32 v164, a120\nv_mul_f32_e32 v164, v164, v152\nv_accvgpr_write_b32 a120, v164\nv_accvgpr_read_b32 v165, a121\nv_mul_f32_e32 v165, v165, v153\nv_accvgpr_write_b32 a121, v165\nv_accvgpr_read_b32 v166, a122\n"
        "v_mul_f32_e32 v166, v166, v154\nv_accvgpr_write_b32 a122, v166\nv_accvgpr_read_b32 v167, a123\nv_mul_f32_e32 v167, v167, v155\nv_accvgpr_write_b32 a123, v167\nv_accvgpr_read_b32 v164, a124\nv_mul_f32_e32 v164, v164, v156\n"
        "v_accvgpr_write_b32 a124, v164\nv_accvgpr_read_b32 v165, a125\nv_mul_f32_e32 v165, v165, v157\nv_accvgpr_write_b32 a125, v165\nv_accvgpr_read_b32 v166, a126\nv_mul_f32_e32 v166, v166, v158\nv_accvgpr_write_b32 a126, v166\n"
        "v_accvgpr_read_b32 v167, a127\nv_mul_f32_e32 v167, v167, v159\nv_accvgpr_write_b32 a127, v167\ns_mov_b32 %[pend], 0\ns_nop 1\ns_branch L_rareBhb0%=\nL_rareBh1%=:\nv_mbcnt_lo_u32_b32 v160, -1, 0\nv_mbcnt_hi_u32_b32 v160, -1, v160\n"
        "s_lshl_b32 %[t0], %[wid], 9\ns_add_u32 %[t0], %[t0], %[ldsb]\ns_add_u32 %[t0], %[t0], 0x10000\nv_and_b32_e32 v161, 31, v160\nv_lshl_add_u32 v161, v161, 2, %[t0]\nv_lshrrev_b32_e32 v162, 5, v160\n"
        "v_lshl_add_u32 v162, v162, 4, %[t0]\nds_write_b32 v161, v12\nds_write_b32 v161, v13 offset:128\ns_waitcnt lgkmcnt(0)\nds_read_b128 v[128:131], v162 offset:0\nds_read_b128 v[132:135], v162 offset:32\n"
        "ds_read_b128 v[136:139], v162 offset:64\nds_read_b128 v[140:143], v162 offset:96\nds_read_b128 v[144:147], v162 offset:128\nds_read_b128 v[148:151], v162 offset:160\nds_read_b128 v[152:155], v162 offset:192\n"
        "ds_read_b128 v[156:159], v162 offset:224\ns_waitcnt lgkmcnt(0)\ns_nop 7\nv_accvgpr_read_b32 v164, a0\nv_mul_f32_e32 v164, v164, v128\nv_accvgpr_write_b32 a0, v164\nv_accvgpr_read_b32 v165, a1\nv_mul_f32_e32 v165, v165, v129\n"
        "v_accvgpr_write_b32 a1, v165\nv_accvgpr_read_b32 v166, a2\nv_mul_f32_e32 v166, v166, v130\nv_accvgpr_write_b32 a2, v166\nv_accvgpr_read_b32 v167, a3\nv_mul_f32_e32 v167, v167, v131\nv_accvgpr_write_b32 a3, v167\n"
        "v_accvgpr_read_b32 v164, a4\nv_mul_f32_e32 v164, v164, v132\nv_accvgpr_write_b32 a4, v164\nv_accvgpr_read_b32 v165, a5\nv_mul_f32_e32 v165, v165, v133\nv_accvgpr_write_b32 a5, v165\nv_accvgpr_read_b32 v166, a6\n"
        "v_mul_f32_e32 v166, v166, v134\nv_accvgpr_write_b32 a6, v166\nv_accvgpr_read_b32 v167, a7\nv_mul_f32_e32 v167, v167, v135\nv_accvgpr_write_b32 a7, v167\nv_accvgpr_read_b32 v164, a8\nv_mul_f32_e32 v164, v164, v136\n"
        "v_accvgpr_write_b32 a8, v164\nv_accvgpr_read_b32 v165, a9\nv_mul_f32_e32 v165, v165, v137\nv_accvgpr_write_b32 a9, v165\nv_accvgpr_read_b32 v166, a10\nv_mul_f32_e32 v166, v166, v138\nv_accvgpr_write_b32 a10, v166\n"
        "v_accvgpr_read_b32 v167, a11\nv_mul_f32_e32 v167, v167, v139\nv_accvgpr_write_b32 a11, v167\nv_accvgpr_read_b32 v164, a12\nv_mul_f32_e32 v164, v164, v140\nv_accvgpr_write_b32 a12, v164\nv_accvgpr_read_b32 v165, a13\n"
        "v_mul_f32_e32 v165, v165, v141\nv_accvgpr_write_b32 a13, v165\nv_accvgpr_read_b32 v166, a14\nv_mul_f32_e32 v166, v166, v142\nv_accvgpr_write_b32 a14, v166\nv_accvgpr_read_b32 v167, a15\nv_mul_f32_e32 v167, v167, v143\n"
        "v_accvgpr_write_b32 a15, v167\nv_accvgpr_read_b32 v164, a16\nv_mul_f32_e32 v164, v164, v128\nv_accvgpr_write_b32 a16, v164\nv_accvgpr_read_b32 v165, a17\nv_mul_f32_e32 v165, v165, v129\nv_accvgpr_write_b32 a17, v165\n"
        "v_accvgpr_read_b32 v166, a18\nv_mul_f32_e32 v166, v166, v130\nv_accvgpr_write_b32 a18, v166\nv_accvgpr_read_b32 v167, a19\nv_mul_f32_e32 v167, v167, v131\nv_accvgpr_write_b32 a19, v167\nv_accvgpr_read_b32 v164, a20\n"
        "v_mul_f32_e32 v164, v164, v132\nv_accvgpr_write_b32 a20, v164\nv_accvgpr_read_b32 v165, a21\nv_mul_f32_e32 v165, v165, v133\nv_accvgpr_write_b32 a21, v165\nv_accvgpr_read_b32 v166, a22\nv_mul_f32_e32 v166, v166, v134\n"
        "v_accvgpr_write_b32 a22, v166\nv_accvgpr_read_b32 v167, a23\nv_mul_f32_e32 v167, v167, v135\nv_accvgpr_write_b32 a23, v167\nv_accvgpr_read_b32 v164, a24\nv_mul_f32_e32 v164, v164, v136\nv_accvgpr_write_b32 a24, v164\n"
        "v_accvgpr_read_b32 v165, a25\nv_mul_f32_e32 v165, v165, v137\nv_accvgpr_write_b32 a25, v165\nv_accvgpr_read_b32 v166, a26\nv_mul_f32_e32 v166, v166, v138\nv_accvgpr_write_b32 a26, v166\nv_accvgpr_read_b32 v167, a27\n"
        "v_mul_f32_e32 v167, v167, v139\nv_accvgpr_write_b32 a27, v167\nv_accvgpr_read_b32 v164, a28\nv_mul_f32_e32 v164, v164, v140\nv_accvgpr_write_b32 a28, v164\nv_accvgpr_read_b32 v165, a29\nv_mul_f32_e32 v165, v165, v141\n"
        "v_accvgpr_write_b32 a29, v165\nv_accvgpr_read_b32 v166, a30\nv_mul_f32_e32 v166, v166, v142\nv_accvgpr_write_b32 a30, v166\nv_accvgpr_read_b32 v167, a31\nv_mul_f32_e32 v167, v167, v143\nv_accvgpr_write_b32 a31, v167\n"
        "v_accvgpr_read_b32 v164, a32\nv_mul_f32_e32 v164, v164, v128\nv_accvgpr_write_b32 a32, v164\nv_accvgpr_read_b32 v165, a33\nv_mul_f32_e32 v165, v165, v129\nv_accvgpr_write_b32 a33, v165\nv_accvgpr_read_b32 v166, a34\n"
        "v_mul_f32_e32 v166, v166, v130\nv_accvgpr_write_b32 a34, v166\nv_accvgpr_read_b32 v167, a35\nv_mul_f32_e32 v167, v167, v131\nv_accvgpr_write_b32 a35, v167\nv_accvgpr_read_b32 v164, a36\nv_mul_f32_e32 v164, v164, v132\n"
        "v_accvgpr_write_b32 a36, v164\nv_accvgpr_read_b32 v165, a37\nv_mul_f32_e32 v165, v165, v133\nv_accvgpr_write_b32 a37, v165\nv_accvgpr_read_b32 v166, a38\nv_mul_f32_e32 v166, v166, v134\nv_accvgpr_write_b32 a38, v166\n"
        "v_accvgpr_read_b32 v167, a39\nv_mul_f32_e32 v167, v167, v135\nv_accvgpr_write_b32 a39, v167\nv_accvgpr_read_b32 v164, a40\nv_mul_f32_e32 v164, v164, v136\nv_accvgpr_write_b32 a40, v164\nv_accvgpr_read_b32 v165, a41\n"
        "v_mul_f32_e32 v165, v165, v137\nv_accvgpr_write_b32 a41, v165\nv_accvgpr_read_b32 v166, a42\nv_mul_f32_e32 v166, v166, v138\nv_accvgpr_write_b32 a42, v166\nv_accvgpr_read_b32 v167, a43\nv_mul_f32_e32 v167, v167, v139\n"
        "v_accvgpr_write_b32 a43, v167\nv_accvgpr_read_b32 v164, a44\nv_mul_f32_e32 v164, v164, v140\nv_accvgpr_write_b32 a44, v164\nv_accvgpr_read_b32 v165, a45\nv_mul_f32_e32 v165, v165, v141\nv_accvgpr_write_b32 a45, v165\n"
        "v_accvgpr_read_b32 v166, a46\nv_mul_f32_e32 v166, v166, v142\nv_accvgpr_write_b32 a46, v166\nv_accvgpr_read_b32 v167, a47\nv_mul_f32_e32 v167, v167, v143\nv_accvgpr_write_b32 a47, v167\nv_accvgpr_read_b32 v164, a48\n"
        "v_mul_f32_e32 v164, v164, v128\nv_accvgpr_write_b32 a48, v164\nv_accvgpr_read_b32 v165, a49\nv_mul_f32_e32 v165, v165, v129\nv_accvgpr_write_b32 a49, v165\nv_accvgpr_read_b32 v166, a50\nv_mul_f32_e32 v166, v166, v130\n"
        "v_accvgpr_write_b32 a50, v166\nv_accvgpr_read_b32 v167, a51\nv_mul_f32_e32 v167, v167, v131\nv_accvgpr_write_b32 a51, v167\nv_accvgpr_read_b32 v164, a52\nv_mul_f32_e32 v164, v164, v132\nv_accvgpr_write_b32 a52, v164\n"
        "v_accvgpr_read_b32 v165, a53\nv_mul_f32_e32 v165, v165, v133\nv_accvgpr_write_b32 a53, v165\nv_accvgpr_read_b32 v166, a54\nv_mul_f32_e32 v166, v166, v134\nv_accvgpr_write_b32 a54, v166\nv_accvgpr_read_b32 v167, a55\n"
        "v_mul_f32_e32 v167, v167, v135\nv_accvgpr_write_b32 a55, v167\nv_accvgpr_read_b32 v164, a56\nv_mul_f32_e32 v164, v164, v136\nv_accvgpr_write_b32 a56, v164\nv_accvgpr_read_b32 v165, a57\nv_mul_f32_e32 v165, v165, v137\n"
        "v_accvgpr_write_b32 a57, v165\nv_accvgpr_read_b32 v166, a58\nv_mul_f32_e32 v166, v166, v138\nv_accvgpr_write_b32 a58, v166\nv_accvgpr_read_b32 v167, a59\nv_mul_f32_e32 v167, v167, v139\nv_accvgpr_write_b32 a59, v167\n"
        "v_accvgpr_read_b32 v164, a60\nv_mul_f32_e32 v164, v164, v140\nv_accvgpr_write_b32 a60, v164\nv_accvgpr_read_b32 v165, a61\nv_mul_f32_e32 v165, v165, v141\nv_accvgpr_write_b32 a61, v165\nv_accvgpr_read_b32 v166, a62\n"
        "v_mul_f32_e32 v166, v166, v142\nv_accvgpr_write_b32 a62, v166\nv_accvgpr_read_b32 v167, a63\nv_mul_f32_e32 v167, v167, v143\nv_accvgpr_write_b32 a63, v167\nv_accvgpr_read_b32 v164, a64\nv_mul_f32_e32 v164, v164, v144\n"
        "v_accvgpr_write_b32 a64, v164\nv_accvgpr_read_b32 v165, a65\nv_mul_f32_e32 v165, v165, v145\nv_accvgpr_write_b32 a65, v165\nv_accvgpr_read_b32 v166, a66\nv_mul_f32_e32 v166, v166, v146\nv_accvgpr_write_b32 a66, v166\n"
        "v_accvgpr_read_b32 v167, a67\nv_mul_f32_e32 v167, v167, v147\nv_accvgpr_write_b32 a67, v167\nv_accvgpr_read_b32 v164, a68\nv_mul_f32_e32 v164, v164, v148\nv_accvgpr_write_b32 a68, v164\nv_accvgpr_read_b32 v165, a69\n"
        "v_mul_f32_e32 v165, v165, v149\nv_accvgpr_write_b32 a69, v165\nv_accvgpr_read_b32 v166, a70\nv_mul_f32_e32 v166, v166, v150\nv_accvgpr_write_b32 a70, v166\nv_accvgpr_read_b32 v167, a71\nv_mul_f32_e32 v167, v167, v151\n"
        "v_accvgpr_write_b32 a71, v167\nv_accvgpr_read_b32 v164, a72\nv_mul_f32_e32 v164, v164, v152\nv_accvgpr_write_b32 a72, v164\nv_accvgpr_read_b32 v165, a73\nv_mul_f32_e32 v165, v165, v153\nv_accvgpr_write_b32 a73, v165\n"
        "v_accvgpr_read_b32 v166, a74\nv_mul_f32_e32 v166, v166, v154\nv_accvgpr_write_b32 a74, v166\nv_accvgpr_read_b32 v167, a75\nv_mul_f32_e32 v167, v167, v155\nv_accvgpr_write_b32 a75, v167\nv_accvgpr_read_b32 v164, a76\n"
        "v_mul_f32_e32 v164, v164, v156\nv_accvgpr_write_b32 a76, v164\nv_accvgpr_read_b32 v165, a77\nv_mul_f32_e32 v165, v165, v157\nv_accvgpr_write_b32 a77, v165\nv_accvgpr_read_b32 v166, a78\nv_mul_f32_e32 v166, v166, v158\n"
        "v_accvgpr_write_b32 a78, v166\nv_accvgpr_read_b32 v167, a79\nv_mul_f32_e32 v167, v167, v159\nv_accvgpr_write_b32 a79, v167\nv_accvgpr_read_b32 v164, a80\nv_mul_f32_e32 v164, v164, v144\nv_accvgpr_write_b32 a80, v164\n"
        "v_accvgpr_read_b32 v165, a81\nv_mul_f32_e32 v165, v165, v145\nv_accvgpr_write_b32 a81, v165\nv_accvgpr_read_b32 v166, a82\nv_mul_f32_e32 v166, v166, v146\nv_accvgpr_write_b32 a82, v166\nv_accvgpr_read_b32 v167, a83\n"
        "v_mul_f32_e32 v167, v167, v147\nv_accvgpr_write_b32 a83, v167\nv_accvgpr_read_b32 v164, a84\nv_mul_f32_e32 v164, v164, v148\nv_accvgpr_write_b32 a84, v164\nv_accvgpr_read_b32 v165, a85\nv_mul_f32_e32 v165, v165, v149\n"
        "v_accvgpr_write_b32 a85, v165\nv_accvgpr_read_b32 v166, a86\nv_mul_f32_e32 v166, v166, v150\nv_accvgpr_write_b32 a86, v166\nv_accvgpr_read_b32 v167, a87\nv_mul_f32_e32 v167, v167, v151\nv_accvgpr_write_b32 a87, v167\n"
        "v_accvgpr_read_b32 v164, a88\nv_mul_f32_e32 v164, v164, v152\nv_accvgpr_write_b32 a88, v164\nv_accvgpr_read_b32 v165, a89\nv_mul_f32_e32 v165, v165, v153\nv_accvgpr_write_b32 a89, v165\nv_accvgpr_read_b32 v166, a90\n"
        "v_mul_f32_e32 v166, v166, v154\nv_accvgpr_write_b32 a90, v166\nv_accvgpr_read_b32 v167, a91\nv_mul_f32_e32 v167, v167, v155\nv_accvgpr_write_b32 a91, v167\nv_accvgpr_read_b32 v164, a92\nv_mul_f32_e32 v164, v164, v156\n"
        "v_accvgpr_write_b32 a92, v164\nv_accvgpr_read_b32 v165, a93\nv_mul_f32_e32 v165, v165, v157\nv_accvgpr_write_b32 a93, v165\nv_accvgpr_read_b32 v166, a94\nv_mul_f32_e32 v166, v166, v158\nv_accvgpr_write_b32 a94, v166\n"
        "v_accvgpr_read_b32 v167, a95\nv_mul_f32_e32 v167, v167, v159\nv_accvgpr_write_b32 a95, v167\nv_accvgpr_read_b32 v164, a96\nv_mul_f32_e32 v164, v164, v144\nv_accvgpr_write_b32 a96, v164\nv_accvgpr_read_b32 v165, a97\n"
        "v_mul_f32_e32 v165, v165, v145\nv_accvgpr_write_b32 a97, v165\nv_accvgpr_read_b32 v166, a98\nv_mul_f32_e32 v166, v166, v146\nv_accvgpr_write_b32 a98, v166\nv_accvgpr_read_b32 v167, a99\nv_mul_f32_e32 v167, v167, v147\n"
        "v_accvgpr_write_b32 a99, v167\nv_accvgpr_read_b32 v164, a100\nv_mul_f32_e32 v164, v164, v148\nv_accvgpr_write_b32 a100, v164\nv_accvgpr_read_b32 v165, a101\nv_mul_f32_e32 v165, v165, v149\nv_accvgpr_write_b32 a101, v165\n"
        "v_accvgpr_read_b32 v166, a102\nv_mul_f32_e32 v166, v166, v150\nv_accvgpr_write_b32 a102, v166\nv_accvgpr_read_b32 v167, a103\nv_mul_f32_e32 v167, v167, v151\nv_accvgpr_write_b32 a103, v167\nv_accvgpr_read_b32 v164, a104\n"
        "v_mul_f32_e32 v164, v164, v152\nv_accvgpr_write_b32 a104, v164\nv_accvgpr_read_b32 v165, a105\nv_mul_f32_e32 v165, v165, v153\nv_accvgpr_write_b32 a105, v165\nv_accvgpr_read_b32 v166, a106\nv_mul_f32_e32 v166, v166, v154\n"
        "v_accvgpr_write_b32 a106, v166\nv_accvgpr_read_b32 v167, a107\nv_mul_f32_e32 v167, v167, v155\nv_accvgpr_write_b32 a107, v167\nv_accvgpr_read_b32 v164, a108\nv_mul_f32_e32 v164, v164, v156\nv_accvgpr_write_b32 a108, v164\n"
        "v_accvgpr_read_b32 v165, a109\nv_mul_f32_e32 v165, v165, v157\nv_accvgpr_write_b32 a109, v165\nv_accvgpr_read_b32 v166, a110\nv_mul_f32_e32 v166, v166, v158\nv_accvgpr_write_b32 a110, v166\nv_accvgpr_read_b32 v167, a111\n"
        "v_mul_f32_e32 v167, v167, v159\nv_accvgpr_write_b32 a111, v167\nv_accvgpr_read_b32 v164, a112\nv_mul_f32_e32 v164, v164, v144\nv_accvgpr_write_b32 a112, v164\nv_accvgpr_read_b32 v165, a113\nv_mul_f32_e32 v165, v165, v145\n"
        "v_accvgpr_write_b32 a113, v165\nv_accvgpr_read_b32 v166, a114\nv_mul_f32_e32 v166, v166, v146\nv_accvgpr_write_b32 a114, v166\nv_accvgpr_read_b32 v167, a115\nv_mul_f32_e32 v167, v167, v147\nv_accvgpr_write_b32 a115, v167\n"
        "v_accvgpr_read_b32 v164, a116\nv_mul_f32_e32 v164, v164, v148\nv_accvgpr_write_b32 a116, v164\nv_accvgpr_read_b32 v165, a117\nv_mul_f32_e32 v165, v165, v149\nv_accvgpr_write_b32 a117, v165\nv_accvgpr_read_b32 v166, a118\n"
        "v_mul_f32_e32 v166, v166, v150\nv_accvgpr_write_b32 a118, v166\nv_accvgpr_read_b32 v167, a119\nv_mul_f32_e32 v167, v167, v151\nv_accvgpr_write_b32 a119, v167\nv_accvgpr_read_b32 v164, a120\nv_mul_f32_e32 v164, v164, v152\n"
        "v_accvgpr_write_b32 a120, v164\nv_accvgpr_read_b32 v165, a121\nv_mul_f32_e32 v165, v165, v153\nv_accvgpr_write_b32 a121, v165\nv_accvgpr_read_b32 v166, a122\nv_mul_f32_e32 v166, v166, v154\nv_accvgpr_write_b32 a122, v166\n"
        "v_accvgpr_read_b32 v167, a123\nv_mul_f32_e32 v167, v167, v155\nv_accvgpr_write_b32 a123, v167\nv_accvgpr_read_b32 v164, a124\nv_mul_f32_e32 v164, v164, v156\nv_accvgpr_write_b32 a124, v164\nv_accvgpr_read_b32 v165, a125\n"
        "v_mul_f32_e32 v165, v165, v157\nv_accvgpr_write_b32 a125, v165\nv_accvgpr_read_b32 v166, a126\nv_mul_f32_e32 v166, v166, v158\nv_accvgpr_write_b32 a126, v166\nv_accvgpr_read_b32 v167, a127\nv_mul_f32_e32 v167, v167, v159\n"
        "v_accvgpr_write_b32 a127, v167\ns_mov_b32 %[pend], 0\ns_nop 1\ns_branch L_rareBhb1%=\nL_udma%=:\ns_bitcmp1_b32 %[ctl], 1\ns_cbranch_scc0 L_nopollh%=\nv_mov_b32_e32 v238, 0\nglobal_load_dword v246, v238, %[flg] sc1\nL_nopollh%=:\n"
        "s_mul_i32 %[t0], %[wid], 0x5000\ns_add_u32 %[dq0], %[t0], %[ldsb]\ns_add_u32 m0, %[dq0], 0x12000\ns_add_u32 %[t0], %[u0], 0x1000\nbuffer_load_dwordx4 v237, %[srdU], %[t0] offen offset:0 lds\ns_add_u32 %[t0], %[u0], 0x21cc00\n"
        "buffer_load_dwordx4 v237, %[srdU], %[t0] offen offset:1024 lds\ns_add_u32 %[t0], %[u0], 0x438800\nbuffer_load_dwordx4 v237, %[srdU], %[t0] offen offset:2048 lds\ns_add_u32 %[t0], %[u0], 0x654400\n"
        "buffer_load_dwordx4 v237, %[srdU], %[t0] offen offset:3072 lds\ns_add_u32 m0, %[dq0], 0x13000\ns_add_u32 %[t0], %[u1], 0x1000\nbuffer_load_dwordx4 v237, %[srdU], %[t0] offen offset:0 lds\ns_add_u32 %[t0], %[u1], 0x21cc00\n"
        "buffer_load_dwordx4 v237, %[srdU], %[t0] offen offset:1024 lds\ns_add_u32 %[t0], %[u1], 0x438800\nbuffer_load_dwordx4 v237, %[srdU], %[t0] offen offset:2048 lds\ns_add_u32 %[t0], %[u1], 0x654400\n"
        "buffer_load_dwordx4 v237, %[srdU], %[t0] offen offset:3072 lds\ns_add_u32 m0, %[dq0], 0x14000\ns_add_u32 %[t0], %[u2], 0x1000\nbuffer_load_dwordx4 v237, %[srdU], %[t0] offen offset:0 lds\ns_add_u32 %[t0], %[u2], 0x21cc00\n"
        "buffer_load_dwordx4 v237, %[srdU], %[t0] offen offset:1024 lds\ns_add_u32 %[t0], %[u2], 0x438800\nbuffer_load_dwordx4 v237, %[srdU], %[t0] offen offset:2048 lds\ns_add_u32 %[t0], %[u2], 0x654400\n"
        "buffer_load_dwordx4 v237, %[srdU], %[t0] offen offset:3072 lds\ns_add_u32 m0, %[dq0], 0x15000\ns_add_u32 %[t0], %[u3], 0x1000\nbuffer_load_dwordx4 v237, %[srdU], %[t0] offen offset:0 lds\ns_add_u32 %[t0], %[u3], 0x21cc00\n"
        "buffer_load_dwordx4 v237, %[srdU], %[t0] offen offset:1024 lds\ns_add_u32 %[t0], %[u3], 0x438800\nbuffer_load_dwordx4 v237, %[srdU], %[t0] offen offset:2048 lds\ns_add_u32 %[t0], %[u3], 0x654400\n"
        "buffer_load_dwordx4 v237, %[srdU], %[t0] offen offset:3072 lds\ns_add_u32 m0, %[dq0], 0x16000\ns_add_u32 %[t0], %[u4], 0x1000\nbuffer_load_dwordx4 v237, %[srdU], %[t0] offen offset:0 lds\ns_add_u32 %[t0], %[u4], 0x21cc00\n"
        "buffer_load_dwordx4 v237, %[srdU], %[t0] offen offset:1024 lds\ns_add_u32 %[t0], %[u4], 0x438800\nbuffer_load_dwordx4 v237, %[srdU], %[t0] offen offset:2048 lds\ns_add_u32 %[t0], %[u4], 0x654400\n"
        "buffer_load_dwordx4 v237, %[srdU], %[t0] offen offset:3072 lds\ns_branch L_udmab%=\nL_exit%=:\ns_cmp_eq_u32 %[mthr], 0\ns_cbranch_scc1 L_drain%=\nv_mfma_f32_32x32x16_bf16 v[16:31], a[192:195], a[160:163], v[208:223]\n"
        "ds_read_b64_tr_b16 v[128:129], v232 offset:16384\nds_read_b64_tr_b16 v[130:131], v232 offset:16896\nds_read_b64_tr_b16 v[132:133], v232 offset:17408\nv_exp_f32_e32 v96, v96\nv_exp_f32_e32 v97, v97\nv_exp_f32_e32 v98, v98\n"
        "v_cvt_pk_bf16_f32 v64, v64, v65\nv_mfma_f32_32x32x16_bf16 v[48:63], a[224:227], a[160:163], v[208:223]\nds_read_b64_tr_b16 v[134:135], v232 offset:17920\nds_read_b64_tr_b16 v[136:137], v232 offset:18432\nv_exp_f32_e32 v99, v99\n"
        "v_exp_f32_e32 v100, v100\nv_add_f32_e32 v242, v242, v96\nv_add_f32_e32 v243, v243, v97\nv_add_f32_e32 v242, v242, v98\nv_cvt_pk_bf16_f32 v65, v66, v67\nv_cvt_pk_bf16_f32 v96, v96, v97\n"
        "v_mfma_f32_32x32x16_bf16 v[16:31], a[196:199], a[164:167], v[16:31]\nds_read_b64_tr_b16 v[138:139], v232 offset:18944\nds_read_b64_tr_b16 v[140:141], v232 offset:19456\nv_exp_f32_e32 v101, v101\nv_exp_f32_e32 v102, v102\n"
        "v_add_f32_e32 v243, v243, v99\nv_add_f32_e32 v242, v242, v100\nv_cvt_pk_bf16_f32 v66, v68, v69\nv_cvt_pk_bf16_f32 v97, v98, v99\nv_mfma_f32_32x32x16_bf16 v[48:63], a[228:231], a[164:167], v[48:63]\n"
        "ds_read_b64_tr_b16 v[142:143], v232 offset:19968\nds_read_b64_tr_b16 v[144:145], v232 offset:20480\nds_read_b64_tr_b16 v[146:147], v232 offset:20992\nv_exp_f32_e32 v103, v103\nv_exp_f32_e32 v104, v104\n"
        "v_add_f32_e32 v243, v243, v101\nv_add_f32_e32 v242, v242, v102\nv_cvt_pk_bf16_f32 v67, v70, v71\nv_cvt_pk_bf16_f32 v98, v100, v101\nv_mfma_f32_32x32x16_bf16 v[16:31], a[200:203], a[168:171], v[16:31]\n"
        "ds_read_b64_tr_b16 v[148:149], v232 offset:21504\nds_read_b64_tr_b16 v[150:151], v232 offset:22016\nv_exp_f32_e32 v105, v105\nv_exp_f32_e32 v106, v106\nv_add_f32_e32 v243, v243, v103\nv_add_f32_e32 v242, v242, v104\n"
        "v_cvt_pk_bf16_f32 v72, v72, v73\nv_cvt_pk_bf16_f32 v99, v102, v103\nv_mfma_f32_32x32x16_bf16 v[48:63], a[232:235], a[168:171], v[48:63]\nds_read_b64_tr_b16 v[152:153], v232 offset:22528\n"
        "ds_read_b64_tr_b16 v[154:155], v232 offset:23040\nv_exp_f32_e32 v107, v107\nv_exp_f32_e32 v108, v108\nv_add_f32_e32 v243, v243, v105\nv_add_f32_e32 v242, v242, v106\nv_cvt_pk_bf16_f32 v73, v74, v75\n"
        "v_cvt_pk_bf16_f32 v104, v104, v105\nv_mfma_f32_32x32x16_bf16 v[16:31], a[204:207], a[172:175], v[16:31]\nds_read_b64_tr_b16 v[156:157], v232 offset:23552\nds_read_b64_tr_b16 v[158:159], v232 offset:24064\n"
        "v_exp_f32_e32 v109, v109\nv_exp_f32_e32 v110, v110\nv_add_f32_e32 v243, v243, v107\nv_add_f32_e32 v242, v242, v108\nv_cvt_pk_bf16_f32 v74, v76, v77\nv_cvt_pk_bf16_f32 v105, v106, v107\n"
        "v_mfma_f32_32x32x16_bf16 v[48:63], a[236:239], a[172:175], v[48:63]\nds_read_b64_tr_b16 v[160:161], v232 offset:24576\nds_read_b64_tr_b16 v[162:163], v232 offset:25088\nds_read_b64_tr_b16 v[164:165], v232 offset:25600\n"
        "v_exp_f32_e32 v111, v111\nv_exp_f32_e32 v112, v112\nv_exp_f32_e32 v113, v113\nv_add_f32_e32 v243, v243, v109\nv_add_f32_e32 v242, v242, v110\nv_cvt_pk_bf16_f32 v75, v78, v79\nv_cvt_pk_bf16_f32 v106, v108, v109\n"
        "v_mfma_f32_32x32x16_bf16 v[16:31], a[208:211], a[176:179], v[16:31]\nds_read_b64_tr_b16 v[166:167], v232 offset:26112\nds_read_b64_tr_b16 v[168:169], v232 offset:26624\nv_exp_f32_e32 v114, v114\nv_exp_f32_e32 v115, v115\n"
        "v_add_f32_e32 v243, v243, v111\nv_add_f32_e32 v244, v244, v112\nv_add_f32_e32 v245, v245, v113\nv_cvt_pk_bf16_f32 v80, v80, v81\nv_cvt_pk_bf16_f32 v107, v110, v111\nv_cvt_pk_bf16_f32 v112, v112, v113\n"
        "v_mfma_f32_32x32x16_bf16 v[48:63], a[240:243], a[176:179], v[48:63]\nds_read_b64_tr_b16 v[170:171], v232 offset:27136\nds_read_b64_tr_b16 v[172:173], v232 offset:27648\nv_exp_f32_e32 v116, v116\nv_exp_f32_e32 v117, v117\n"
        "v_add_f32_e32 v244, v244, v114\nv_add_f32_e32 v245, v245, v115\nv_cvt_pk_bf16_f32 v81, v82, v83\nv_cvt_pk_bf16_f32 v113, v114, v115\nv_mfma_f32_32x32x16_bf16 v[16:31], a[212:215], a[180:183], v[16:31]\n"
        "ds_read_b64_tr_b16 v[174:175], v232 offset:28160\nds_read_b64_tr_b16 v[176:177], v232 offset:28672\nds_read_b64_tr_b16 v[178:179], v232 offset:29184\nv_exp_f32_e32 v118, v118\nv_exp_f32_e32 v119, v119\n"
        "v_add_f32_e32 v244, v244, v116\nv_add_f32_e32 v245, v245, v117\nv_cvt_pk_bf16_f32 v82, v84, v85\nv_cvt_pk_bf16_f32 v114, v116, v117\nv_mfma_f32_32x32x16_bf16 v[48:63], a[244:247], a[180:183], v[48:63]\n"
        "ds_read_b64_tr_b16 v[180:181], v232 offset:29696\nds_read_b64_tr_b16 v[182:183], v232 offset:30208\nv_exp_f32_e32 v120, v120\nv_exp_f32_e32 v121, v121\nv_add_f32_e32 v244, v244, v118\nv_add_f32_e32 v245, v245, v119\n"
        "v_cvt_pk_bf16_f32 v83, v86, v87\nv_cvt_pk_bf16_f32 v115, v118, v119\nv_mfma_f32_32x32x16_bf16 v[16:31], a[216:219], a[184:187], v[16:31]\nds_read_b64_tr_b16 v[184:185], v232 offset:30720\n"
        "ds_read_b64_tr_b16 v[186:187], v232 offset:31232\nv_exp_f32_e32 v122, v122\nv_exp_f32_e32 v123, v123\nv_add_f32_e32 v244, v244, v120\nv_add_f32_e32 v245, v245, v121\nv_cvt_pk_bf16_f32 v88, v88, v89\n"
        "v_cvt_pk_bf16_f32 v120, v120, v121\nv_mfma_f32_32x32x16_bf16 v[48:63], a[248:251], a[184:187], v[48:63]\nds_read_b64_tr_b16 v[188:189], v232 offset:31744\nds_read_b64_tr_b16 v[190:191], v232 offset:32256\n"
        "v_exp_f32_e32 v124, v124\nv_exp_f32_e32 v125, v125\nv_add_f32_e32 v244, v244, v122\nv_add_f32_e32 v245, v245, v123\nv_cvt_pk_bf16_f32 v89, v90, v91\nv_cvt_pk_bf16_f32 v121, v122, v123\n"
        "v_mfma_f32_32x32x16_bf16 v[16:31], a[220:223], a[188:191], v[16:31]\nv_exp_f32_e32 v126, v126\nv_exp_f32_e32 v127, v127\nv_add_f32_e32 v244, v244, v124\nv_add_f32_e32 v245, v245, v125\nv_cvt_pk_bf16_f32 v90, v92, v93\n"
        "v_cvt_pk_bf16_f32 v122, v124, v125\nv_mfma_f32_32x32x16_bf16 v[48:63], a[252:255], a[188:191], v[48:63]\nv_add_f32_e32 v244, v244, v126\nv_add_f32_e32 v245, v245, v127\nv_cvt_pk_bf16_f32 v91, v94, v95\n"
        "v_cvt_pk_bf16_f32 v123, v126, v127\ns_nop 1\ns_waitcnt vmcnt(0) lgkmcnt(0)\ns_barrier\nv_mfma_f32_32x32x16_bf16 a[0:15], v[64:67], v[128:131], a[0:15]\ns_branch L_maskh0%=\nL_maskhb0%=:\nv_max_f32_e32 v70, v16, v17\n"
        "v_max_f32_e32 v71, v18, v19\nv_max3_f32 v70, v70, v20, v21\nv_mfma_f32_32x32x16_bf16 a[64:79], v[80:83], v[128:131], a[64:79]\ns_add_u32 m0, %[m0k], 0xc000\ns_nop 0\nbuffer_load_dwordx4 v237, %[srdV], %[sk1] offen offset:0 lds\n"
        "ds_read_b128 a[192:195], v224 offset:16384\nv_max3_f32 v71, v71, v22, v23\nv_max3_f32 v70, v70, v24, v25\nv_mfma_f32_32x32x16_bf16 a[16:31], v[64:67], v[132:135], a[16:31]\nds_read_b128 a[224:227], v224 offset:24576\n"
        "v_max3_f32 v71, v71, v26, v27\nv_max3_f32 v70, v70, v28, v29\nv_mfma_f32_32x32x16_bf16 a[80:95], v[80:83], v[132:135], a[80:95]\nbuffer_load_dwordx4 v237, %[srdV], %[sk1] offen offset:1024 lds\nv_max3_f32 v71, v71, v30, v31\n"
        "v_max3_f32 v70, v70, v48, v49\nv_max3_f32 v71, v71, v50, v51\nv_mfma_f32_32x32x16_bf16 a[32:47], v[64:67], v[136:139], a[32:47]\nds_read_b128 a[196:199], v225 offset:16384\nv_max3_f32 v70, v70, v52, v53\n"
        "v_max3_f32 v71, v71, v54, v55\nv_mfma_f32_32x32x16_bf16 a[96:111], v[80:83], v[136:139], a[96:111]\nbuffer_load_dwordx4 v237, %[srdV], %[sk1] offen offset:2048 lds\nv_max3_f32 v70, v70, v56, v57\nv_max3_f32 v71, v71, v58, v59\n"
        "v_mfma_f32_32x32x16_bf16 a[48:63], v[64:67], v[140:143], a[48:63]\nds_read_b128 a[228:231], v225 offset:24576\nv_max3_f32 v70, v70, v60, v61\nv_max3_f32 v71, v71, v62, v63\n"
        "v_mfma_f32_32x32x16_bf16 a[112:127], v[80:83], v[140:143], a[112:127]\nbuffer_load_dwordx4 v237, %[srdV], %[sk1] offen offset:3072 lds\nv_max_f32_e32 v78, v70, v71\nv_cmp_lt_f32_e32 vcc, 0x41800000, v78\n"
        "s_cbranch_vccnz L_rareAh0%=\nL_rareAhb0%=:\nv_mfma_f32_32x32x16_bf16 a[0:15], v[72:75], v[144:147], a[0:15]\nds_read_b128 a[200:203], v226 offset:16384\nv_exp_f32_e32 v16, v16\n"
        "v_mfma_f32_32x32x16_bf16 a[64:79], v[88:91], v[144:147], a[64:79]\nds_read_b128 a[232:235], v226 offset:24576\nv_exp_f32_e32 v17, v17\nv_add_f32_e32 v244, v244, v16\n"
        "v_mfma_f32_32x32x16_bf16 a[16:31], v[72:75], v[148:151], a[16:31]\nv_exp_f32_e32 v18, v18\nv_add_f32_e32 v245, v245, v17\nv_mfma_f32_32x32x16_bf16 a[80:95], v[88:91], v[148:151], a[80:95]\n"
        "ds_read_b128 a[204:207], v227 offset:16384\nv_add_f32_e32 v244, v244, v18\nv_mfma_f32_32x32x16_bf16 a[32:47], v[72:75], v[152:155], a[32:47]\nv_exp_f32_e32 v19, v19\n"
        "v_mfma_f32_32x32x16_bf16 a[96:111], v[88:91], v[152:155], a[96:111]\nds_read_b128 a[236:239], v227 offset:24576\nv_exp_f32_e32 v20, v20\nv_add_f32_e32 v245, v245, v19\n"
        "v_mfma_f32_32x32x16_bf16 a[48:63], v[72:75], v[156:159], a[48:63]\nv_add_f32_e32 v244, v244, v20\nv_mfma_f32_32x32x16_bf16 a[112:127], v[88:91], v[156:159], a[112:127]\nds_read_b128 a[208:211], v228 offset:16384\n"
        "v_exp_f32_e32 v21, v21\nv_mfma_f32_32x32x16_bf16 a[0:15], v[96:99], v[160:163], a[0:15]\nds_read_b128 a[240:243], v228 offset:24576\nv_exp_f32_e32 v22, v22\nv_add_f32_e32 v245, v245, v21\n"
        "v_mfma_f32_32x32x16_bf16 a[64:79], v[112:115], v[160:163], a[64:79]\nv_add_f32_e32 v244, v244, v22\nv_mfma_f32_32x32x16_bf16 a[16:31], v[96:99], v[164:167], a[16:31]\nds_read_b128 a[212:215], v229 offset:16384\n"
        "v_exp_f32_e32 v23, v23\nv_mfma_f32_32x32x16_bf16 a[80:95], v[112:115], v[164:167], a[80:95]\nv_exp_f32_e32 v24, v24\nv_add_f32_e32 v245, v245, v23\nv_mfma_f32_32x32x16_bf16 a[32:47], v[96:99], v[168:171], a[32:47]\n"
        "ds_read_b128 a[244:247], v229 offset:24576\nv_exp_f32_e32 v25, v25\nv_add_f32_e32 v244, v244, v24\nv_mfma_f32_32x32x16_bf16 a[96:111], v[112:115], v[168:171], a[96:111]\nv_add_f32_e32 v245, v245, v25\n"
        "v_mfma_f32_32x32x16_bf16 a[48:63], v[96:99], v[172:175], a[48:63]\nds_read_b128 a[216:219], v230 offset:16384\nv_exp_f32_e32 v26, v26\nv_mfma_f32_32x32x16_bf16 a[112:127], v[112:115], v[172:175], a[112:127]\n"
        "ds_read_b128 a[248:251], v230 offset:24576\nv_exp_f32_e32 v27, v27\nv_add_f32_e32 v244, v244, v26\nv_mfma_f32_32x32x16_bf16 a[0:15], v[104:107], v[176:179], a[0:15]\nv_add_f32_e32 v245, v245, v27\n"
        "v_mfma_f32_32x32x16_bf16 a[64:79], v[120:123], v[176:179], a[64:79]\nds_read_b128 a[220:223], v231 offset:16384\nv_exp_f32_e32 v28, v28\nv_mfma_f32_32x32x16_bf16 a[16:31], v[104:107], v[180:183], a[16:31]\nv_exp_f32_e32 v29, v29\n"
        "v_add_f32_e32 v244, v244, v28\nv_mfma_f32_32x32x16_bf16 a[80:95], v[120:123], v[180:183], a[80:95]\nds_read_b128 a[252:255], v231 offset:24576\nv_add_f32_e32 v245, v245, v29\n"
        "v_mfma_f32_32x32x16_bf16 a[32:47], v[104:107], v[184:187], a[32:47]\nv_exp_f32_e32 v30, v30\nv_mfma_f32_32x32x16_bf16 a[96:111], v[120:123], v[184:187], a[96:111]\nv_exp_f32_e32 v31, v31\nv_add_f32_e32 v244, v244, v30\n"
        "v_mfma_f32_32x32x16_bf16 a[48:63], v[104:107], v[188:191], a[48:63]\nv_add_f32_e32 v245, v245, v31\nv_mfma_f32_32x32x16_bf16 a[112:127], v[120:123], v[188:191], a[112:127]\ns_cmp_lg_u32 %[pend], 0\ns_cbranch_scc1 L_rareBh0%=\n"
        "L_rareBhb0%=:\ns_waitcnt lgkmcnt(0)\nv_mfma_f32_32x32x16_bf16 v[80:95], a[192:195], a[160:163], v[208:223]\nds_read_b64_tr_b16 v[128:129], v232 offset:0\nds_read_b64_tr_b16 v[130:131], v232 offset:512\n"
        "ds_read_b64_tr_b16 v[132:133], v232 offset:1024\nv_exp_f32_e32 v48, v48\nv_exp_f32_e32 v49, v49\nv_cvt_pk_bf16_f32 v16, v16, v17\nv_mfma_f32_32x32x16_bf16 v[112:127], a[224:227], a[160:163], v[208:223]\n"
        "ds_read_b64_tr_b16 v[134:135], v232 offset:1536\nds_read_b64_tr_b16 v[136:137], v232 offset:2048\nv_exp_f32_e32 v50, v50\nv_add_f32_e32 v244, v244, v48\nv_add_f32_e32 v245, v245, v49\nv_cvt_pk_bf16_f32 v48, v48, v49\n"
        "v_mfma_f32_32x32x16_bf16 v[80:95], a[196:199], a[164:167], v[80:95]\nds_read_b64_tr_b16 v[138:139], v232 offset:2560\nds_read_b64_tr_b16 v[140:141], v232 offset:3072\nv_exp_f32_e32 v51, v51\nv_add_f32_e32 v244, v244, v50\n"
        "v_cvt_pk_bf16_f32 v17, v18, v19\nv_mfma_f32_32x32x16_bf16 v[112:127], a[228:231], a[164:167], v[112:127]\nds_read_b64_tr_b16 v[142:143], v232 offset:3584\nds_read_b64_tr_b16 v[144:145], v232 offset:4096\n"
        "ds_read_b64_tr_b16 v[146:147], v232 offset:4608\nv_exp_f32_e32 v52, v52\nv_add_f32_e32 v245, v245, v51\nv_cvt_pk_bf16_f32 v49, v50, v51\nv_mfma_f32_32x32x16_bf16 v[80:95], a[200:203], a[168:171], v[80:95]\n"
        "ds_read_b64_tr_b16 v[148:149], v232 offset:5120\nds_read_b64_tr_b16 v[150:151], v232 offset:5632\nv_exp_f32_e32 v53, v53\nv_add_f32_e32 v244, v244, v52\nv_cvt_pk_bf16_f32 v18, v20, v21\n"
        "v_mfma_f32_32x32x16_bf16 v[112:127], a[232:235], a[168:171], v[112:127]\nds_read_b64_tr_b16 v[152:153], v232 offset:6144\nds_read_b64_tr_b16 v[154:155], v232 offset:6656\nv_exp_f32_e32 v54, v54\nv_add_f32_e32 v245, v245, v53\n"
        "v_cvt_pk_bf16_f32 v50, v52, v53\nv_mfma_f32_32x32x16_bf16 v[80:95], a[204:207], a[172:175], v[80:95]\nds_read_b64_tr_b16 v[156:157], v232 offset:7168\nds_read_b64_tr_b16 v[158:159], v232 offset:7680\nv_exp_f32_e32 v55, v55\n"
        "v_add_f32_e32 v244, v244, v54\nv_cvt_pk_bf16_f32 v19, v22, v23\nv_mfma_f32_32x32x16_bf16 v[112:127], a[236:239], a[172:175], v[112:127]\nds_read_b64_tr_b16 v[160:161], v232 offset:8192\n"
        "ds_read_b64_tr_b16 v[162:163], v232 offset:8704\nds_read_b64_tr_b16 v[164:165], v232 offset:9216\nv_exp_f32_e32 v56, v56\nv_add_f32_e32 v245, v245, v55\nv_cvt_pk_bf16_f32 v51, v54, v55\n"
        "v_mfma_f32_32x32x16_bf16 v[80:95], a[208:211], a[176:179], v[80:95]\nds_read_b64_tr_b16 v[166:167], v232 offset:9728\nds_read_b64_tr_b16 v[168:169], v232 offset:10240\nv_exp_f32_e32 v57, v57\nv_add_f32_e32 v244, v244, v56\n"
        "v_cvt_pk_bf16_f32 v24, v24, v25\nv_mfma_f32_32x32x16_bf16 v[112:127], a[240:243], a[176:179], v[112:127]\nds_read_b64_tr_b16 v[170:171], v232 offset:10752\nds_read_b64_tr_b16 v[172:173], v232 offset:11264\nv_exp_f32_e32 v58, v58\n"
        "v_add_f32_e32 v245, v245, v57\nv_cvt_pk_bf16_f32 v56, v56, v57\nv_mfma_f32_32x32x16_bf16 v[80:95], a[212:215], a[180:183], v[80:95]\nds_read_b64_tr_b16 v[174:175], v232 offset:11776\n"
        "ds_read_b64_tr_b16 v[176:177], v232 offset:12288\nds_read_b64_tr_b16 v[178:179], v232 offset:12800\nv_exp_f32_e32 v59, v59\nv_add_f32_e32 v244, v244, v58\nv_cvt_pk_bf16_f32 v25, v26, v27\n"
        "v_mfma_f32_32x32x16_bf16 v[112:127], a[244:247], a[180:183], v[112:127]\nds_read_b64_tr_b16 v[180:181], v232 offset:13312\nds_read_b64_tr_b16 v[182:183], v232 offset:13824\nv_exp_f32_e32 v60, v60\nv_add_f32_e32 v245, v245, v59\n"
        "v_cvt_pk_bf16_f32 v57, v58, v59\nv_mfma_f32_32x32x16_bf16 v[80:95], a[216:219], a[184:187], v[80:95]\nds_read_b64_tr_b16 v[184:185], v232 offset:14336\nds_read_b64_tr_b16 v[186:187], v232 offset:14848\nv_exp_f32_e32 v61, v61\n"
        "v_add_f32_e32 v244, v244, v60\nv_cvt_pk_bf16_f32 v26, v28, v29\nv_mfma_f32_32x32x16_bf16 v[112:127], a[248:251], a[184:187], v[112:127]\nds_read_b64_tr_b16 v[188:189], v232 offset:15360\n"
        "ds_read_b64_tr_b16 v[190:191], v232 offset:15872\nv_exp_f32_e32 v62, v62\nv_add_f32_e32 v245, v245, v61\nv_cvt_pk_bf16_f32 v58, v60, v61\nv_mfma_f32_32x32x16_bf16 v[80:95], a[220:223], a[188:191], v[80:95]\n"
        "v_exp_f32_e32 v63, v63\nv_add_f32_e32 v244, v244, v62\nv_cvt_pk_bf16_f32 v27, v30, v31\nv_mfma_f32_32x32x16_bf16 v[112:127], a[252:255], a[188:191], v[112:127]\nv_add_f32_e32 v245, v245, v63\nv_cvt_pk_bf16_f32 v59, v62, v63\n"
        "s_nop 1\ns_waitcnt vmcnt(0) lgkmcnt(0)\ns_barrier\nv_mfma_f32_32x32x16_bf16 a[64:79], v[16:19], v[128:131], a[64:79]\ns_branch L_maskh1%=\nL_maskhb1%=:\nv_max_f32_e32 v6, v80, v81\nv_max_f32_e32 v7, v82, v83\n"
        "v_max3_f32 v6, v6, v84, v85\nv_max3_f32 v7, v7, v86, v87\nv_mfma_f32_32x32x16_bf16 a[80:95], v[16:19], v[132:135], a[80:95]\nv_max3_f32 v6, v6, v88, v89\nv_max3_f32 v7, v7, v90, v91\nv_max3_f32 v6, v6, v92, v93\n"
        "v_max3_f32 v7, v7, v94, v95\nv_mfma_f32_32x32x16_bf16 a[96:111], v[16:19], v[136:139], a[96:111]\nv_max3_f32 v6, v6, v112, v113\nv_max3_f32 v7, v7, v114, v115\nv_max3_f32 v6, v6, v116, v117\nv_max3_f32 v7, v7, v118, v119\n"
        "v_mfma_f32_32x32x16_bf16 a[112:127], v[16:19], v[140:143], a[112:127]\nv_max3_f32 v6, v6, v120, v121\nv_max3_f32 v7, v7, v122, v123\nv_max3_f32 v6, v6, v124, v125\nv_max3_f32 v7, v7, v126, v127\n"
        "v_mfma_f32_32x32x16_bf16 a[64:79], v[24:27], v[144:147], a[64:79]\nv_max_f32_e32 v14, v6, v7\nv_cmp_lt_f32_e32 vcc, 0x41800000, v14\ns_cbranch_vccnz L_rareAh1%=\nL_rareAhb1%=:\n"
        "v_mfma_f32_32x32x16_bf16 a[80:95], v[24:27], v[148:151], a[80:95]\ns_bitcmp1_b32 %[ctl], 3\ns_cbranch_scc1 L_udma%=\nL_udmab%=:\nv_exp_f32_e32 v80, v80\nv_exp_f32_e32 v81, v81\n"
        "v_mfma_f32_32x32x16_bf16 a[96:111], v[24:27], v[152:155], a[96:111]\nv_exp_f32_e32 v82, v82\nv_exp_f32_e32 v83, v83\nv_add_f32_e32 v244, v244, v80\nv_add_f32_e32 v245, v245, v81\n"
        "v_mfma_f32_32x32x16_bf16 a[112:127], v[24:27], v[156:159], a[112:127]\nv_exp_f32_e32 v84, v84\nv_add_f32_e32 v244, v244, v82\nv_add_f32_e32 v245, v245, v83\nv_mfma_f32_32x32x16_bf16 a[64:79], v[48:51], v[160:163], a[64:79]\n"
        "v_exp_f32_e32 v85, v85\nv_exp_f32_e32 v86, v86\nv_add_f32_e32 v244, v244, v84\nv_mfma_f32_32x32x16_bf16 a[80:95], v[48:51], v[164:167], a[80:95]\nv_exp_f32_e32 v87, v87\nv_add_f32_e32 v245, v245, v85\n"
        "v_add_f32_e32 v244, v244, v86\nv_mfma_f32_32x32x16_bf16 a[96:111], v[48:51], v[168:171], a[96:111]\nv_exp_f32_e32 v88, v88\nv_exp_f32_e32 v89, v89\nv_add_f32_e32 v245, v245, v87\n"
        "v_mfma_f32_32x32x16_bf16 a[112:127], v[48:51], v[172:175], a[112:127]\nv_exp_f32_e32 v90, v90\nv_exp_f32_e32 v91, v91\nv_add_f32_e32 v244, v244, v88\nv_add_f32_e32 v245, v245, v89\n"
        "v_mfma_f32_32x32x16_bf16 a[64:79], v[56:59], v[176:179], a[64:79]\nv_exp_f32_e32 v92, v92\nv_add_f32_e32 v244, v244, v90\nv_add_f32_e32 v245, v245, v91\nv_mfma_f32_32x32x16_bf16 a[80:95], v[56:59], v[180:183], a[80:95]\n"
        "v_exp_f32_e32 v93, v93\nv_exp_f32_e32 v94, v94\nv_add_f32_e32 v244, v244, v92\nv_mfma_f32_32x32x16_bf16 a[96:111], v[56:59], v[184:187], a[96:111]\nv_exp_f32_e32 v95, v95\nv_add_f32_e32 v245, v245, v93\n"
        "v_add_f32_e32 v244, v244, v94\nv_mfma_f32_32x32x16_bf16 a[112:127], v[56:59], v[188:191], a[112:127]\nv_add_f32_e32 v245, v245, v95\ns_cmp_lg_u32 %[pend], 0\ns_cbranch_scc1 L_rareBh1%=\nL_rareBhb1%=:\ns_waitcnt lgkmcnt(0)\n"
        "ds_read_b64_tr_b16 v[128:129], v232 offset:16384\nds_read_b64_tr_b16 v[130:131], v232 offset:16896\nds_read_b64_tr_b16 v[132:133], v232 offset:17408\nds_read_b64_tr_b16 v[134:135], v232 offset:17920\n"
        "ds_read_b64_tr_b16 v[136:137], v232 offset:18432\nds_read_b64_tr_b16 v[138:139], v232 offset:18944\nds_read_b64_tr_b16 v[140:141], v232 offset:19456\nds_read_b64_tr_b16 v[142:143], v232 offset:19968\n"
        "ds_read_b64_tr_b16 v[144:145], v232 offset:20480\nds_read_b64_tr_b16 v[146:147], v232 offset:20992\nds_read_b64_tr_b16 v[148:149], v232 offset:21504\nds_read_b64_tr_b16 v[150:151], v232 offset:22016\n"
        "ds_read_b64_tr_b16 v[152:153], v232 offset:22528\nds_read_b64_tr_b16 v[154:155], v232 offset:23040\nds_read_b64_tr_b16 v[156:157], v232 offset:23552\nds_read_b64_tr_b16 v[158:159], v232 offset:24064\n"
        "ds_read_b64_tr_b16 v[160:161], v232 offset:24576\nds_read_b64_tr_b16 v[162:163], v232 offset:25088\nds_read_b64_tr_b16 v[164:165], v232 offset:25600\nds_read_b64_tr_b16 v[166:167], v232 offset:26112\n"
        "ds_read_b64_tr_b16 v[168:169], v232 offset:26624\nds_read_b64_tr_b16 v[170:171], v232 offset:27136\nds_read_b64_tr_b16 v[172:173], v232 offset:27648\nds_read_b64_tr_b16 v[174:175], v232 offset:28160\n"
        "ds_read_b64_tr_b16 v[176:177], v232 offset:28672\nds_read_b64_tr_b16 v[178:179], v232 offset:29184\nds_read_b64_tr_b16 v[180:181], v232 offset:29696\nds_read_b64_tr_b16 v[182:183], v232 offset:30208\n"
        "ds_read_b64_tr_b16 v[184:185], v232 offset:30720\nds_read_b64_tr_b16 v[186:187], v232 offset:31232\nds_read_b64_tr_b16 v[188:189], v232 offset:31744\nds_read_b64_tr_b16 v[190:191], v232 offset:32256\n"
        "v_cvt_pk_bf16_f32 v80, v80, v81\nv_cvt_pk_bf16_f32 v81, v82, v83\nv_cvt_pk_bf16_f32 v82, v84, v85\nv_cvt_pk_bf16_f32 v83, v86, v87\nv_cvt_pk_bf16_f32 v88, v88, v89\nv_cvt_pk_bf16_f32 v89, v90, v91\n"
        "v_cvt_pk_bf16_f32 v90, v92, v93\nv_cvt_pk_bf16_f32 v91, v94, v95\nv_exp_f32_e32 v112, v112\nv_exp_f32_e32 v113, v113\nv_exp_f32_e32 v114, v114\nv_exp_f32_e32 v115, v115\nv_exp_f32_e32 v116, v116\nv_exp_f32_e32 v117, v117\n"
        "v_exp_f32_e32 v118, v118\nv_exp_f32_e32 v119, v119\ns_waitcnt lgkmcnt(15)\ns_waitcnt lgkmcnt(0)\nv_mfma_f32_32x32x16_bf16 a[64:79], v[80:83], v[128:131], a[64:79]\nv_exp_f32_e32 v120, v120\nv_exp_f32_e32 v121, v121\n"
        "v_add_f32_e32 v244, v244, v112\nv_add_f32_e32 v245, v245, v113\nv_mfma_f32_32x32x16_bf16 a[80:95], v[80:83], v[132:135], a[80:95]\nv_exp_f32_e32 v122, v122\nv_exp_f32_e32 v123, v123\nv_add_f32_e32 v244, v244, v114\n"
        "v_add_f32_e32 v245, v245, v115\nv_add_f32_e32 v244, v244, v120\nv_add_f32_e32 v245, v245, v121\nv_cvt_pk_bf16_f32 v112, v112, v113\nv_mfma_f32_32x32x16_bf16 a[96:111], v[80:83], v[136:139], a[96:111]\nv_exp_f32_e32 v124, v124\n"
        "v_exp_f32_e32 v125, v125\nv_add_f32_e32 v244, v244, v116\nv_add_f32_e32 v245, v245, v117\nv_add_f32_e32 v244, v244, v122\nv_add_f32_e32 v245, v245, v123\nv_cvt_pk_bf16_f32 v113, v114, v115\nv_cvt_pk_bf16_f32 v120, v120, v121\n"
        "v_mfma_f32_32x32x16_bf16 a[112:127], v[80:83], v[140:143], a[112:127]\nv_exp_f32_e32 v126, v126\nv_exp_f32_e32 v127, v127\nv_add_f32_e32 v244, v244, v118\nv_add_f32_e32 v245, v245, v119\nv_add_f32_e32 v244, v244, v124\n"
        "v_add_f32_e32 v245, v245, v125\nv_cvt_pk_bf16_f32 v114, v116, v117\nv_cvt_pk_bf16_f32 v121, v122, v123\nv_mfma_f32_32x32x16_bf16 a[64:79], v[88:91], v[144:147], a[64:79]\nv_add_f32_e32 v244, v244, v126\n"
        "v_add_f32_e32 v245, v245, v127\nv_cvt_pk_bf16_f32 v115, v118, v119\nv_cvt_pk_bf16_f32 v122, v124, v125\nv_mfma_f32_32x32x16_bf16 a[80:95], v[88:91], v[148:151], a[80:95]\nv_cvt_pk_bf16_f32 v123, v126, v127\n"
        "v_mfma_f32_32x32x16_bf16 a[96:111], v[88:91], v[152:155], a[96:111]\nv_mfma_f32_32x32x16_bf16 a[112:127], v[88:91], v[156:159], a[112:127]\ns_nop 1\nv_mfma_f32_32x32x16_bf16 a[64:79], v[112:115], v[160:163], a[64:79]\n"
        "v_mfma_f32_32x32x16_bf16 a[80:95], v[112:115], v[164:167], a[80:95]\nv_mfma_f32_32x32x16_bf16 a[96:111], v[112:115], v[168:171], a[96:111]\nv_mfma_f32_32x32x16_bf16 a[112:127], v[112:115], v[172:175], a[112:127]\n"
        "v_mfma_f32_32x32x16_bf16 a[64:79], v[120:123], v[176:179], a[64:79]\nv_mfma_f32_32x32x16_bf16 a[80:95], v[120:123], v[180:183], a[80:95]\nv_mfma_f32_32x32x16_bf16 a[96:111], v[120:123], v[184:187], a[96:111]\n"
        "v_mfma_f32_32x32x16_bf16 a[112:127], v[120:123], v[188:191], a[112:127]\nv_add_f32_e32 v242, v242, v243\nv_add_f32_e32 v244, v244, v245\ns_waitcnt vmcnt(20)\ns_nop 7\ns_nop 3\ns_branch L_end%=\nL_drain%=:\n"
        "s_bitcmp1_b32 %[ctl], 1\ns_cbranch_scc0 L_nopoll%=\nv_mov_b32_e32 v238, 0\nglobal_load_dword v246, v238, %[flg] sc1\nL_nopoll%=:\nds_read_b64_tr_b16 v[128:129], v232 offset:16384\nds_read_b64_tr_b16 v[130:131], v232 offset:16896\n"
        "ds_read_b64_tr_b16 v[132:133], v232 offset:17408\nds_read_b64_tr_b16 v[134:135], v232 offset:17920\nds_read_b64_tr_b16 v[136:137], v232 offset:18432\nds_read_b64_tr_b16 v[138:139], v232 offset:18944\n"
        "ds_read_b64_tr_b16 v[140:141], v232 offset:19456\nds_read_b64_tr_b16 v[142:143], v232 offset:19968\nds_read_b64_tr_b16 v[144:145], v232 offset:20480\nds_read_b64_tr_b16 v[146:147], v232 offset:20992\n"
        "ds_read_b64_tr_b16 v[148:149], v232 offset:21504\nds_read_b64_tr_b16 v[150:151], v232 offset:22016\nds_read_b64_tr_b16 v[152:153], v232 offset:22528\nds_read_b64_tr_b16 v[154:155], v232 offset:23040\n"
        "ds_read_b64_tr_b16 v[156:157], v232 offset:23552\nds_read_b64_tr_b16 v[158:159], v232 offset:24064\ns_bitcmp1_b32 %[ctl], 2\ns_cbranch_scc0 L_noq%=\nv_mbcnt_lo_u32_b32 v6, -1, 0\nv_mbcnt_hi_u32_b32 v6, -1, v6\n"
        "v_bfe_u32 v4, v6, 4, 1\nv_lshlrev_b32_e32 v4, 12, v4\nv_and_b32_e32 v5, 15, v6\nv_lshl_add_u32 v4, v5, 6, v4\nv_lshrrev_b32_e32 v5, 5, v6\nv_lshl_add_u32 v4, v5, 4, v4\ns_lshl_b32 %[t0], %[wid], 14\n"
        "s_sub_u32 %[t0], 0xe000, %[t0]\nv_add_u32_e32 v5, %[t0], v4\nglobal_load_dwordx4 a[128:131], v4, %[qn] offset:0\nglobal_load_dwordx4 a[132:135], v4, %[qn] offset:32\nglobal_load_dwordx4 a[136:139], v4, %[qn] offset:1024\n"
        "global_load_dwordx4 a[140:143], v4, %[qn] offset:1056\nglobal_load_dwordx4 a[144:147], v4, %[qn] offset:2048\nglobal_load_dwordx4 a[148:151], v4, %[qn] offset:2080\nglobal_load_dwordx4 a[152:155], v4, %[qn] offset:3072\n"
        "global_load_dwordx4 a[156:159], v4, %[qn] offset:3104\nglobal_load_dwordx4 a[160:163], v5, %[qn] offset:0\nglobal_load_dwordx4 a[164:167], v5, %[qn] offset:32\nglobal_load_dwordx4 a[168:171], v5, %[qn] offset:1024\n"
        "global_load_dwordx4 a[172:175], v5, %[qn] offset:1056\nglobal_load_dwordx4 a[176:179], v5, %[qn] offset:2048\nglobal_load_dwordx4 a[180:183], v5, %[qn] offset:2080\nglobal_load_dwordx4 a[184:187], v5, %[qn] offset:3072\n"
        "global_load_dwordx4 a[188:191], v5, %[qn] offset:3104\nL_noq%=:\nv_cvt_pk_bf16_f32 v64, v64, v65\nv_cvt_pk_bf16_f32 v65, v66, v67\nv_cvt_pk_bf16_f32 v66, v68, v69\nv_cvt_pk_bf16_f32 v67, v70, v71\nv_cvt_pk_bf16_f32 v72, v72, v73\n"
        "v_cvt_pk_bf16_f32 v73, v74, v75\nv_cvt_pk_bf16_f32 v74, v76, v77\nv_cvt_pk_bf16_f32 v75, v78, v79\nv_cvt_pk_bf16_f32 v80, v80, v81\nv_cvt_pk_bf16_f32 v81, v82, v83\nv_cvt_pk_bf16_f32 v82, v84, v85\n"
        "v_cvt_pk_bf16_f32 v83, v86, v87\nv_cvt_pk_bf16_f32 v88, v88, v89\nv_cvt_pk_bf16_f32 v89, v90, v91\nv_cvt_pk_bf16_f32 v90, v92, v93\nv_cvt_pk_bf16_f32 v91, v94, v95\ns_waitcnt lgkmcnt(0)\n"
        "v_mfma_f32_32x32x16_bf16 a[0:15], v[64:67], v[128:131], a[0:15]\nds_read_b64_tr_b16 v[160:161], v232 offset:24576\nds_read_b64_tr_b16 v[162:163], v232 offset:25088\nv_exp_f32_e32 v96, v96\nv_exp_f32_e32 v97, v97\n"
        "v_exp_f32_e32 v98, v98\nv_mfma_f32_32x32x16_bf16 a[64:79], v[80:83], v[128:131], a[64:79]\nds_read_b64_tr_b16 v[164:165], v232 offset:25600\nds_read_b64_tr_b16 v[166:167], v232 offset:26112\nv_exp_f32_e32 v99, v99\n"
        "v_exp_f32_e32 v100, v100\nv_add_f32_e32 v242, v242, v96\nv_add_f32_e32 v243, v243, v97\nv_add_f32_e32 v242, v242, v98\nv_cvt_pk_bf16_f32 v96, v96, v97\nv_mfma_f32_32x32x16_bf16 a[16:31], v[64:67], v[132:135], a[16:31]\n"
        "ds_read_b64_tr_b16 v[168:169], v232 offset:26624\nds_read_b64_tr_b16 v[170:171], v232 offset:27136\nv_exp_f32_e32 v101, v101\nv_exp_f32_e32 v102, v102\nv_add_f32_e32 v243, v243, v99\nv_add_f32_e32 v242, v242, v100\n"
        "v_cvt_pk_bf16_f32 v97, v98, v99\nv_mfma_f32_32x32x16_bf16 a[80:95], v[80:83], v[132:135], a[80:95]\nds_read_b64_tr_b16 v[172:173], v232 offset:27648\nds_read_b64_tr_b16 v[174:175], v232 offset:28160\nv_exp_f32_e32 v103, v103\n"
        "v_exp_f32_e32 v104, v104\nv_exp_f32_e32 v105, v105\nv_add_f32_e32 v243, v243, v101\nv_add_f32_e32 v242, v242, v102\nv_cvt_pk_bf16_f32 v98, v100, v101\nv_mfma_f32_32x32x16_bf16 a[32:47], v[64:67], v[136:139], a[32:47]\n"
        "ds_read_b64_tr_b16 v[176:177], v232 offset:28672\nds_read_b64_tr_b16 v[178:179], v232 offset:29184\nv_exp_f32_e32 v106, v106\nv_exp_f32_e32 v107, v107\nv_add_f32_e32 v243, v243, v103\nv_add_f32_e32 v242, v242, v104\n"
        "v_add_f32_e32 v243, v243, v105\nv_cvt_pk_bf16_f32 v99, v102, v103\nv_cvt_pk_bf16_f32 v104, v104, v105\nv_mfma_f32_32x32x16_bf16 a[96:111], v[80:83], v[136:139], a[96:111]\nds_read_b64_tr_b16 v[180:181], v232 offset:29696\n"
        "ds_read_b64_tr_b16 v[182:183], v232 offset:30208\nv_exp_f32_e32 v108, v108\nv_exp_f32_e32 v109, v109\nv_add_f32_e32 v242, v242, v106\nv_add_f32_e32 v243, v243, v107\nv_cvt_pk_bf16_f32 v105, v106, v107\n"
        "v_mfma_f32_32x32x16_bf16 a[48:63], v[64:67], v[140:143], a[48:63]\nds_read_b64_tr_b16 v[184:185], v232 offset:30720\nds_read_b64_tr_b16 v[186:187], v232 offset:31232\nv_exp_f32_e32 v110, v110\nv_exp_f32_e32 v111, v111\n"
        "v_add_f32_e32 v242, v242, v108\nv_add_f32_e32 v243, v243, v109\nv_cvt_pk_bf16_f32 v106, v108, v109\nv_mfma_f32_32x32x16_bf16 a[112:127], v[80:83], v[140:143], a[112:127]\nds_read_b64_tr_b16 v[188:189], v232 offset:31744\n"
        "ds_read_b64_tr_b16 v[190:191], v232 offset:32256\nv_exp_f32_e32 v112, v112\nv_exp_f32_e32 v113, v113\nv_exp_f32_e32 v114, v114\nv_add_f32_e32 v242, v242, v110\nv_add_f32_e32 v243, v243, v111\nv_cvt_pk_bf16_f32 v107, v110, v111\n"
        "v_mfma_f32_32x32x16_bf16 a[0:15], v[72:75], v[144:147], a[0:15]\nv_exp_f32_e32 v115, v115\nv_exp_f32_e32 v116, v116\nv_add_f32_e32 v244, v244, v112\nv_add_f32_e32 v245, v245, v113\nv_add_f32_e32 v244, v244, v114\n"
        "v_cvt_pk_bf16_f32 v112, v112, v113\nv_mfma_f32_32x32x16_bf16 a[64:79], v[88:91], v[144:147], a[64:79]\nv_exp_f32_e32 v117, v117\nv_exp_f32_e32 v118, v118\nv_add_f32_e32 v245, v245, v115\nv_add_f32_e32 v244, v244, v116\n"
        "v_cvt_pk_bf16_f32 v113, v114, v115\nv_mfma_f32_32x32x16_bf16 a[16:31], v[72:75], v[148:151], a[16:31]\nv_exp_f32_e32 v119, v119\nv_exp_f32_e32 v120, v120\nv_exp_f32_e32 v121, v121\nv_add_f32_e32 v245, v245, v117\n"
        "v_add_f32_e32 v244, v244, v118\nv_cvt_pk_bf16_f32 v114, v116, v117\nv_mfma_f32_32x32x16_bf16 a[80:95], v[88:91], v[148:151], a[80:95]\nv_exp_f32_e32 v122, v122\nv_exp_f32_e32 v123, v123\nv_add_f32_e32 v245, v245, v119\n"
        "v_add_f32_e32 v244, v244, v120\nv_add_f32_e32 v245, v245, v121\nv_cvt_pk_bf16_f32 v115, v118, v119\nv_cvt_pk_bf16_f32 v120, v120, v121\nv_mfma_f32_32x32x16_bf16 a[32:47], v[72:75], v[152:155], a[32:47]\nv_exp_f32_e32 v124, v124\n"
        "v_exp_f32_e32 v125, v125\nv_add_f32_e32 v244, v244, v122\nv_add_f32_e32 v245, v245, v123\nv_cvt_pk_bf16_f32 v121, v122, v123\nv_mfma_f32_32x32x16_bf16 a[96:111], v[88:91], v[152:155], a[96:111]\nv_exp_f32_e32 v126, v126\n"
        "v_exp_f32_e32 v127, v127\nv_add_f32_e32 v244, v244, v124\nv_add_f32_e32 v245, v245, v125\nv_cvt_pk_bf16_f32 v122, v124, v125\nv_mfma_f32_32x32x16_bf16 a[48:63], v[72:75], v[156:159], a[48:63]\nv_add_f32_e32 v244, v244, v126\n"
        "v_add_f32_e32 v245, v245, v127\nv_cvt_pk_bf16_f32 v123, v126, v127\nv_mfma_f32_32x32x16_bf16 a[112:127], v[88:91], v[156:159], a[112:127]\ns_waitcnt lgkmcnt(0)\ns_nop 1\n"
        "v_mfma_f32_32x32x16_bf16 a[0:15], v[96:99], v[160:163], a[0:15]\nv_mfma_f32_32x32x16_bf16 a[64:79], v[112:115], v[160:163], a[64:79]\nv_mfma_f32_32x32x16_bf16 a[16:31], v[96:99], v[164:167], a[16:31]\n"
        "v_mfma_f32_32x32x16_bf16 a[80:95], v[112:115], v[164:167], a[80:95]\nv_mfma_f32_32x32x16_bf16 a[32:47], v[96:99], v[168:171], a[32:47]\nv_mfma_f32_32x32x16_bf16 a[96:111], v[112:115], v[168:171], a[96:111]\n"
        "v_mfma_f32_32x32x16_bf16 a[48:63], v[96:99], v[172:175], a[48:63]\nv_mfma_f32_32x32x16_bf16 a[112:127], v[112:115], v[172:175], a[112:127]\nv_mfma_f32_32x32x16_bf16 a[0:15], v[104:107], v[176:179], a[0:15]\n"
        "v_mfma_f32_32x32x16_bf16 a[64:79], v[120:123], v[176:179], a[64:79]\nv_mfma_f32_32x32x16_bf16 a[16:31], v[104:107], v[180:183], a[16:31]\nv_mfma_f32_32x32x16_bf16 a[80:95], v[120:123], v[180:183], a[80:95]\n"
        "v_mfma_f32_32x32x16_bf16 a[32:47], v[104:107], v[184:187], a[32:47]\nv_mfma_f32_32x32x16_bf16 a[96:111], v[120:123], v[184:187], a[96:111]\nv_mfma_f32_32x32x16_bf16 a[48:63], v[104:107], v[188:191], a[48:63]\n"
        "v_mfma_f32_32x32x16_bf16 a[112:127], v[120:123], v[188:191], a[112:127]\nv_add_f32_e32 v242, v242, v243\nv_add_f32_e32 v244, v244, v245\ns_waitcnt vmcnt(0)\ns_nop 7\ns_nop 3\nL_end%=:\n"
            : "={v240}"(m0_), "={v241}"(m1_), "={v242}"(l0_), "={v244}"(l1_), "={v246}"(seen), "={v247}"(old_),
              [t0] "=&s"(t0_), [pend] "=&s"(pend_), [dq0] "=&s"(dq0_), [dq1] "=&s"(dq1_), [mk0] "=&s"(mk0_), [mk1] "=&s"(mk1_), [mk2] "=&s"(mk2_),
              "+{s[40:41]}"(tab0), "+{s[42:43]}"(tab1), [sk0] "+s"(sk0), [sk1] "+s"(sk1), [cnt] "+s"(cnt), [sp] "+s"(spf)
            : [srdK] "s"(srdK), [srdV] "s"(srdV), [m0k] "s"(m0k), [ldsb] "s"(ldsb), [wid] "s"(wid), [flg] "s"(flg), [qn] "s"(qn), [ctl] "s"(ctl), [mthr] "s"(mthr),
              [srdU] "s"(srdU), [u0] "s"(uo[0]), [u1] "s"(uo[1]), [u2] "s"(uo[2]), [u3] "s"(uo[3]), [u4] "s"(uo[4]), [srdP] "s"(srdP)
            : "memory", "vcc", "scc", "m0", VCL, ACL_LO, ACL_HI);
        const int lane = lane_id(), r32 = lane & 31, hi = lane >> 5;
        if (u == 1 && wid == 0 && lane == 0) xw[1] = sig ? old_ : 0u;
        sig = 0;
        int comb = 0;
        if (split) {
            if (wid == 0 && lane == 0) xw[0] = seen;
            WAIT_L(0); BAR;
            comb = __builtin_amdgcn_readfirstlane(xw[0]) != 0;
        }
        const unsigned xo = (unsigned)(wid * 16384 + lane * 16);
        u32x4 pw[2][4][2] = {}; u32x2 pml[2] = {};
        auto ldp = [&]() {
            const unsigned xs = (unsigned)((role ^ 1) * 69632);
#pragma unroll
            for (int q = 0; q < 2; ++q) pml[q] = __builtin_amdgcn_raw_buffer_load_b64(srdX, (unsigned)(65536 + ((wid * 2 + q) * 32 + r32) * 8), xs, 16);
#pragma unroll
            for (int i = 0; i < 16; ++i) pw[i >> 3][(i >> 1) & 3][i & 1] = __builtin_amdgcn_raw_buffer_load_b128(srdX, xo + i * 1024, xs, 16);
        };
        if (comb) { ldp(); WAIT_V(18); } else WAIT_V(0);
        if (role == 0) {
            LAS unsigned char* ub = lds + UOFF + wid * 20480;
#pragma unroll 1
            for (int n = 0; n < (gw < 112 ? 5 : 4); ++n) {
                {
                    LAS u32x4* pu = (LAS u32x4*)(ub + n * 4096 + lane * 16);
                    const u32x4 p0 = pu[0], p1 = pu[64], p2 = pu[128], p3 = pu[192];
                    const float bq = bf2f(*(const LAS u16*)(lds + BOFF + ((gw + n * 512) / 20) * 32 + (lane >> 2) * 2));
                    u32x4 gv;
#pragma unroll
                    for (int e = 0; e < 4; ++e) {
                        const float ul = ((bf2f(p0[e] & 0xffffu) + bf2f(p1[e] & 0xffffu)) + bf2f(p2[e] & 0xffffu)) + bf2f(p3[e] & 0xffffu);
                        const float uh = ((__uint_as_float(p0[e] & 0xffff0000u) + __uint_as_float(p1[e] & 0xffff0000u)) + __uint_as_float(p2[e] & 0xffff0000u)) + __uint_as_float(p3[e] & 0xffff0000u);
                        const unsigned ru = cvtpk(ul, uh);
                        const unsigned rx = cvtpk(__uint_as_float(ru << 16) + bq, __uint_as_float(ru & 0xffff0000u) + bq);
                        const f32x2 xg = {__uint_as_float(rx << 16), __uint_as_float(rx & 0xffff0000u)};
                        const f32x2 z = xg * (xg * xg * (-2.8853900817779268f * 0.7978845608028654f * 0.044715f) + (-2.8853900817779268f * 0.7978845608028654f));
                        const f32x2 dn = (f32x2){__builtin_amdgcn_exp2f(z[0]), __builtin_amdgcn_exp2f(z[1])} + 1.0f;
                        const f32x2 g = xg * (f32x2){__builtin_amdgcn_rcpf(dn[0]), __builtin_amdgcn_rcpf(dn[1])};
                        gv[e] = cvtpk(g[0], g[1]);
                    }
                    pu[0] = gv;
                }
            }
            unsigned again = 0;
            if (!comb && wid == 0) again = __hip_atomic_load(flg, __ATOMIC_RELAXED, __HIP_MEMORY_SCOPE_AGENT);
            if (!comb) {
                if (wid == 0 && lane == 0) xw[2] = again;
                WAIT_L(0); BAR;
                comb = __builtin_amdgcn_readfirstlane(xw[2]) != 0;
                if (comb) ldp();
            }
        }
        const unsigned xs = (unsigned)(role * 69632);
        auto epi = [&](auto mode_) {
            constexpr int MODE = decltype(mode_)::value;
#pragma unroll
            for (int q = 0; q < 2; ++q) {
                const float ls = swap_add(q ? l1_ : l0_), mq = q ? m1_ : m0_;
                float f = 1.0f / ls, gq = 0.f;
                const unsigned mo = (unsigned)(65536 + ((wid * 2 + q) * 32 + r32) * 8);
                if constexpr (MODE == 2) {
                    const u32x2 ml = pml[q];
                    const float m1 = __uint_as_float(ml[0]), l1 = __uint_as_float(ml[1]), mm = fmaxf(mq, m1);
                    const float a0 = __builtin_amdgcn_exp2f(mq - mm), a1 = __builtin_amdgcn_exp2f(m1 - mm) * l1, il = 1.0f / (ls * a0 + a1);
                    f = a0 * il; gq = a1 * il;
                }
                if constexpr (MODE == 1) { u32x2 ml; ml[0] = __float_as_uint(mq); ml[1] = __float_as_uint(ls); if (hi == 0) __builtin_amdgcn_raw_buffer_store_b64(ml, srdX, mo, xs, 16); }
                if (hi == 0) { al[q * 64 + r32] = f; if constexpr (MODE == 2) al[q * 64 + 32 + r32] = gq; }
            }
            WAIT_L(0);
#define AT_EPI(q, cc) do { float o[16]; acc_read16<AO_(q, cc)>(o, mkseq<16>::type{}); \
            _Pragma("unroll") for (int r = 0; r < 16; ++r) o[r] *= fr[r]; \
            if constexpr (MODE == 2) { _Pragma("unroll") for (int r = 0; r < 16; ++r) { const unsigned w_ = pw[q][cc][r >> 3][(r >> 1) & 3]; o[r] += gr[r] * bf2f((r & 1) ? (w_ >> 16) : (w_ & 0xffffu)); } } \
            _Pragma("unroll") for (int kk = 0; kk < 2; ++kk) { u32x4 w; \
                if constexpr (MODE == 1) { _Pragma("unroll") for (int e = 0; e < 4; ++e) w[e] = cvtpk(o[8 * kk + 2 * e], o[8 * kk + 2 * e + 1]); \
                    __builtin_amdgcn_raw_buffer_store_b128(w, srdX, xo + (unsigned)(((q) * 8 + (cc) * 2 + kk) * 1024), xs, 16); } \
                else { const auto rx = __builtin_amdgcn_permlane32_swap(cvtpk(o[8 * kk], o[8 * kk + 1]), cvtpk(o[8 * kk + 4], o[8 * kk + 5]), false, false); \
                    const auto ry = __builtin_amdgcn_permlane32_swap(cvtpk(o[8 * kk + 2], o[8 * kk + 3]), cvtpk(o[8 * kk + 6], o[8 * kk + 7]), false, false); \
                    w[0] = rx[0]; w[1] = ry[0]; w[2] = rx[1]; w[3] = ry[1]; __builtin_amdgcn_raw_buffer_store_b128(w, srdC, ob + kk * 1024, (cc) * 131072, CTX_AUX); } } } while (0)
            float fr[16], gr[16];
#pragma unroll
            for (int r = 0; r < 16; ++r) { fr[r] = al[crow(r, hi)]; if constexpr (MODE == 2) gr[r] = al[32 + crow(r, hi)]; }
            const __amdgpu_buffer_rsrc_t srdC = __builtin_amdgcn_make_buffer_rsrc((void*)(ctxT + (size_t)head * 262144), (short)0, 524288, 0x00020000);
            unsigned ob = (unsigned)((qblk * 16 + wid * 2) * 1024 + r32 * 32 + hi * 16);
            AT_EPI(0, 0); AT_EPI(0, 1); AT_EPI(0, 2); AT_EPI(0, 3);
#pragma unroll
            for (int r = 0; r < 16; ++r) { fr[r] = al[64 + crow(r, hi)]; if constexpr (MODE == 2) gr[r] = al[96 + crow(r, hi)]; }
            ob += (unsigned)((14 - 4 * wid) * 1024);
            AT_EPI(1, 0); AT_EPI(1, 1); AT_EPI(1, 2); AT_EPI(1, 3);
        };
        __builtin_amdgcn_s_waitcnt(0x0F70);
        if (comb) epi(icst<2>{}); else if (split) epi(icst<1>{}); else epi(icst<0>{});
        WAIT_L(0);
        if (role == 0) {
            int g2 = gw; asm volatile("" : "+s"(g2));
            const __amdgpu_buffer_rsrc_t srdG = __builtin_amdgcn_make_buffer_rsrc((void*)gT, (short)0, F_ * M_ * 2, 0x00020000);
#pragma unroll 1
            for (int n = 0; n < (g2 < 112 ? 5 : 4); ++n)
                { const unsigned item = (unsigned)(g2 + n * 512), fb = item / 20u, db = item - fb * 20u, d5 = db / 5u;
                    __builtin_amdgcn_raw_buffer_store_b128(*(const LAS u32x4*)(lds + UOFF + wid * 20480 + n * 4096 + lane * 16), srdG, (unsigned)((lane >> 2) * 320 + (lane & 3) * 16),
                                                           (d5 * F_ + fb * 16u) * 320u + (db - d5 * 5u) * 64u, 16); }
        }
        if (split && !comb) sig = 1;
    }
    if (sig) {
        WAIT_V(0);
        BAR;
        if (wid == 0 && lane_id() == 0) xw[1] = __hip_atomic_fetch_add(flg, 1u, __ATOMIC_RELAXED, __HIP_MEMORY_SCOPE_AGENT);
    } else if (role == 0) return;
    WAIT_L(0); BAR;
    if (__builtin_amdgcn_readfirstlane(xw[1]) != 0) {
        const int lane = lane_id(), r32 = lane & 31, hi = lane >> 5;
#pragma unroll 1
        for (int q = 0; q < 2; ++q) {
            const unsigned mo = (unsigned)(65536 + ((wid * 2 + q) * 32 + r32) * 8);
            const u32x2 ma = __builtin_amdgcn_raw_buffer_load_b64(srdX, mo, 0, 16), mb = __builtin_amdgcn_raw_buffer_load_b64(srdX, mo, 69632, 16);
            const float m0 = __uint_as_float(ma[0]), m1 = __uint_as_float(mb[0]), mm = fmaxf(m0, m1);
            const float a0 = __builtin_amdgcn_exp2f(m0 - mm) * __uint_as_float(ma[1]), a1 = __builtin_amdgcn_exp2f(m1 - mm) * __uint_as_float(mb[1]), il = 1.0f / (a0 + a1);
            if (hi == 0) { al[r32] = a0 * il; al[32 + r32] = a1 * il; }
            WAIT_L(0);
            u16* ob = ctxT + ((size_t)(head * 4) * 128 + q0 * 16 + (q ? 14 - 2 * wid : 2 * wid)) * 512 + r32 * 16 + hi * 8;
#pragma unroll 1
            for (int i = 0; i < 8; ++i) {
                const unsigned xo = (unsigned)(wid * 16384 + lane * 16 + (q * 8 + i) * 1024);
                const u32x4 wa = __builtin_amdgcn_raw_buffer_load_b128(srdX, xo, 0, 16), wb = __builtin_amdgcn_raw_buffer_load_b128(srdX, xo, 69632, 16);
                float o[8];
#pragma unroll
                for (int r = 0; r < 8; ++r) { const int row = (i & 1) * 16 + crow(r, hi);
                    o[r] = al[row] * bf2f((r & 1) ? (wa[r >> 1] >> 16) : (wa[r >> 1] & 0xffffu)) + al[32 + row] * bf2f((r & 1) ? (wb[r >> 1] >> 16) : (wb[r >> 1] & 0xffffu)); }
                const auto rx = __builtin_amdgcn_permlane32_swap(cvtpk(o[0], o[1]), cvtpk(o[4], o[5]), false, false);
                const auto ry = __builtin_amdgcn_permlane32_swap(cvtpk(o[2], o[3]), cvtpk(o[6], o[7]), false, false);
                u32x4 w; w[0] = rx[0]; w[1] = ry[0]; w[2] = rx[1]; w[3] = ry[1];
                *(u32x4*)(ob + (size_t)((i >> 1) * 128 + (i & 1)) * 512) = w;
            }
            WAIT_L(0);
        }
    }
#undef AT_EPI
}
#define O_SLEEP 2
#define LATE_X
namespace g3 {
constexpr int NST = 4, STG = 36864, LDS_BYTES = NST * STG, NK1 = 64, NKT = 91, KSPL = 23;
}
__global__ __launch_bounds__(512, 2) void k_out(const u16* __restrict__ ctxT, const u16* __restrict__ wo, const u16* __restrict__ w2, const u16* __restrict__ gT,
                                                u16* __restrict__ part, unsigned* __restrict__ cnt, const u16* __restrict__ x, const u16* __restrict__ b2, u16* __restrict__ out) {
    using namespace g3;
    extern __shared__ __attribute__((aligned(16))) unsigned char shm_[];
    LAS unsigned char* lds = (LAS unsigned char*)shm_;
    const int tid = threadIdx.x, wid = __builtin_amdgcn_readfirstlane(tid >> 6), lane = tid & 63, fr = lane & 15, fq = lane >> 4;
    const int c_ = blockIdx.x, xcd = c_ & 7, jj = c_ >> 3;
    const int z = xcd >> 1, st = (xcd & 1) * 8 + (jj & 7), nt = jj >> 3, tile = st * 4 + nt;
    const int s0 = st * 128, n0 = nt * 160;
    const int kt0 = z * KSPL, kt1 = (kt0 + KSPL < NKT) ? kt0 + KSPL : NKT, nk = kt1 - kt0;
    if (wid >= 4) {
        const int lw = wid - 4;
        const unsigned voffC = (unsigned)(((lw >> 1) * 128 + (s0 >> 4) + (lane >> 5)) * 1024 + (lw & 1) * 512 + (lane & 31) * 16);
        unsigned voffW[2], voffB[5], voffG[5];
#pragma unroll
        for (int j = 0; j < 2; ++j) { const int r = 4 * j + (lane >> 4); voffW[j] = (unsigned)((lw * 16 + r) * (S_ * 2) + s0 * 2 + (((lane & 15) ^ (r << 1)) * 16)); }
#pragma unroll
        for (int j = 0; j < 5; ++j) { const int c = j * 64 + lane, r = c / 20, ch = (c % 20) ^ (((r >> 2) & 1) << 1);
            voffB[j] = (unsigned)((lw * 16 + r) * (M_ * 2) + n0 * 2 + ch * 16); voffG[j] = (unsigned)(nt * (F_ * 320) + (lw * 16 + r) * 320 + ch * 16); }
        auto stage = [&](int i) {
            const int kt = kt0 + i;
            LAS unsigned char* dst = lds + (i & (NST - 1)) * STG;
            if (kt < NK1) { const char* ga = (const char*)ctxT + (size_t)kt * 262144 + voffC; const char* gb = (const char*)wo + (size_t)kt * 81920;
#pragma unroll
                for (int j = 0; j < 4; ++j) GLDS16(ga + j * 2048, dst + lw * 4096 + j * 1024);
#pragma unroll
                for (int j = 0; j < 5; ++j) GLDS16(gb + voffB[j], dst + 16384 + lw * 5120 + j * 1024); }
            else { const char* ga = (const char*)w2 + (size_t)(kt - NK1) * 262144; const char* gb = (const char*)gT + (size_t)(kt - NK1) * 20480;
#pragma unroll
                for (int j = 0; j < 4; ++j) GLDS16(ga + (j >> 1) * 32768 + voffW[j & 1], dst + lw * 4096 + j * 1024);
#pragma unroll
                for (int j = 0; j < 5; ++j) GLDS16(gb + voffG[j], dst + 16384 + lw * 5120 + j * 1024); }
        };
        stage(0); stage(1); stage(2); stage(3);
        WAIT_V(27); BAR;
        WAIT_V(18); BAR;
        for (int it = 0; it < nk; ++it) {
            if (it + 4 < nk) { stage(it + 4); WAIT_V(18); } else if (it + 3 < nk) WAIT_V(9); else WAIT_V(0);
            BAR;
        }
        return;
    }
    const int wm = wid & 1, wn = wid >> 1;
    int aC, aW, bE, bO, lb;
    { const int q = fr >> 2, p = lane & 3, l0 = (int)(uintptr_t)lds;
      aC = (wm * 2048 + lane * 8) ^ (512 * z);
      aW = ((4 * fq + q) * 256 + (((wm ^ (fq & 1)) * 4 + q) * 32) + p * 8) ^ (32 * z);
      lb = l0;
      const int rb = l0 + 16384 + (4 * fq + q) * 320 + p * 8 + wn * 160, y = (fq & 1) * 32;
      bE = wn ? rb - y : rb + y; bO = wn ? rb + y : rb - y; }
    const size_t ob = (size_t)(s0 + wm * 64 + fr) * M_ + n0 + wn * 80 + fq * 4;
    u32x2 xv[5]; u16 bv;
    auto ldx = [&](int o) {
        bv = b2[s0 + wm * 64 + o * 16 + fr];
#pragma unroll
        for (int n = 0; n < 5; ++n) xv[n] = *(const u32x2*)(x + ob + (size_t)o * 16 * M_ + n * 16); };
    f32x4 acc[4][5];
#pragma unroll
    for (int i = 0; i < 4; ++i)
#pragma unroll
        for (int n = 0; n < 5; ++n) acc[i][n] = (f32x4){0.f, 0.f, 0.f, 0.f};
    s16x4 fa[2][2][4][2], fb[2][2][5][2];
#define O_RA(B_, kh, i) do { TRRD(fa[B_][kh][i][0], a_[i], (kh) * 8192); TRRD(fa[B_][kh][i][1], a_[i], (kh) * 8192 + 4096); } while (0)
#define O_RB(B_, kh, n) do { TRRD(fb[B_][kh][n][0], ((n) & 1) ? b1_ : b0_, (n) * 32 + (kh) * 10240); TRRD(fb[B_][kh][n][1], ((n) & 1) ? b1_ : b0_, (n) * 32 + (kh) * 10240 + 5120); } while (0)
#define O_ADDR(i) const int so_ = ((i) & (NST - 1)) * STG; const bool cx_ = kt0 + (i) < NK1; const int b0_ = bE + so_, b1_ = bO + so_; int a_[4]; \
        _Pragma("unroll") for (int e = 0; e < 4; ++e) a_[e] = (cx_ ? aC ^ (512 * e) : aW ^ (32 * e)) + (so_ + lb)
#define O_WT(B_) do { _Pragma("unroll") for (int kh = 0; kh < 2; ++kh) { \
        asm volatile("s_waitcnt lgkmcnt(0)" : "+v"(fa[B_][kh][0][0]), "+v"(fa[B_][kh][0][1]), "+v"(fa[B_][kh][1][0]), "+v"(fa[B_][kh][1][1]), "+v"(fa[B_][kh][2][0]), "+v"(fa[B_][kh][2][1]), "+v"(fa[B_][kh][3][0]), "+v"(fa[B_][kh][3][1]) :: "memory"); \
        asm volatile("" : "+v"(fb[B_][kh][0][0]), "+v"(fb[B_][kh][0][1]), "+v"(fb[B_][kh][1][0]), "+v"(fb[B_][kh][1][1]), "+v"(fb[B_][kh][2][0]), "+v"(fb[B_][kh][2][1]), "+v"(fb[B_][kh][3][0]), "+v"(fb[B_][kh][3][1]), "+v"(fb[B_][kh][4][0]), "+v"(fb[B_][kh][4][1]) :: "memory"); } } while (0)
#define O_M(B_, kh, n, i) acc[i][n] = __builtin_amdgcn_mfma_f32_16x16x32_bf16(cat4(fb[B_][kh][n][0], fb[B_][kh][n][1]), cat4(fa[B_][kh][i][0], fa[B_][kh][i][1]), acc[i][n], 0, 0, 0)
#define O_G(B_, kh, n, R0, R1) do { O_M(B_, kh, n, 0); O_M(B_, kh, n, 1); R0; SCHED; O_M(B_, kh, n, 2); O_M(B_, kh, n, 3); R1; SCHED; } while (0)
#define O_IT(B_, i) do { O_ADDR((i) + 1); constexpr int N_ = (B_) ^ 1; \
        O_G(B_, 0, 0, O_RA(N_, 0, 0), O_RA(N_, 0, 1)); \
        O_G(B_, 0, 1, O_RA(N_, 0, 2), O_RA(N_, 0, 3)); \
        O_G(B_, 0, 2, O_RB(N_, 0, 0); O_RB(N_, 0, 1), O_RB(N_, 0, 2)); \
        O_G(B_, 0, 3, O_RB(N_, 0, 3); O_RB(N_, 0, 4), O_RA(N_, 1, 0)); \
        O_G(B_, 0, 4, O_RA(N_, 1, 1); O_RA(N_, 1, 2), O_RA(N_, 1, 3)); \
        O_G(B_, 1, 0, O_RB(N_, 1, 0); O_RB(N_, 1, 1), O_RB(N_, 1, 2)); \
        O_G(B_, 1, 1, O_RB(N_, 1, 3), O_RB(N_, 1, 4)); \
        O_G(B_, 1, 2, , ); O_G(B_, 1, 3, , ); O_G(B_, 1, 4, , ); \
        O_WT(N_); BAR; SCHED; } while (0)
#pragma unroll
    for (int i = 0; i < 4; ++i) asm volatile("" : "+v"(acc[i][0]), "+v"(acc[i][1]), "+v"(acc[i][2]), "+v"(acc[i][3]), "+v"(acc[i][4]));
    BAR;
    { O_ADDR(0);
#pragma unroll
      for (int kh = 0; kh < 2; ++kh) {
        O_RA(0, kh, 0); O_RA(0, kh, 1); O_RA(0, kh, 2); O_RA(0, kh, 3); O_RB(0, kh, 0); O_RB(0, kh, 1); O_RB(0, kh, 2); O_RB(0, kh, 3); O_RB(0, kh, 4); }
      O_WT(0); }
    BAR; SCHED;
    __builtin_amdgcn_s_setprio(1);
    int it = 0;
    for (; it + 1 < nk; it += 2) { O_IT(0, it); O_IT(1, it + 1); }
    if (it < nk) O_IT(0, it);
    __builtin_amdgcn_s_setprio(0);
    struct Sl { u32x4 a, b; u32x2 c; };
    const int g = tile * 4 + wid;
    const __amdgpu_buffer_rsrc_t srdP = __builtin_amdgcn_make_buffer_rsrc((void*)part, (short)0, 4 * S_ * M_ * 2, 0x00020000);
    auto pk = [&](int i, int n, int h) { const unsigned w = cvt_pk(acc[i][n][2 * h], acc[i][n][2 * h + 1]); return w < 0xfffffffeu ? w : 0xfffffffeu; };
    auto pack = [&](int i) { Sl s; s.a = (u32x4){pk(i, 0, 0), pk(i, 0, 1), pk(i, 1, 0), pk(i, 1, 1)}; s.b = (u32x4){pk(i, 2, 0), pk(i, 2, 1), pk(i, 3, 0), pk(i, 3, 1)}; s.c = (u32x2){pk(i, 4, 0), pk(i, 4, 1)}; return s; };
    auto put = [&](const Sl& s, unsigned so) {
        __builtin_amdgcn_raw_buffer_store_b128(s.a, srdP, lane * 16, so, 16); __builtin_amdgcn_raw_buffer_store_b128(s.b, srdP, lane * 16 + 1024, so, 16);
        __builtin_amdgcn_raw_buffer_store_b64(s.c, srdP, lane * 8 + 2048, so, 16); };
    auto fin = [&](int o, const Sl& p0, const Sl& p1, const Sl& p2, const Sl& p3) {
        const float bias = bf2f(bv);
#pragma unroll
        for (int n = 0; n < 5; ++n) { u32x2 wv;
#pragma unroll
            for (int e = 0; e < 2; ++e) {
                auto w = [&](const Sl& p) { return n < 2 ? p.a[2 * n + e] : n < 4 ? p.b[2 * (n & 1) + e] : p.c[e]; };
                const float lo = (bf2f(w(p0) & 0xffffu) + bf2f(w(p1) & 0xffffu)) + (bf2f(w(p2) & 0xffffu) + bf2f(w(p3) & 0xffffu));
                const float hi = (bf2f(w(p0) >> 16) + bf2f(w(p1) >> 16)) + (bf2f(w(p2) >> 16) + bf2f(w(p3) >> 16));
                wv[e] = cvt_pk(lo + bias + bf2f(xv[n][e] & 0xffffu), hi + bias + bf2f(xv[n][e] >> 16)); }
            *(u32x2*)(out + ob + (size_t)o * 16 * M_ + n * 16) = wv; } };
#pragma unroll
    for (int i = 1; i < 4; ++i) put(pack(i), (unsigned)(((g * 4 + (z ^ i)) * 3 + i - 1) * 2560));
    ldx(z);
    const Sl own = pack(0);
    Sl r[3];
#pragma unroll
    for (int i = 0; i < 3; ++i) asm volatile("" : "=v"(r[i].a), "=v"(r[i].b), "=v"(r[i].c));
#define O_GET(so, need) asm volatile( \
        "s_bitcmp1_b32 %[nd], 0\n\ts_cbranch_scc0 L_o1%=\n\tbuffer_load_dwordx4 %0, %[va], %[srd], %[s0] offen sc1\n\tbuffer_load_dwordx4 %1, %[va], %[srd], %[s0] offen offset:1024 sc1\n\tbuffer_load_dwordx2 %2, %[vb], %[srd], %[s0] offen offset:2048 sc1\n" \
        "L_o1%=:\n\ts_add_u32 %[t], %[s0], 2560\n\ts_bitcmp1_b32 %[nd], 1\n\ts_cbranch_scc0 L_o2%=\n\tbuffer_load_dwordx4 %3, %[va], %[srd], %[t] offen sc1\n\tbuffer_load_dwordx4 %4, %[va], %[srd], %[t] offen offset:1024 sc1\n\tbuffer_load_dwordx2 %5, %[vb], %[srd], %[t] offen offset:2048 sc1\n" \
        "L_o2%=:\n\ts_add_u32 %[t], %[s0], 5120\n\ts_bitcmp1_b32 %[nd], 2\n\ts_cbranch_scc0 L_o3%=\n\tbuffer_load_dwordx4 %6, %[va], %[srd], %[t] offen sc1\n\tbuffer_load_dwordx4 %7, %[va], %[srd], %[t] offen offset:1024 sc1\n\tbuffer_load_dwordx2 %8, %[vb], %[srd], %[t] offen offset:2048 sc1\n" \
        "L_o3%=:\n\ts_waitcnt vmcnt(0)" \
        : "+v"(r[0].a), "+v"(r[0].b), "+v"(r[0].c), "+v"(r[1].a), "+v"(r[1].b), "+v"(r[1].c), "+v"(r[2].a), "+v"(r[2].b), "+v"(r[2].c), [t] "=&s"(tmp_) \
        : [nd] "s"(need), [va] "v"(lane * 16), [vb] "v"(lane * 8), [srd] "s"(srdP), [s0] "s"(so) : "memory", "scc")
    unsigned need = 7u, tmp_;
    const unsigned long long tw = __builtin_amdgcn_s_memrealtime();
#pragma unroll 1
    for (;;) {
        O_GET((unsigned)((g * 4 + z) * 7680), need);
#pragma unroll
        for (int i = 0; i < 3; ++i) {
            const unsigned m = max(max(max(max(r[i].a[0], r[i].a[1]), max(r[i].a[2], r[i].a[3])), max(max(r[i].b[0], r[i].b[1]), max(r[i].b[2], r[i].b[3]))), max(r[i].c[0], r[i].c[1]));
            if (__builtin_amdgcn_ballot_w64(m == 0xffffffffu) == 0) need &= ~(1u << i); }
        if (need == 0 || __builtin_amdgcn_s_memrealtime() - tw > 20000u) break;
        __builtin_amdgcn_s_sleep(O_SLEEP);
    }
    unsigned add = 1u, old = 0u;
    if (need) { put(own, (unsigned)(7864320 + (g * 4 + z) * 2560)); WAIT_V(0); add += 256u << z; }
    if (lane == 0) old = __hip_atomic_fetch_add(cnt + g * 64, add, __ATOMIC_RELAXED, __HIP_MEMORY_SCOPE_AGENT);
    if (!need) fin(z, own, r[0], r[1], r[2]);
    old = __builtin_amdgcn_readfirstlane(old);
    if ((old & 255u) == 3u) {
        const unsigned left = (old + add) >> 8;
#pragma unroll 1
        for (int o = 0; o < 4; ++o) if ((left >> o) & 1u) {
            ldx(o);
            O_GET((unsigned)(7864320 + (g * 4 + o) * 2560), 1u);
            const Sl p0 = r[0];
            O_GET((unsigned)((g * 4 + o) * 7680), 7u);
            fin(o, p0, r[0], r[1], r[2]);
        }
    }
#undef O_GET
#undef O_RA
#undef O_RB
#undef O_ADDR
#undef O_WT
#undef O_M
#undef O_G
#undef O_IT
}

extern "C" void kernel_launch(void* const* d_in, const int* in_sizes, int n_in, void* d_out, int out_size, void* d_ws, size_t ws_size,
                              hipStream_t stream) {
    const u16* x = (const u16*)d_in[0];
    const u16* ckv = (const u16*)d_in[1];
    const u16* wq = (const u16*)d_in[2];
    const u16* wuk = (const u16*)d_in[3];
    const u16* wuv = (const u16*)d_in[4];
    const u16* wo = (const u16*)d_in[5];
    const u16* w1 = (const u16*)d_in[7];
    const u16* b1 = (const u16*)d_in[8];
    const u16* w2 = (const u16*)d_in[9];
    const u16* b2 = (const u16*)d_in[10];
    char* ws = (char*)d_ws;
    u16* xn = (u16*)(ws + wsl::XN); u16* hb = (u16*)(ws + wsl::HB); u16* q = (u16*)(ws + wsl::QO); u16* k = (u16*)(ws + wsl::KO); u16* v = (u16*)(ws + wsl::VO);
    u16* upart = (u16*)(ws + wsl::UP); u16* gT = (u16*)(ws + wsl::GT); u16* ctxT = (u16*)(ws + wsl::CT); u16* part = (u16*)(ws + wsl::PA);
    unsigned* flags = (unsigned*)(ws + wsl::FL); unsigned* cnt = (unsigned*)(ws + wsl::CN);
    (void)hipFuncSetAttribute((const void*)k_proj, hipFuncAttributeMaxDynamicSharedMemorySize, 163840);
    (void)hipFuncSetAttribute((const void*)k_attn, hipFuncAttributeMaxDynamicSharedMemorySize, at::LDS_BYTES);
    (void)hipFuncSetAttribute((const void*)k_out, hipFuncAttributeMaxDynamicSharedMemorySize, g3::LDS_BYTES);
    k_proj<<<dim3(256), dim3(256), 163840, stream>>>(x, ckv, wuk, wuv, wq, w1, xn, hb, k, v, q, upart, flags);
    k_attn<<<dim3(256), dim3(256), at::LDS_BYTES, stream>>>(ws, b1, wo, w2);
    k_out<<<dim3(256), dim3(512), g3::LDS_BYTES, stream>>>(ctxT, wo, w2, gT, part, cnt, x, b2, (u16*)d_out);
}
```
